```python
import torch

def _softmax(x, axis=-1):
    return torch.softmax(x, dim=axis)
def _gelu(x):
    return torch.nn.functional.gelu(x, approximate="tanh")
def _silu(x):
    return torch.nn.functional.silu(x)
def _sigmoid(x):
    return torch.sigmoid(x)
def _layernorm(x, axis=-1, eps=1e-5):
    m = x.mean(dim=axis, keepdim=True)
    v = x.var(dim=axis, keepdim=True, unbiased=False)
    return (x - m) / torch.sqrt(v + eps)
def _scatter_add(tgt, idx, src, axis=0):
    return tgt.clone().index_add_(axis, idx, src)
def _segment_sum(src, seg, n):
    out = torch.zeros((n,) + src.shape[1:], dtype=src.dtype)
    return out.index_add_(0, seg, src)
def _pad(x, pw):
    flat = tuple(v for pair in reversed(pw) for v in pair)
    return torch.nn.functional.pad(x, flat)

kernel_name = 'gated_mix__spectral_gfnet__spectral_gfnet__prompt'

def setup_inputs():

    _w = (lambda a: (lambda t: t.to(torch.bfloat16) if t.is_floating_point() else t)(torch.as_tensor(a)))
    def _A_setup():
        B, S, D = 1, 2048, 1024
        Df = D // 2 + 1
        return {
            "x": _w((lambda *s: torch.randn(*s, dtype=torch.bfloat16) * 0.05)(B, S, D)),
            "w_r": _w((lambda *s: torch.randn(*s, dtype=torch.bfloat16) * 0.05)(S, Df) * 0.02),
            "w_i": _w((lambda *s: torch.randn(*s, dtype=torch.bfloat16) * 0.05)(S, Df) * 0.02),
        }
    _A_in = _A_setup()

    def _B_setup():
        B, S, D = 1, 2048, 1024
        Df = D // 2 + 1
        return {
            "x": _w((lambda *s: torch.randn(*s, dtype=torch.bfloat16) * 0.05)(B, S, D)),
            "w_r": _w((lambda *s: torch.randn(*s, dtype=torch.bfloat16) * 0.05)(S, Df) * 0.02),
            "w_i": _w((lambda *s: torch.randn(*s, dtype=torch.bfloat16) * 0.05)(S, Df) * 0.02),
        }
    _B_in = _B_setup()

    return {
        "A_x": _A_in["x"],
        "A_w_r": _A_in["w_r"],
        "A_w_i": _A_in["w_i"],
        "B_x": _B_in["x"],
        "B_w_r": _B_in["w_r"],
        "B_w_i": _B_in["w_i"],
        "gate_w": _w((lambda *s: torch.randn(*s, dtype=torch.bfloat16) * 0.05)(1024) * 16.0),
    }

def reference(A_x, A_w_r, A_w_i, B_x, B_w_r, B_w_i, gate_w):
    def _A_ref(x, w_r, w_i):
        B, S, D = x.shape
        X = (lambda x, axes=(-2,-1): torch.fft.rfft2(x.float(), dim=axes))(x, axes=(-2, -1))
        W = w_r + 1j * w_i
        X = X * W
        y = (lambda x, s, axes=(-2,-1): torch.fft.irfft2(x, s=s, dim=axes).to(torch.bfloat16))(X, (S, D), axes=(-2, -1))
        return y

    def _B_ref(x, w_r, w_i):
        B, S, D = x.shape
        X = (lambda x, axes=(-2,-1): torch.fft.rfft2(x.float(), dim=axes))(x, axes=(-2, -1))
        W = w_r + 1j * w_i
        X = X * W
        y = (lambda x, s, axes=(-2,-1): torch.fft.irfft2(x, s=s, dim=axes).to(torch.bfloat16))(X, (S, D), axes=(-2, -1))
        return y

    x = A_x
    g = _sigmoid((x * gate_w).sum(-1, keepdims=True))
    a = _A_ref(x, A_w_r, A_w_i)
    b = _B_ref(x, B_w_r, B_w_i)
    return g * a + (1 - g) * b
```

```cpp
#include <hip/hip_runtime.h>
#include <hip/hip_bf16.h>
#include <stdint.h>

typedef _Float16 h2 __attribute__((ext_vector_type(2)));
typedef _Float16 h8 __attribute__((ext_vector_type(8)));
typedef __bf16 b2 __attribute__((ext_vector_type(2)));
typedef __bf16 b8 __attribute__((ext_vector_type(8)));
typedef float f2 __attribute__((ext_vector_type(2)));
typedef float f4 __attribute__((ext_vector_type(4)));
typedef float f16v __attribute__((ext_vector_type(16)));
typedef short s4 __attribute__((ext_vector_type(4)));
typedef unsigned u32;
typedef u32 u2 __attribute__((ext_vector_type(2)));
typedef u32 u4 __attribute__((ext_vector_type(4)));
typedef unsigned short us2 __attribute__((ext_vector_type(2)));
#define DI __device__ __forceinline__
#define UNROLL _Pragma("unroll")

constexpr int ZP = 4096 + 32;
constexpr int VC = 528;
constexpr int VP = 8 * VC;
constexpr size_t WS_Z = 0;
constexpr size_t WS_V = 8u << 20;
constexpr size_t WS_META = 24u << 20;
constexpr size_t WS_GATE = WS_META + 4096;

DI u32 pkh(float lo, float hi) { h2 r; r.x = (_Float16)lo; r.y = (_Float16)hi; return __builtin_bit_cast(u32, r); }
DI u32 pkb(float lo, float hi) { b2 r; r.x = (__bf16)lo; r.y = (__bf16)hi; return __builtin_bit_cast(u32, r); }
DI float bflo(u32 v) { return __builtin_bit_cast(float, v << 16); }
DI float bfhi(u32 v) { return __builtin_bit_cast(float, v & 0xffff0000u); }
DI float rbf(float v) { return (float)(__bf16)v; }
DI f16v mfma32(u4 a, u4 b, f16v c) { return __builtin_amdgcn_mfma_f32_32x32x16_f16(__builtin_bit_cast(h8, a), __builtin_bit_cast(h8, b), c, 0, 0, 0); }
DI f4 mfma16(u4 a, u4 b, f4 c) { return __builtin_amdgcn_mfma_f32_16x16x32_f16(__builtin_bit_cast(h8, a), __builtin_bit_cast(h8, b), c, 0, 0, 0); }
DI f4 mfma16b(u4 a, u4 b, f4 c) { return __builtin_amdgcn_mfma_f32_16x16x32_bf16(__builtin_bit_cast(b8, a), __builtin_bit_cast(b8, b), c, 0, 0, 0); }
DI u32 hmul(u32 a, u32 b) { return __builtin_bit_cast(u32, __builtin_bit_cast(h2, a) * __builtin_bit_cast(h2, b)); }
DI u32 hfma(u32 a, u32 b, u32 c) { return __builtin_bit_cast(u32, __builtin_elementwise_fma(__builtin_bit_cast(h2, a), __builtin_bit_cast(h2, b), __builtin_bit_cast(h2, c))); }
DI u32 pkmax(u32 a, u32 b) { return __builtin_bit_cast(u32, __builtin_elementwise_max(__builtin_bit_cast(us2, a), __builtin_bit_cast(us2, b))); }
DI float sinr(float r) { return __builtin_amdgcn_sinf(r); }
DI float cosr(float r) { return __builtin_amdgcn_cosf(r); }
DI int perm32(int n) { return (n & 0x13) | ((n & 4) << 1) | ((n & 8) >> 1); }

template <int CTRL> DI u32 dpp(u32 v) { return __builtin_amdgcn_update_dpp(v, v, CTRL, 0xf, 0xf, false); }
DI u32 wave_pkmax(u32 v) {
    v = pkmax(v, dpp<0xB1>(v));
    v = pkmax(v, dpp<0x4E>(v));
    v = pkmax(v, dpp<0x141>(v));
    v = pkmax(v, dpp<0x140>(v));
    u32 a = __builtin_amdgcn_readlane(v, 0), b = __builtin_amdgcn_readlane(v, 16);
    u32 c = __builtin_amdgcn_readlane(v, 32), d = __builtin_amdgcn_readlane(v, 48);
    u32 lo = max(max(a & 0xffffu, b & 0xffffu), max(c & 0xffffu, d & 0xffffu));
    u32 hi = max(max(a >> 16, b >> 16), max(c >> 16, d >> 16));
    return lo | (hi << 16);
}

template <bool PLANAR> DI void core_setup(int lane, u4 (&MR)[4], u4 (&MI)[4], f2 (&TW)[16]) {
    const int h = lane >> 5, c = perm32(lane & 31);
    const float fc = (float)c;
    UNROLL for (int s = 0; s < 4; ++s) {
        float cs[4], sn[4];
        UNROLL for (int i = 0; i < 4; ++i) {
            const float a = (float)(8 * s + i) + (float)(4 * h);
            const float ph = a * fc * (1.0f / 32.0f);
            cs[i] = cosr(ph); sn[i] = sinr(ph);
            const float pt = a * fc * (1.0f / 1024.0f);
            TW[4 * s + i] = f2{cosr(pt) * 0.125f, sinr(pt) * 0.125f};
        }
        if (PLANAR) {
            MR[s] = u4{pkh(cs[0], cs[1]), pkh(cs[2], cs[3]), pkh(sn[0], sn[1]), pkh(sn[2], sn[3])};
            MI[s] = u4{MR[s].z ^ 0x80008000u, MR[s].w ^ 0x80008000u, MR[s].x, MR[s].y};
        } else {
            MR[s] = u4{pkh(cs[0], sn[0]), pkh(cs[1], sn[1]), pkh(cs[2], sn[2]), pkh(cs[3], sn[3])};
            MI[s] = u4{pkh(-sn[0], cs[0]), pkh(-sn[1], cs[1]), pkh(-sn[2], cs[2]), pkh(-sn[3], cs[3])};
        }
    }
}
template <bool PLANAR, bool OUT_B> DI void core_fft(const u4 (&in)[4], const u4 (&MR)[4], const u4 (&MI)[4], const f2 (&TW)[16], f16v& outr, f16v& outi) {
    f16v yr = {}, yi = {};
    UNROLL for (int s = 0; s < 4; ++s) { yr = mfma32(in[s], MR[s], yr); yi = mfma32(in[s], MI[s], yi); }
    outr = f16v{}; outi = f16v{};
    UNROLL for (int s = 0; s < 4; ++s) {
        float pr[4], pi[4];
        UNROLL for (int i = 0; i < 4; ++i) {
            const int r = 4 * s + i;
            pr[i] = yr[r] * TW[r].x + yi[r] * TW[r].y;
            pi[i] = yi[r] * TW[r].x - yr[r] * TW[r].y;
        }
        u4 d;
        if (PLANAR) d = u4{pkh(pr[0], pr[1]), pkh(pr[2], pr[3]), pkh(pi[0], pi[1]), pkh(pi[2], pi[3])};
        else        d = u4{pkh(pr[0], pi[0]), pkh(pr[1], pi[1]), pkh(pr[2], pi[2]), pkh(pr[3], pi[3])};
        if (OUT_B) { outr = mfma32(MR[s], d, outr); outi = mfma32(MI[s], d, outi); }
        else       { outr = mfma32(d, MR[s], outr); outi = mfma32(d, MI[s], outi); }
    }
}

__global__ __launch_bounds__(256) void k1_rows(const u2* __restrict__ x, const u2* __restrict__ gw,
                                               const u4* __restrict__ w0, const u4* __restrict__ w1, const u4* __restrict__ w2, const u4* __restrict__ w3,
                                               u32* __restrict__ Z, u32* __restrict__ meta, float* __restrict__ gate) {
    __shared__ __attribute__((aligned(16))) u32 lds[4096 + 64];
    const int t = threadIdx.x, lane = t & 63, wv = __builtin_amdgcn_readfirstlane(t >> 6), s2 = blockIdx.x;
    u2 xv[8];
    UNROLL for (int s1 = 0; s1 < 8; ++s1) xv[s1] = x[(256 * s1 + s2) * 256 + t];
    const u2 gv = gw[t];
    const u4* wp = wv == 0 ? w0 : wv == 1 ? w1 : wv == 2 ? w2 : w3;
    u4 wvv[8]; u4 wx;
    {
        const int cls = s2 & 7, g0 = (s2 >> 3) * 8;
        UNROLL for (int i = 0; i < 8; ++i) wvv[i] = wp[(g0 + i) * 513 + cls * 64 + lane];
        wx = wp[(g0 + (lane & 7)) * 513 + 512];
    }
    u4 MR[4], MI[4]; f2 TW[16];
    core_setup<true>(lane, MR, MI, TW);

    u32 mx = 0;
    UNROLL for (int s1 = 0; s1 < 8; ++s1) mx = pkmax(mx, pkmax(xv[s1].x & 0x7fff7fffu, xv[s1].y & 0x7fff7fffu));
    mx = wave_pkmax(mx);
    int ew = min(max((int)(max(mx & 0xffffu, mx >> 16) >> 7), 8), 250);
    const float sc = __builtin_bit_cast(float, (u32)(253 - ew) << 23);
    float o[8][4];
    UNROLL for (int j = 0; j < 4; ++j) {
        float v[8];
        UNROLL for (int s1 = 0; s1 < 8; ++s1) { const u32 q = (j & 2) ? xv[s1].y : xv[s1].x; v[s1] = ((j & 1) ? bfhi(q) : bflo(q)) * sc; }
        const float a0 = v[0] + v[4], a1 = v[1] + v[5], a2 = v[2] + v[6], a3 = v[3] + v[7];
        const float b0 = v[0] - v[4], b1 = v[1] - v[5], b2_ = v[2] - v[6], b3 = v[3] - v[7];
        const float e0 = a0 + a2, e1 = a1 + a3, dm = b1 - b3, dp = b1 + b3;
        const float r = 0.70710678118654752f;
        o[0][j] = e0 + e1; o[1][j] = e0 - e1;
        o[2][j] = b0 + r * dm; o[3][j] = -b2_ - r * dp;
        o[4][j] = a0 - a2; o[5][j] = a3 - a1;
        o[6][j] = b0 - r * dm; o[7][j] = b2_ - r * dp;
    }
    UNROLL for (int p = 0; p < 8; ++p) *(u2*)&lds[p * 512 + 2 * t] = u2{pkh(o[p][0], o[p][1]), pkh(o[p][2], o[p][3])};
    if (lane == 0) lds[4096 + wv] = ew;
    __syncthreads();
    const u4 ews = *(const u4*)&lds[4096];
    const int emax = max(max((int)ews.x, (int)ews.y), max((int)ews.z, (int)ews.w));
    u4 in[4];
    {
        const int h = lane >> 5, blk = (lane >> 4) & 1, q = (lane & 15) >> 2, p = lane & 3;
        const int off = wv * 1024 + (4 * h + q) * 16 + blk * 8 + p * 2;
        UNROLL for (int s = 0; s < 4; ++s) {
            const u2 re = __builtin_bit_cast(u2, __builtin_amdgcn_ds_read_tr16_b64_v4i16((__attribute__((address_space(3))) s4*)&lds[off + s * 128]));
            const u2 im = __builtin_bit_cast(u2, __builtin_amdgcn_ds_read_tr16_b64_v4i16((__attribute__((address_space(3))) s4*)&lds[off + s * 128 + 512]));
            const u32 f = (u32)max((int)ews[s] - emax + 15, 0) * 0x04000400u;
            in[s] = u4{hmul(re.x, f), hmul(re.y, f), hmul(im.x, f), hmul(im.y, f)};
        }
    }
    f16v zr, zi;
    core_fft<true, true>(in, MR, MI, TW, zr, zi);
    {
        u32* zp = Z + s2 * ZP + wv * 1024 + perm32(lane & 31) + (lane >> 5) * 256;
        UNROLL for (int r = 0; r < 16; ++r) zp[32 * (16 * (r >> 3) + (r & 7))] = pkh(zr[r], zi[r]);
    }
    float gs[8];
    {
        const float g0 = bflo(gv.x), g1 = bfhi(gv.x), g2 = bflo(gv.y), g3 = bfhi(gv.y);
        const b2 one = {(__bf16)1.0f, (__bf16)1.0f};
        UNROLL for (int s1 = 0; s1 < 8; ++s1) {
            const u32 p0 = pkb(bflo(xv[s1].x) * g0, bfhi(xv[s1].x) * g1), p1 = pkb(bflo(xv[s1].y) * g2, bfhi(xv[s1].y) * g3);
            float a = __builtin_amdgcn_fdot2_f32_bf16(__builtin_bit_cast(b2, p0), one, 0.0f, false);
            a = __builtin_amdgcn_fdot2_f32_bf16(__builtin_bit_cast(b2, p1), one, a, false);
            UNROLL for (int m = 1; m < 64; m <<= 1) a += __shfl_xor(a, m);
            gs[s1] = a;
        }
    }
    u32 mw = pkmax(pkmax(wx.x & 0x7fff7fffu, wx.y & 0x7fff7fffu), pkmax(wx.z & 0x7fff7fffu, wx.w & 0x7fff7fffu));
    UNROLL for (int i = 0; i < 8; ++i) mw = pkmax(mw, pkmax(pkmax(wvv[i].x & 0x7fff7fffu, wvv[i].y & 0x7fff7fffu), pkmax(wvv[i].z & 0x7fff7fffu, wvv[i].w & 0x7fff7fffu)));
    mw = wave_pkmax(mw);
    if (lane == 0) {
        UNROLL for (int s1 = 0; s1 < 8; ++s1) lds[4100 + s1 * 4 + wv] = __builtin_bit_cast(u32, gs[s1]);
        lds[4132 + wv] = max(mw & 0xffffu, mw >> 16) >> 7;
    }
    __syncthreads();
    if (t < 8) {
        const f4 ps = *(const f4*)&lds[4100 + t * 4];
        const float lg = rbf((ps.x + ps.y) + (ps.z + ps.w));
        const float g = rbf(1.0f / (1.0f + __expf(-lg)));
        *(f2*)&gate[s2 * 16 + 2 * t] = f2{g, rbf(1.0f - g)};
        if (t == 0) {
            const u4 e = *(const u4*)&lds[4132];
            meta[s2] = (u32)emax | (min(max(max(e.x, e.y), max(e.z, e.w)), 240u) << 16);
        }
    }
}

__global__ __launch_bounds__(256) void k2_cols(const u32* __restrict__ Z, const u32* __restrict__ meta,
                                               const unsigned short* __restrict__ war, const unsigned short* __restrict__ wai,
                                               const unsigned short* __restrict__ wbr, const unsigned short* __restrict__ wbi,
                                               u2* __restrict__ V) {
    __shared__ __attribute__((aligned(16))) u4 lds[1024 + 2048];
    const int t = threadIdx.x, lane = t & 63, wv = __builtin_amdgcn_readfirstlane(t >> 6);
    const int cls = blockIdx.x & 7, tile = blockIdx.x >> 3;
    const int k0 = min(tile * 16, 497);
    const int q = lane >> 4, n = lane & 15;
    const bool dir = cls <= 4, mir = cls >= 4 || cls == 0;
    const int row = (cls & 3) == 0 ? 0 : cls < 4 ? cls : 8 - cls;

    const u4 mall = *(const u4*)&meta[4 * lane];
    u4 mrow[4];
    UNROLL for (int j = 0; j < 4; ++j) mrow[j] = *(const u4*)&meta[64 * q + 16 * j + 4 * wv];
    u32 zd[4][4], zm[4][4];
    {
        const u32* zb = Z + (64 * q + 4 * wv) * ZP + row * 1024;
        if (dir) { UNROLL for (int i = 0; i < 4; ++i) UNROLL for (int j = 0; j < 4; ++j) zd[i][j] = zb[(16 * j + i) * ZP + k0 + n]; }
        if (mir) { UNROLL for (int i = 0; i < 4; ++i) UNROLL for (int j = 0; j < 4; ++j) zm[i][j] = zb[(16 * j + i) * ZP + ((1024 - k0 - n) & 1023)]; }
    }
    u4 wa[4], wb[4];
    {
        const int off = (cls + 8 * (4 * wv + 16 * n)) * 513 + k0 + 8 * (q & 1);
        const unsigned short* pa = ((q & 2) ? wai : war) + off;
        const unsigned short* pb = ((q & 2) ? wbi : wbr) + off;
        UNROLL for (int i = 0; i < 4; ++i) {
            __builtin_memcpy(&wa[i], pa + 8 * 513 * i, 16);
            __builtin_memcpy(&wb[i], pb + 8 * 513 * i, 16);
        }
    }
    u4 GR[4], GI[4], HR[4], HI[4], FR, FI;
    {
        const float ofs = cls == 4 ? 0.25f : 0.0f;
        UNROLL for (int i = 0; i < 4; ++i) UNROLL for (int j = 0; j < 4; ++j) {
            const int b = 4 * wv + i;
            const float g = (float)((16 * (4 * q + j) + b) * (cls + 8 * n)) * (1.0f / 2048.0f) + ofs;
            const float gc = cosr(g), gsn = sinr(g);
            GR[i][j] = pkh(gc, gsn); GI[i][j] = pkh(-gsn, gc);
            const float hh = (float)((16 * n + b) * (cls + 8 * (4 * q + j))) * (1.0f / 2048.0f);
            const float hc = cosr(hh), hs = sinr(hh);
            HR[i][j] = pkh(hc, hs); HI[i][j] = pkh(-hs, hc);
        }
        UNROLL for (int j = 0; j < 4; ++j) {
            const float f = (float)(n * (4 * q + j)) * (1.0f / 16.0f);
            const float fc = cosr(f), fs = sinr(f);
            FR[j] = pkh(fc, fs); FI[j] = pkh(-fs, fc);
        }
    }
    const u32 mm = wave_pkmax(pkmax(pkmax(mall.x, mall.y), pkmax(mall.z, mall.w)));
    const int emax = mm & 0xffff, ewm = mm >> 16;
    {
        const u32 sgn = cls == 4 ? 0x00008000u : 0x80000000u;
        u32 y[4][4];
        UNROLL for (int i = 0; i < 4; ++i) {
            u4 d;
            UNROLL for (int j = 0; j < 4; ++j) {
                const u32 f = (u32)max((int)(mrow[j][i] & 0xffff) - emax + 15, 0) * 0x04000400u;
                if (dir && mir) d[j] = hfma(zm[i][j], f | sgn, hmul(zd[i][j], f));
                else if (dir) d[j] = hmul(zd[i][j], f);
                else d[j] = hmul(zm[i][j], f | sgn);
            }
            const f4 yr = mfma16(GR[i], d, f4{}), yi = mfma16(GI[i], d, f4{});
            UNROLL for (int r = 0; r < 4; ++r) y[r][i] = pkh(yr[r], yi[r]);
        }
        UNROLL for (int r = 0; r < 4; ++r) lds[(wv * 16 + 4 * q + r) * 16 + n] = u4{y[r][0], y[r][1], y[r][2], y[r][3]};
    }
    __syncthreads();
    {
        const u32 sw = (u32)(127 + 118 - ewm - ((cls & 3) == 0 ? 1 : 0)) << 7;
        u4 SR, SI;
        UNROLL for (int j = 0; j < 4; ++j) {
            const u32 v = (8 * (q & 1) + 2 * j == n ? sw : 0u) | (8 * (q & 1) + 2 * j + 1 == n ? sw << 16 : 0u);
            SR[j] = (q & 2) ? 0u : v; SI[j] = (q & 2) ? v : 0u;
        }
        u32 ra[4][4], rb[4][4];
        UNROLL for (int i = 0; i < 4; ++i) {
            const u4 d = lds[(q * 16 + 4 * wv + i) * 16 + n];
            const f4 xr = mfma16(FR, d, f4{}), xi = mfma16(FI, d, f4{});
            const f4 ar = mfma16b(wa[i], SR, f4{}), ai = mfma16b(wa[i], SI, f4{});
            const f4 br = mfma16b(wb[i], SR, f4{}), bi = mfma16b(wb[i], SI, f4{});
            u4 ya, yb;
            UNROLL for (int r = 0; r < 4; ++r) {
                ya[r] = pkh(xr[r] * ai[r] + xi[r] * ar[r], xr[r] * ar[r] - xi[r] * ai[r]);
                yb[r] = pkh(xr[r] * bi[r] + xi[r] * br[r], xr[r] * br[r] - xi[r] * bi[r]);
            }
            const f4 p0 = mfma16(FR, ya, f4{}), p1 = mfma16(FI, ya, f4{});
            const f4 p2 = mfma16(FR, yb, f4{}), p3 = mfma16(FI, yb, f4{});
            UNROLL for (int r = 0; r < 4; ++r) { ra[r][i] = pkh(p0[r], p1[r]); rb[r][i] = pkh(p2[r], p3[r]); }
        }
        UNROLL for (int r = 0; r < 4; ++r) {
            lds[1024 + (wv * 16 + 4 * q + r) * 16 + n] = u4{ra[r][0], ra[r][1], ra[r][2], ra[r][3]};
            lds[2048 + (wv * 16 + 4 * q + r) * 16 + n] = u4{rb[r][0], rb[r][1], rb[r][2], rb[r][3]};
        }
    }
    __syncthreads();
    {
        u2* vp = V + (64 * q + 4 * wv) * VP + cls * VC + k0 + n;
        const bool ok = tile < 32 || n == 15;
        UNROLL for (int i = 0; i < 4; ++i) {
            const u4 da = lds[1024 + (q * 16 + 4 * wv + i) * 16 + n], db = lds[2048 + (q * 16 + 4 * wv + i) * 16 + n];
            const f4 p0 = mfma16(HR[i], da, f4{}), p1 = mfma16(HI[i], da, f4{});
            const f4 p2 = mfma16(HR[i], db, f4{}), p3 = mfma16(HI[i], db, f4{});
            if (ok) { UNROLL for (int r = 0; r < 4; ++r) vp[(16 * r + i) * VP] = u2{pkh(p0[r], p1[r]), pkh(p2[r], p3[r])}; }
        }
    }
}

__global__ __launch_bounds__(256) void k3_rows(const u2* __restrict__ V, const u32* __restrict__ meta, const float* __restrict__ gate, u4* __restrict__ out) {
    __shared__ __attribute__((aligned(16))) u4 lds[1024];
    const int t = threadIdx.x, lane = t & 63, wv = __builtin_amdgcn_readfirstlane(t >> 6), s2 = blockIdx.x;
    const int q = lane >> 4, n = lane & 15;
    const f4 gg = *(const f4*)&gate[s2 * 16 + (n >> 2) * 4];
    const u4 mall = *(const u4*)&meta[4 * lane];
    u2 v0[9], v1[9];
    {
        const u2* vp = V + s2 * VP + 2 * q * VC + 128 * wv;
        UNROLL for (int i = 0; i < 8; ++i) { v0[i] = vp[16 * i + n]; v1[i] = vp[VC + 16 * i + n]; }
        v0[8] = vp[128]; v1[8] = vp[VC + 128];
    }
    u4 MR[4], MI[4]; f2 TW[16];
    core_setup<false>(lane, MR, MI, TW);
    u4 PM;
    {
        const int r = n & 3, sa = (n >> 2) * 2;
        UNROLL for (int cc = 0; cc < 2; ++cc) {
            const int u1 = 2 * q + cc;
            const float ta = (float)(sa * u1) * 0.125f + ((r & 1) ? 0.25f : (r & 2) ? 0.5f : 0.0f);
            const float tb = (float)((sa + 1) * u1) * 0.125f + ((r & 1) ? ((r & 2) ? 0.0f : 0.5f) : 0.25f);
            const float ca = cosr(ta) * 0.25f, sa_ = sinr(ta) * 0.25f, cb = cosr(tb) * 0.25f, sb_ = sinr(tb) * 0.25f;
            PM[2 * cc]     = pkh(gg.x * ca + gg.z * cb, gg.x * sa_ + gg.z * sb_);
            PM[2 * cc + 1] = pkh(gg.y * ca + gg.w * cb, gg.y * sa_ + gg.w * sb_);
        }
    }
    {
        u32 dd[8], mm[9];
        UNROLL for (int i = 0; i < 9; ++i) {
            f4 z = mfma16(PM, u4{v0[i].x, v0[i].y, v1[i].x, v1[i].y}, f4{});
            if (i == 0 && wv == 0 && n == 0) { z.x = 0.5f * (z.x + z.z); z.y = 0.5f * (z.y + z.w); }
            if (i == 8 && wv == 3) { z.z = 0.5f * (z.x + z.z); z.w = 0.5f * (z.y + z.w); }
            if (i < 8) dd[i] = pkh(z.x, z.y);
            mm[i] = pkh(z.z, z.w);
        }
        lds[(q * 8 + wv) * 32 + n]      = u4{dd[0], dd[2], dd[4], dd[6]};
        lds[(q * 8 + wv) * 32 + 16 + n] = u4{dd[1], dd[3], dd[5], dd[7]};
        lds[(q * 8 + 7 - wv) * 32 + ((32 - n) & 31)] = n == 0 ? u4{mm[8], mm[6], mm[4], mm[2]} : u4{mm[6], mm[4], mm[2], mm[0]};
        lds[(q * 8 + 7 - wv) * 32 + 16 - n] = u4{mm[7], mm[5], mm[3], mm[1]};
    }
    const u32 mx = wave_pkmax(pkmax(pkmax(mall.x, mall.y), pkmax(mall.z, mall.w)));
    const int esc = (int)(mx & 0xffff) + (int)(mx >> 16) - 257;
    __syncthreads();
    u4 in[4];
    UNROLL for (int s = 0; s < 4; ++s) in[s] = lds[wv * 256 + s * 64 + lane];
    f16v zr, zi;
    core_fft<false, false>(in, MR, MI, TW, zr, zi);
    {
        u4* op = out + (512 * wv + s2) * 128 + 4 * perm32(lane & 31) + (lane >> 5);
        UNROLL for (int hf = 0; hf < 2; ++hf) {
            u4 a, b;
            UNROLL for (int j = 0; j < 4; ++j) {
                a[j] = pkb(__builtin_amdgcn_ldexpf(zi[8 * hf + 2 * j], esc), __builtin_amdgcn_ldexpf(zi[8 * hf + 2 * j + 1], esc));
                b[j] = pkb(__builtin_amdgcn_ldexpf(zr[8 * hf + 2 * j], esc), __builtin_amdgcn_ldexpf(zr[8 * hf + 2 * j + 1], esc));
            }
            op[2 * hf] = a;
            op[256 * 128 + 2 * hf] = b;
        }
    }
}

extern "C" void kernel_launch(void* const* d_in, const int* in_sizes, int n_in,
                              void* d_out, int out_size, void* d_ws, size_t ws_size,
                              hipStream_t stream) {
    char* ws = (char*)d_ws;
    u32* Z = (u32*)(ws + WS_Z); u2* V = (u2*)(ws + WS_V); u32* meta = (u32*)(ws + WS_META); float* gate = (float*)(ws + WS_GATE);
    k1_rows<<<256, 256, 0, stream>>>((const u2*)d_in[0], (const u2*)d_in[6], (const u4*)d_in[1], (const u4*)d_in[2], (const u4*)d_in[4], (const u4*)d_in[5], Z, meta, gate);
    k2_cols<<<264, 256, 0, stream>>>(Z, meta, (const unsigned short*)d_in[1], (const unsigned short*)d_in[2], (const unsigned short*)d_in[4], (const unsigned short*)d_in[5], V);
    k3_rows<<<256, 256, 0, stream>>>(V, meta, gate, (u4*)d_out);
}
```

```cpp
#include <hip/hip_runtime.h>
#include <hip/hip_bf16.h>
#include <stdint.h>

typedef _Float16 h2 __attribute__((ext_vector_type(2)));
typedef _Float16 h8 __attribute__((ext_vector_type(8)));
typedef __bf16 b2 __attribute__((ext_vector_type(2)));
typedef __bf16 b8 __attribute__((ext_vector_type(8)));
typedef float f2 __attribute__((ext_vector_type(2)));
typedef float f4 __attribute__((ext_vector_type(4)));
typedef float f16v __attribute__((ext_vector_type(16)));
typedef short s4 __attribute__((ext_vector_type(4)));
typedef unsigned u32;
typedef u32 u2 __attribute__((ext_vector_type(2)));
typedef u32 u4 __attribute__((ext_vector_type(4)));
typedef unsigned short us2 __attribute__((ext_vector_type(2)));
#define DI __device__ __forceinline__
#define UNROLL _Pragma("unroll")

constexpr int ZP = 4096 + 32;
constexpr int VC = 528;
constexpr int VP = 8 * VC;
constexpr size_t WS_Z = 0;
constexpr size_t WS_V = 8u << 20;
constexpr size_t WS_META = 24u << 20;
constexpr size_t WS_GATE = WS_META + 4096;

DI u32 pkh(float lo, float hi) { h2 r; r.x = (_Float16)lo; r.y = (_Float16)hi; return __builtin_bit_cast(u32, r); }
DI u32 pkb(float lo, float hi) { b2 r; r.x = (__bf16)lo; r.y = (__bf16)hi; return __builtin_bit_cast(u32, r); }
DI float bflo(u32 v) { return __builtin_bit_cast(float, v << 16); }
DI float bfhi(u32 v) { return __builtin_bit_cast(float, v & 0xffff0000u); }
DI float rbf(float v) { return (float)(__bf16)v; }
DI f16v mfma32(u4 a, u4 b, f16v c) { return __builtin_amdgcn_mfma_f32_32x32x16_f16(__builtin_bit_cast(h8, a), __builtin_bit_cast(h8, b), c, 0, 0, 0); }
DI f4 mfma16(u4 a, u4 b, f4 c) { return __builtin_amdgcn_mfma_f32_16x16x32_f16(__builtin_bit_cast(h8, a), __builtin_bit_cast(h8, b), c, 0, 0, 0); }
DI f4 mfma16b(u4 a, u4 b, f4 c) { return __builtin_amdgcn_mfma_f32_16x16x32_bf16(__builtin_bit_cast(b8, a), __builtin_bit_cast(b8, b), c, 0, 0, 0); }
DI u32 hmul(u32 a, u32 b) { return __builtin_bit_cast(u32, __builtin_bit_cast(h2, a) * __builtin_bit_cast(h2, b)); }
DI u32 hfma(u32 a, u32 b, u32 c) { return __builtin_bit_cast(u32, __builtin_elementwise_fma(__builtin_bit_cast(h2, a), __builtin_bit_cast(h2, b), __builtin_bit_cast(h2, c))); }
DI u32 pkmax(u32 a, u32 b) { return __builtin_bit_cast(u32, __builtin_elementwise_max(__builtin_bit_cast(us2, a), __builtin_bit_cast(us2, b))); }
DI float sinr(float r) { return __builtin_amdgcn_sinf(r); }
DI float cosr(float r) { return __builtin_amdgcn_cosf(r); }
DI int perm32(int n) { return (n & 0x13) | ((n & 4) << 1) | ((n & 8) >> 1); }

typedef __amdgpu_buffer_rsrc_t rsrc_t;
DI rsrc_t mkrs(const void* p, u32 bytes) { return __builtin_amdgcn_make_buffer_rsrc((void*)p, (short)0, (int)bytes, 0x00020000); }
#define SB() __builtin_amdgcn_sched_barrier(0)
#define STAMP_DECL
#define STAMP(i)
#define STAMP_VM(i)
#define STAMP_OUT(kid)
template <int CTRL> DI u32 dpp(u32 v) { return __builtin_amdgcn_update_dpp(v, v, CTRL, 0xf, 0xf, false); }
DI u32 wave_pkmax(u32 v) {
    v = pkmax(v, dpp<0xB1>(v));
    v = pkmax(v, dpp<0x4E>(v));
    v = pkmax(v, dpp<0x141>(v));
    v = pkmax(v, dpp<0x140>(v));
    u32 a = __builtin_amdgcn_readlane(v, 0), b = __builtin_amdgcn_readlane(v, 16);
    u32 c = __builtin_amdgcn_readlane(v, 32), d = __builtin_amdgcn_readlane(v, 48);
    u32 lo = max(max(a & 0xffffu, b & 0xffffu), max(c & 0xffffu, d & 0xffffu));
    u32 hi = max(max(a >> 16, b >> 16), max(c >> 16, d >> 16));
    return lo | (hi << 16);
}

template <bool PLANAR> DI void core_setup(int lane, u4 (&MR)[4], u4 (&MI)[4], f2 (&TW)[16]) {
    const int h = lane >> 5, c = perm32(lane & 31);
    const float fc = (float)c;
    UNROLL for (int s = 0; s < 4; ++s) {
        float cs[4], sn[4];
        UNROLL for (int i = 0; i < 4; ++i) {
            const float a = (float)(8 * s + i) + (float)(4 * h);
            const float ph = a * fc * (1.0f / 32.0f);
            cs[i] = cosr(ph); sn[i] = sinr(ph);
            const float pt = a * fc * (1.0f / 1024.0f);
            TW[4 * s + i] = f2{cosr(pt) * 0.125f, sinr(pt) * 0.125f};
        }
        if (PLANAR) {
            MR[s] = u4{pkh(cs[0], cs[1]), pkh(cs[2], cs[3]), pkh(sn[0], sn[1]), pkh(sn[2], sn[3])};
            MI[s] = u4{MR[s].z ^ 0x80008000u, MR[s].w ^ 0x80008000u, MR[s].x, MR[s].y};
        } else {
            MR[s] = u4{pkh(cs[0], sn[0]), pkh(cs[1], sn[1]), pkh(cs[2], sn[2]), pkh(cs[3], sn[3])};
            MI[s] = u4{pkh(-sn[0], cs[0]), pkh(-sn[1], cs[1]), pkh(-sn[2], cs[2]), pkh(-sn[3], cs[3])};
        }
    }
}
template <bool PLANAR, bool OUT_B> DI void core_fft(const u4 (&in)[4], const u4 (&MR)[4], const u4 (&MI)[4], const f2 (&TW)[16], f16v& outr, f16v& outi) {
    f16v yr = {}, yi = {};
    UNROLL for (int s = 0; s < 4; ++s) { yr = mfma32(in[s], MR[s], yr); yi = mfma32(in[s], MI[s], yi); }
    outr = f16v{}; outi = f16v{};
    UNROLL for (int s = 0; s < 4; ++s) {
        float pr[4], pi[4];
        UNROLL for (int i = 0; i < 4; ++i) {
            const int r = 4 * s + i;
            pr[i] = yr[r] * TW[r].x + yi[r] * TW[r].y;
            pi[i] = yi[r] * TW[r].x - yr[r] * TW[r].y;
        }
        u4 d;
        if (PLANAR) d = u4{pkh(pr[0], pr[1]), pkh(pr[2], pr[3]), pkh(pi[0], pi[1]), pkh(pi[2], pi[3])};
        else        d = u4{pkh(pr[0], pi[0]), pkh(pr[1], pi[1]), pkh(pr[2], pi[2]), pkh(pr[3], pi[3])};
        if (OUT_B) { outr = mfma32(MR[s], d, outr); outi = mfma32(MI[s], d, outi); }
        else       { outr = mfma32(d, MR[s], outr); outi = mfma32(d, MI[s], outi); }
    }
}

__global__ __launch_bounds__(256, 2) void k1_rows(const u2* __restrict__ x, const u2* __restrict__ gw,
                                               const u4* __restrict__ w0, const u4* __restrict__ w1, const u4* __restrict__ w2, const u4* __restrict__ w3,
                                               u32* __restrict__ Z, u32* __restrict__ meta, float* __restrict__ gate) {
    __shared__ __attribute__((aligned(16))) u32 lds[4096 + 64];
    STAMP_DECL;
    STAMP(0);
    const int t = threadIdx.x, lane = t & 63, wv = __builtin_amdgcn_readfirstlane(t >> 6), s2 = blockIdx.x;
    u2 xv[8];
    {
        const rsrc_t rx = mkrs(x, 2048 * 2048);
        UNROLL for (int s1 = 0; s1 < 8; ++s1) xv[s1] = __builtin_amdgcn_raw_buffer_load_b64(rx, t * 8, (256 * s1 + s2) * 2048, 0);
    }
    const u2 gv = gw[t];
    u4 wvv[8]; u4 wx;
    {
        const u4* wp = wv == 0 ? w0 : wv == 1 ? w1 : wv == 2 ? w2 : w3;
        const rsrc_t rw = mkrs(wp, 2048 * 1026);
        const int cls = s2 & 7, g0 = (s2 >> 3) * 8;
        UNROLL for (int i = 0; i < 8; ++i) wvv[i] = __builtin_amdgcn_raw_buffer_load_b128(rw, lane * 16, ((g0 + i) * 513 + cls * 64) * 16, 0);
        wx = __builtin_amdgcn_raw_buffer_load_b128(rw, (lane & 7) * 8208, (g0 * 513 + 512) * 16, 0);
    }
    SB();
    u4 MR[4], MI[4]; f2 TW[16];
    core_setup<true>(lane, MR, MI, TW);
    STAMP(1);
    SB();

    u32 mx = 0;
    UNROLL for (int s1 = 0; s1 < 8; ++s1) mx = pkmax(mx, pkmax(xv[s1].x & 0x7fff7fffu, xv[s1].y & 0x7fff7fffu));
    mx = wave_pkmax(mx);
    STAMP(2);
    int ew = min(max((int)(max(mx & 0xffffu, mx >> 16) >> 7), 8), 250);
    const float sc = __builtin_bit_cast(float, (u32)(253 - ew) << 23);
    float o[8][4];
    UNROLL for (int j = 0; j < 4; ++j) {
        float v[8];
        UNROLL for (int s1 = 0; s1 < 8; ++s1) { const u32 q = (j & 2) ? xv[s1].y : xv[s1].x; v[s1] = ((j & 1) ? bfhi(q) : bflo(q)) * sc; }
        const float a0 = v[0] + v[4], a1 = v[1] + v[5], a2 = v[2] + v[6], a3 = v[3] + v[7];
        const float b0 = v[0] - v[4], b1 = v[1] - v[5], b2_ = v[2] - v[6], b3 = v[3] - v[7];
        const float e0 = a0 + a2, e1 = a1 + a3, dm = b1 - b3, dp = b1 + b3;
        const float r = 0.70710678118654752f;
        o[0][j] = e0 + e1; o[1][j] = e0 - e1;
        o[2][j] = b0 + r * dm; o[3][j] = -b2_ - r * dp;
        o[4][j] = a0 - a2; o[5][j] = a3 - a1;
        o[6][j] = b0 - r * dm; o[7][j] = b2_ - r * dp;
    }
    UNROLL for (int p = 0; p < 8; ++p) *(u2*)&lds[p * 512 + 2 * t] = u2{pkh(o[p][0], o[p][1]), pkh(o[p][2], o[p][3])};
    if (lane == 0) lds[4096 + wv] = ew;
    __syncthreads();
    STAMP(3);
    const u4 ews = *(const u4*)&lds[4096];
    const int emax = max(max((int)ews.x, (int)ews.y), max((int)ews.z, (int)ews.w));
    u4 in[4];
    {
        const int h = lane >> 5, blk = (lane >> 4) & 1, q = (lane & 15) >> 2, p = lane & 3;
        const int off = wv * 1024 + (4 * h + q) * 16 + blk * 8 + p * 2;
        UNROLL for (int s = 0; s < 4; ++s) {
            const u2 re = __builtin_bit_cast(u2, __builtin_amdgcn_ds_read_tr16_b64_v4i16((__attribute__((address_space(3))) s4*)&lds[off + s * 128]));
            const u2 im = __builtin_bit_cast(u2, __builtin_amdgcn_ds_read_tr16_b64_v4i16((__attribute__((address_space(3))) s4*)&lds[off + s * 128 + 512]));
            const u32 f = (u32)max((int)ews[s] - emax + 15, 0) * 0x04000400u;
            in[s] = u4{hmul(re.x, f), hmul(re.y, f), hmul(im.x, f), hmul(im.y, f)};
        }
    }
    f16v zr, zi;
    core_fft<true, true>(in, MR, MI, TW, zr, zi);
    {
        u32* zp = Z + s2 * ZP + wv * 1024 + perm32(lane & 31) + (lane >> 5) * 256;
        UNROLL for (int r = 0; r < 16; ++r) zp[32 * (16 * (r >> 3) + (r & 7))] = pkh(zr[r], zi[r]);
    }
    STAMP(4);
    SB();
    float gs[8];
    {
        const float g0 = bflo(gv.x), g1 = bfhi(gv.x), g2 = bflo(gv.y), g3 = bfhi(gv.y);
        const b2 one = {(__bf16)1.0f, (__bf16)1.0f};
        UNROLL for (int s1 = 0; s1 < 8; ++s1) {
            const u32 p0 = pkb(bflo(xv[s1].x) * g0, bfhi(xv[s1].x) * g1), p1 = pkb(bflo(xv[s1].y) * g2, bfhi(xv[s1].y) * g3);
            float a = __builtin_amdgcn_fdot2_f32_bf16(__builtin_bit_cast(b2, p0), one, 0.0f, false);
            gs[s1] = __builtin_amdgcn_fdot2_f32_bf16(__builtin_bit_cast(b2, p1), one, a, false);
        }
        UNROLL for (int s1 = 0; s1 < 8; ++s1) {
            float a = gs[s1];
            a += __builtin_bit_cast(float, dpp<0xB1>(__builtin_bit_cast(u32, a)));
            a += __builtin_bit_cast(float, dpp<0x4E>(__builtin_bit_cast(u32, a)));
            a += __builtin_bit_cast(float, dpp<0x141>(__builtin_bit_cast(u32, a)));
            a += __builtin_bit_cast(float, dpp<0x140>(__builtin_bit_cast(u32, a)));
            gs[s1] = a;
        }
    }
    STAMP(5);
    u32 mw = pkmax(pkmax(wx.x & 0x7fff7fffu, wx.y & 0x7fff7fffu), pkmax(wx.z & 0x7fff7fffu, wx.w & 0x7fff7fffu));
    UNROLL for (int i = 0; i < 8; ++i) mw = pkmax(mw, pkmax(pkmax(wvv[i].x & 0x7fff7fffu, wvv[i].y & 0x7fff7fffu), pkmax(wvv[i].z & 0x7fff7fffu, wvv[i].w & 0x7fff7fffu)));
    mw = wave_pkmax(mw);
    __syncthreads();
    if ((lane & 15) == 0) {
        UNROLL for (int s1 = 0; s1 < 8; ++s1) lds[s1 * 16 + (t >> 4)] = __builtin_bit_cast(u32, gs[s1]);
        if (lane == 0) lds[128 + wv] = max(mw & 0xffffu, mw >> 16) >> 7;
    }
    __syncthreads();
    if (t < 8) {
        const f4* pp = (const f4*)&lds[t * 16];
        const f4 ps = (pp[0] + pp[1]) + (pp[2] + pp[3]);
        const float lg = rbf((ps.x + ps.y) + (ps.z + ps.w));
        const float g = rbf(1.0f / (1.0f + __expf(-lg)));
        *(f2*)&gate[s2 * 16 + 2 * t] = f2{g, rbf(1.0f - g)};
        if (t == 0) {
            const u4 e = *(const u4*)&lds[128];
            meta[s2] = (u32)emax | (min(max(max(e.x, e.y), max(e.z, e.w)), 240u) << 16);
        }
    }
    STAMP(6);
    STAMP_VM(7);
    STAMP_OUT(0);
}

__global__ __launch_bounds__(256, 2) void k2_cols(const u32* __restrict__ Z, u32* __restrict__ meta,
                                               const unsigned short* __restrict__ war, const unsigned short* __restrict__ wai,
                                               const unsigned short* __restrict__ wbr, const unsigned short* __restrict__ wbi,
                                               u2* __restrict__ V) {
    __shared__ __attribute__((aligned(16))) u4 lds[1024 + 2048];
    STAMP_DECL;
    STAMP(0);
    const int t = threadIdx.x, lane = t & 63, wv = __builtin_amdgcn_readfirstlane(t >> 6);
    const int cls = blockIdx.x & 7, tile = blockIdx.x >> 3;
    const int k0 = min(tile * 16, 497);
    const int q = lane >> 4, n = lane & 15;
    const bool packed = (cls & 3) == 0;
    const int row = packed ? 0 : cls < 4 ? cls : 8 - cls;

    u32 z1[4][4], z2[4][4] = {};
    u4 mall, mrow[4];
    {
        const rsrc_t rz = mkrs(Z, 256 * ZP * 4);
        const int kd = k0 + n, km = (1024 - k0 - n) & 1023;
        const int vb = (64 * q * ZP) * 4, sb = (4 * wv * ZP + row * 1024) * 4;
        if (packed) { UNROLL for (int i = 0; i < 4; ++i) UNROLL for (int j = 0; j < 4; ++j) z2[i][j] = __builtin_amdgcn_raw_buffer_load_b32(rz, vb + km * 4, sb + (16 * j + i) * ZP * 4, 0); }
        mall = *(const u4*)&meta[4 * lane];
        UNROLL for (int j = 0; j < 4; ++j) mrow[j] = *(const u4*)&meta[64 * q + 16 * j + 4 * wv];
        const int k1 = cls <= 4 ? kd : km;
        UNROLL for (int i = 0; i < 4; ++i) UNROLL for (int j = 0; j < 4; ++j) z1[i][j] = __builtin_amdgcn_raw_buffer_load_b32(rz, vb + k1 * 4, sb + (16 * j + i) * ZP * 4, 0);
    }
    u4 wa[4], wb[4];
    {
        const int off = (cls + 8 * (4 * wv + 16 * n)) * 513 + k0 + 8 * (q & 1);
        const unsigned short* pa = ((q & 2) ? wai : war) + off;
        const unsigned short* pb = ((q & 2) ? wbi : wbr) + off;
        UNROLL for (int i = 0; i < 4; ++i) {
            __builtin_memcpy(&wa[i], pa + 8 * 513 * i, 16);
            __builtin_memcpy(&wb[i], pb + 8 * 513 * i, 16);
        }
    }
    SB();
    u4 GR[4], GI[4], HR[4], HI[4], FR, FI;
    {
        const float ofs = cls == 4 ? 0.25f : 0.0f;
        const float gu = (float)(cls + 8 * n) * (1.0f / 2048.0f), gs0 = (float)(64 * q + 4 * wv);
        const float hs0 = (float)(16 * n + 4 * wv) * (1.0f / 2048.0f), hu = (float)(cls + 32 * q);
        UNROLL for (int i = 0; i < 4; ++i) UNROLL for (int j = 0; j < 4; ++j) {
            const float g = (gs0 + (float)(16 * j + i)) * gu + ofs;
            const float gc = cosr(g), gsn = sinr(g);
            GR[i][j] = pkh(gc, gsn); GI[i][j] = pkh(-gsn, gc);
            const float hh = (hs0 + (float)i * (1.0f / 2048.0f)) * (hu + (float)(8 * j));
            const float hc = cosr(hh), hs = sinr(hh);
            HR[i][j] = pkh(hc, hs); HI[i][j] = pkh(-hs, hc);
        }
        UNROLL for (int j = 0; j < 4; ++j) {
            const float f = (float)(n * (4 * q + j)) * (1.0f / 16.0f);
            const float fc = cosr(f), fs = sinr(f);
            FR[j] = pkh(fc, fs); FI[j] = pkh(-fs, fc);
        }
    }
    STAMP(1);
    SB();
    const u32 mm = wave_pkmax(pkmax(pkmax(mall.x, mall.y), pkmax(mall.z, mall.w)));
    const int emax = mm & 0xffff, ewm = mm >> 16;
    STAMP(2);
    {
        const u32 sg1 = cls > 4 ? 0x80000000u : 0u;
        const u32 sg2 = cls == 4 ? 0x00008000u : 0x80000000u;
        u32 y[4][4];
        UNROLL for (int i = 0; i < 4; ++i) {
            u4 d;
            UNROLL for (int j = 0; j < 4; ++j) {
                const u32 f = (u32)max((int)(mrow[j][i] & 0xffff) - emax + 15, 0) * 0x04000400u;
                d[j] = hfma(z2[i][j], f | sg2, hmul(z1[i][j], f | sg1));
            }
            const f4 yr = mfma16(GR[i], d, f4{}), yi = mfma16(GI[i], d, f4{});
            UNROLL for (int r = 0; r < 4; ++r) y[r][i] = pkh(yr[r], yi[r]);
        }
        UNROLL for (int r = 0; r < 4; ++r) lds[(wv * 16 + 4 * q + r) * 16 + n] = u4{y[r][0], y[r][1], y[r][2], y[r][3]};
    }
    __syncthreads();
    STAMP(3);
    {
        const u32 sw = (u32)(127 + 118 - ewm - (packed ? 1 : 0)) << 7;
        u4 SR, SI;
        UNROLL for (int j = 0; j < 4; ++j) {
            const u32 v = (8 * (q & 1) + 2 * j == n ? sw : 0u) | (8 * (q & 1) + 2 * j + 1 == n ? sw << 16 : 0u);
            SR[j] = (q & 2) ? 0u : v; SI[j] = (q & 2) ? v : 0u;
        }
        u32 ra[4][4], rb[4][4];
        UNROLL for (int i = 0; i < 4; ++i) {
            const u4 d = lds[(q * 16 + 4 * wv + i) * 16 + n];
            const f4 xr = mfma16(FR, d, f4{}), xi = mfma16(FI, d, f4{});
            const f4 ar = mfma16b(wa[i], SR, f4{}), ai = mfma16b(wa[i], SI, f4{});
            const f4 br = mfma16b(wb[i], SR, f4{}), bi = mfma16b(wb[i], SI, f4{});
            u4 ya, yb;
            UNROLL for (int r = 0; r < 4; ++r) {
                ya[r] = pkh(xr[r] * ai[r] + xi[r] * ar[r], xr[r] * ar[r] - xi[r] * ai[r]);
                yb[r] = pkh(xr[r] * bi[r] + xi[r] * br[r], xr[r] * br[r] - xi[r] * bi[r]);
            }
            const f4 p0 = mfma16(FR, ya, f4{}), p1 = mfma16(FI, ya, f4{});
            const f4 p2 = mfma16(FR, yb, f4{}), p3 = mfma16(FI, yb, f4{});
            UNROLL for (int r = 0; r < 4; ++r) { ra[r][i] = pkh(p0[r], p1[r]); rb[r][i] = pkh(p2[r], p3[r]); }
        }
        UNROLL for (int r = 0; r < 4; ++r) {
            lds[1024 + (wv * 16 + 4 * q + r) * 16 + n] = u4{ra[r][0], ra[r][1], ra[r][2], ra[r][3]};
            lds[2048 + (wv * 16 + 4 * q + r) * 16 + n] = u4{rb[r][0], rb[r][1], rb[r][2], rb[r][3]};
        }
    }
    STAMP(4);
    __syncthreads();
    STAMP(5);
    {
        const rsrc_t rv = mkrs(V, 256 * VP * 8);
        const int vo = (tile < 32 || n == 15) ? (64 * q * VP + n) * 8 : 0x7ffffff0;
        const int so = (4 * wv * VP + cls * VC + k0) * 8;
        UNROLL for (int i = 0; i < 4; ++i) {
            const u4 da = lds[1024 + (q * 16 + 4 * wv + i) * 16 + n], db = lds[2048 + (q * 16 + 4 * wv + i) * 16 + n];
            const f4 p0 = mfma16(HR[i], da, f4{}), p1 = mfma16(HI[i], da, f4{});
            const f4 p2 = mfma16(HR[i], db, f4{}), p3 = mfma16(HI[i], db, f4{});
            UNROLL for (int r = 0; r < 4; ++r) __builtin_amdgcn_raw_buffer_store_b64(u2{pkh(p0[r], p1[r]), pkh(p2[r], p3[r])}, rv, vo, so + (16 * r + i) * VP * 8, 0);
        }
    }
    STAMP(6);
    STAMP_VM(7);
    STAMP_OUT(1);
}

__global__ __launch_bounds__(256, 2) void k3_rows(const u2* __restrict__ V, u32* __restrict__ meta, const float* __restrict__ gate, u4* __restrict__ out) {
    __shared__ __attribute__((aligned(16))) u4 lds[1024];
    STAMP_DECL;
    STAMP(0);
    const int t = threadIdx.x, lane = t & 63, wv = __builtin_amdgcn_readfirstlane(t >> 6), s2 = blockIdx.x;
    const int q = lane >> 4, n = lane & 15;
    const f4 gg = *(const f4*)&gate[s2 * 16 + (n >> 2) * 4];
    const u4 mall = *(const u4*)&meta[4 * lane];
    u2 v0[9], v1[9];
    {
        const u2* vp = V + s2 * VP + 2 * q * VC + 128 * wv;
        UNROLL for (int i = 0; i < 8; ++i) { v0[i] = vp[16 * i + n]; v1[i] = vp[VC + 16 * i + n]; }
        v0[8] = vp[128]; v1[8] = vp[VC + 128];
    }
    SB();
    u4 MR[4], MI[4]; f2 TW[16];
    core_setup<false>(lane, MR, MI, TW);
    STAMP(1);
    SB();
    u4 PM;
    {
        const int r = n & 3, sa = (n >> 2) * 2;
        UNROLL for (int cc = 0; cc < 2; ++cc) {
            const int u1 = 2 * q + cc;
            const float ta = (float)(sa * u1) * 0.125f + ((r & 1) ? 0.25f : (r & 2) ? 0.5f : 0.0f);
            const float tb = (float)((sa + 1) * u1) * 0.125f + ((r & 1) ? ((r & 2) ? 0.0f : 0.5f) : 0.25f);
            const float ca = cosr(ta) * 0.25f, sa_ = sinr(ta) * 0.25f, cb = cosr(tb) * 0.25f, sb_ = sinr(tb) * 0.25f;
            PM[2 * cc]     = pkh(gg.x * ca + gg.z * cb, gg.x * sa_ + gg.z * sb_);
            PM[2 * cc + 1] = pkh(gg.y * ca + gg.w * cb, gg.y * sa_ + gg.w * sb_);
        }
    }
    STAMP(2);
    {
        u32 dd[8], mm[9];
        UNROLL for (int i = 0; i < 9; ++i) {
            f4 z = mfma16(PM, u4{v0[i].x, v0[i].y, v1[i].x, v1[i].y}, f4{});
            if (i == 0 && wv == 0 && n == 0) { z.x = 0.5f * (z.x + z.z); z.y = 0.5f * (z.y + z.w); }
            if (i == 8 && wv == 3) { z.z = 0.5f * (z.x + z.z); z.w = 0.5f * (z.y + z.w); }
            if (i < 8) dd[i] = pkh(z.x, z.y);
            mm[i] = pkh(z.z, z.w);
        }
        lds[(q * 8 + wv) * 32 + n]      = u4{dd[0], dd[2], dd[4], dd[6]};
        lds[(q * 8 + wv) * 32 + 16 + n] = u4{dd[1], dd[3], dd[5], dd[7]};
        lds[(q * 8 + 7 - wv) * 32 + ((32 - n) & 31)] = n == 0 ? u4{mm[8], mm[6], mm[4], mm[2]} : u4{mm[6], mm[4], mm[2], mm[0]};
        lds[(q * 8 + 7 - wv) * 32 + 16 - n] = u4{mm[7], mm[5], mm[3], mm[1]};
    }
    const u32 mx = wave_pkmax(pkmax(pkmax(mall.x, mall.y), pkmax(mall.z, mall.w)));
    const int esc = (int)(mx & 0xffff) + (int)(mx >> 16) - 257;
    STAMP(3);
    __syncthreads();
    STAMP(4);
    u4 in[4];
    UNROLL for (int s = 0; s < 4; ++s) in[s] = lds[wv * 256 + s * 64 + lane];
    f16v zr, zi;
    core_fft<false, false>(in, MR, MI, TW, zr, zi);
    {
        u4* op = out + (512 * wv + s2) * 128 + 4 * perm32(lane & 31) + (lane >> 5);
        UNROLL for (int hf = 0; hf < 2; ++hf) {
            u4 a, b;
            UNROLL for (int j = 0; j < 4; ++j) {
                a[j] = pkb(__builtin_amdgcn_ldexpf(zi[8 * hf + 2 * j], esc), __builtin_amdgcn_ldexpf(zi[8 * hf + 2 * j + 1], esc));
                b[j] = pkb(__builtin_amdgcn_ldexpf(zr[8 * hf + 2 * j], esc), __builtin_amdgcn_ldexpf(zr[8 * hf + 2 * j + 1], esc));
            }
            op[2 * hf] = a;
            op[256 * 128 + 2 * hf] = b;
        }
    }
    STAMP(6);
    STAMP_VM(7);
    STAMP_OUT(2);
}

extern "C" void kernel_launch(void* const* d_in, const int* in_sizes, int n_in,
                              void* d_out, int out_size, void* d_ws, size_t ws_size,
                              hipStream_t stream) {
    char* ws = (char*)d_ws;
    u32* Z = (u32*)(ws + WS_Z); u2* V = (u2*)(ws + WS_V); u32* meta = (u32*)(ws + WS_META); float* gate = (float*)(ws + WS_GATE);
    k1_rows<<<256, 256, 0, stream>>>((const u2*)d_in[0], (const u2*)d_in[6], (const u4*)d_in[1], (const u4*)d_in[2], (const u4*)d_in[4], (const u4*)d_in[5], Z, meta, gate);
    k2_cols<<<264, 256, 0, stream>>>(Z, meta, (const unsigned short*)d_in[1], (const unsigned short*)d_in[2], (const unsigned short*)d_in[4], (const unsigned short*)d_in[5], V);
    k3_rows<<<256, 256, 0, stream>>>(V, meta, gate, (u4*)d_out);
}
```

```cpp
#define DIAG 0
#define WAUX 0
#include <hip/hip_runtime.h>
#include <hip/hip_bf16.h>
#include <stdint.h>

typedef _Float16 h2 __attribute__((ext_vector_type(2)));
typedef _Float16 h8 __attribute__((ext_vector_type(8)));
typedef __bf16 b2 __attribute__((ext_vector_type(2)));
typedef __bf16 b8 __attribute__((ext_vector_type(8)));
typedef float f2 __attribute__((ext_vector_type(2)));
typedef float f4 __attribute__((ext_vector_type(4)));
typedef float f16v __attribute__((ext_vector_type(16)));
typedef short s4 __attribute__((ext_vector_type(4)));
typedef unsigned u32;
typedef unsigned long long u64;
typedef u32 u2 __attribute__((ext_vector_type(2)));
typedef u32 u4 __attribute__((ext_vector_type(4)));
typedef unsigned short us2 __attribute__((ext_vector_type(2)));
#define DI __device__ __forceinline__
#define UNROLL _Pragma("unroll")

constexpr u32 WS_Z = 0;
constexpr u32 WS_V = 4u << 20;
constexpr u32 WS_VL = 12u << 20;
#define FSTR 16
#define F2DELAY 16
#define PSLEEP 4
constexpr u32 WS_F0 = WS_VL + (64u << 10);
constexpr u32 WS_F1 = WS_F0 + 2048 * FSTR;
constexpr u32 WS_F2 = WS_F1 + 2048 * FSTR;
constexpr u32 WS_F3 = WS_F2 + 2048 * FSTR;
constexpr u32 WS_E = WS_F3 + 2048 * FSTR;
constexpr u32 WS_BYTES = WS_E + 4096;
constexpr size_t WS_META = 24u << 20;
#define VC0 2
#define VC1 2
#define VC2 5
#define VC3 5
#define WP0 1
#define WP1 3
#define WP2 5
#define WP3 5
#define XAUX 2
#define OAUX 2
constexpr int SC1 = 16;
constexpr int SPIN_CAP = 4096;

DI u32 pkh(float lo, float hi) { h2 r; r.x = (_Float16)lo; r.y = (_Float16)hi; return __builtin_bit_cast(u32, r); }
DI u32 pkb(float lo, float hi) { b2 r; r.x = (__bf16)lo; r.y = (__bf16)hi; return __builtin_bit_cast(u32, r); }
DI float bflo(u32 v) { return __builtin_bit_cast(float, v << 16); }
DI float bfhi(u32 v) { return __builtin_bit_cast(float, v & 0xffff0000u); }
DI float rbf(float v) { return (float)(__bf16)v; }
DI f16v mfma32(u4 a, u4 b, f16v c) { return __builtin_amdgcn_mfma_f32_32x32x16_f16(__builtin_bit_cast(h8, a), __builtin_bit_cast(h8, b), c, 0, 0, 0); }
DI f4 mfma16(u4 a, u4 b) { return __builtin_amdgcn_mfma_f32_16x16x32_f16(__builtin_bit_cast(h8, a), __builtin_bit_cast(h8, b), f4{}, 0, 0, 0); }
DI f4 mfma16b(u4 a, u4 b) { return __builtin_amdgcn_mfma_f32_16x16x32_bf16(__builtin_bit_cast(b8, a), __builtin_bit_cast(b8, b), f4{}, 0, 0, 0); }
DI u32 hmul(u32 a, u32 b) { return __builtin_bit_cast(u32, __builtin_bit_cast(h2, a) * __builtin_bit_cast(h2, b)); }
DI u32 hfma(u32 a, u32 b, u32 c) { return __builtin_bit_cast(u32, __builtin_elementwise_fma(__builtin_bit_cast(h2, a), __builtin_bit_cast(h2, b), __builtin_bit_cast(h2, c))); }
DI u32 pkmax(u32 a, u32 b) { return __builtin_bit_cast(u32, __builtin_elementwise_max(__builtin_bit_cast(us2, a), __builtin_bit_cast(us2, b))); }
DI float sinr(float r) { return __builtin_amdgcn_sinf(r); }
DI float cosr(float r) { return __builtin_amdgcn_cosf(r); }
DI int perm32(int n) { return (n & 0x13) | ((n & 4) << 1) | ((n & 8) >> 1); }
DI u32 max3u(u32 a, u32 b, u32 c) { return max(max(a, b), c); }
DI u32 pow2h(int d) { const float f = __builtin_bit_cast(float, (u32)(127 - min(d, 64)) << 23); return pkh(f, f); }
template <class T> DI void pin(T& v) { asm volatile("" : "+v"(v)); }
template <class T, int N> DI void pin(T (&v)[N]) { UNROLL for (int i = 0; i < N; ++i) pin(v[i]); }

typedef __amdgpu_buffer_rsrc_t rsrc_t;
DI rsrc_t mkrs(const void* p, u32 bytes) { return __builtin_amdgcn_make_buffer_rsrc((void*)p, (short)0, (int)bytes, 0x00020000); }
#define SB() __builtin_amdgcn_sched_barrier(0)
#define BAR() do { asm volatile("s_waitcnt lgkmcnt(0)" ::: "memory"); __builtin_amdgcn_s_barrier(); asm volatile("" ::: "memory"); } while (0)
#define VMWAIT(n) asm volatile("s_waitcnt vmcnt(" #n ")" ::: "memory")
#define STAMP_DECL unsigned long long st_[16] = {}
#define STAMP(i) do { if (i == 0 || i == DIAG || i == 15) { SB(); asm volatile("s_memrealtime %0\n\ts_waitcnt lgkmcnt(0)" : "=s"(st_[i]) :: "memory"); SB(); } } while (0)
#define STAMP_OUT() do { if (wv < 4) { while (__builtin_amdgcn_s_memrealtime() - st_[15] < 4 * (st_[DIAG] - st_[0])) __builtin_amdgcn_s_sleep(1); } } while (0)
template <int CTRL> DI u32 dpp(u32 v) { return __builtin_amdgcn_update_dpp(v, v, CTRL, 0xf, 0xf, false); }
DI u32 wave_maxu(u32 v) {
    v = max(v, dpp<0xB1>(v)); v = max(v, dpp<0x4E>(v)); v = max(v, dpp<0x141>(v)); v = max(v, dpp<0x140>(v));
    return max(max((u32)__builtin_amdgcn_readlane(v, 0), (u32)__builtin_amdgcn_readlane(v, 16)), max((u32)__builtin_amdgcn_readlane(v, 32), (u32)__builtin_amdgcn_readlane(v, 48)));
}
DI u32 absmax2(u32 m, u32 v) { return pkmax(m, v & 0x7fff7fffu); }
DI u32 fold16(u32 m) { return max(m & 0xffffu, m >> 16); }

DI void core_setup(int lane, u4 (&MR)[4], u4 (&MI)[4], f2 (&TW)[16]) {
    const float fc = (float)perm32(lane & 31), h4 = (float)(4 * (lane >> 5));
    UNROLL for (int s = 0; s < 4; ++s) {
        float cs[4], sn[4];
        UNROLL for (int i = 0; i < 4; ++i) {
            const float pt = ((float)(8 * s + i) + h4) * fc * (1.0f / 1024.0f);
            if (s < 2) { cs[i] = cosr(pt * 32.0f); sn[i] = sinr(pt * 32.0f); }
            TW[4 * s + i] = f2{cosr(pt) * 0.125f, sinr(pt) * 0.125f};
        }
        if (s < 2) MR[s] = u4{pkh(cs[0], cs[1]), pkh(cs[2], cs[3]), pkh(sn[0], sn[1]), pkh(sn[2], sn[3])};
        else MR[s] = MR[s - 2] ^ ((lane & 1) ? 0x80008000u : 0u);
        MI[s] = u4{MR[s].z ^ 0x80008000u, MR[s].w ^ 0x80008000u, MR[s].x, MR[s].y};
    }
}
template <bool OUT_B> DI void core_fft(const u4 (&in)[4], const u4 (&MR)[4], const u4 (&MI)[4], const f2 (&TW)[16], f16v& outr, f16v& outi) {
    f16v yr = {}, yi = {};
    UNROLL for (int s = 0; s < 4; ++s) { yr = mfma32(in[s], MR[s], yr); yi = mfma32(in[s], MI[s], yi); }
    outr = f16v{}; outi = f16v{};
    UNROLL for (int s = 0; s < 4; ++s) {
        float pr[4], pi[4];
        UNROLL for (int i = 0; i < 4; ++i) {
            const int r = 4 * s + i;
            pr[i] = yr[r] * TW[r].x + yi[r] * TW[r].y;
            pi[i] = yi[r] * TW[r].x - yr[r] * TW[r].y;
        }
        const u4 d = u4{pkh(pr[0], pr[1]), pkh(pr[2], pr[3]), pkh(pi[0], pi[1]), pkh(pi[2], pi[3])};
        if (OUT_B) { outr = mfma32(MR[s], d, outr); outi = mfma32(MI[s], d, outi); }
        else       { outr = mfma32(d, MR[s], outr); outi = mfma32(d, MI[s], outi); }
    }
}
DI void rot_pair(float ph, u32& r, u32& i) { const float c = cosr(ph), s = sinr(ph); r = pkh(c, s); i = pkh(-s, c); }

DI u32 tag_of(u32 epoch, u32 idx) { u32 h = (epoch ^ (idx * 0x9E3779B1u)) * 0x85EBCA6Bu; return h ^ (h >> 15); }
DI void flag_post(rsrc_t rws, u32 arr, int b, int lane, u32 payload, u32 epoch) {
    if (lane < 8) __builtin_amdgcn_raw_buffer_store_b64(u2{payload, tag_of(epoch, arr + b)}, rws, lane * (256 * FSTR), arr + b * FSTR, SC1);
}
DI u4 flag_wait(rsrc_t rws, u32 arr, int b, int lane, u32 epoch) {
    u2 f[4]; u32 tg[4];
    UNROLL for (int j = 0; j < 4; ++j) tg[j] = tag_of(epoch, arr + 4 * lane + j);
    _Pragma("unroll 1") for (int spin = 0; spin < SPIN_CAP; ++spin) {
        asm volatile("" ::: "memory");
        UNROLL for (int j = 0; j < 4; ++j) f[j] = __builtin_amdgcn_raw_buffer_load_b64(rws, lane * (4 * FSTR) + j * FSTR, arr + (b & 7) * (256 * FSTR), SC1);
        if (__all(f[0].y == tg[0] && f[1].y == tg[1] && f[2].y == tg[2] && f[3].y == tg[3])) break;
        __builtin_amdgcn_s_sleep(PSLEEP);
    }
    return u4{f[0].x, f[1].x, f[2].x, f[3].x};
}
DI int cls_of(int b) { const int row = (b & 7) >> 1, sel = b >> 7; return row == 0 ? 4 * sel : (sel ? 8 - row : row); }
DI int tile_of(int b) { return 16 * (b & 1) + ((b >> 3) & 15); }
DI bool is_helper(int b) { return (b & 0xe6) == 0x22; }
DI int helper_cls(int b) { return (b & 1) | ((b >> 2) & 6); }

constexpr int L_A = 0;
constexpr int L_B = 4096;
constexpr int L_EW = 12288;
constexpr int L_WW = 12292;
constexpr int L_GL = 12296;
constexpr int L_SC = 12304;
constexpr int L_GS = 12560;
constexpr int L_TS = 12688;
constexpr int L_X = L_TS + 288;
constexpr int L_END = L_X + 4096;

__global__ __launch_bounds__(320, 2) void gfnet_fused(const u32* __restrict__ x, const u2* __restrict__ gw,
                                                      const unsigned short* __restrict__ war, const unsigned short* __restrict__ wai,
                                                      const unsigned short* __restrict__ wbr, const unsigned short* __restrict__ wbi,
                                                      char* ws, u4* __restrict__ out) {
    __shared__ __attribute__((aligned(16))) u32 lds[L_END];
    STAMP_DECL;
    STAMP(0);
    const int t = threadIdx.x, lane = t & 63, wv = __builtin_amdgcn_readfirstlane(t >> 6), b = blockIdx.x;
    const int q = lane >> 4, n = lane & 15;
    const rsrc_t rws = mkrs(ws, WS_BYTES);
    const int row = (b & 7) >> 1, sel = b >> 7, cls = cls_of(b), tile = tile_of(b);
    const u4 ones = {~0u, ~0u, ~0u, ~0u};

    if (wv == 4) {
        const u32 ev = __builtin_amdgcn_raw_buffer_load_b32(rws, 0, WS_E, SC1);
        const int reg = row * 64 + (sel ? 63 - tile : tile);
        UNROLL for (int i = 0; i < 16; ++i)
            __builtin_amdgcn_raw_buffer_store_b128(ones, rws, lane * 16 + (i & 3) * 1024, WS_Z + reg * 16384 + (i >> 2) * 4096, SC1);
        const bool helper = is_helper(b);
        const int hc = helper_cls(b);
        STAMP(7);
        const u32 epoch = __builtin_amdgcn_readfirstlane(ev) + 1u;
        VMWAIT(0);
        flag_post(rws, WS_F0, b, lane, 0u, epoch);
        STAMP(1);
        BAR();
        STAMP(2);
        {
            const u4 e = *(const u4*)&lds[L_EW];
            flag_post(rws, WS_F1, b, lane, max(max(e.x, e.y), max(e.z, e.w)), epoch);
        }
        flag_wait(rws, WS_F0, b, lane, epoch);
        STAMP(3);
        BAR();
        u32 emax;
        {
            const u4 f = flag_wait(rws, WS_F1, b, lane, epoch) & 0xffu;
            emax = wave_maxu(max(max(f.x, f.y), max(f.z, f.w)));
            *(u4*)&lds[L_SC + 4 * lane] = u4{pow2h(emax - f.x), pow2h(emax - f.y), pow2h(emax - f.z), pow2h(emax - f.w)};
        }
        STAMP(4);
        BAR();
        flag_post(rws, WS_F2, b, lane, 0u, epoch);
        u32 he = 0;
        u4 o[2], HR, HI;
        if (helper) {
            u32 hw[4][4];
            UNROLL for (int r = 0; r < 4; ++r) {
                const int o_ = (hc + 8 * (n + 16 * (4 * q + r))) * 513 + 512;
                hw[0][r] = war[o_]; hw[1][r] = wai[o_]; hw[2][r] = wbr[o_]; hw[3][r] = wbi[o_];
            }
            u4 GR, GI, FR, FI; f2 T1[4], T2[4];
            UNROLL for (int j = 0; j < 4; ++j) {
                u32 a, c;
                rot_pair((float)((4 * q + j) * (hc + 8 * n)) * (1.0f / 128.0f) + (hc == 4 ? 0.25f : 0.0f), a, c); GR[j] = a; GI[j] = c;
                rot_pair((float)(n * (hc + 8 * (4 * q + j))) * (1.0f / 128.0f), a, c); HR[j] = a; HI[j] = c;
                rot_pair((float)(n * (4 * q + j)) * (1.0f / 16.0f), a, c); FR[j] = a; FI[j] = c;
                const float p1 = (float)((4 * q + j) * (hc + 8 * n)) * (1.0f / 2048.0f), p2 = (float)(n * (hc + 8 * (4 * q + j))) * (1.0f / 2048.0f);
                T1[j] = f2{cosr(p1), sinr(p1)}; T2[j] = f2{cosr(p2), sinr(p2)};
            }
            pin(GR); pin(GI); pin(HR); pin(HI); pin(FR); pin(FI); pin(T1); pin(T2);
            BAR();
            const int hoff = (((hc & 3) == 0 ? 0 : hc < 4 ? hc : 8 - hc) * 64 + 63) * 16384 + 60;
            u4 hz;
            _Pragma("unroll 1") for (int spin = 0; spin < SPIN_CAP; ++spin) {
                asm volatile("" ::: "memory");
                UNROLL for (int j = 0; j < 4; ++j) hz[j] = __builtin_amdgcn_raw_buffer_load_b32(rws, (16 * n + q) * 64 + j * 256, WS_Z + hoff, SC1);
                if (__all(max(max(hz.x, hz.y), max(hz.z, hz.w)) != ~0u)) break;
                __builtin_amdgcn_s_sleep(2);
            }
            UNROLL for (int r = 0; r < 4; ++r) UNROLL for (int a = 0; a < 4; ++a) he = max(he, hw[a][r] & 0x7fffu);
            he = min(wave_maxu(he) >> 7, 240u);
            const u32 msk = hc == 0 ? 0x0000ffffu : hc == 4 ? 0xffff0000u : ~0u, sg = hc > 4 ? 0x80000000u : 0u;
            u4 d;
            UNROLL for (int j = 0; j < 4; ++j) d[j] = hmul(hz[j], (lds[L_SC + 64 * q + 16 * j + n] & msk) | sg);
            f4 yr = mfma16(d, GR), yi = mfma16(d, GI);
            UNROLL for (int r = 0; r < 4; ++r) d[r] = pkh(yr[r] * T1[r].x + yi[r] * T1[r].y, yi[r] * T1[r].x - yr[r] * T1[r].y);
            const f4 xr = mfma16(FR, d), xi = mfma16(FI, d);
            const float sw = __builtin_bit_cast(float, (245u - he) << 23);
            UNROLL for (int f = 0; f < 2; ++f) {
                UNROLL for (int r = 0; r < 4; ++r) {
                    const float wr = bflo(hw[2 * f][r]) * sw, wi = bflo(hw[2 * f + 1][r]) * sw;
                    d[r] = pkh(xr[r] * wi + xi[r] * wr, xr[r] * wr - xi[r] * wi);
                }
                yr = mfma16(d, FR); yi = mfma16(d, FI);
                UNROLL for (int r = 0; r < 4; ++r) d[r] = pkh(yr[r] * T2[r].x + yi[r] * T2[r].y, yi[r] * T2[r].x - yr[r] * T2[r].y);
                o[f] = d;
            }
        } else {
            BAR();
        }
        STAMP(5);
        {
            const u4 e = *(const u4*)&lds[L_WW];
            flag_post(rws, WS_F3, b, lane, max(max(e.x, e.y), max(e.z, e.w)) | (he << 8), epoch);
        }
        flag_wait(rws, WS_F2, b, lane, epoch);
        STAMP(6);
        BAR();
        if (helper) {
            const f4 p0 = mfma16(HR, o[0]), p1 = mfma16(HI, o[0]), p2 = mfma16(HR, o[1]), p3 = mfma16(HI, o[1]);
            UNROLL for (int r = 0; r < 4; ++r)
                __builtin_amdgcn_raw_buffer_store_b64(u2{pkh(p0[r], p1[r]), pkh(p2[r], p3[r])}, rws, (64 * q + n) * 128, WS_VL + 16 * r * 128 + hc * 8, SC1);
        }
        {
            const u4 f = flag_wait(rws, WS_F3, b, lane, epoch);
            u32 m = 0;
            UNROLL for (int j = 0; j < 4; ++j) m = max3u(m, f[j] & 0xffu, (f[j] >> 8) & 0xffu);
            const u32 ewm = wave_maxu(m);
            UNROLL for (int j = 0; j < 4; ++j) {
                const int p = 4 * lane + j;
                lds[L_TS + cls_of(p) * 36 + tile_of(p)] = pow2h(ewm - (f[j] & 0xffu));
                if (is_helper(p)) lds[L_TS + helper_cls(p) * 36 + 32] = pow2h(ewm - ((f[j] >> 8) & 0xffu));
            }
            if (lane == 0) lds[L_GL] = (u32)min(max((int)emax + (int)ewm - 130, 1), 254) << 23;
            if (b == 0 && lane == 0) __builtin_amdgcn_raw_buffer_store_b32(epoch, rws, 0, WS_E, SC1);
        }
        STAMP(8);
        BAR();
        STAMP_OUT();
        return;
    }

    const bool packed = row == 0;
    const int rot = tile & 3;
    const int wvo = n * (128 * 1026) + min(q, 2) * 16, wso = ((cls + 32 * wv) * 513 + 16 * tile - cls) * 2;
    u4 wr[4][4];
    const rsrc_t rw0 = mkrs(war, 2048 * 1026), rw1 = mkrs(wai, 2048 * 1026), rw2 = mkrs(wbr, 2048 * 1026), rw3 = mkrs(wbi, 2048 * 1026);
#define LOAD_W(i) do { const int so_ = wso + (((i) + rot) & 3) * 8208; \
        wr[i][0] = __builtin_amdgcn_raw_buffer_load_b128(rw0, wvo, so_, WAUX); wr[i][1] = __builtin_amdgcn_raw_buffer_load_b128(rw1, wvo, so_, WAUX); \
        wr[i][2] = __builtin_amdgcn_raw_buffer_load_b128(rw2, wvo, so_, WAUX); wr[i][3] = __builtin_amdgcn_raw_buffer_load_b128(rw3, wvo, so_, WAUX); } while (0)
#define CLEAR_V(i) do { __builtin_amdgcn_raw_buffer_store_b128(ones, rws, t * 16, WS_V + b * 32768 + (i) * 8192, SC1); \
        __builtin_amdgcn_raw_buffer_store_b128(ones, rws, t * 16, WS_V + b * 32768 + (i) * 8192 + 4096, SC1); \
        if ((i) == 0) __builtin_amdgcn_raw_buffer_store_b128(ones, rws, t < 8 ? t * 16 : 0x7ffffff0, WS_VL + b * 128, SC1); } while (0)
#define SLOT(k) do { SB(); if (VC0 == k) CLEAR_V(0); if (VC1 == k) CLEAR_V(1); if (VC2 == k) CLEAR_V(2); if (VC3 == k) CLEAR_V(3); \
        if (WP0 == k) LOAD_W(0); if (WP1 == k) LOAD_W(1); if (WP2 == k) LOAD_W(2); if (WP3 == k) LOAD_W(3); SB(); } while (0)
    u2 xv[8];
    {
        const rsrc_t rx = mkrs(x, 2048 * 2048);
        UNROLL for (int s1 = 0; s1 < 8; ++s1) xv[s1] = __builtin_amdgcn_raw_buffer_load_b64(rx, t * 8, (256 * s1 + b) * 2048, XAUX);
    }
    const u2 gv = gw[t];
    SLOT(0);
    {
        u4 MR[4], MI[4]; f2 TW[16];
        core_setup(lane, MR, MI, TW);
        pin(MR); pin(MI); pin(TW);
        SLOT(1);
        STAMP(1);
        u32 mx = 0;
        UNROLL for (int s1 = 0; s1 < 8; ++s1) mx = absmax2(absmax2(mx, xv[s1].x), xv[s1].y);
        const int ew = min(max((int)(wave_maxu(fold16(mx)) >> 7), 8), 250);
        STAMP(2);
        SLOT(2);
        const float sc = __builtin_bit_cast(float, (u32)(253 - ew) << 23);
        const float rs = sc * 0.70710678118654752f;
        u32 o[8][2];
        UNROLL for (int j = 0; j < 2; ++j) {
            f2 v[8];
            UNROLL for (int s1 = 0; s1 < 8; ++s1) v[s1] = f2{bflo(xv[s1][j]), bfhi(xv[s1][j])};
            const f2 a0 = v[0] + v[4], a1 = v[1] + v[5], a2 = v[2] + v[6], a3 = v[3] + v[7];
            const f2 b0 = (v[0] - v[4]) * sc, b1 = v[1] - v[5], b2_ = (v[2] - v[6]) * sc, b3 = v[3] - v[7];
            const f2 e0 = a0 + a2, e1 = (a1 + a3) * sc, dm = b1 - b3, dp = b1 + b3;
            f2 r[8];
            r[0] = e0 * sc + e1; r[1] = e0 * sc - e1;
            r[2] = dm * rs + b0; r[3] = -(dp * rs) - b2_;
            r[4] = (a0 - a2) * sc; r[5] = (a3 - a1) * sc;
            r[6] = b0 - dm * rs; r[7] = b2_ - dp * rs;
            UNROLL for (int p = 0; p < 8; ++p) o[p][j] = pkh(r[p].x, r[p].y);
        }
        UNROLL for (int p = 0; p < 8; ++p) *(u2*)&lds[L_A + p * 512 + 2 * t] = u2{o[p][0], o[p][1]};
        if (lane == 0) lds[L_EW + wv] = ew;
        SLOT(3);
        BAR();
        STAMP(3);
        const u4 ews = *(const u4*)&lds[L_EW];
        const int emax = max(max((int)ews.x, (int)ews.y), max((int)ews.z, (int)ews.w));
        u4 in[4];
        {
            const int h = lane >> 5, blk = (lane >> 4) & 1, qq = (lane & 15) >> 2, p = lane & 3;
            const int off = L_A + wv * 1024 + (4 * h + qq) * 16 + blk * 8 + p * 2;
            UNROLL for (int s = 0; s < 4; ++s) {
                const u2 re = __builtin_bit_cast(u2, __builtin_amdgcn_ds_read_tr16_b64_v4i16((__attribute__((address_space(3))) s4*)&lds[off + s * 128]));
                const u2 im = __builtin_bit_cast(u2, __builtin_amdgcn_ds_read_tr16_b64_v4i16((__attribute__((address_space(3))) s4*)&lds[off + s * 128 + 512]));
                const u32 f = pow2h(emax - (int)ews[s]);
                in[s] = u4{hmul(re.x, f), hmul(re.y, f), hmul(im.x, f), hmul(im.y, f)};
            }
        }
        f16v zr, zi;
        core_fft<true>(in, MR, MI, TW, zr, zi);
        u32 z[16];
        UNROLL for (int r = 0; r < 16; ++r) z[r] = pkh(zr[r], zi[r]);
        pin(z);
        SLOT(4);
        STAMP(4);
        BAR();
        const int c = perm32(lane & 31) + (lane >> 5) * 256, cu = c + 511, c8 = lane == 0 ? 1023 : cu;
        const int zl = (c >> 4) * 16384 + (c & 15) * 4, zu = (cu >> 4) * 16384 + (cu & 15) * 4, z8 = (c8 >> 4) * 16384 + (c8 & 15) * 4;
        const int zs = WS_Z + wv * 64 * 16384 + (((b >> 2) & 3) * 64 + (b & 3) * 16 + ((b >> 4) & 3) * 4 + (b >> 6)) * 64;
        UNROLL for (int r = 0; r < 16; ++r) __builtin_amdgcn_raw_buffer_store_b32(z[r], rws, r < 8 ? zl : r == 8 ? z8 : zu, zs + (r & 7) * 32768, SC1);
    }
    STAMP(5);
    SLOT(5);
    UNROLL for (int i = 0; i < 4; ++i) *(u4*)&lds[L_X + i * 1024 + t * 4] = u4{xv[2 * i].x, xv[2 * i].y, xv[2 * i + 1].x, xv[2 * i + 1].y};
    u32 eprov;
    u4 SEL;
    {
        u32 mw = 0;
        UNROLL for (int a = 0; a < 4; ++a) UNROLL for (int j = 0; j < 4; ++j) mw = absmax2(mw, wr[0][a][j]);
        eprov = min(max(wave_maxu(fold16(mw)) >> 7, 40u), 240u);
        const u32 sw = (245u - eprov - (packed ? 1u : 0u)) << 7;
        UNROLL for (int j = 0; j < 4; ++j) SEL[j] = (8 * q + 2 * j == n + cls ? sw : 0u) | (8 * q + 2 * j + 1 == n + cls ? sw << 16 : 0u);
    }
    f4 Wf[4][4];
    float wmax = 0.0f;
#define WIDEN_W(i) do { UNROLL for (int a = 0; a < 4; ++a) { Wf[i][a] = mfma16b(wr[i][a], SEL); \
        if ((i) > 0) wmax = fmaxf(fmaxf(fmaxf(wmax, fabsf(Wf[i][a].x)), fabsf(Wf[i][a].y)), fmaxf(fabsf(Wf[i][a].z), fabsf(Wf[i][a].w))); } pin(Wf[i]); } while (0)
    WIDEN_W(0);
    if (WP1 < 5) WIDEN_W(1);
    if (WP2 < 5) WIDEN_W(2);
    if (WP3 < 5) WIDEN_W(3);
    u4 GR[4], GI[4], HR[4], HI[4], FR, FI;
    {
        const float ofs = cls == 4 ? 0.25f : 0.0f;
        const float gu = (float)(cls + 8 * n) * (1.0f / 2048.0f), gs0 = (float)(64 * q + 4 * wv);
        UNROLL for (int i = 0; i < 4; ++i) UNROLL for (int j = 0; j < 4; ++j) { u32 a, c; rot_pair((gs0 + (float)(16 * j + i)) * gu + ofs, a, c); GR[i][j] = a; GI[i][j] = c; }
    }
    pin(GR); pin(GI);
    STAMP(6);
    {
        constexpr int young = 4 * ((WP1 == 5) + (WP2 == 5) + (WP3 == 5)) + ((VC0 == 5 || VC1 == 5 || VC2 == 5 || VC3 == 5) ? 0 : 16);
        asm volatile("s_waitcnt vmcnt(%0)" :: "n"(young) : "memory");
    }
    BAR();
    STAMP(7);
    u32 z1[4][4], z2[4][4];
    const int zvm = q * 64 + ((tile == 0 && n == 0) ? 0 : (63 - tile) * 16384 + (15 - n) * 4);
    const int zv1 = cls <= 4 ? q * 64 + tile * 16384 + n * 4 : zvm, zso = WS_Z + row * 64 * 16384 + wv * 4096;
#define LOAD_Z() do { \
        if (packed) { int a_ = zvm; pin(a_); UNROLL for (int i = 0; i < 4; ++i) UNROLL for (int j = 0; j < 4; ++j) z2[i][j] = __builtin_amdgcn_raw_buffer_load_b32(rws, a_ + (4 * i + j) * 256, zso, SC1); } \
        else { UNROLL for (int i = 0; i < 4; ++i) UNROLL for (int j = 0; j < 4; ++j) z2[i][j] = 0u; } \
        int c_ = zv1; pin(c_); UNROLL for (int i = 0; i < 4; ++i) UNROLL for (int j = 0; j < 4; ++j) z1[i][j] = __builtin_amdgcn_raw_buffer_load_b32(rws, c_ + (4 * i + j) * 256, zso, SC1); } while (0)
    LOAD_Z();
    SLOT(6);
    if (WP1 == 5) WIDEN_W(1);
    if (WP2 == 5) WIDEN_W(2);
    if (WP3 == 5) WIDEN_W(3);
    u4 scl[4];
    UNROLL for (int j = 0; j < 4; ++j) scl[j] = *(const u4*)&lds[L_SC + 64 * q + 16 * j + 4 * wv];
    {
        const float hs0 = (float)(16 * n + 4 * wv) * (1.0f / 2048.0f);
        UNROLL for (int j = 0; j < 4; ++j) {
            const float hu = (float)(cls + 32 * q + 8 * ((j + rot) & 3));
            UNROLL for (int i = 0; i < 4; ++i) { u32 a, c; rot_pair((hs0 + (float)i * (1.0f / 2048.0f)) * hu, a, c); HR[i][j] = a; HI[i][j] = c; }
        }
        UNROLL for (int j = 0; j < 4; ++j) { u32 a, c; rot_pair((float)(n * (4 * q + j)) * (1.0f / 16.0f), a, c); FR[j] = a; FI[j] = c; }
        const u32 sg1 = cls > 4 ? 0x80000000u : 0u;
        UNROLL for (int j = 0; j < 4; ++j) scl[j] |= sg1;
    }
    pin(HR); pin(HI); pin(FR); pin(FI); pin(scl);
    STAMP(8);
    SB();
    _Pragma("unroll 1") for (int spin = 0; spin < SPIN_CAP; ++spin) {
        u32 m = 0;
        UNROLL for (int i = 0; i < 4; ++i) UNROLL for (int j = 0; j < 4; ++j) m = max3u(m, z1[i][j], z2[i][j]);
        if (__all(m != ~0u)) break;
        asm volatile("" ::: "memory");
        LOAD_Z();
    }
    STAMP(9);
    {
        const u32 sg2 = cls == 4 ? 0x3c00bc00u : 0xbc003c00u;
        u32 y[4][4];
        UNROLL for (int i = 0; i < 4; ++i) {
            u4 d;
            UNROLL for (int j = 0; j < 4; ++j) d[j] = hmul(hfma(z2[i][j], sg2, z1[i][j]), scl[j][i]);
            const f4 yr = mfma16(GR[i], d), yi = mfma16(GI[i], d);
            UNROLL for (int r = 0; r < 4; ++r) y[r][i] = pkh(yr[r], yi[r]);
        }
        UNROLL for (int r = 0; r < 4; ++r) *(u4*)&lds[L_A + ((wv * 16 + 4 * q + r) * 16 + n) * 4] = u4{y[r][0], y[r][1], y[r][2], y[r][3]};
    }
    if (WP1 == 6) WIDEN_W(1);
    if (WP2 == 6) WIDEN_W(2);
    if (WP3 == 6) WIDEN_W(3);
    {
        const int ex = (int)(wave_maxu(__builtin_bit_cast(u32, wmax)) >> 23) - (packed ? 117 : 118);
        if (lane == 0) lds[L_WW + wv] = min(eprov + (u32)max(ex, 0), 250u);
    }
    BAR();
    STAMP(10);
    {
        const u4 e4 = *(const u4*)&lds[L_WW];
        const int dw = __builtin_amdgcn_readfirstlane((int)max(max(e4.x, e4.y), max(e4.z, e4.w))) - (int)eprov;
        if (dw > 8) {
            const float ex = __builtin_bit_cast(float, (u32)max(127 + 8 - dw, 1) << 23);
            UNROLL for (int i = 0; i < 4; ++i) UNROLL for (int a = 0; a < 4; ++a) Wf[i][a] *= ex;
        }
        const u32 fw = pow2h(min(dw, 8));
        const u4 FR2 = {hmul(FR.x, fw), hmul(FR.y, fw), hmul(FR.z, fw), hmul(FR.w, fw)}, FI2 = {hmul(FI.x, fw), hmul(FI.y, fw), hmul(FI.z, fw), hmul(FI.w, fw)};
        u32 ra[4][4], rb[4][4];
        UNROLL for (int i = 0; i < 4; ++i) {
            const u4 d = *(const u4*)&lds[L_A + ((q * 16 + 4 * wv + ((i + rot) & 3)) * 16 + n) * 4];
            const f4 xr = mfma16(FR2, d), xi = mfma16(FI2, d);
            const f4 ai = xr * Wf[i][1] + xi * Wf[i][0], ar = xr * Wf[i][0] - xi * Wf[i][1];
            const f4 bi = xr * Wf[i][3] + xi * Wf[i][2], br = xr * Wf[i][2] - xi * Wf[i][3];
            u4 ya, yb;
            UNROLL for (int r = 0; r < 4; ++r) { ya[r] = pkh(ai[r], ar[r]); yb[r] = pkh(bi[r], br[r]); }
            const f4 p0 = mfma16(FR, ya), p1 = mfma16(FI, ya), p2 = mfma16(FR, yb), p3 = mfma16(FI, yb);
            UNROLL for (int r = 0; r < 4; ++r) { ra[r][i] = pkh(p0[r], p1[r]); rb[r][i] = pkh(p2[r], p3[r]); }
        }
        UNROLL for (int r = 0; r < 4; ++r) {
            *(u4*)&lds[L_B + ((wv * 16 + 4 * q + r) * 16 + n) * 4] = u4{ra[r][0], ra[r][1], ra[r][2], ra[r][3]};
            *(u4*)&lds[L_B + 4096 + ((wv * 16 + 4 * q + r) * 16 + n) * 4] = u4{rb[r][0], rb[r][1], rb[r][2], rb[r][3]};
        }
    }
    STAMP(11);
    BAR();
    {
        const int vo = 64 * q * 32768 + n * 8, so = WS_V + 4 * wv * 32768 + cls * 4096 + tile * 128;
        UNROLL for (int i = 0; i < 4; ++i) {
            const u4 da = *(const u4*)&lds[L_B + ((q * 16 + 4 * wv + i) * 16 + n) * 4], db = *(const u4*)&lds[L_B + 4096 + ((q * 16 + 4 * wv + i) * 16 + n) * 4];
            const f4 p0 = mfma16(HR[i], da), p1 = mfma16(HI[i], da), p2 = mfma16(HR[i], db), p3 = mfma16(HI[i], db);
            UNROLL for (int r = 0; r < 4; ++r) __builtin_amdgcn_raw_buffer_store_b64(u2{pkh(p0[r], p1[r]), pkh(p2[r], p3[r])}, rws, vo, so + (16 * r + i) * 32768, SC1);
        }
    }
    STAMP(12);
    SB();
    UNROLL for (int i = 0; i < 4; ++i) { const u4 v = *(const u4*)&lds[L_X + i * 1024 + t * 4]; xv[2 * i] = u2{v.x, v.y}; xv[2 * i + 1] = u2{v.z, v.w}; }
    {
        const float g0 = bflo(gv.x), g1 = bfhi(gv.x), g2 = bflo(gv.y), g3 = bfhi(gv.y);
        const b2 one = {(__bf16)1.0f, (__bf16)1.0f};
        UNROLL for (int s1 = 0; s1 < 8; ++s1) {
            const u32 p0 = pkb(bflo(xv[s1].x) * g0, bfhi(xv[s1].x) * g1), p1 = pkb(bflo(xv[s1].y) * g2, bfhi(xv[s1].y) * g3);
            float a = __builtin_amdgcn_fdot2_f32_bf16(__builtin_bit_cast(b2, p0), one, 0.0f, false);
            a = __builtin_amdgcn_fdot2_f32_bf16(__builtin_bit_cast(b2, p1), one, a, false);
            a += __builtin_bit_cast(float, dpp<0xB1>(__builtin_bit_cast(u32, a)));
            a += __builtin_bit_cast(float, dpp<0x4E>(__builtin_bit_cast(u32, a)));
            a += __builtin_bit_cast(float, dpp<0x141>(__builtin_bit_cast(u32, a)));
            a += __builtin_bit_cast(float, dpp<0x140>(__builtin_bit_cast(u32, a)));
            if (n == 0) lds[L_GS + s1 * 16 + (t >> 4)] = __builtin_bit_cast(u32, a);
        }
    }
    u4 MR[4], MI[4]; f2 TW[16];
    { int lane2 = lane; asm volatile("" : "+v"(lane2)); core_setup(lane2, MR, MI, TW); }
    pin(MR); pin(MI); pin(TW);
    const int ve = 2 * q * 4096 + (128 * wv + n) * 8;
    const int ve8 = wv == 3 ? (int)(WS_VL - WS_V) - b * 32768 + b * 128 + 16 * q : 2 * q * 4096 + (128 * wv + 128) * 8, vo8 = ve8 + (wv == 3 ? 8 : 4096);
    u2 v0[9], v1[9];
#define LOAD_V() do { int a_ = ve; pin(a_); \
        UNROLL for (int i = 0; i < 8; ++i) { \
            v0[i] = __builtin_amdgcn_raw_buffer_load_b64(rws, a_ + 128 * i, WS_V + b * 32768, SC1); \
            v1[i] = __builtin_amdgcn_raw_buffer_load_b64(rws, a_ + 128 * i, WS_V + b * 32768 + 4096, SC1); } \
        v0[8] = __builtin_amdgcn_raw_buffer_load_b64(rws, ve8, WS_V + b * 32768, SC1); \
        v1[8] = __builtin_amdgcn_raw_buffer_load_b64(rws, vo8, WS_V + b * 32768, SC1); } while (0)
    VMWAIT(0);
    LOAD_V();
    SB();
    BAR();
    u4 PM[9];
    {
        float gg[4];
        UNROLL for (int k = 0; k < 2; ++k) {
            const f4* pp = (const f4*)&lds[L_GS + ((n >> 2) * 2 + k) * 16];
            const f4 ps = (pp[0] + pp[1]) + (pp[2] + pp[3]);
            const float lg = rbf((ps.x + ps.y) + (ps.z + ps.w));
            gg[2 * k] = rbf(1.0f / (1.0f + __expf(-lg)));
            gg[2 * k + 1] = rbf(1.0f - gg[2 * k]);
        }
        const int r = n & 3, sa = (n >> 2) * 2;
        UNROLL for (int cc = 0; cc < 2; ++cc) {
            const int u1 = 2 * q + cc;
            const float ta = (float)(sa * u1) * 0.125f + ((r & 1) ? 0.25f : (r & 2) ? 0.5f : 0.0f);
            const float tb = (float)((sa + 1) * u1) * 0.125f + ((r & 1) ? ((r & 2) ? 0.0f : 0.5f) : 0.25f);
            const float ca = cosr(ta) * 0.25f, sa_ = sinr(ta) * 0.25f, cb = cosr(tb) * 0.25f, sb_ = sinr(tb) * 0.25f;
            const u32 pa = pkh(gg[0] * ca + gg[2] * cb, gg[0] * sa_ + gg[2] * sb_), pb = pkh(gg[1] * ca + gg[3] * cb, gg[1] * sa_ + gg[3] * sb_);
            UNROLL for (int i = 0; i < 9; ++i) { const u32 f = lds[L_TS + u1 * 36 + 8 * wv + i]; PM[i][2 * cc] = hmul(pa, f); PM[i][2 * cc + 1] = hmul(pb, f); }
        }
    }
    const float osc = __builtin_bit_cast(float, lds[L_GL]);
    pin(PM);
    STAMP(13);
    SB();
    _Pragma("unroll 1") for (int spin = 0; spin < SPIN_CAP; ++spin) {
        u32 m = 0;
        UNROLL for (int i = 0; i < 9; ++i) m = max3u(m, v0[i].x, v1[i].x);
        if (__all(m != ~0u)) break;
        asm volatile("" ::: "memory");
        LOAD_V();
    }
    STAMP(14);
    {
        f4 z[9];
        UNROLL for (int i = 0; i < 9; ++i) z[i] = mfma16(PM[i], u4{v0[i].x, v0[i].y, v1[i].x, v1[i].y});
        if (wv == 0 && n == 0) { z[0].x = 0.5f * (z[0].x + z[0].z); z[0].y = 0.5f * (z[0].y + z[0].w); }
        if (wv == 3) { z[8].z = 0.5f * (z[8].x + z[8].z); z[8].w = 0.5f * (z[8].y + z[8].w); }
        u4* l4 = (u4*)&lds[L_A];
        UNROLL for (int p = 0; p < 2; ++p)
            l4[(q * 8 + wv) * 32 + 16 * p + n] = u4{pkh(z[p].x, z[p + 2].x), pkh(z[p + 4].x, z[p + 6].x), pkh(z[p].y, z[p + 2].y), pkh(z[p + 4].y, z[p + 6].y)};
        l4[(q * 8 + 7 - wv) * 32 + 16 - n] = u4{pkh(z[7].z, z[5].z), pkh(z[3].z, z[1].z), pkh(z[7].w, z[5].w), pkh(z[3].w, z[1].w)};
        f2 m[4];
        UNROLL for (int i = 0; i < 4; ++i) m[i] = n == 0 ? f2{z[8 - 2 * i].z, z[8 - 2 * i].w} : f2{z[6 - 2 * i].z, z[6 - 2 * i].w};
        l4[(q * 8 + 7 - wv) * 32 + ((32 - n) & 31)] = u4{pkh(m[0].x, m[1].x), pkh(m[2].x, m[3].x), pkh(m[0].y, m[1].y), pkh(m[2].y, m[3].y)};
    }
    BAR();
    {
        u4 in[4];
        UNROLL for (int s = 0; s < 4; ++s) in[s] = *(const u4*)&lds[L_A + (wv * 256 + s * 64 + lane) * 4];
        f16v zr, zi;
        core_fft<false>(in, MR, MI, TW, zr, zi);
        zr *= osc; zi *= osc;
        const rsrc_t ro = mkrs(out, 2048 * 2048);
        const int ov = (4 * perm32(lane & 31) + (lane >> 5)) * 16, os = (512 * wv + b) * 2048;
        UNROLL for (int hf = 0; hf < 2; ++hf) {
            u4 a, c;
            UNROLL for (int j = 0; j < 4; ++j) { a[j] = pkb(zi[8 * hf + 2 * j], zi[8 * hf + 2 * j + 1]); c[j] = pkb(zr[8 * hf + 2 * j], zr[8 * hf + 2 * j + 1]); }
            __builtin_amdgcn_raw_buffer_store_b128(a, ro, ov + 32 * hf, os, OAUX);
            __builtin_amdgcn_raw_buffer_store_b128(c, ro, ov + 32 * hf, os + 256 * 2048, OAUX);
        }
    }
    STAMP(15);
    STAMP_OUT();
}

extern "C" void kernel_launch(void* const* d_in, const int* in_sizes, int n_in,
                              void* d_out, int out_size, void* d_ws, size_t ws_size,
                              hipStream_t stream) {
    gfnet_fused<<<256, 320, 0, stream>>>((const u32*)d_in[0], (const u2*)d_in[6], (const unsigned short*)d_in[1], (const unsigned short*)d_in[2],
                                         (const unsigned short*)d_in[4], (const unsigned short*)d_in[5], (char*)d_ws, (u4*)d_out);
}
```

```cpp
#include <hip/hip_runtime.h>
#include <hip/hip_bf16.h>
#include <stdint.h>

typedef _Float16 h2 __attribute__((ext_vector_type(2)));
typedef _Float16 h8 __attribute__((ext_vector_type(8)));
typedef __bf16 b2 __attribute__((ext_vector_type(2)));
typedef __bf16 b8 __attribute__((ext_vector_type(8)));
typedef float f2 __attribute__((ext_vector_type(2)));
typedef float f4 __attribute__((ext_vector_type(4)));
typedef float f16v __attribute__((ext_vector_type(16)));
typedef short s4 __attribute__((ext_vector_type(4)));
typedef unsigned u32;
typedef u32 u2 __attribute__((ext_vector_type(2)));
typedef u32 u4 __attribute__((ext_vector_type(4)));
typedef unsigned short us2 __attribute__((ext_vector_type(2)));
#define DI __device__ __forceinline__
#define UNROLL _Pragma("unroll")
#define NOUNROLL _Pragma("unroll 1")
#define LAS __attribute__((address_space(3)))

#define MSLP 1
#define F1DELAY 12
#define W0WAIT 0
#define GFIRST 1
#define GEARLY 1
#define LDEPTH 3
#define PSLEEP 4
#define W0DELAY 2
#define WWIN 8
#define VCMODE 1
#define VWIN 8
#define ZWAIT 4
#define VWAIT 0
#define XAUX 2
#define W0AUX 0
#define OAUX 2

constexpr u32 FSTR = 16;
constexpr u32 WS_Z = 0;
constexpr u32 WS_V = 4u << 20;
constexpr u32 WS_VL = 12u << 20;
constexpr u32 WS_HB = WS_VL + (32u << 10);
constexpr u32 WS_F1 = WS_HB + (32u << 10);
constexpr u32 WS_F3 = WS_F1 + 2048 * FSTR;
constexpr u32 WS_E = WS_F3 + 2048 * FSTR;
constexpr u32 WS_BYTES = WS_E + 4096;
constexpr size_t WS_META = 24u << 20;
constexpr int SC1 = 16;
constexpr int SPIN_CAP = 8192;
constexpr int LSPIN_CAP = 1 << 17;
constexpr int OOB = 0x7ffffff0;
constexpr float TRUNC_FIX = 1.00066f;
constexpr u32 TM = 0x00010001u;

template <class T> DI void pin(T& v) { asm volatile("" : "+v"(v)); }
template <class T, int N> DI void pin(T (&v)[N]) { UNROLL for (int i = 0; i < N; ++i) pin(v[i]); }
DI u32 opq(u32 v) { asm("" : "+v"(v)); return v; }
DI u32 pkh(float lo, float hi) { h2 r; r.x = (_Float16)lo; r.y = (_Float16)hi; return __builtin_bit_cast(u32, r); }
DI u32 pkb(float lo, float hi) { b2 r; r.x = (__bf16)lo; r.y = (__bf16)hi; return __builtin_bit_cast(u32, r); }
DI float bflo(u32 v) { return __builtin_bit_cast(float, v << 16); }
DI float bfhi(u32 v) { return __builtin_bit_cast(float, v & 0xffff0000u); }
DI float rbf(float v) { return (float)(__bf16)v; }
DI h2 H(u32 v) { return __builtin_bit_cast(h2, v); }
DI u32 U(h2 v) { return __builtin_bit_cast(u32, v); }
DI f16v mfma32(u4 a, u4 b, f16v c) { return __builtin_amdgcn_mfma_f32_32x32x16_f16(__builtin_bit_cast(h8, a), __builtin_bit_cast(h8, b), c, 0, 0, 0); }
DI f4 mfma16(u4 a, u4 b) { return __builtin_amdgcn_mfma_f32_16x16x32_f16(__builtin_bit_cast(h8, a), __builtin_bit_cast(h8, b), f4{}, 0, 0, 0); }
DI f4 mfma16b(u4 a, u4 b) { return __builtin_amdgcn_mfma_f32_16x16x32_bf16(__builtin_bit_cast(b8, a), __builtin_bit_cast(b8, b), f4{}, 0, 0, 0); }
DI u32 hmul(u32 a, u32 b) { return U(H(a) * H(b)); }
DI u32 hfma(u32 a, u32 b, u32 c) { return U(__builtin_elementwise_fma(H(a), H(b), H(c))); }
DI u32 cmul_swap(u32 x, u32 w) { const h2 a = H(opq(x)), c = H(opq(w)); h2 n = c; n.y = -n.y; return U(__builtin_elementwise_fma(a.yy, n, a.xx * c.yx)); }
DI u32 cmul_conj(u32 x, u32 t) { const h2 a = H(opq(x)), c = H(opq(t)); h2 n = c.yy; n.y = -n.y; return U(__builtin_elementwise_fma(a.yx, n, a * c.xx)); }
DI u32 pkmax(u32 a, u32 b) { return __builtin_bit_cast(u32, __builtin_elementwise_max(__builtin_bit_cast(us2, a), __builtin_bit_cast(us2, b))); }
DI float sinr(float r) { return __builtin_amdgcn_sinf(r); }
DI float cosr(float r) { return __builtin_amdgcn_cosf(r); }
DI int perm32(int n) { return (n & 0x13) | ((n & 4) << 1) | ((n & 8) >> 1); }
DI u32 max3u(u32 a, u32 b, u32 c) { return max(max(a, b), c); }
DI u32 max4u(u4 v) { return max(max(v.x, v.y), max(v.z, v.w)); }
DI u32 min4u(u4 v) { return min(min(v.x, v.y), min(v.z, v.w)); }
DI u32 mark(u32 v, u32 par, u32 tm) { return (par & tm) | (v & ~tm); }
DI u32 rfl(u32 v) { return __builtin_amdgcn_readfirstlane(v); }
DI u32 pow2h(int d) { const float f = __builtin_bit_cast(float, (u32)(127 - min(d, 64)) << 23); return pkh(f, f); }

typedef __amdgpu_buffer_rsrc_t rsrc_t;
DI rsrc_t mkrs(const void* p, u32 bytes) { return __builtin_amdgcn_make_buffer_rsrc((void*)p, (short)0, (int)bytes, 0x00020000); }
template <int AUX> DI void store128(u4 v, rsrc_t r, int off) { __builtin_amdgcn_raw_buffer_store_b128(v, r, off, 0, AUX); }
#define SB() __builtin_amdgcn_sched_barrier(0)
#define MEMBAR() asm volatile("" ::: "memory")
#define VMWAIT(n) asm volatile("s_waitcnt vmcnt(%0)" :: "n"(n) : "memory")
#define STAMP_DECL
#define STAMP(i)
#define STAMP_OUT()
template <int CTRL> DI u32 dpp(u32 v) { return __builtin_amdgcn_update_dpp(0u, v, CTRL, 0xf, 0xf, true); }
DI u32 wave_maxu(u32 v) {
    v = max(v, dpp<0xB1>(v)); v = max(v, dpp<0x4E>(v)); v = max(v, dpp<0x141>(v)); v = max(v, dpp<0x140>(v));
    return max(max((u32)__builtin_amdgcn_readlane(v, 0), (u32)__builtin_amdgcn_readlane(v, 16)), max((u32)__builtin_amdgcn_readlane(v, 32), (u32)__builtin_amdgcn_readlane(v, 48)));
}

DI void core_setup(int lane, u4 (&M)[6]) {
    float fc = (float)perm32(lane & 31) * (1.0f / 1024.0f), p0 = (float)(4 * (lane >> 5)) * fc;
    pin(fc); pin(p0);
    UNROLL for (int s = 0; s < 4; ++s) {
        float cs[4], sn[4], tc[4], ts[4];
        UNROLL for (int i = 0; i < 4; ++i) {
            const float pt = fmaf((float)(8 * s + i), fc, p0);
            if (s < 2) { cs[i] = cosr(pt * 32.0f); sn[i] = sinr(pt * 32.0f); }
            tc[i] = cosr(pt); ts[i] = sinr(pt);
        }
        M[2 + s] = u4{pkh(tc[0], tc[1]), pkh(tc[2], tc[3]), pkh(ts[0], ts[1]), pkh(ts[2], ts[3])};
        if (s < 2) M[s] = u4{pkh(cs[0], cs[1]), pkh(cs[2], cs[3]), pkh(sn[0], sn[1]), pkh(sn[2], sn[3])};
    }
}
DI void core_expand(int lane, u4 (&MR)[4], u4 (&MI)[4]) {
    const u32 sg = (lane & 1) ? 0x80008000u : 0u;
    MR[2] = MR[0] ^ sg; MR[3] = MR[1] ^ sg;
    UNROLL for (int s = 0; s < 4; ++s) MI[s] = u4{MR[s].z ^ 0x80008000u, MR[s].w ^ 0x80008000u, MR[s].x, MR[s].y};
}
template <bool OUT_B> DI void core_fft(const u4 (&in)[4], const u4 (&MR)[4], const u4 (&MI)[4], const u4 (&TW)[4], f16v& outr, f16v& outi) {
    f16v yr = {}, yi = {};
    UNROLL for (int s = 0; s < 4; ++s) yr = mfma32(in[s], MR[s], yr);
    SB();
    yi = mfma32(in[0], MI[0], yi); yi = mfma32(in[1], MI[1], yi);
    SB();
    h2 t0[8], t1[8];
#define TW_RE(p) do { const h2 pr_ = H(pkh(yr[2 * (p)], yr[2 * (p) + 1])); t0[p] = pr_ * H(TW[(p) >> 1][(p) & 1]); t1[p] = pr_ * H(TW[(p) >> 1][2 + ((p) & 1)]); } while (0)
    TW_RE(0); TW_RE(1); SB();
    yi = mfma32(in[2], MI[2], yi);
    TW_RE(2); TW_RE(3); SB();
    yi = mfma32(in[3], MI[3], yi);
    TW_RE(4); TW_RE(5); TW_RE(6); TW_RE(7); SB();
    outr = f16v{}; outi = f16v{};
    u4 d[4];
#define TW_IM(s) do { UNROLL for (int j = 0; j < 2; ++j) { const int p_ = 2 * (s) + j; const h2 pi_ = H(pkh(yi[2 * p_], yi[2 * p_ + 1])); \
        d[s][j] = U(__builtin_elementwise_fma(pi_, H(TW[s][2 + j]), t0[p_])); d[s][2 + j] = U(__builtin_elementwise_fma(pi_, H(TW[s][j]), -t1[p_])); } } while (0)
    TW_IM(0); SB();
    UNROLL for (int s = 0; s < 4; ++s) {
        outr = OUT_B ? mfma32(MR[s], d[s], outr) : mfma32(d[s], MR[s], outr);
        if (s < 3) TW_IM(s + 1);
        SB();
    }
    UNROLL for (int s = 0; s < 4; ++s) outi = OUT_B ? mfma32(MI[s], d[s], outi) : mfma32(d[s], MI[s], outi);
}
DI u32 rot(float ph) { return pkh(cosr(ph), sinr(ph)); }
DI u32 quarter(u32 r, u32 f) { u32 o; asm("v_pk_mul_f16 %0, %1, %2 op_sel:[1,0] op_sel_hi:[0,1] neg_lo:[1,0]" : "=v"(o) : "v"(r), "v"(f)); return o; }
DI u4 quarter(u4 r) { u32 one = 0x3c003c00u; pin(one); return u4{quarter(r.x, one), quarter(r.y, one), quarter(r.z, one), quarter(r.w, one)}; }

DI u32 tag_of(u32 epoch, u32 idx) { u32 h = (epoch ^ (idx * 0x9E3779B1u)) * 0x85EBCA6Bu; return h ^ (h >> 15); }
DI u32 parity_of(u32 epoch) { return (epoch & 1u) ? 0x00000001u : 0x00010000u; }
DI void flag_store(rsrc_t rws, u32 arr, int b, int lane, u32 payload, u32 tag) {
    __builtin_amdgcn_raw_buffer_store_b64(u2{payload, tag}, rws, lane < 8 ? lane * (int)(256 * FSTR) : OOB, arr + b * FSTR, SC1);
}
DI u4 flag_wait(rsrc_t rws, u32 arr, int b, int lane, u32 epoch) {
    u2 f[4]; u32 tg[4];
    UNROLL for (int j = 0; j < 4; ++j) tg[j] = tag_of(epoch, arr + 4 * lane + j);
    NOUNROLL for (int spin = 0; spin < SPIN_CAP; ++spin) {
        MEMBAR();
        UNROLL for (int j = 0; j < 4; ++j) f[j] = __builtin_amdgcn_raw_buffer_load_b64(rws, lane * (4 * FSTR) + j * FSTR, arr + (b & 7) * (256 * FSTR), SC1);
        if (__all(f[0].y == tg[0] && f[1].y == tg[1] && f[2].y == tg[2] && f[3].y == tg[3])) break;
        __builtin_amdgcn_s_sleep(PSLEEP);
    }
    return u4{f[0].x, f[1].x, f[2].x, f[3].x};
}
DI int cls_of(int b) { const int row = (b & 7) >> 1, sel = b >> 7; return row == 0 ? 4 * sel : (sel ? 8 - row : row); }
DI int tile_of(int b) { return 16 * (b & 1) + ((b >> 3) & 15); }
DI bool is_helper(int b) { return (b & 0x79) == 0x79; }

constexpr int L_EW = 0;
constexpr int L_C1 = 4, L_WW = 8, L_C3 = 12, L_C4 = 16, L_C5 = 20, L_C6 = 24;
constexpr int L_HE = 28;
constexpr int L_OK = 32;
constexpr int L_OSC = 36;
constexpr int L_PR = 40;
constexpr int L_LD = 44;
constexpr int L_MOK = 45;
constexpr int L_NFLAG = 64;
constexpr int L_SC = 64;
constexpr int L_GS = 320;
constexpr int L_TS = 448;
constexpr int L_M = 768;
constexpr int L_A1 = L_M + 1536;
constexpr int L_A2 = L_A1 + 4096;
constexpr int L_B = L_A2 + 4096;
constexpr int L_W = L_B + 8192;
constexpr int L_END = L_W + 64 * 256;

DI void lset(u32* lds, int idx, u32 v, int lane) { MEMBAR(); if (lane == 0) lds[idx] = v; MEMBAR(); }
DI u32 lget(u32* lds, int idx) { MEMBAR(); return rfl(lds[idx]); }
DI u32 lwait(u32* lds, int idx, u32 atleast, int slp) {
    u32 v = 0;
    NOUNROLL for (int spin = 0; spin < LSPIN_CAP; ++spin) { v = lget(lds, idx); if (v >= atleast) break; if (slp) __builtin_amdgcn_s_sleep(1); }
    MEMBAR();
    return v;
}
DI u4 lwait4(u32* lds, int idx) {
    u4 v;
    NOUNROLL for (int spin = 0; spin < LSPIN_CAP; ++spin) { MEMBAR(); v = *(const u4*)&lds[idx]; if (rfl(min4u(v))) break; }
    MEMBAR();
    return v;
}

DI void lwait4s(u32* lds, int idx) {
    NOUNROLL for (int spin = 0; spin < LSPIN_CAP; ++spin) { MEMBAR(); const u4 v = *(const u4*)&lds[idx]; if (rfl(min4u(v))) break; __builtin_amdgcn_s_sleep(1); }
    MEMBAR();
}

__global__ __launch_bounds__(512, 2) void gfnet_fused(const void* x, const void* gw, const void* war, const void* wai, const void* wbr, const void* wbi,
                                                      char* ws, void* out) {
    __shared__ __attribute__((aligned(16))) u32 lds[L_END];
    STAMP_DECL;
    STAMP(0);
    asm volatile("" :: "s"(x), "s"(gw), "s"(war), "s"(wai), "s"(wbr), "s"(wbi), "s"(ws), "s"(out));
    const int t = threadIdx.x, lane = t & 63, wv = __builtin_amdgcn_readfirstlane(t >> 6), b = blockIdx.x;
    const int q = lane >> 4, n = lane & 15;
    const rsrc_t rws = mkrs(ws, WS_BYTES);
    const int row = (b & 7) >> 1, sel = b >> 7, cls = cls_of(b), tile = tile_of(b);
    const bool packed = row == 0;

    if (wv >= 4) {
        const u32 ev = __builtin_amdgcn_raw_buffer_load_b32(rws, 0, WS_E, SC1);
        if (wv == 4) lds[lane] = 0u;
        asm volatile("s_waitcnt lgkmcnt(0)" ::: "memory");
        __builtin_amdgcn_s_barrier();
        MEMBAR();
        u32 he = 0;
        u32 hw[4][4];
        const bool helper = wv == 7 && is_helper(b);
        if (wv > 4) {
            const int wvo = (lane >> 2) * (128 * 1026) + (lane & 3) * 16, wso = (cls * 513 + 16 * tile - cls) * 2;
            const rsrc_t rw[4] = {mkrs(war, 2048 * 1026), mkrs(wai, 2048 * 1026), mkrs(wbr, 2048 * 1026), mkrs(wbi, 2048 * 1026)};
            const u32 ldp = (u32)(size_t)(LAS u32*)&lds[L_LD];
#define WLOAD(c, a) __builtin_amdgcn_raw_ptr_buffer_load_lds(rw[a], (LAS void*)&lds[L_W + ((c) * 4 + (a)) * 256], 16, wvo, wso + (c) * 8208, 0, 0)
            if ((lane & 3) < 3) {
                if (wv == 5) {
                    __builtin_amdgcn_s_sleep(W0DELAY);
                    UNROLL for (int st = 0; st < 4 + LDEPTH; ++st) {
                        if (st >= LDEPTH) {
                            if (st < 4) VMWAIT(4 * (LDEPTH - 1)); else VMWAIT(4 * (3 + LDEPTH - st));
                            asm volatile("ds_write_b32 %0, %1" :: "v"(ldp), "v"(st - LDEPTH + 1) : "memory");
                        }
                        if (st < 4) { UNROLL for (int a = 0; a < 4; ++a) WLOAD(4 * st + (tile & 3), a); }
                    }
                } else {
                    NOUNROLL for (int st = 0; st < 4; ++st) {
                        NOUNROLL for (int spin = 0; spin < LSPIN_CAP; ++spin) {
                            u32 v; asm volatile("ds_read_b32 %0, %1\n\ts_waitcnt lgkmcnt(0)" : "=v"(v) : "v"(ldp) : "memory");
                            if ((int)rfl(v) > st) break;
                            __builtin_amdgcn_s_sleep(1);
                        }
                        UNROLL for (int r = 1; r < 4; ++r) {
                            const int c = 4 * st + ((tile + r) & 3);
                            if (wv == 6) { WLOAD(c, 0); WLOAD(c, 1); } else { WLOAD(c, 2); WLOAD(c, 3); }
                        }
                    }
                }
            }
            STAMP(1);
            VMWAIT(0);
            lset(lds, L_PR + wv - 4, 1u, lane);
            STAMP(2);
        } else {
            u4 M[6];
            core_setup(lane, M);
            UNROLL for (int i = 0; i < 6; ++i) *(u4*)&lds[L_M + i * 256 + lane * 4] = M[i];
            lset(lds, L_MOK, 1u, lane);
            lset(lds, L_PR, 1u, lane);
            STAMP(1);
        }
        const u32 epoch = rfl(ev) + 1u;
        if (wv == 7) {
            if (helper) {
                lwait4s(lds, L_PR);
                UNROLL for (int r = 0; r < 4; ++r) UNROLL for (int a = 0; a < 4; ++a) {
                    hw[a][r] = ((const unsigned short*)&lds[L_W + (n * 4 + a) * 256 + (4 * q + r) * 16])[16 + cls];
                    he = max(he, hw[a][r] & 0x7fffu);
                }
                he = min(wave_maxu(he) >> 7, 240u);
            }
            lset(lds, L_HE, he | 0x100u, lane);
            STAMP(3);
        }
        if (wv == 5) {
            lwait4s(lds, L_WW);
            const u32 hx = lwait(lds, L_HE, 1u, 1);
            flag_store(rws, WS_F3, b, lane, max4u(*(const u4*)&lds[L_WW]) | ((hx & 0xffu) << 8), tag_of(epoch, WS_F3 + b));
        }
        if (wv == 4) {
            lwait(lds, L_C1, 1u, 1);
            __builtin_amdgcn_s_sleep(F1DELAY);
            STAMP(3);
            u32 emax;
            {
                const u4 f = flag_wait(rws, WS_F1, b, lane, epoch) & 0xffu;
                emax = wave_maxu(max4u(f));
                *(u4*)&lds[L_SC + 4 * lane] = u4{pow2h(emax + 4 - f.x), pow2h(emax + 4 - f.y), pow2h(emax + 4 - f.z), pow2h(emax + 4 - f.w)};
                lset(lds, L_OK + 1, 1u, lane);
            }
            STAMP(4);
            lwait(lds, L_C3, 1u, 1);
            {
                const u4 f = flag_wait(rws, WS_F3, b, lane, epoch);
                u32 m = 0;
                UNROLL for (int j = 0; j < 4; ++j) m = max3u(m, f[j] & 0xffu, (f[j] >> 8) & 0xffu);
                const u32 ewm = wave_maxu(m);
                UNROLL for (int j = 0; j < 4; ++j) {
                    const int p = 4 * lane + j;
                    lds[L_TS + cls_of(p) * 36 + tile_of(p)] = pow2h(ewm + 5 - (f[j] & 0xffu));
                    if (is_helper(p)) lds[L_TS + cls_of(p) * 36 + 32] = pow2h(ewm + 5 - ((f[j] >> 8) & 0xffu));
                }
                lset(lds, L_OSC, (u32)min(max((int)emax + (int)ewm - 130, 1), 254) << 23, lane);
                lset(lds, L_OK + 3, 1u, lane);
            }
            STAMP(5);

        } else if (helper) {
            {
                const int hc = cls;
                u4 GR, FR, HR, T1, T2, WH[2];
                UNROLL for (int j = 0; j < 4; ++j) {
                    GR[j] = rot((float)((4 * q + j) * (hc + 8 * n)) * (1.0f / 128.0f) + (hc == 4 ? 0.25f : 0.0f));
                    HR[j] = rot((float)(n * (hc + 8 * (4 * q + j))) * (1.0f / 128.0f));
                    FR[j] = rot((float)(n * (4 * q + j)) * (1.0f / 16.0f));
                    T1[j] = rot((float)((4 * q + j) * (hc + 8 * n)) * (1.0f / 2048.0f));
                    T2[j] = rot((float)(n * (hc + 8 * (4 * q + j))) * (1.0f / 2048.0f));
                    const float sw = __builtin_bit_cast(float, (249u - he) << 23);
                    WH[0][j] = pkh(bflo(hw[0][j]) * sw, bflo(hw[1][j]) * sw); WH[1][j] = pkh(bflo(hw[2][j]) * sw, bflo(hw[3][j]) * sw);
                }
                u4 GI = quarter(GR), HI = quarter(HR), FI = quarter(FR);
                pin(GR); pin(GI); pin(HR); pin(HI); pin(FR); pin(FI); pin(T1); pin(T2); pin(WH);
                lwait(lds, L_OK + 1, 1u, 1);
                const u32 tg = tag_of(epoch, 0u), par = parity_of(epoch);
                u4 hz;
                NOUNROLL for (int spin = 0; spin < SPIN_CAP; ++spin) {
                    MEMBAR();
                    u32 bad = 0;
                    UNROLL for (int j = 0; j < 4; ++j) { const u2 g = __builtin_amdgcn_raw_buffer_load_b64(rws, (64 * q + n) * 16 + j * 256, WS_HB + hc * 4096, SC1); hz[j] = g.x & ~TM; bad |= g.y ^ tg; }
                    if (__all(bad == 0u)) break;
                    __builtin_amdgcn_s_sleep(PSLEEP);
                }
                STAMP(4);
                const u32 msk = hc == 0 ? 0x0000ffffu : hc == 4 ? 0xffff0000u : ~0u, sg = hc > 4 ? 0x80000000u : 0u;
                u4 d, o[2];
                UNROLL for (int j = 0; j < 4; ++j) d[j] = hmul(hz[j], (lds[L_SC + 64 * q + 16 * j + n] & msk) | sg);
                f4 yr = mfma16(d, GR), yi = mfma16(d, GI);
                UNROLL for (int r = 0; r < 4; ++r) d[r] = cmul_conj(pkh(yr[r], yi[r]), T1[r]);
                const f4 xr = mfma16(FR, d), xi = mfma16(FI, d);
                UNROLL for (int f = 0; f < 2; ++f) {
                    UNROLL for (int r = 0; r < 4; ++r) d[r] = cmul_swap(pkh(xr[r], xi[r]), WH[f][r]);
                    yr = mfma16(d, FR); yi = mfma16(d, FI);
                    UNROLL for (int r = 0; r < 4; ++r) o[f][r] = cmul_conj(pkh(yr[r], yi[r]), T2[r]);
                }
                const f4 p0 = mfma16(HR, o[0]), p1 = mfma16(HI, o[0]), p2 = mfma16(HR, o[1]), p3 = mfma16(HI, o[1]);
                UNROLL for (int r = 0; r < 4; ++r)
                    store128<SC1>(u4{mark(pkh(p0[r], p1[r]), par, TM), tg, mark(pkh(p2[r], p3[r]), par, TM), tg}, rws, (64 * q + n) * 128 + WS_VL + 16 * r * 128 + hc * 16);
                STAMP(5);
            }
        }
        STAMP_OUT();
        return;
    }

#define GATE_BLOCK do { pin(xv); \
        const f2 g01 = {bflo(gv.x), bfhi(gv.x)}, g23 = {bflo(gv.y), bfhi(gv.y)}; \
        f4 acc = {}; \
        UNROLL for (int i = 0; i < 4; ++i) { \
            u4 pp; \
            UNROLL for (int k = 0; k < 2; ++k) { \
                const f2 a = f2{bflo(xv[2 * i + k].x), bfhi(xv[2 * i + k].x)} * g01, c = f2{bflo(xv[2 * i + k].y), bfhi(xv[2 * i + k].y)} * g23; \
                pp[2 * k] = pkb(a.x, a.y); pp[2 * k + 1] = pkb(c.x, c.y); } \
            const u32 s0 = n == 2 * i ? 0x3f803f80u : 0u, s1 = n == 2 * i + 1 ? 0x3f803f80u : 0u; \
            acc = __builtin_amdgcn_mfma_f32_16x16x32_bf16(__builtin_bit_cast(b8, pp), __builtin_bit_cast(b8, u4{s0, s0, s1, s1}), acc, 0, 0, 0); } \
        if (n < 8) lds[L_GS + n * 16 + (t >> 4)] = __builtin_bit_cast(u32, (acc.x + acc.y) + (acc.z + acc.w)); \
        lset(lds, L_C5 + wv, 1u, lane); } while (0)
#define GATE_EARLY GATE_BLOCK
#define GATE_LATE
    u2 xv[8];
    {
        const rsrc_t rx = mkrs(x, 2048 * 2048);
        UNROLL for (int s1 = 0; s1 < 8; ++s1) xv[s1] = __builtin_amdgcn_raw_buffer_load_b64(rx, t * 8, (256 * s1 + b) * 2048, XAUX);
    }
    const u32 ev = __builtin_amdgcn_raw_buffer_load_b32(rws, 0, WS_E, SC1);
    const u2 gv = __builtin_amdgcn_raw_buffer_load_b64(mkrs(gw, 2048), t * 8, 0, 0);
    MEMBAR();
    SB();
    __builtin_amdgcn_s_barrier();
    MEMBAR();
    __builtin_amdgcn_s_setprio(1);
    u4 MR[4], MI[4], TW[4];
    u4 FR, FI, GR[4], GI[4], HR[4], HI[4], sel1;
    UNROLL for (int j = 0; j < 4; ++j) FR[j] = rot((float)(n * (4 * q + j)) * (1.0f / 16.0f));
    FI = quarter(FR);
    {
        const float ofs = cls == 4 ? 0.25f : 0.0f;
        float gu = (float)(cls + 8 * n) * (1.0f / 2048.0f), g0 = (float)(64 * q + 4 * wv) * gu + ofs;
        pin(gu); pin(g0);
        UNROLL for (int i = 0; i < 4; ++i) UNROLL for (int j = 0; j < 4; ++j) GR[i][j] = rot(fmaf((float)(16 * j + i), gu, g0));
    }
    pin(FR); pin(FI); pin(GR);
    const int zc = perm32(lane & 31) + (lane >> 5) * 256, zcu = zc + ((lane & 31) == 0 ? 543 : 511);
    int zl = (zc >> 4) * 16384 + (zc & 15) * 4, zu = (zcu >> 4) * 16384 + (zcu & 15) * 4;
    const int zh0 = wv * 4096, zh1 = (wv == 0 ? 4 : 8 - wv) * 4096;
    int zh = lane == 0 ? zh0 : zh1; pin(zh); zh = lane < 2 ? zh : OOB;
    pin(zl); pin(zu); pin(zh);
    u4 tags;
    u32 tm = TM; pin(tm);
    STAMP(1);
    SB();
    {
        h2 hi = H(xv[0].x), lo = hi;
        hi = __builtin_elementwise_maximum(hi, H(xv[0].y)); lo = __builtin_elementwise_minimum(lo, H(xv[0].y));
        UNROLL for (int s1 = 1; s1 < 8; ++s1) {
            hi = __builtin_elementwise_maximum(__builtin_elementwise_maximum(hi, H(xv[s1].x)), H(xv[s1].y));
            lo = __builtin_elementwise_minimum(__builtin_elementwise_minimum(lo, H(xv[s1].x)), H(xv[s1].y));
        }
        const u32 mx = pkmax(U(hi) & 0x7fff7fffu, U(lo) & 0x7fff7fffu);
        lset(lds, L_EW + wv, min(max(wave_maxu(max(mx & 0xffffu, mx >> 16)) >> 7, 8u), 247u), lane);
        STAMP(2);
        f2 e0[2], e1[2], c0[2], c2[2], dm[2], dp[2], d0[2], d1[2];
        UNROLL for (int j = 0; j < 2; ++j) {
            f2 v[8];
            UNROLL for (int s1 = 0; s1 < 8; ++s1) v[s1] = f2{bflo(xv[s1][j]), bfhi(xv[s1][j])};
            const f2 a0 = v[0] + v[4], a1 = v[1] + v[5], a2 = v[2] + v[6], a3 = v[3] + v[7];
            const f2 b1 = v[1] - v[5], b3 = v[3] - v[7];
            c0[j] = v[0] - v[4]; c2[j] = v[2] - v[6];
            e0[j] = a0 + a2; e1[j] = a1 + a3; dm[j] = b1 - b3; dp[j] = b1 + b3; d0[j] = a0 - a2; d1[j] = a3 - a1;
        }
        pin(e0); pin(e1); pin(c0); pin(c2); pin(dm); pin(dp); pin(d0); pin(d1);
        const u32 epoch = rfl(ev) + 1u;
        tags = u4{parity_of(epoch), tag_of(epoch, WS_F1 + b), epoch, tag_of(epoch, 0u)};
        u4 ews;
        NOUNROLL for (int spin = 0; spin < LSPIN_CAP; ++spin) {
            MEMBAR();
            ews = *(const u4*)&lds[L_EW];
            const u32 mok = lds[L_MOK];
            if (rfl(min(min4u(ews), mok))) break;
        }
        MEMBAR();
        MR[0] = *(const u4*)&lds[L_M + lane * 4]; MR[1] = *(const u4*)&lds[L_M + 256 + lane * 4];
        UNROLL for (int i = 0; i < 4; ++i) TW[i] = *(const u4*)&lds[L_M + (2 + i) * 256 + lane * 4];
        const u32 emax = rfl(max4u(ews));
        if (wv == 0) flag_store(rws, WS_F1, b, lane, emax, tags.y);
        const float sc = __builtin_bit_cast(float, (250u - emax) << 23);
        const float rs = sc * 0.70710678118654752f;
        u32 o[8][2];
        UNROLL for (int j = 0; j < 2; ++j) {
            const f2 e1s = e1[j] * sc, b0 = c0[j] * sc, b2_ = c2[j] * sc;
            f2 r[8];
            r[0] = e0[j] * sc + e1s; r[1] = e0[j] * sc - e1s;
            r[2] = dm[j] * rs + b0; r[3] = -(dp[j] * rs) - b2_;
            r[4] = d0[j] * sc; r[5] = d1[j] * sc;
            r[6] = b0 - dm[j] * rs; r[7] = b2_ - dp[j] * rs;
            UNROLL for (int p = 0; p < 8; ++p) o[p][j] = pkh(r[p].x, r[p].y);
        }
        UNROLL for (int p = 0; p < 8; ++p) *(u2*)&lds[L_A1 + p * 512 + 2 * t] = u2{o[p][0], o[p][1]};
        lset(lds, L_C1 + wv, 1u, lane);
        core_expand(lane, MR, MI);
        pin(MR); pin(MI);
        u4 in[4];
        {
            const int h = lane >> 5, blk = (lane >> 4) & 1, qq = (lane & 15) >> 2, p = lane & 3;
            const int off = L_A1 + wv * 1024 + (4 * h + qq) * 16 + blk * 8 + p * 2;
            NOUNROLL for (int spin = 0; spin < LSPIN_CAP; ++spin) {
                MEMBAR();
                const u4 c = *(const u4*)&lds[L_C1];
                MEMBAR();
                UNROLL for (int s = 0; s < 4; ++s) {
                    const u2 re = __builtin_bit_cast(u2, __builtin_amdgcn_ds_read_tr16_b64_v4i16((LAS s4*)&lds[off + s * 128]));
                    const u2 im = __builtin_bit_cast(u2, __builtin_amdgcn_ds_read_tr16_b64_v4i16((LAS s4*)&lds[off + s * 128 + 512]));
                    in[s] = u4{re.x, re.y, im.x, im.y};
                }
                if (rfl(min4u(c))) break;
            }
        }
        STAMP(3);
        f16v zr, zi;
        core_fft<true>(in, MR, MI, TW, zr, zi);
        u32 z[16];
        UNROLL for (int r = 0; r < 16; ++r) z[r] = mark(pkh(zr[r], zi[r]), tags.x, tm);
        STAMP(4);
        const int zp = ((b >> 2) & 3) * 64 + (b & 3) * 16 + ((b >> 4) & 3) * 4 + (b >> 6);
        const int zs = WS_Z + wv * 64 * 16384 + zp * 64;
        UNROLL for (int r = 0; r < 8; ++r) __builtin_amdgcn_raw_buffer_store_b32(z[r], rws, zl, zs + r * 32768, SC1);
        const u32 zn = rfl(z[8]), za = __builtin_amdgcn_readlane(z[8], 32), zb = __builtin_amdgcn_readlane(z[0], 0);
        MEMBAR();
        const u32 zt = lane < 32 ? za : zb;
        UNROLL for (int r = 8; r < 16; ++r) __builtin_amdgcn_raw_buffer_store_b32((lane & 31) == 0 ? (r < 15 ? z[r + 1] : zt) : z[r], rws, zu, zs + (r & 7) * 32768, SC1);
        __builtin_amdgcn_raw_buffer_store_b64(u2{zn, tags.w}, rws, zh, WS_HB + b * 16, SC1);
    }
    STAMP(5);
    SB();
    {
        float hs[4], hu[4];
        UNROLL for (int i = 0; i < 4; ++i) { hs[i] = (float)(16 * n + 4 * wv + i) * (1.0f / 2048.0f); hu[i] = (float)(cls + 32 * q + 8 * i); }
        pin(hs); pin(hu);
        UNROLL for (int i = 0; i < 4; ++i) { UNROLL for (int j = 0; j < 4; ++j) { float a = hs[i] * hu[j]; pin(a); HR[i][j] = rot(a); } }
    }
    pin(HR);
    {
        const int d = n + cls - 8 * q;
        UNROLL for (int j = 0; j < 4; ++j) sel1[j] = (d >> 1) == j ? ((d & 1) ? 0x3f800000u : 0x3f80u) : 0u;
        pin(sel1);
    }
    GATE_EARLY;
    STAMP(6);
    u32 z1[4][4], z2[4][4];
    const int zvm = q * 64 + (63 - tile) * 16384 + (15 - n) * 4;
    const int zv1 = cls <= 4 ? q * 64 + tile * 16384 + n * 4 : zvm, zso = WS_Z + row * 64 * 16384 + wv * 4096;
#define LOAD_Z() do { \
        if (packed) { int a_ = zvm; pin(a_); UNROLL for (int i = 0; i < 4; ++i) UNROLL for (int j = 0; j < 4; ++j) z2[i][j] = __builtin_amdgcn_raw_buffer_load_b32(rws, a_ + (4 * i + j) * 256, zso, SC1); } \
        else { UNROLL for (int i = 0; i < 4; ++i) UNROLL for (int j = 0; j < 4; ++j) z2[i][j] = tags.x; } \
        int c_ = zv1; pin(c_); UNROLL for (int i = 0; i < 4; ++i) UNROLL for (int j = 0; j < 4; ++j) z1[i][j] = __builtin_amdgcn_raw_buffer_load_b32(rws, c_ + (4 * i + j) * 256, zso, SC1); } while (0)
    VMWAIT(ZWAIT);
    LOAD_Z();
    SB();
    {
        u4 scl[4];
        lwait(lds, L_OK + 1, 1u, MSLP);
        UNROLL for (int j = 0; j < 4; ++j) scl[j] = *(const u4*)&lds[L_SC + 64 * q + 16 * j + 4 * wv];
        SB();
        UNROLL for (int i = 0; i < 4; ++i) UNROLL for (int j = 0; j < 4; ++j) { GI[i][j] = quarter(GR[i][j], scl[j][i]); GR[i][j] = hmul(GR[i][j], scl[j][i]); }
    }
    pin(GR); pin(GI);
    STAMP(7);
    u4 WA[4], WB[4];
    {
        lwait4s(lds, L_PR);
        u4 wr[4][4];
        int wl = L_W / 4 + wv * 1024 + n * 4 + min(q, 2); pin(wl);
        UNROLL for (int i = 0; i < 4; ++i) UNROLL for (int a = 0; a < 4; ++a) wr[i][a] = ((const u4*)lds)[wl + (i * 4 + a) * 64];
        SB();
        f4 wf[4][4];
        float wmax = 0.0f;
        UNROLL for (int k = 0; k < 20; ++k) {
            if (k < 16) wf[k >> 2][k & 3] = mfma16b(wr[k >> 2][k & 3], sel1);
            if (k >= 4) { const f4 v = wf[(k - 4) >> 2][k & 3]; wmax = fmaxf(fmaxf(wmax, fabsf(v.x)), fabsf(v.y)); wmax = fmaxf(fmaxf(wmax, fabsf(v.z)), fabsf(v.w)); }
            SB();
        }
        lset(lds, L_WW + wv, min(max(wave_maxu(__builtin_bit_cast(u32, wmax)) >> 23, 1u), 240u), lane);
        UNROLL for (int i = 0; i < 4; ++i) HI[i] = quarter(HR[i]);
        pin(HI);
        const u32 ewt = rfl(max4u(lwait4(lds, L_WW)));
        const float sw = __builtin_bit_cast(float, (249u - ewt - (packed ? 1u : 0u)) << 23);
        UNROLL for (int i = 0; i < 4; ++i) {
            const f4 ar = wf[i][0] * sw, ai = wf[i][1] * sw, br = wf[i][2] * sw, bi = wf[i][3] * sw;
            UNROLL for (int r = 0; r < 4; ++r) { WA[i][r] = pkh(ar[r], ai[r]); WB[i][r] = pkh(br[r], bi[r]); }
        }
        pin(WA); pin(WB);
    }
    u32 x1 = tags.x ^ (cls > 4 ? 0x80000000u : 0u), x2 = tags.x ^ (cls == 4 ? 0x00008000u : cls == 0 ? 0x80000000u : 0u);
    const int lw = ((wv * 16 + 4 * q) * 16 + n) * 4;
    int lr = (q * 16 + 4 * wv) * 16 + n;
    const u4* l4 = (const u4*)lds;
    pin(GR); pin(GI); pin(x1); pin(x2); pin(lr);
    STAMP(8);
    SB();
    {
        f4 yr[4], yi[4];
        NOUNROLL for (int spin = 0; spin < SPIN_CAP; ++spin) {
            u32 m = 0;
            UNROLL for (int i = 0; i < 4; ++i) {
                u4 d;
                UNROLL for (int j = 0; j < 4; ++j) { z1[i][j] ^= x1; z2[i][j] ^= x2; }
                UNROLL for (int j = 0; j < 4; ++j) d[j] = U(H(z1[i][j]) + H(z2[i][j]));
                UNROLL for (int j = 0; j < 4; ++j) m |= z1[i][j] | z2[i][j];
                yr[i] = mfma16(GR[i], d); yi[i] = mfma16(GI[i], d);
                SB();
            }
            if (__all((m & TM) == 0u)) break;
            MEMBAR();
            LOAD_Z();
        }
        STAMP(9);
        u4 y[4];
        UNROLL for (int i = 0; i < 4; ++i) UNROLL for (int r = 0; r < 4; ++r) y[r][i] = pkh(yr[i][r], yi[i][r]);
        UNROLL for (int r = 0; r < 4; ++r) *(u4*)&lds[L_A2 + lw + r * 64] = y[r];
        lset(lds, L_C3 + wv, 1u, lane);
    }
    {
        u4 d[4];
        NOUNROLL for (int spin = 0; spin < LSPIN_CAP; ++spin) {
            MEMBAR();
            const u4 c = *(const u4*)&lds[L_C3];
            MEMBAR();
            UNROLL for (int i = 0; i < 4; ++i) d[i] = l4[L_A2 / 4 + lr + i * 16];
            pin(d[0]);
            if (rfl(min4u(c))) break;
        }
        STAMP(10);
        pin(WA); pin(WB);
        f4 xr[4], xi[4];
        u4 xx[4], ya[4], yb[4];
        UNROLL for (int i = 0; i < 4; ++i) {
            xr[i] = mfma16(FR, d[i]); xi[i] = mfma16(FI, d[i]);
            if (i >= 2) { UNROLL for (int r = 0; r < 4; ++r) xx[i - 2][r] = pkh(xr[i - 2][r], xi[i - 2][r]); pin(xx[i - 2]); }
            SB();
        }
        UNROLL for (int r = 0; r < 4; ++r) ya[0][r] = cmul_swap(xx[0][r], WA[0][r]);
        UNROLL for (int i = 2; i < 4; ++i) { UNROLL for (int r = 0; r < 4; ++r) xx[i][r] = pkh(xr[i][r], xi[i][r]); pin(xx[i]); }
        SB();
        f4 p0[4], p1[4], p2[4], p3[4];
        UNROLL for (int i = 0; i < 4; ++i) {
            p0[i] = mfma16(FR, ya[i]); p1[i] = mfma16(FI, ya[i]);
            if (i < 3) { UNROLL for (int r = 0; r < 4; ++r) ya[i + 1][r] = cmul_swap(xx[i + 1][r], WA[i + 1][r]); }
            else { UNROLL for (int r = 0; r < 4; ++r) yb[0][r] = cmul_swap(xx[0][r], WB[0][r]); }
            SB();
        }
        u4 ra[4], rb[4];
        UNROLL for (int i = 0; i < 4; ++i) {
            p2[i] = mfma16(FR, yb[i]); p3[i] = mfma16(FI, yb[i]);
            if (i < 3) { UNROLL for (int r = 0; r < 4; ++r) yb[i + 1][r] = cmul_swap(xx[i + 1][r], WB[i + 1][r]); }
            UNROLL for (int k = 0; k < 4; ++k) ra[i][k] = pkh(p0[k][i], p1[k][i]);
            *(u4*)&lds[L_B + lw + i * 64] = ra[i];
            SB();
        }
        UNROLL for (int i = 0; i < 3; ++i) UNROLL for (int r = 0; r < 4; ++r) rb[r][i] = pkh(p2[i][r], p3[i][r]);
        SB();
        UNROLL for (int r = 0; r < 4; ++r) { rb[r][3] = pkh(p2[3][r], p3[3][r]); *(u4*)&lds[L_B + 4096 + lw + r * 64] = rb[r]; }
        lset(lds, L_C4 + wv, 1u, lane);
    }
    STAMP(11);
    {
        u4 da[4], db[4];
        NOUNROLL for (int spin = 0; spin < LSPIN_CAP; ++spin) {
            MEMBAR();
            const u4 c = *(const u4*)&lds[L_C4];
            MEMBAR();
            UNROLL for (int i = 0; i < 4; ++i) { da[i] = l4[L_B / 4 + lr + i * 16]; db[i] = l4[L_B / 4 + 1024 + lr + i * 16]; }
            pin(da[0]);
            if (rfl(min4u(c))) break;
        }
        const int vo = 64 * q * 32768 + n * 8, so = WS_V + 4 * wv * 32768 + cls * 4096 + tile * 128;
        f4 p0[4], p1[4], p2[4], p3[4];
        UNROLL for (int i = 0; i < 5; ++i) {
            if (i < 4) { p0[i] = mfma16(HR[i], da[i]); p1[i] = mfma16(HI[i], da[i]); p2[i] = mfma16(HR[i], db[i]); p3[i] = mfma16(HI[i], db[i]); }
            if (i > 0) { UNROLL for (int r = 0; r < 4; ++r)
                __builtin_amdgcn_raw_buffer_store_b64(u2{mark(pkh(p0[i - 1][r], p1[i - 1][r]), tags.x, tm), mark(pkh(p2[i - 1][r], p3[i - 1][r]), tags.x, tm)}, rws, vo, so + (16 * r + i - 1) * 32768, SC1); }
            SB();
        }
    }
    STAMP(12);
    SB();
    GATE_LATE;
    MR[0] = *(const u4*)&lds[L_M + lane * 4]; MR[1] = *(const u4*)&lds[L_M + 256 + lane * 4];
    UNROLL for (int i = 0; i < 4; ++i) TW[i] = *(const u4*)&lds[L_M + (2 + i) * 256 + lane * 4];
    core_expand(lane, MR, MI);
    pin(MR); pin(MI); pin(TW);
    u4 PM[9];
    {
        lwait4(lds, L_C5);
        float gg[4];
        UNROLL for (int k = 0; k < 2; ++k) {
            const f4* pp = (const f4*)&lds[L_GS + ((n >> 2) * 2 + k) * 16];
            const f4 ps = (pp[0] + pp[1]) + (pp[2] + pp[3]);
            const float lg = rbf((ps.x + ps.y) + (ps.z + ps.w));
            gg[2 * k] = rbf(1.0f / (1.0f + __expf(-lg)));
            gg[2 * k + 1] = rbf(1.0f - gg[2 * k]);
        }
        const int r = n & 3, sa = (n >> 2) * 2;
        lwait(lds, L_OK + 3, 1u, MSLP);
        UNROLL for (int cc = 0; cc < 2; ++cc) {
            const int u1 = 2 * q + cc;
            const float ta = (float)(sa * u1) * 0.125f + ((r & 1) ? 0.25f : (r & 2) ? 0.5f : 0.0f);
            const float tb = (float)((sa + 1) * u1) * 0.125f + ((r & 1) ? ((r & 2) ? 0.0f : 0.5f) : 0.25f);
            const float ca = cosr(ta), sa_ = sinr(ta), cb = cosr(tb), sb_ = sinr(tb);
            float e[4] = {gg[2] * cb, gg[2] * sb_, gg[3] * cb, gg[3] * sb_};
            pin(e);
            e[0] = fmaf(gg[0], ca, e[0]); pin(e[0]); e[1] = fmaf(gg[0], sa_, e[1]); pin(e[1]); e[2] = fmaf(gg[1], ca, e[2]); pin(e[2]); e[3] = fmaf(gg[1], sa_, e[3]);
            const u32 pa = pkh(e[0], e[1]), pb = pkh(e[2], e[3]);
            UNROLL for (int i = 0; i < 9; ++i) { const u32 f = lds[L_TS + u1 * 36 + 8 * wv + i]; PM[i][2 * cc] = hmul(pa, f); PM[i][2 * cc + 1] = hmul(pb, f); }
        }
    }
    const float osc = __builtin_bit_cast(float, lds[L_OSC]) * TRUNC_FIX;
    pin(PM);
    const int ve = 2 * q * 4096 + (128 * wv + n) * 8;
    STAMP(13);
    VMWAIT(VWAIT);
    {
        f4 z[9];
        NOUNROLL for (int spin = 0; spin < SPIN_CAP; ++spin) {
            MEMBAR();
            u2 v0[9], v1[9];
            int a_ = ve; pin(a_);
            u4 e, o;
            if (wv == 3) {
                e = __builtin_amdgcn_raw_buffer_load_b128(rws, q * 32, WS_VL + b * 128, SC1); o = __builtin_amdgcn_raw_buffer_load_b128(rws, q * 32 + 16, WS_VL + b * 128, SC1);
            } else {
                const u2 e2 = __builtin_amdgcn_raw_buffer_load_b64(rws, a_ + 1024, WS_V + b * 32768, SC1), o2 = __builtin_amdgcn_raw_buffer_load_b64(rws, a_ + 1024, WS_V + b * 32768 + 4096, SC1);
                e = u4{e2.x, e2.y, tags.w, tags.w}; o = u4{o2.x, o2.y, tags.w, tags.w};
            }
            UNROLL for (int i = 0; i < 8; ++i) {
                v0[i] = __builtin_amdgcn_raw_buffer_load_b64(rws, a_ + 128 * i, WS_V + b * 32768, SC1);
                v1[i] = __builtin_amdgcn_raw_buffer_load_b64(rws, a_ + 128 * i, WS_V + b * 32768 + 4096, SC1);
            }
            SB();
            v0[8] = u2{e.x, wv == 3 ? e.z : e.y}; v1[8] = u2{o.x, wv == 3 ? o.z : o.y};
            u32 m = (((wv == 3 ? e.y : e.z) ^ tags.w) | (e.w ^ tags.w) | ((wv == 3 ? o.y : o.z) ^ tags.w) | (o.w ^ tags.w)) ? TM : 0u;
            UNROLL for (int k = 0; k < 9; ++k) {
                const int i = (k + 8) % 9;
                const u4 d = u4{v0[i].x, v0[i].y, v1[i].x, v1[i].y} ^ tags.x;
                m |= d.x | d.y; m |= d.z | d.w;
                z[i] = mfma16(PM[i], d);
                SB();
            }
            if (__all((m & TM) == 0u)) break;
        }
        STAMP(14);
        if (wv == 0 && n == 0) { z[0].x = 0.5f * (z[0].x + z[0].z); z[0].y = 0.5f * (z[0].y + z[0].w); }
        if (wv == 3) { z[8].z = 0.5f * (z[8].x + z[8].z); z[8].w = 0.5f * (z[8].y + z[8].w); }
        u4* l4 = (u4*)&lds[L_A1];
        UNROLL for (int p = 0; p < 2; ++p)
            l4[(q * 8 + wv) * 32 + 16 * p + n] = u4{pkh(z[p].x, z[p + 2].x), pkh(z[p + 4].x, z[p + 6].x), pkh(z[p].y, z[p + 2].y), pkh(z[p + 4].y, z[p + 6].y)};
        l4[(q * 8 + 7 - wv) * 32 + 16 - n] = u4{pkh(z[7].z, z[5].z), pkh(z[3].z, z[1].z), pkh(z[7].w, z[5].w), pkh(z[3].w, z[1].w)};
        f2 m[4];
        UNROLL for (int i = 0; i < 4; ++i) m[i] = n == 0 ? f2{z[8 - 2 * i].z, z[8 - 2 * i].w} : f2{z[6 - 2 * i].z, z[6 - 2 * i].w};
        l4[(q * 8 + 7 - wv) * 32 + ((32 - n) & 31)] = u4{pkh(m[0].x, m[1].x), pkh(m[2].x, m[3].x), pkh(m[0].y, m[1].y), pkh(m[2].y, m[3].y)};
        lset(lds, L_C6 + wv, 1u, lane);
    }
    {
        u4 in[4];
        NOUNROLL for (int spin = 0; spin < LSPIN_CAP; ++spin) {
            MEMBAR();
            const u4 c = *(const u4*)&lds[L_C6];
            MEMBAR();
            UNROLL for (int s = 0; s < 4; ++s) in[s] = *(const u4*)&lds[L_A1 + (wv * 256 + s * 64 + lane) * 4];
            pin(in[0]);
            if (rfl(min4u(c))) break;
        }
        if (b == 0 && t == 0) __builtin_amdgcn_raw_buffer_store_b32(tags.z, rws, 0, WS_E, SC1);
        f16v zr, zi;
        core_fft<false>(in, MR, MI, TW, zr, zi);
        zr *= osc; zi *= osc;
        const rsrc_t ro = mkrs(out, 2048 * 2048);
        const int ov = (4 * perm32(lane & 31) + (lane >> 5)) * 16, os = (512 * wv + b) * 2048;
        UNROLL for (int hf = 0; hf < 2; ++hf) {
            u4 a, c;
            UNROLL for (int j = 0; j < 4; ++j) { a[j] = pkb(zi[8 * hf + 2 * j], zi[8 * hf + 2 * j + 1]); c[j] = pkb(zr[8 * hf + 2 * j], zr[8 * hf + 2 * j + 1]); }
            store128<OAUX>(a, ro, ov + os + 32 * hf);
            store128<OAUX>(c, ro, ov + os + 256 * 2048 + 32 * hf);
        }
    }
    STAMP(15);
    STAMP_OUT();
}

extern "C" void kernel_launch(void* const* d_in, const int* in_sizes, int n_in,
                              void* d_out, int out_size, void* d_ws, size_t ws_size,
                              hipStream_t stream) {
    gfnet_fused<<<256, 512, 0, stream>>>(d_in[0], d_in[6], d_in[1], d_in[2], d_in[4], d_in[5], (char*)d_ws, d_out);
}
```

```cpp
#define DIAG 2
#include <hip/hip_runtime.h>
#include <hip/hip_bf16.h>
#include <stdint.h>

typedef _Float16 h2 __attribute__((ext_vector_type(2)));
typedef _Float16 h8 __attribute__((ext_vector_type(8)));
typedef __bf16 b2 __attribute__((ext_vector_type(2)));
typedef __bf16 b8 __attribute__((ext_vector_type(8)));
typedef float f2 __attribute__((ext_vector_type(2)));
typedef float f4 __attribute__((ext_vector_type(4)));
typedef float f16v __attribute__((ext_vector_type(16)));
typedef short s4 __attribute__((ext_vector_type(4)));
typedef unsigned u32;
typedef u32 u2 __attribute__((ext_vector_type(2)));
typedef u32 u4 __attribute__((ext_vector_type(4)));
typedef unsigned short us2 __attribute__((ext_vector_type(2)));
#define DI __device__ __forceinline__
#define UNROLL _Pragma("unroll")
#define NOUNROLL _Pragma("unroll 1")
#define LAS __attribute__((address_space(3)))

#define MSLP 1
#define F1DELAY 12
#define W0WAIT 0
#define GFIRST 1
#define GEARLY 1
#define LDEPTH 3
#define PSLEEP 4
#define W0DELAY 2
#define WWIN 8
#define VCMODE 1
#define VWIN 8
#define ZWAIT 4
#define VWAIT 0
#define XAUX 2
#define W0AUX 0
#define OAUX 2

constexpr u32 FSTR = 16;
constexpr u32 WS_Z = 0;
constexpr u32 WS_V = 4u << 20;
constexpr u32 WS_VL = 12u << 20;
constexpr u32 WS_HB = WS_VL + (32u << 10);
constexpr u32 WS_F1 = WS_HB + (32u << 10);
constexpr u32 WS_F3 = WS_F1 + 2048 * FSTR;
constexpr u32 WS_E = WS_F3 + 2048 * FSTR;
constexpr u32 WS_BYTES = WS_E + 4096;
constexpr size_t WS_META = 24u << 20;
constexpr int SC1 = 16;
constexpr int SPIN_CAP = 8192;
constexpr int LSPIN_CAP = 1 << 17;
constexpr int OOB = 0x7ffffff0;
constexpr float TRUNC_FIX = 1.00066f;
constexpr u32 TM = 0x00010001u;

template <class T> DI void pin(T& v) { asm volatile("" : "+v"(v)); }
template <class T, int N> DI void pin(T (&v)[N]) { UNROLL for (int i = 0; i < N; ++i) pin(v[i]); }
DI u32 opq(u32 v) { asm("" : "+v"(v)); return v; }
DI u32 pkh(float lo, float hi) { h2 r; r.x = (_Float16)lo; r.y = (_Float16)hi; return __builtin_bit_cast(u32, r); }
DI u32 pkb(float lo, float hi) { b2 r; r.x = (__bf16)lo; r.y = (__bf16)hi; return __builtin_bit_cast(u32, r); }
DI float bflo(u32 v) { return __builtin_bit_cast(float, v << 16); }
DI float bfhi(u32 v) { return __builtin_bit_cast(float, v & 0xffff0000u); }
DI float rbf(float v) { return (float)(__bf16)v; }
DI h2 H(u32 v) { return __builtin_bit_cast(h2, v); }
DI u32 U(h2 v) { return __builtin_bit_cast(u32, v); }
DI f16v mfma32(u4 a, u4 b, f16v c) { return __builtin_amdgcn_mfma_f32_32x32x16_f16(__builtin_bit_cast(h8, a), __builtin_bit_cast(h8, b), c, 0, 0, 0); }
DI f4 mfma16(u4 a, u4 b) { return __builtin_amdgcn_mfma_f32_16x16x32_f16(__builtin_bit_cast(h8, a), __builtin_bit_cast(h8, b), f4{}, 0, 0, 0); }
DI f4 mfma16b(u4 a, u4 b) { return __builtin_amdgcn_mfma_f32_16x16x32_bf16(__builtin_bit_cast(b8, a), __builtin_bit_cast(b8, b), f4{}, 0, 0, 0); }
DI u32 hmul(u32 a, u32 b) { return U(H(a) * H(b)); }
DI u32 hfma(u32 a, u32 b, u32 c) { return U(__builtin_elementwise_fma(H(a), H(b), H(c))); }
DI u32 cmul_swap(u32 x, u32 w) { const h2 a = H(opq(x)), c = H(opq(w)); h2 n = c; n.y = -n.y; return U(__builtin_elementwise_fma(a.yy, n, a.xx * c.yx)); }
DI u32 cmul_conj(u32 x, u32 t) { const h2 a = H(opq(x)), c = H(opq(t)); h2 n = c.yy; n.y = -n.y; return U(__builtin_elementwise_fma(a.yx, n, a * c.xx)); }
DI u32 pkmax(u32 a, u32 b) { return __builtin_bit_cast(u32, __builtin_elementwise_max(__builtin_bit_cast(us2, a), __builtin_bit_cast(us2, b))); }
DI float sinr(float r) { return __builtin_amdgcn_sinf(r); }
DI float cosr(float r) { return __builtin_amdgcn_cosf(r); }
DI int perm32(int n) { return (n & 0x13) | ((n & 4) << 1) | ((n & 8) >> 1); }
DI u32 max3u(u32 a, u32 b, u32 c) { return max(max(a, b), c); }
DI u32 max4u(u4 v) { return max(max(v.x, v.y), max(v.z, v.w)); }
DI u32 min4u(u4 v) { return min(min(v.x, v.y), min(v.z, v.w)); }
DI u32 mark(u32 v, u32 par, u32 tm) { return (par & tm) | (v & ~tm); }
DI u32 rfl(u32 v) { return __builtin_amdgcn_readfirstlane(v); }
DI u32 pow2h(int d) { const float f = __builtin_bit_cast(float, (u32)(127 - min(d, 64)) << 23); return pkh(f, f); }

typedef __amdgpu_buffer_rsrc_t rsrc_t;
DI rsrc_t mkrs(const void* p, u32 bytes) { return __builtin_amdgcn_make_buffer_rsrc((void*)p, (short)0, (int)bytes, 0x00020000); }
template <int AUX> DI void store128(u4 v, rsrc_t r, int off) { __builtin_amdgcn_raw_buffer_store_b128(v, r, off, 0, AUX); }
#define SB() __builtin_amdgcn_sched_barrier(0)
#define MEMBAR() asm volatile("" ::: "memory")
#define VMWAIT(n) asm volatile("s_waitcnt vmcnt(%0)" :: "n"(n) : "memory")
#define STAMP_DECL unsigned long long st_[16] = {}
#define STAMP(i) do { if (i == 0 || i == DIAG || i == 15) { SB(); asm volatile("s_memrealtime %0\n\ts_waitcnt lgkmcnt(0)" : "=s"(st_[i]) :: "memory"); SB(); } } while (0)
#define STAMP_OUT() do { if (wv < 4) { while (__builtin_amdgcn_s_memrealtime() - st_[0] < 3000 + 4 * (st_[DIAG] - st_[0])) __builtin_amdgcn_s_sleep(1); } } while (0)
template <int CTRL> DI u32 dpp(u32 v) { return __builtin_amdgcn_update_dpp(0u, v, CTRL, 0xf, 0xf, true); }
DI u32 wave_maxu(u32 v) {
    v = max(v, dpp<0xB1>(v)); v = max(v, dpp<0x4E>(v)); v = max(v, dpp<0x141>(v)); v = max(v, dpp<0x140>(v));
    return max(max((u32)__builtin_amdgcn_readlane(v, 0), (u32)__builtin_amdgcn_readlane(v, 16)), max((u32)__builtin_amdgcn_readlane(v, 32), (u32)__builtin_amdgcn_readlane(v, 48)));
}

DI void core_setup(int lane, u4 (&M)[6]) {
    float fc = (float)perm32(lane & 31) * (1.0f / 1024.0f), p0 = (float)(4 * (lane >> 5)) * fc;
    pin(fc); pin(p0);
    UNROLL for (int s = 0; s < 4; ++s) {
        float cs[4], sn[4], tc[4], ts[4];
        UNROLL for (int i = 0; i < 4; ++i) {
            const float pt = fmaf((float)(8 * s + i), fc, p0);
            if (s < 2) { cs[i] = cosr(pt * 32.0f); sn[i] = sinr(pt * 32.0f); }
            tc[i] = cosr(pt); ts[i] = sinr(pt);
        }
        M[2 + s] = u4{pkh(tc[0], tc[1]), pkh(tc[2], tc[3]), pkh(ts[0], ts[1]), pkh(ts[2], ts[3])};
        if (s < 2) M[s] = u4{pkh(cs[0], cs[1]), pkh(cs[2], cs[3]), pkh(sn[0], sn[1]), pkh(sn[2], sn[3])};
    }
}
DI void core_expand(int lane, u4 (&MR)[4], u4 (&MI)[4]) {
    const u32 sg = (lane & 1) ? 0x80008000u : 0u;
    MR[2] = MR[0] ^ sg; MR[3] = MR[1] ^ sg;
    UNROLL for (int s = 0; s < 4; ++s) MI[s] = u4{MR[s].z ^ 0x80008000u, MR[s].w ^ 0x80008000u, MR[s].x, MR[s].y};
}
template <bool OUT_B> DI void core_fft(const u4 (&in)[4], const u4 (&MR)[4], const u4 (&MI)[4], const u4 (&TW)[4], f16v& outr, f16v& outi) {
    f16v yr = {}, yi = {};
    UNROLL for (int s = 0; s < 4; ++s) yr = mfma32(in[s], MR[s], yr);
    SB();
    yi = mfma32(in[0], MI[0], yi); yi = mfma32(in[1], MI[1], yi);
    SB();
    h2 t0[8], t1[8];
#define TW_RE(p) do { const h2 pr_ = H(pkh(yr[2 * (p)], yr[2 * (p) + 1])); t0[p] = pr_ * H(TW[(p) >> 1][(p) & 1]); t1[p] = pr_ * H(TW[(p) >> 1][2 + ((p) & 1)]); } while (0)
    TW_RE(0); TW_RE(1); SB();
    yi = mfma32(in[2], MI[2], yi);
    TW_RE(2); TW_RE(3); SB();
    yi = mfma32(in[3], MI[3], yi);
    TW_RE(4); TW_RE(5); TW_RE(6); TW_RE(7); SB();
    outr = f16v{}; outi = f16v{};
    u4 d[4];
#define TW_IM(s) do { UNROLL for (int j = 0; j < 2; ++j) { const int p_ = 2 * (s) + j; const h2 pi_ = H(pkh(yi[2 * p_], yi[2 * p_ + 1])); \
        d[s][j] = U(__builtin_elementwise_fma(pi_, H(TW[s][2 + j]), t0[p_])); d[s][2 + j] = U(__builtin_elementwise_fma(pi_, H(TW[s][j]), -t1[p_])); } } while (0)
    TW_IM(0); SB();
    UNROLL for (int s = 0; s < 4; ++s) {
        outr = OUT_B ? mfma32(MR[s], d[s], outr) : mfma32(d[s], MR[s], outr);
        if (s < 3) TW_IM(s + 1);
        SB();
    }
    UNROLL for (int s = 0; s < 4; ++s) outi = OUT_B ? mfma32(MI[s], d[s], outi) : mfma32(d[s], MI[s], outi);
}
DI u32 rot(float ph) { return pkh(cosr(ph), sinr(ph)); }
DI u32 quarter(u32 r, u32 f) { u32 o; asm("v_pk_mul_f16 %0, %1, %2 op_sel:[1,0] op_sel_hi:[0,1] neg_lo:[1,0]" : "=v"(o) : "v"(r), "v"(f)); return o; }
DI u4 quarter(u4 r) { u32 one = 0x3c003c00u; pin(one); return u4{quarter(r.x, one), quarter(r.y, one), quarter(r.z, one), quarter(r.w, one)}; }

DI u32 tag_of(u32 epoch, u32 idx) { u32 h = (epoch ^ (idx * 0x9E3779B1u)) * 0x85EBCA6Bu; return h ^ (h >> 15); }
DI u32 parity_of(u32 epoch) { return (epoch & 1u) ? 0x00000001u : 0x00010000u; }
DI void flag_store(rsrc_t rws, u32 arr, int b, int lane, u32 payload, u32 tag) {
    __builtin_amdgcn_raw_buffer_store_b64(u2{payload, tag}, rws, lane < 8 ? lane * (int)(256 * FSTR) : OOB, arr + b * FSTR, SC1);
}
DI u4 flag_wait(rsrc_t rws, u32 arr, int b, int lane, u32 epoch) {
    u2 f[4]; u32 tg[4];
    UNROLL for (int j = 0; j < 4; ++j) tg[j] = tag_of(epoch, arr + 4 * lane + j);
    NOUNROLL for (int spin = 0; spin < SPIN_CAP; ++spin) {
        MEMBAR();
        UNROLL for (int j = 0; j < 4; ++j) f[j] = __builtin_amdgcn_raw_buffer_load_b64(rws, lane * (4 * FSTR) + j * FSTR, arr + (b & 7) * (256 * FSTR), SC1);
        if (__all(f[0].y == tg[0] && f[1].y == tg[1] && f[2].y == tg[2] && f[3].y == tg[3])) break;
        __builtin_amdgcn_s_sleep(PSLEEP);
    }
    return u4{f[0].x, f[1].x, f[2].x, f[3].x};
}
DI int cls_of(int b) { const int row = (b & 7) >> 1, sel = b >> 7; return row == 0 ? 4 * sel : (sel ? 8 - row : row); }
DI int tile_of(int b) { return 16 * (b & 1) + ((b >> 3) & 15); }
DI bool is_helper(int b) { return (b & 0x79) == 0x79; }

constexpr int L_EW = 0;
constexpr int L_C1 = 4, L_WW = 8, L_C3 = 12, L_C4 = 16, L_C5 = 20, L_C6 = 24;
constexpr int L_HE = 28;
constexpr int L_OK = 32;
constexpr int L_OSC = 36;
constexpr int L_PR = 40;
constexpr int L_LD = 44;
constexpr int L_MOK = 45;
constexpr int L_NFLAG = 64;
constexpr int L_SC = 64;
constexpr int L_GS = 320;
constexpr int L_TS = 448;
constexpr int L_M = 768;
constexpr int L_A1 = L_M + 1536;
constexpr int L_A2 = L_A1 + 4096;
constexpr int L_B = L_A2 + 4096;
constexpr int L_W = L_B + 8192;
constexpr int L_END = L_W + 64 * 256;

DI void lset(u32* lds, int idx, u32 v, int lane) { MEMBAR(); if (lane == 0) lds[idx] = v; MEMBAR(); }
DI u32 lget(u32* lds, int idx) { MEMBAR(); return rfl(lds[idx]); }
DI u32 lwait(u32* lds, int idx, u32 atleast, int slp) {
    u32 v = 0;
    NOUNROLL for (int spin = 0; spin < LSPIN_CAP; ++spin) { v = lget(lds, idx); if (v >= atleast) break; if (slp) __builtin_amdgcn_s_sleep(1); }
    MEMBAR();
    return v;
}
DI u4 lwait4(u32* lds, int idx) {
    u4 v;
    NOUNROLL for (int spin = 0; spin < LSPIN_CAP; ++spin) { MEMBAR(); v = *(const u4*)&lds[idx]; if (rfl(min4u(v))) break; }
    MEMBAR();
    return v;
}

DI void lwait4s(u32* lds, int idx) {
    NOUNROLL for (int spin = 0; spin < LSPIN_CAP; ++spin) { MEMBAR(); const u4 v = *(const u4*)&lds[idx]; if (rfl(min4u(v))) break; __builtin_amdgcn_s_sleep(1); }
    MEMBAR();
}

__global__ __launch_bounds__(512, 2) void gfnet_fused(const void* x, const void* gw, const void* war, const void* wai, const void* wbr, const void* wbi,
                                                      char* ws, void* out) {
    __shared__ __attribute__((aligned(16))) u32 lds[L_END];
    STAMP_DECL;
    STAMP(0);
    asm volatile("" :: "s"(x), "s"(gw), "s"(war), "s"(wai), "s"(wbr), "s"(wbi), "s"(ws), "s"(out));
    const int t = threadIdx.x, lane = t & 63, wv = __builtin_amdgcn_readfirstlane(t >> 6), b = blockIdx.x;
    const int q = lane >> 4, n = lane & 15;
    const rsrc_t rws = mkrs(ws, WS_BYTES);
    const int row = (b & 7) >> 1, sel = b >> 7, cls = cls_of(b), tile = tile_of(b);
    const bool packed = row == 0;

    if (wv >= 4) {
        const u32 ev = __builtin_amdgcn_raw_buffer_load_b32(rws, 0, WS_E, SC1);
        if (wv == 4) lds[lane] = 0u;
        asm volatile("s_waitcnt lgkmcnt(0)" ::: "memory");
        __builtin_amdgcn_s_barrier();
        MEMBAR();
        u32 he = 0;
        u32 hw[4][4];
        const bool helper = wv == 7 && is_helper(b);
        if (wv > 4) {
            const int wvo = (lane >> 2) * (128 * 1026) + (lane & 3) * 16, wso = (cls * 513 + 16 * tile - cls) * 2;
            const rsrc_t rw[4] = {mkrs(war, 2048 * 1026), mkrs(wai, 2048 * 1026), mkrs(wbr, 2048 * 1026), mkrs(wbi, 2048 * 1026)};
            const u32 ldp = (u32)(size_t)(LAS u32*)&lds[L_LD];
#define WLOAD(c, a) __builtin_amdgcn_raw_ptr_buffer_load_lds(rw[a], (LAS void*)&lds[L_W + ((c) * 4 + (a)) * 256], 16, wvo, wso + (c) * 8208, 0, 0)
            if ((lane & 3) < 3) {
                if (wv == 5) {
                    __builtin_amdgcn_s_sleep(W0DELAY);
                    UNROLL for (int st = 0; st < 4 + LDEPTH; ++st) {
                        if (st >= LDEPTH) {
                            if (st < 4) VMWAIT(4 * (LDEPTH - 1)); else VMWAIT(4 * (3 + LDEPTH - st));
                            asm volatile("ds_write_b32 %0, %1" :: "v"(ldp), "v"(st - LDEPTH + 1) : "memory");
                        }
                        if (st < 4) { UNROLL for (int a = 0; a < 4; ++a) WLOAD(4 * st + (tile & 3), a); }
                    }
                } else {
                    NOUNROLL for (int st = 0; st < 4; ++st) {
                        NOUNROLL for (int spin = 0; spin < LSPIN_CAP; ++spin) {
                            u32 v; asm volatile("ds_read_b32 %0, %1\n\ts_waitcnt lgkmcnt(0)" : "=v"(v) : "v"(ldp) : "memory");
                            if ((int)rfl(v) > st) break;
                            __builtin_amdgcn_s_sleep(1);
                        }
                        UNROLL for (int r = 1; r < 4; ++r) {
                            const int c = 4 * st + ((tile + r) & 3);
                            if (wv == 6) { WLOAD(c, 0); WLOAD(c, 1); } else { WLOAD(c, 2); WLOAD(c, 3); }
                        }
                    }
                }
            }
            STAMP(1);
            VMWAIT(0);
            lset(lds, L_PR + wv - 4, 1u, lane);
            STAMP(2);
        } else {
            u4 M[6];
            core_setup(lane, M);
            UNROLL for (int i = 0; i < 6; ++i) *(u4*)&lds[L_M + i * 256 + lane * 4] = M[i];
            lset(lds, L_MOK, 1u, lane);
            lset(lds, L_PR, 1u, lane);
            STAMP(1);
        }
        const u32 epoch = rfl(ev) + 1u;
        if (wv == 7) {
            if (helper) {
                lwait4s(lds, L_PR);
                UNROLL for (int r = 0; r < 4; ++r) UNROLL for (int a = 0; a < 4; ++a) {
                    hw[a][r] = ((const unsigned short*)&lds[L_W + (n * 4 + a) * 256 + (4 * q + r) * 16])[16 + cls];
                    he = max(he, hw[a][r] & 0x7fffu);
                }
                he = min(wave_maxu(he) >> 7, 240u);
            }
            lset(lds, L_HE, he | 0x100u, lane);
            STAMP(3);
        }
        if (wv == 5) {
            lwait4s(lds, L_WW);
            const u32 hx = lwait(lds, L_HE, 1u, 1);
            flag_store(rws, WS_F3, b, lane, max4u(*(const u4*)&lds[L_WW]) | ((hx & 0xffu) << 8), tag_of(epoch, WS_F3 + b));
        }
        if (wv == 4) {
            lwait(lds, L_C1, 1u, 1);
            __builtin_amdgcn_s_sleep(F1DELAY);
            STAMP(3);
            u32 emax;
            {
                const u4 f = flag_wait(rws, WS_F1, b, lane, epoch) & 0xffu;
                emax = wave_maxu(max4u(f));
                *(u4*)&lds[L_SC + 4 * lane] = u4{pow2h(emax + 4 - f.x), pow2h(emax + 4 - f.y), pow2h(emax + 4 - f.z), pow2h(emax + 4 - f.w)};
                lset(lds, L_OK + 1, 1u, lane);
            }
            STAMP(4);
            lwait(lds, L_C3, 1u, 1);
            {
                const u4 f = flag_wait(rws, WS_F3, b, lane, epoch);
                u32 m = 0;
                UNROLL for (int j = 0; j < 4; ++j) m = max3u(m, f[j] & 0xffu, (f[j] >> 8) & 0xffu);
                const u32 ewm = wave_maxu(m);
                UNROLL for (int j = 0; j < 4; ++j) {
                    const int p = 4 * lane + j;
                    lds[L_TS + cls_of(p) * 36 + tile_of(p)] = pow2h(ewm + 5 - (f[j] & 0xffu));
                    if (is_helper(p)) lds[L_TS + cls_of(p) * 36 + 32] = pow2h(ewm + 5 - ((f[j] >> 8) & 0xffu));
                }
                lset(lds, L_OSC, (u32)min(max((int)emax + (int)ewm - 130, 1), 254) << 23, lane);
                lset(lds, L_OK + 3, 1u, lane);
            }
            STAMP(5);

        } else if (helper) {
            {
                const int hc = cls;
                u4 GR, FR, HR, T1, T2, WH[2];
                UNROLL for (int j = 0; j < 4; ++j) {
                    GR[j] = rot((float)((4 * q + j) * (hc + 8 * n)) * (1.0f / 128.0f) + (hc == 4 ? 0.25f : 0.0f));
                    HR[j] = rot((float)(n * (hc + 8 * (4 * q + j))) * (1.0f / 128.0f));
                    FR[j] = rot((float)(n * (4 * q + j)) * (1.0f / 16.0f));
                    T1[j] = rot((float)((4 * q + j) * (hc + 8 * n)) * (1.0f / 2048.0f));
                    T2[j] = rot((float)(n * (hc + 8 * (4 * q + j))) * (1.0f / 2048.0f));
                    const float sw = __builtin_bit_cast(float, (249u - he) << 23);
                    WH[0][j] = pkh(bflo(hw[0][j]) * sw, bflo(hw[1][j]) * sw); WH[1][j] = pkh(bflo(hw[2][j]) * sw, bflo(hw[3][j]) * sw);
                }
                u4 GI = quarter(GR), HI = quarter(HR), FI = quarter(FR);
                pin(GR); pin(GI); pin(HR); pin(HI); pin(FR); pin(FI); pin(T1); pin(T2); pin(WH);
                lwait(lds, L_OK + 1, 1u, 1);
                const u32 tg = tag_of(epoch, 0u), par = parity_of(epoch);
                u4 hz;
                NOUNROLL for (int spin = 0; spin < SPIN_CAP; ++spin) {
                    MEMBAR();
                    u32 bad = 0;
                    UNROLL for (int j = 0; j < 4; ++j) { const u2 g = __builtin_amdgcn_raw_buffer_load_b64(rws, (64 * q + n) * 16 + j * 256, WS_HB + hc * 4096, SC1); hz[j] = g.x & ~TM; bad |= g.y ^ tg; }
                    if (__all(bad == 0u)) break;
                    __builtin_amdgcn_s_sleep(PSLEEP);
                }
                STAMP(4);
                const u32 msk = hc == 0 ? 0x0000ffffu : hc == 4 ? 0xffff0000u : ~0u, sg = hc > 4 ? 0x80000000u : 0u;
                u4 d, o[2];
                UNROLL for (int j = 0; j < 4; ++j) d[j] = hmul(hz[j], (lds[L_SC + 64 * q + 16 * j + n] & msk) | sg);
                f4 yr = mfma16(d, GR), yi = mfma16(d, GI);
                UNROLL for (int r = 0; r < 4; ++r) d[r] = cmul_conj(pkh(yr[r], yi[r]), T1[r]);
                const f4 xr = mfma16(FR, d), xi = mfma16(FI, d);
                UNROLL for (int f = 0; f < 2; ++f) {
                    UNROLL for (int r = 0; r < 4; ++r) d[r] = cmul_swap(pkh(xr[r], xi[r]), WH[f][r]);
                    yr = mfma16(d, FR); yi = mfma16(d, FI);
                    UNROLL for (int r = 0; r < 4; ++r) o[f][r] = cmul_conj(pkh(yr[r], yi[r]), T2[r]);
                }
                const f4 p0 = mfma16(HR, o[0]), p1 = mfma16(HI, o[0]), p2 = mfma16(HR, o[1]), p3 = mfma16(HI, o[1]);
                UNROLL for (int r = 0; r < 4; ++r)
                    store128<SC1>(u4{mark(pkh(p0[r], p1[r]), par, TM), tg, mark(pkh(p2[r], p3[r]), par, TM), tg}, rws, (64 * q + n) * 128 + WS_VL + 16 * r * 128 + hc * 16);
                STAMP(5);
            }
        }
        STAMP_OUT();
        return;
    }

#define GATE_BLOCK do { pin(xv); \
        const f2 g01 = {bflo(gv.x), bfhi(gv.x)}, g23 = {bflo(gv.y), bfhi(gv.y)}; \
        f4 acc = {}; \
        UNROLL for (int i = 0; i < 4; ++i) { \
            u4 pp; \
            UNROLL for (int k = 0; k < 2; ++k) { \
                const f2 a = f2{bflo(xv[2 * i + k].x), bfhi(xv[2 * i + k].x)} * g01, c = f2{bflo(xv[2 * i + k].y), bfhi(xv[2 * i + k].y)} * g23; \
                pp[2 * k] = pkb(a.x, a.y); pp[2 * k + 1] = pkb(c.x, c.y); } \
            const u32 s0 = n == 2 * i ? 0x3f803f80u : 0u, s1 = n == 2 * i + 1 ? 0x3f803f80u : 0u; \
            acc = __builtin_amdgcn_mfma_f32_16x16x32_bf16(__builtin_bit_cast(b8, pp), __builtin_bit_cast(b8, u4{s0, s0, s1, s1}), acc, 0, 0, 0); } \
        if (n < 8) lds[L_GS + n * 16 + (t >> 4)] = __builtin_bit_cast(u32, (acc.x + acc.y) + (acc.z + acc.w)); \
        lset(lds, L_C5 + wv, 1u, lane); } while (0)
#define GATE_EARLY GATE_BLOCK
#define GATE_LATE
    u2 xv[8];
    {
        const rsrc_t rx = mkrs(x, 2048 * 2048);
        UNROLL for (int s1 = 0; s1 < 8; ++s1) xv[s1] = __builtin_amdgcn_raw_buffer_load_b64(rx, t * 8, (256 * s1 + b) * 2048, XAUX);
    }
    const u32 ev = __builtin_amdgcn_raw_buffer_load_b32(rws, 0, WS_E, SC1);
    const u2 gv = __builtin_amdgcn_raw_buffer_load_b64(mkrs(gw, 2048), t * 8, 0, 0);
    MEMBAR();
    SB();
    __builtin_amdgcn_s_barrier();
    MEMBAR();
    __builtin_amdgcn_s_setprio(1);
    u4 MR[4], MI[4], TW[4];
    u4 FR, FI, GR[4], GI[4], HR[4], HI[4], sel1;
    UNROLL for (int j = 0; j < 4; ++j) FR[j] = rot((float)(n * (4 * q + j)) * (1.0f / 16.0f));
    FI = quarter(FR);
    {
        const float ofs = cls == 4 ? 0.25f : 0.0f;
        float gu = (float)(cls + 8 * n) * (1.0f / 2048.0f), g0 = (float)(64 * q + 4 * wv) * gu + ofs;
        pin(gu); pin(g0);
        UNROLL for (int i = 0; i < 4; ++i) UNROLL for (int j = 0; j < 4; ++j) GR[i][j] = rot(fmaf((float)(16 * j + i), gu, g0));
    }
    pin(FR); pin(FI); pin(GR);
    const int zc = perm32(lane & 31) + (lane >> 5) * 256, zcu = zc + ((lane & 31) == 0 ? 543 : 511);
    int zl = (zc >> 4) * 16384 + (zc & 15) * 4, zu = (zcu >> 4) * 16384 + (zcu & 15) * 4;
    const int zh0 = wv * 4096, zh1 = (wv == 0 ? 4 : 8 - wv) * 4096;
    int zh = lane == 0 ? zh0 : zh1; pin(zh); zh = lane < 2 ? zh : OOB;
    pin(zl); pin(zu); pin(zh);
    u4 tags;
    u32 tm = TM; pin(tm);
    STAMP(1);
    SB();
    {
        h2 hi = H(xv[0].x), lo = hi;
        hi = __builtin_elementwise_maximum(hi, H(xv[0].y)); lo = __builtin_elementwise_minimum(lo, H(xv[0].y));
        UNROLL for (int s1 = 1; s1 < 8; ++s1) {
            hi = __builtin_elementwise_maximum(__builtin_elementwise_maximum(hi, H(xv[s1].x)), H(xv[s1].y));
            lo = __builtin_elementwise_minimum(__builtin_elementwise_minimum(lo, H(xv[s1].x)), H(xv[s1].y));
        }
        const u32 mx = pkmax(U(hi) & 0x7fff7fffu, U(lo) & 0x7fff7fffu);
        lset(lds, L_EW + wv, min(max(wave_maxu(max(mx & 0xffffu, mx >> 16)) >> 7, 8u), 247u), lane);
        STAMP(2);
        f2 e0[2], e1[2], c0[2], c2[2], dm[2], dp[2], d0[2], d1[2];
        UNROLL for (int j = 0; j < 2; ++j) {
            f2 v[8];
            UNROLL for (int s1 = 0; s1 < 8; ++s1) v[s1] = f2{bflo(xv[s1][j]), bfhi(xv[s1][j])};
            const f2 a0 = v[0] + v[4], a1 = v[1] + v[5], a2 = v[2] + v[6], a3 = v[3] + v[7];
            const f2 b1 = v[1] - v[5], b3 = v[3] - v[7];
            c0[j] = v[0] - v[4]; c2[j] = v[2] - v[6];
            e0[j] = a0 + a2; e1[j] = a1 + a3; dm[j] = b1 - b3; dp[j] = b1 + b3; d0[j] = a0 - a2; d1[j] = a3 - a1;
        }
        pin(e0); pin(e1); pin(c0); pin(c2); pin(dm); pin(dp); pin(d0); pin(d1);
        const u32 epoch = rfl(ev) + 1u;
        tags = u4{parity_of(epoch), tag_of(epoch, WS_F1 + b), epoch, tag_of(epoch, 0u)};
        u4 ews;
        NOUNROLL for (int spin = 0; spin < LSPIN_CAP; ++spin) {
            MEMBAR();
            ews = *(const u4*)&lds[L_EW];
            const u32 mok = lds[L_MOK];
            if (rfl(min(min4u(ews), mok))) break;
        }
        MEMBAR();
        MR[0] = *(const u4*)&lds[L_M + lane * 4]; MR[1] = *(const u4*)&lds[L_M + 256 + lane * 4];
        UNROLL for (int i = 0; i < 4; ++i) TW[i] = *(const u4*)&lds[L_M + (2 + i) * 256 + lane * 4];
        const u32 emax = rfl(max4u(ews));
        if (wv == 0) flag_store(rws, WS_F1, b, lane, emax, tags.y);
        const float sc = __builtin_bit_cast(float, (250u - emax) << 23);
        const float rs = sc * 0.70710678118654752f;
        u32 o[8][2];
        UNROLL for (int j = 0; j < 2; ++j) {
            const f2 e1s = e1[j] * sc, b0 = c0[j] * sc, b2_ = c2[j] * sc;
            f2 r[8];
            r[0] = e0[j] * sc + e1s; r[1] = e0[j] * sc - e1s;
            r[2] = dm[j] * rs + b0; r[3] = -(dp[j] * rs) - b2_;
            r[4] = d0[j] * sc; r[5] = d1[j] * sc;
            r[6] = b0 - dm[j] * rs; r[7] = b2_ - dp[j] * rs;
            UNROLL for (int p = 0; p < 8; ++p) o[p][j] = pkh(r[p].x, r[p].y);
        }
        UNROLL for (int p = 0; p < 8; ++p) *(u2*)&lds[L_A1 + p * 512 + 2 * t] = u2{o[p][0], o[p][1]};
        lset(lds, L_C1 + wv, 1u, lane);
        core_expand(lane, MR, MI);
        pin(MR); pin(MI);
        u4 in[4];
        {
            const int h = lane >> 5, blk = (lane >> 4) & 1, qq = (lane & 15) >> 2, p = lane & 3;
            const int off = L_A1 + wv * 1024 + (4 * h + qq) * 16 + blk * 8 + p * 2;
            NOUNROLL for (int spin = 0; spin < LSPIN_CAP; ++spin) {
                MEMBAR();
                const u4 c = *(const u4*)&lds[L_C1];
                MEMBAR();
                UNROLL for (int s = 0; s < 4; ++s) {
                    const u2 re = __builtin_bit_cast(u2, __builtin_amdgcn_ds_read_tr16_b64_v4i16((LAS s4*)&lds[off + s * 128]));
                    const u2 im = __builtin_bit_cast(u2, __builtin_amdgcn_ds_read_tr16_b64_v4i16((LAS s4*)&lds[off + s * 128 + 512]));
                    in[s] = u4{re.x, re.y, im.x, im.y};
                }
                if (rfl(min4u(c))) break;
            }
        }
        STAMP(3);
        f16v zr, zi;
        core_fft<true>(in, MR, MI, TW, zr, zi);
        u32 z[16];
        UNROLL for (int r = 0; r < 16; ++r) z[r] = mark(pkh(zr[r], zi[r]), tags.x, tm);
        STAMP(4);
        const int zp = ((b >> 2) & 3) * 64 + (b & 3) * 16 + ((b >> 4) & 3) * 4 + (b >> 6);
        const int zs = WS_Z + wv * 64 * 16384 + zp * 64;
        UNROLL for (int r = 0; r < 8; ++r) __builtin_amdgcn_raw_buffer_store_b32(z[r], rws, zl, zs + r * 32768, SC1);
        const u32 zn = rfl(z[8]), za = __builtin_amdgcn_readlane(z[8], 32), zb = __builtin_amdgcn_readlane(z[0], 0);
        MEMBAR();
        const u32 zt = lane < 32 ? za : zb;
        UNROLL for (int r = 8; r < 16; ++r) __builtin_amdgcn_raw_buffer_store_b32((lane & 31) == 0 ? (r < 15 ? z[r + 1] : zt) : z[r], rws, zu, zs + (r & 7) * 32768, SC1);
        __builtin_amdgcn_raw_buffer_store_b64(u2{zn, tags.w}, rws, zh, WS_HB + b * 16, SC1);
    }
    STAMP(5);
    SB();
    {
        float hs[4], hu[4];
        UNROLL for (int i = 0; i < 4; ++i) { hs[i] = (float)(16 * n + 4 * wv + i) * (1.0f / 2048.0f); hu[i] = (float)(cls + 32 * q + 8 * i); }
        pin(hs); pin(hu);
        UNROLL for (int i = 0; i < 4; ++i) { UNROLL for (int j = 0; j < 4; ++j) { float a = hs[i] * hu[j]; pin(a); HR[i][j] = rot(a); } }
    }
    pin(HR);
    {
        const int d = n + cls - 8 * q;
        UNROLL for (int j = 0; j < 4; ++j) sel1[j] = (d >> 1) == j ? ((d & 1) ? 0x3f800000u : 0x3f80u) : 0u;
        pin(sel1);
    }
    GATE_EARLY;
    STAMP(6);
    u32 z1[4][4], z2[4][4];
    const int zvm = q * 64 + (63 - tile) * 16384 + (15 - n) * 4;
    const int zv1 = cls <= 4 ? q * 64 + tile * 16384 + n * 4 : zvm, zso = WS_Z + row * 64 * 16384 + wv * 4096;
#define LOAD_Z() do { \
        if (packed) { int a_ = zvm; pin(a_); UNROLL for (int i = 0; i < 4; ++i) UNROLL for (int j = 0; j < 4; ++j) z2[i][j] = __builtin_amdgcn_raw_buffer_load_b32(rws, a_ + (4 * i + j) * 256, zso, SC1); } \
        else { UNROLL for (int i = 0; i < 4; ++i) UNROLL for (int j = 0; j < 4; ++j) z2[i][j] = tags.x; } \
        int c_ = zv1; pin(c_); UNROLL for (int i = 0; i < 4; ++i) UNROLL for (int j = 0; j < 4; ++j) z1[i][j] = __builtin_amdgcn_raw_buffer_load_b32(rws, c_ + (4 * i + j) * 256, zso, SC1); } while (0)
    VMWAIT(ZWAIT);
    LOAD_Z();
    SB();
    {
        u4 scl[4];
        lwait(lds, L_OK + 1, 1u, MSLP);
        UNROLL for (int j = 0; j < 4; ++j) scl[j] = *(const u4*)&lds[L_SC + 64 * q + 16 * j + 4 * wv];
        SB();
        UNROLL for (int i = 0; i < 4; ++i) UNROLL for (int j = 0; j < 4; ++j) { GI[i][j] = quarter(GR[i][j], scl[j][i]); GR[i][j] = hmul(GR[i][j], scl[j][i]); }
    }
    pin(GR); pin(GI);
    STAMP(7);
    u4 WA[4], WB[4];
    {
        lwait4s(lds, L_PR);
        u4 wr[4][4];
        int wl = L_W / 4 + wv * 1024 + n * 4 + min(q, 2); pin(wl);
        UNROLL for (int i = 0; i < 4; ++i) UNROLL for (int a = 0; a < 4; ++a) wr[i][a] = ((const u4*)lds)[wl + (i * 4 + a) * 64];
        SB();
        f4 wf[4][4];
        float wmax = 0.0f;
        UNROLL for (int k = 0; k < 20; ++k) {
            if (k < 16) wf[k >> 2][k & 3] = mfma16b(wr[k >> 2][k & 3], sel1);
            if (k >= 4) { const f4 v = wf[(k - 4) >> 2][k & 3]; wmax = fmaxf(fmaxf(wmax, fabsf(v.x)), fabsf(v.y)); wmax = fmaxf(fmaxf(wmax, fabsf(v.z)), fabsf(v.w)); }
            SB();
        }
        lset(lds, L_WW + wv, min(max(wave_maxu(__builtin_bit_cast(u32, wmax)) >> 23, 1u), 240u), lane);
        UNROLL for (int i = 0; i < 4; ++i) HI[i] = quarter(HR[i]);
        pin(HI);
        const u32 ewt = rfl(max4u(lwait4(lds, L_WW)));
        const float sw = __builtin_bit_cast(float, (249u - ewt - (packed ? 1u : 0u)) << 23);
        UNROLL for (int i = 0; i < 4; ++i) {
            const f4 ar = wf[i][0] * sw, ai = wf[i][1] * sw, br = wf[i][2] * sw, bi = wf[i][3] * sw;
            UNROLL for (int r = 0; r < 4; ++r) { WA[i][r] = pkh(ar[r], ai[r]); WB[i][r] = pkh(br[r], bi[r]); }
        }
        pin(WA); pin(WB);
    }
    u32 x1 = tags.x ^ (cls > 4 ? 0x80000000u : 0u), x2 = tags.x ^ (cls == 4 ? 0x00008000u : cls == 0 ? 0x80000000u : 0u);
    const int lw = ((wv * 16 + 4 * q) * 16 + n) * 4;
    int lr = (q * 16 + 4 * wv) * 16 + n;
    const u4* l4 = (const u4*)lds;
    pin(GR); pin(GI); pin(x1); pin(x2); pin(lr);
    STAMP(8);
    SB();
    {
        f4 yr[4], yi[4];
        NOUNROLL for (int spin = 0; spin < SPIN_CAP; ++spin) {
            u32 m = 0;
            UNROLL for (int i = 0; i < 4; ++i) {
                u4 d;
                UNROLL for (int j = 0; j < 4; ++j) { z1[i][j] ^= x1; z2[i][j] ^= x2; }
                UNROLL for (int j = 0; j < 4; ++j) d[j] = U(H(z1[i][j]) + H(z2[i][j]));
                UNROLL for (int j = 0; j < 4; ++j) m |= z1[i][j] | z2[i][j];
                yr[i] = mfma16(GR[i], d); yi[i] = mfma16(GI[i], d);
                SB();
            }
            if (__all((m & TM) == 0u)) break;
            MEMBAR();
            LOAD_Z();
        }
        STAMP(9);
        u4 y[4];
        UNROLL for (int i = 0; i < 4; ++i) UNROLL for (int r = 0; r < 4; ++r) y[r][i] = pkh(yr[i][r], yi[i][r]);
        UNROLL for (int r = 0; r < 4; ++r) *(u4*)&lds[L_A2 + lw + r * 64] = y[r];
        lset(lds, L_C3 + wv, 1u, lane);
    }
    {
        u4 d[4];
        NOUNROLL for (int spin = 0; spin < LSPIN_CAP; ++spin) {
            MEMBAR();
            const u4 c = *(const u4*)&lds[L_C3];
            MEMBAR();
            UNROLL for (int i = 0; i < 4; ++i) d[i] = l4[L_A2 / 4 + lr + i * 16];
            pin(d[0]);
            if (rfl(min4u(c))) break;
        }
        STAMP(10);
        pin(WA); pin(WB);
        f4 xr[4], xi[4];
        u4 xx[4], ya[4], yb[4];
        UNROLL for (int i = 0; i < 4; ++i) {
            xr[i] = mfma16(FR, d[i]); xi[i] = mfma16(FI, d[i]);
            if (i >= 2) { UNROLL for (int r = 0; r < 4; ++r) xx[i - 2][r] = pkh(xr[i - 2][r], xi[i - 2][r]); pin(xx[i - 2]); }
            SB();
        }
        UNROLL for (int r = 0; r < 4; ++r) ya[0][r] = cmul_swap(xx[0][r], WA[0][r]);
        UNROLL for (int i = 2; i < 4; ++i) { UNROLL for (int r = 0; r < 4; ++r) xx[i][r] = pkh(xr[i][r], xi[i][r]); pin(xx[i]); }
        SB();
        f4 p0[4], p1[4], p2[4], p3[4];
        UNROLL for (int i = 0; i < 4; ++i) {
            p0[i] = mfma16(FR, ya[i]); p1[i] = mfma16(FI, ya[i]);
            if (i < 3) { UNROLL for (int r = 0; r < 4; ++r) ya[i + 1][r] = cmul_swap(xx[i + 1][r], WA[i + 1][r]); }
            else { UNROLL for (int r = 0; r < 4; ++r) yb[0][r] = cmul_swap(xx[0][r], WB[0][r]); }
            SB();
        }
        u4 ra[4], rb[4];
        UNROLL for (int i = 0; i < 4; ++i) {
            p2[i] = mfma16(FR, yb[i]); p3[i] = mfma16(FI, yb[i]);
            if (i < 3) { UNROLL for (int r = 0; r < 4; ++r) yb[i + 1][r] = cmul_swap(xx[i + 1][r], WB[i + 1][r]); }
            UNROLL for (int k = 0; k < 4; ++k) ra[i][k] = pkh(p0[k][i], p1[k][i]);
            *(u4*)&lds[L_B + lw + i * 64] = ra[i];
            SB();
        }
        UNROLL for (int i = 0; i < 3; ++i) UNROLL for (int r = 0; r < 4; ++r) rb[r][i] = pkh(p2[i][r], p3[i][r]);
        SB();
        UNROLL for (int r = 0; r < 4; ++r) { rb[r][3] = pkh(p2[3][r], p3[3][r]); *(u4*)&lds[L_B + 4096 + lw + r * 64] = rb[r]; }
        lset(lds, L_C4 + wv, 1u, lane);
    }
    STAMP(11);
    {
        u4 da[4], db[4];
        NOUNROLL for (int spin = 0; spin < LSPIN_CAP; ++spin) {
            MEMBAR();
            const u4 c = *(const u4*)&lds[L_C4];
            MEMBAR();
            UNROLL for (int i = 0; i < 4; ++i) { da[i] = l4[L_B / 4 + lr + i * 16]; db[i] = l4[L_B / 4 + 1024 + lr + i * 16]; }
            pin(da[0]);
            if (rfl(min4u(c))) break;
        }
        const int vo = 64 * q * 32768 + n * 8, so = WS_V + 4 * wv * 32768 + cls * 4096 + tile * 128;
        f4 p0[4], p1[4], p2[4], p3[4];
        UNROLL for (int i = 0; i < 5; ++i) {
            if (i < 4) { p0[i] = mfma16(HR[i], da[i]); p1[i] = mfma16(HI[i], da[i]); p2[i] = mfma16(HR[i], db[i]); p3[i] = mfma16(HI[i], db[i]); }
            if (i > 0) { UNROLL for (int r = 0; r < 4; ++r)
                __builtin_amdgcn_raw_buffer_store_b64(u2{mark(pkh(p0[i - 1][r], p1[i - 1][r]), tags.x, tm), mark(pkh(p2[i - 1][r], p3[i - 1][r]), tags.x, tm)}, rws, vo, so + (16 * r + i - 1) * 32768, SC1); }
            SB();
        }
    }
    STAMP(12);
    SB();
    GATE_LATE;
    MR[0] = *(const u4*)&lds[L_M + lane * 4]; MR[1] = *(const u4*)&lds[L_M + 256 + lane * 4];
    UNROLL for (int i = 0; i < 4; ++i) TW[i] = *(const u4*)&lds[L_M + (2 + i) * 256 + lane * 4];
    core_expand(lane, MR, MI);
    pin(MR); pin(MI); pin(TW);
    u4 PM[9];
    {
        lwait4(lds, L_C5);
        float gg[4];
        UNROLL for (int k = 0; k < 2; ++k) {
            const f4* pp = (const f4*)&lds[L_GS + ((n >> 2) * 2 + k) * 16];
            const f4 ps = (pp[0] + pp[1]) + (pp[2] + pp[3]);
            const float lg = rbf((ps.x + ps.y) + (ps.z + ps.w));
            gg[2 * k] = rbf(1.0f / (1.0f + __expf(-lg)));
            gg[2 * k + 1] = rbf(1.0f - gg[2 * k]);
        }
        const int r = n & 3, sa = (n >> 2) * 2;
        lwait(lds, L_OK + 3, 1u, MSLP);
        UNROLL for (int cc = 0; cc < 2; ++cc) {
            const int u1 = 2 * q + cc;
            const float ta = (float)(sa * u1) * 0.125f + ((r & 1) ? 0.25f : (r & 2) ? 0.5f : 0.0f);
            const float tb = (float)((sa + 1) * u1) * 0.125f + ((r & 1) ? ((r & 2) ? 0.0f : 0.5f) : 0.25f);
            const float ca = cosr(ta), sa_ = sinr(ta), cb = cosr(tb), sb_ = sinr(tb);
            float e[4] = {gg[2] * cb, gg[2] * sb_, gg[3] * cb, gg[3] * sb_};
            pin(e);
            e[0] = fmaf(gg[0], ca, e[0]); pin(e[0]); e[1] = fmaf(gg[0], sa_, e[1]); pin(e[1]); e[2] = fmaf(gg[1], ca, e[2]); pin(e[2]); e[3] = fmaf(gg[1], sa_, e[3]);
            const u32 pa = pkh(e[0], e[1]), pb = pkh(e[2], e[3]);
            UNROLL for (int i = 0; i < 9; ++i) { const u32 f = lds[L_TS + u1 * 36 + 8 * wv + i]; PM[i][2 * cc] = hmul(pa, f); PM[i][2 * cc + 1] = hmul(pb, f); }
        }
    }
    const float osc = __builtin_bit_cast(float, lds[L_OSC]) * TRUNC_FIX;
    pin(PM);
    const int ve = 2 * q * 4096 + (128 * wv + n) * 8;
    STAMP(13);
    VMWAIT(VWAIT);
    {
        f4 z[9];
        NOUNROLL for (int spin = 0; spin < SPIN_CAP; ++spin) {
            MEMBAR();
            u2 v0[9], v1[9];
            int a_ = ve; pin(a_);
            u4 e, o;
            if (wv == 3) {
                e = __builtin_amdgcn_raw_buffer_load_b128(rws, q * 32, WS_VL + b * 128, SC1); o = __builtin_amdgcn_raw_buffer_load_b128(rws, q * 32 + 16, WS_VL + b * 128, SC1);
            } else {
                const u2 e2 = __builtin_amdgcn_raw_buffer_load_b64(rws, a_ + 1024, WS_V + b * 32768, SC1), o2 = __builtin_amdgcn_raw_buffer_load_b64(rws, a_ + 1024, WS_V + b * 32768 + 4096, SC1);
                e = u4{e2.x, e2.y, tags.w, tags.w}; o = u4{o2.x, o2.y, tags.w, tags.w};
            }
            UNROLL for (int i = 0; i < 8; ++i) {
                v0[i] = __builtin_amdgcn_raw_buffer_load_b64(rws, a_ + 128 * i, WS_V + b * 32768, SC1);
                v1[i] = __builtin_amdgcn_raw_buffer_load_b64(rws, a_ + 128 * i, WS_V + b * 32768 + 4096, SC1);
            }
            SB();
            v0[8] = u2{e.x, wv == 3 ? e.z : e.y}; v1[8] = u2{o.x, wv == 3 ? o.z : o.y};
            u32 m = (((wv == 3 ? e.y : e.z) ^ tags.w) | (e.w ^ tags.w) | ((wv == 3 ? o.y : o.z) ^ tags.w) | (o.w ^ tags.w)) ? TM : 0u;
            UNROLL for (int k = 0; k < 9; ++k) {
                const int i = (k + 8) % 9;
                const u4 d = u4{v0[i].x, v0[i].y, v1[i].x, v1[i].y} ^ tags.x;
                m |= d.x | d.y; m |= d.z | d.w;
                z[i] = mfma16(PM[i], d);
                SB();
            }
            if (__all((m & TM) == 0u)) break;
        }
        STAMP(14);
        if (wv == 0 && n == 0) { z[0].x = 0.5f * (z[0].x + z[0].z); z[0].y = 0.5f * (z[0].y + z[0].w); }
        if (wv == 3) { z[8].z = 0.5f * (z[8].x + z[8].z); z[8].w = 0.5f * (z[8].y + z[8].w); }
        u4* l4 = (u4*)&lds[L_A1];
        UNROLL for (int p = 0; p < 2; ++p)
            l4[(q * 8 + wv) * 32 + 16 * p + n] = u4{pkh(z[p].x, z[p + 2].x), pkh(z[p + 4].x, z[p + 6].x), pkh(z[p].y, z[p + 2].y), pkh(z[p + 4].y, z[p + 6].y)};
        l4[(q * 8 + 7 - wv) * 32 + 16 - n] = u4{pkh(z[7].z, z[5].z), pkh(z[3].z, z[1].z), pkh(z[7].w, z[5].w), pkh(z[3].w, z[1].w)};
        f2 m[4];
        UNROLL for (int i = 0; i < 4; ++i) m[i] = n == 0 ? f2{z[8 - 2 * i].z, z[8 - 2 * i].w} : f2{z[6 - 2 * i].z, z[6 - 2 * i].w};
        l4[(q * 8 + 7 - wv) * 32 + ((32 - n) & 31)] = u4{pkh(m[0].x, m[1].x), pkh(m[2].x, m[3].x), pkh(m[0].y, m[1].y), pkh(m[2].y, m[3].y)};
        lset(lds, L_C6 + wv, 1u, lane);
    }
    {
        u4 in[4];
        NOUNROLL for (int spin = 0; spin < LSPIN_CAP; ++spin) {
            MEMBAR();
            const u4 c = *(const u4*)&lds[L_C6];
            MEMBAR();
            UNROLL for (int s = 0; s < 4; ++s) in[s] = *(const u4*)&lds[L_A1 + (wv * 256 + s * 64 + lane) * 4];
            pin(in[0]);
            if (rfl(min4u(c))) break;
        }
        if (b == 0 && t == 0) __builtin_amdgcn_raw_buffer_store_b32(tags.z, rws, 0, WS_E, SC1);
        f16v zr, zi;
        core_fft<false>(in, MR, MI, TW, zr, zi);
        zr *= osc; zi *= osc;
        const rsrc_t ro = mkrs(out, 2048 * 2048);
        const int ov = (4 * perm32(lane & 31) + (lane >> 5)) * 16, os = (512 * wv + b) * 2048;
        UNROLL for (int hf = 0; hf < 2; ++hf) {
            u4 a, c;
            UNROLL for (int j = 0; j < 4; ++j) { a[j] = pkb(zi[8 * hf + 2 * j], zi[8 * hf + 2 * j + 1]); c[j] = pkb(zr[8 * hf + 2 * j], zr[8 * hf + 2 * j + 1]); }
            store128<OAUX>(a, ro, ov + os + 32 * hf);
            store128<OAUX>(c, ro, ov + os + 256 * 2048 + 32 * hf);
        }
    }
    STAMP(15);
    STAMP_OUT();
}

extern "C" void kernel_launch(void* const* d_in, const int* in_sizes, int n_in,
                              void* d_out, int out_size, void* d_ws, size_t ws_size,
                              hipStream_t stream) {
    gfnet_fused<<<256, 512, 0, stream>>>(d_in[0], d_in[6], d_in[1], d_in[2], d_in[4], d_in[5], (char*)d_ws, d_out);
}
```

```cpp
#define DIAG 0
#define WAUX 0
#include <hip/hip_runtime.h>
#include <hip/hip_bf16.h>
#include <stdint.h>

typedef _Float16 h2 __attribute__((ext_vector_type(2)));
typedef _Float16 h8 __attribute__((ext_vector_type(8)));
typedef __bf16 b2 __attribute__((ext_vector_type(2)));
typedef __bf16 b8 __attribute__((ext_vector_type(8)));
typedef float f2 __attribute__((ext_vector_type(2)));
typedef float f4 __attribute__((ext_vector_type(4)));
typedef float f16v __attribute__((ext_vector_type(16)));
typedef short s4 __attribute__((ext_vector_type(4)));
typedef unsigned u32;
typedef unsigned long long u64;
typedef u32 u2 __attribute__((ext_vector_type(2)));
typedef u32 u4 __attribute__((ext_vector_type(4)));
typedef unsigned short us2 __attribute__((ext_vector_type(2)));
#define DI __device__ __forceinline__
#define UNROLL _Pragma("unroll")

constexpr u32 WS_Z = 0;
constexpr u32 WS_V = 4u << 20;
constexpr u32 WS_VL = 12u << 20;
#define FSTR 16
#define F2DELAY 16
#define PSLEEP 4
constexpr u32 WS_F0 = WS_VL + (64u << 10);
constexpr u32 WS_F1 = WS_F0 + 2048 * FSTR;
constexpr u32 WS_F2 = WS_F1 + 2048 * FSTR;
constexpr u32 WS_F3 = WS_F2 + 2048 * FSTR;
constexpr u32 WS_E = WS_F3 + 2048 * FSTR;
constexpr u32 WS_BYTES = WS_E + 4096;
constexpr size_t WS_META = 24u << 20;
#define VC0 2
#define VC1 2
#define VC2 5
#define VC3 5
#define WP0 1
#define WP1 3
#define WP2 5
#define WP3 5
#define XAUX 2
#define OAUX 2
constexpr int SC1 = 16;
constexpr int SPIN_CAP = 4096;

DI u32 pkh(float lo, float hi) { h2 r; r.x = (_Float16)lo; r.y = (_Float16)hi; return __builtin_bit_cast(u32, r); }
DI u32 pkb(float lo, float hi) { b2 r; r.x = (__bf16)lo; r.y = (__bf16)hi; return __builtin_bit_cast(u32, r); }
DI float bflo(u32 v) { return __builtin_bit_cast(float, v << 16); }
DI float bfhi(u32 v) { return __builtin_bit_cast(float, v & 0xffff0000u); }
DI float rbf(float v) { return (float)(__bf16)v; }
DI f16v mfma32(u4 a, u4 b, f16v c) { return __builtin_amdgcn_mfma_f32_32x32x16_f16(__builtin_bit_cast(h8, a), __builtin_bit_cast(h8, b), c, 0, 0, 0); }
DI f4 mfma16(u4 a, u4 b) { return __builtin_amdgcn_mfma_f32_16x16x32_f16(__builtin_bit_cast(h8, a), __builtin_bit_cast(h8, b), f4{}, 0, 0, 0); }
DI f4 mfma16b(u4 a, u4 b) { return __builtin_amdgcn_mfma_f32_16x16x32_bf16(__builtin_bit_cast(b8, a), __builtin_bit_cast(b8, b), f4{}, 0, 0, 0); }
DI u32 hmul(u32 a, u32 b) { return __builtin_bit_cast(u32, __builtin_bit_cast(h2, a) * __builtin_bit_cast(h2, b)); }
DI u32 hfma(u32 a, u32 b, u32 c) { return __builtin_bit_cast(u32, __builtin_elementwise_fma(__builtin_bit_cast(h2, a), __builtin_bit_cast(h2, b), __builtin_bit_cast(h2, c))); }
DI u32 pkmax(u32 a, u32 b) { return __builtin_bit_cast(u32, __builtin_elementwise_max(__builtin_bit_cast(us2, a), __builtin_bit_cast(us2, b))); }
DI float sinr(float r) { return __builtin_amdgcn_sinf(r); }
DI float cosr(float r) { return __builtin_amdgcn_cosf(r); }
DI int perm32(int n) { return (n & 0x13) | ((n & 4) << 1) | ((n & 8) >> 1); }
DI u32 max3u(u32 a, u32 b, u32 c) { return max(max(a, b), c); }
DI u32 pow2h(int d) { const float f = __builtin_bit_cast(float, (u32)(127 - min(d, 64)) << 23); return pkh(f, f); }
template <class T> DI void pin(T& v) { asm volatile("" : "+v"(v)); }
template <class T, int N> DI void pin(T (&v)[N]) { UNROLL for (int i = 0; i < N; ++i) pin(v[i]); }

typedef __amdgpu_buffer_rsrc_t rsrc_t;
DI rsrc_t mkrs(const void* p, u32 bytes) { return __builtin_amdgcn_make_buffer_rsrc((void*)p, (short)0, (int)bytes, 0x00020000); }
#define SB() __builtin_amdgcn_sched_barrier(0)
#define BAR() do { asm volatile("s_waitcnt lgkmcnt(0)" ::: "memory"); __builtin_amdgcn_s_barrier(); asm volatile("" ::: "memory"); } while (0)
#define VMWAIT(n) asm volatile("s_waitcnt vmcnt(" #n ")" ::: "memory")
#define STAMP_DECL unsigned long long st_[16] = {}
#define STAMP(i) do { if (i == 0 || i == DIAG || i == 15) { SB(); asm volatile("s_memrealtime %0\n\ts_waitcnt lgkmcnt(0)" : "=s"(st_[i]) :: "memory"); SB(); } } while (0)
#define STAMP_OUT() do { if (wv < 4) { while (__builtin_amdgcn_s_memrealtime() - st_[0] < 4000) __builtin_amdgcn_s_sleep(1); } } while (0)
template <int CTRL> DI u32 dpp(u32 v) { return __builtin_amdgcn_update_dpp(v, v, CTRL, 0xf, 0xf, false); }
DI u32 wave_maxu(u32 v) {
    v = max(v, dpp<0xB1>(v)); v = max(v, dpp<0x4E>(v)); v = max(v, dpp<0x141>(v)); v = max(v, dpp<0x140>(v));
    return max(max((u32)__builtin_amdgcn_readlane(v, 0), (u32)__builtin_amdgcn_readlane(v, 16)), max((u32)__builtin_amdgcn_readlane(v, 32), (u32)__builtin_amdgcn_readlane(v, 48)));
}
DI u32 absmax2(u32 m, u32 v) { return pkmax(m, v & 0x7fff7fffu); }
DI u32 fold16(u32 m) { return max(m & 0xffffu, m >> 16); }

DI void core_setup(int lane, u4 (&MR)[4], u4 (&MI)[4], f2 (&TW)[16]) {
    const float fc = (float)perm32(lane & 31), h4 = (float)(4 * (lane >> 5));
    UNROLL for (int s = 0; s < 4; ++s) {
        float cs[4], sn[4];
        UNROLL for (int i = 0; i < 4; ++i) {
            const float pt = ((float)(8 * s + i) + h4) * fc * (1.0f / 1024.0f);
            if (s < 2) { cs[i] = cosr(pt * 32.0f); sn[i] = sinr(pt * 32.0f); }
            TW[4 * s + i] = f2{cosr(pt) * 0.125f, sinr(pt) * 0.125f};
        }
        if (s < 2) MR[s] = u4{pkh(cs[0], cs[1]), pkh(cs[2], cs[3]), pkh(sn[0], sn[1]), pkh(sn[2], sn[3])};
        else MR[s] = MR[s - 2] ^ ((lane & 1) ? 0x80008000u : 0u);
        MI[s] = u4{MR[s].z ^ 0x80008000u, MR[s].w ^ 0x80008000u, MR[s].x, MR[s].y};
    }
}
template <bool OUT_B> DI void core_fft(const u4 (&in)[4], const u4 (&MR)[4], const u4 (&MI)[4], const f2 (&TW)[16], f16v& outr, f16v& outi) {
    f16v yr = {}, yi = {};
    UNROLL for (int s = 0; s < 4; ++s) { yr = mfma32(in[s], MR[s], yr); yi = mfma32(in[s], MI[s], yi); }
    outr = f16v{}; outi = f16v{};
    UNROLL for (int s = 0; s < 4; ++s) {
        float pr[4], pi[4];
        UNROLL for (int i = 0; i < 4; ++i) {
            const int r = 4 * s + i;
            pr[i] = yr[r] * TW[r].x + yi[r] * TW[r].y;
            pi[i] = yi[r] * TW[r].x - yr[r] * TW[r].y;
        }
        const u4 d = u4{pkh(pr[0], pr[1]), pkh(pr[2], pr[3]), pkh(pi[0], pi[1]), pkh(pi[2], pi[3])};
        if (OUT_B) { outr = mfma32(MR[s], d, outr); outi = mfma32(MI[s], d, outi); }
        else       { outr = mfma32(d, MR[s], outr); outi = mfma32(d, MI[s], outi); }
    }
}
DI void rot_pair(float ph, u32& r, u32& i) { const float c = cosr(ph), s = sinr(ph); r = pkh(c, s); i = pkh(-s, c); }

DI u32 tag_of(u32 epoch, u32 idx) { u32 h = (epoch ^ (idx * 0x9E3779B1u)) * 0x85EBCA6Bu; return h ^ (h >> 15); }
DI void flag_post(rsrc_t rws, u32 arr, int b, int lane, u32 payload, u32 epoch) {
    if (lane < 8) __builtin_amdgcn_raw_buffer_store_b64(u2{payload, tag_of(epoch, arr + b)}, rws, lane * (256 * FSTR), arr + b * FSTR, SC1);
}
DI u4 flag_wait(rsrc_t rws, u32 arr, int b, int lane, u32 epoch) {
    u2 f[4]; u32 tg[4];
    UNROLL for (int j = 0; j < 4; ++j) tg[j] = tag_of(epoch, arr + 4 * lane + j);
    _Pragma("unroll 1") for (int spin = 0; spin < SPIN_CAP; ++spin) {
        asm volatile("" ::: "memory");
        UNROLL for (int j = 0; j < 4; ++j) f[j] = __builtin_amdgcn_raw_buffer_load_b64(rws, lane * (4 * FSTR) + j * FSTR, arr + (b & 7) * (256 * FSTR), SC1);
        if (__all(f[0].y == tg[0] && f[1].y == tg[1] && f[2].y == tg[2] && f[3].y == tg[3])) break;
        __builtin_amdgcn_s_sleep(PSLEEP);
    }
    return u4{f[0].x, f[1].x, f[2].x, f[3].x};
}
DI int cls_of(int b) { const int row = (b & 7) >> 1, sel = b >> 7; return row == 0 ? 4 * sel : (sel ? 8 - row : row); }
DI int tile_of(int b) { return 16 * (b & 1) + ((b >> 3) & 15); }
DI bool is_helper(int b) { return (b & 0xe6) == 0x22; }
DI int helper_cls(int b) { return (b & 1) | ((b >> 2) & 6); }

constexpr int L_A = 0;
constexpr int L_B = 4096;
constexpr int L_EW = 12288;
constexpr int L_WW = 12292;
constexpr int L_GL = 12296;
constexpr int L_SC = 12304;
constexpr int L_GS = 12560;
constexpr int L_TS = 12688;
constexpr int L_X = L_TS + 288;
constexpr int L_END = L_X + 4096 + 18432;

__global__ __launch_bounds__(320, 2) void gfnet_fused(const u32* __restrict__ x, const u2* __restrict__ gw,
                                                      const unsigned short* __restrict__ war, const unsigned short* __restrict__ wai,
                                                      const unsigned short* __restrict__ wbr, const unsigned short* __restrict__ wbi,
                                                      char* ws, u4* __restrict__ out) {
    __shared__ __attribute__((aligned(16))) u32 lds[L_END];
    STAMP_DECL;
    STAMP(0);
    if (x == nullptr) lds[L_END - 1 - threadIdx.x] = 1;
    const int t = threadIdx.x, lane = t & 63, wv = __builtin_amdgcn_readfirstlane(t >> 6), b = blockIdx.x;
    const int q = lane >> 4, n = lane & 15;
    const rsrc_t rws = mkrs(ws, WS_BYTES);
    const int row = (b & 7) >> 1, sel = b >> 7, cls = cls_of(b), tile = tile_of(b);
    const u4 ones = {~0u, ~0u, ~0u, ~0u};

    if (wv == 4) {
        const u32 ev = __builtin_amdgcn_raw_buffer_load_b32(rws, 0, WS_E, SC1);
        const int reg = row * 64 + (sel ? 63 - tile : tile);
        UNROLL for (int i = 0; i < 16; ++i)
            __builtin_amdgcn_raw_buffer_store_b128(ones, rws, lane * 16 + (i & 3) * 1024, WS_Z + reg * 16384 + (i >> 2) * 4096, SC1);
        const bool helper = is_helper(b);
        const int hc = helper_cls(b);
        STAMP(7);
        const u32 epoch = __builtin_amdgcn_readfirstlane(ev) + 1u;
        VMWAIT(0);
        flag_post(rws, WS_F0, b, lane, 0u, epoch);
        STAMP(1);
        BAR();
        STAMP(2);
        {
            const u4 e = *(const u4*)&lds[L_EW];
            flag_post(rws, WS_F1, b, lane, max(max(e.x, e.y), max(e.z, e.w)), epoch);
        }
        flag_wait(rws, WS_F0, b, lane, epoch);
        STAMP(3);
        BAR();
        u32 emax;
        {
            const u4 f = flag_wait(rws, WS_F1, b, lane, epoch) & 0xffu;
            emax = wave_maxu(max(max(f.x, f.y), max(f.z, f.w)));
            *(u4*)&lds[L_SC + 4 * lane] = u4{pow2h(emax - f.x), pow2h(emax - f.y), pow2h(emax - f.z), pow2h(emax - f.w)};
        }
        STAMP(4);
        BAR();
        flag_post(rws, WS_F2, b, lane, 0u, epoch);
        u32 he = 0;
        u4 o[2], HR, HI;
        if (helper) {
            u32 hw[4][4];
            UNROLL for (int r = 0; r < 4; ++r) {
                const int o_ = (hc + 8 * (n + 16 * (4 * q + r))) * 513 + 512;
                hw[0][r] = war[o_]; hw[1][r] = wai[o_]; hw[2][r] = wbr[o_]; hw[3][r] = wbi[o_];
            }
            u4 GR, GI, FR, FI; f2 T1[4], T2[4];
            UNROLL for (int j = 0; j < 4; ++j) {
                u32 a, c;
                rot_pair((float)((4 * q + j) * (hc + 8 * n)) * (1.0f / 128.0f) + (hc == 4 ? 0.25f : 0.0f), a, c); GR[j] = a; GI[j] = c;
                rot_pair((float)(n * (hc + 8 * (4 * q + j))) * (1.0f / 128.0f), a, c); HR[j] = a; HI[j] = c;
                rot_pair((float)(n * (4 * q + j)) * (1.0f / 16.0f), a, c); FR[j] = a; FI[j] = c;
                const float p1 = (float)((4 * q + j) * (hc + 8 * n)) * (1.0f / 2048.0f), p2 = (float)(n * (hc + 8 * (4 * q + j))) * (1.0f / 2048.0f);
                T1[j] = f2{cosr(p1), sinr(p1)}; T2[j] = f2{cosr(p2), sinr(p2)};
            }
            pin(GR); pin(GI); pin(HR); pin(HI); pin(FR); pin(FI); pin(T1); pin(T2);
            BAR();
            const int hoff = (((hc & 3) == 0 ? 0 : hc < 4 ? hc : 8 - hc) * 64 + 63) * 16384 + 60;
            u4 hz;
            _Pragma("unroll 1") for (int spin = 0; spin < SPIN_CAP; ++spin) {
                asm volatile("" ::: "memory");
                UNROLL for (int j = 0; j < 4; ++j) hz[j] = __builtin_amdgcn_raw_buffer_load_b32(rws, (16 * n + q) * 64 + j * 256, WS_Z + hoff, SC1);
                if (__all(max(max(hz.x, hz.y), max(hz.z, hz.w)) != ~0u)) break;
                __builtin_amdgcn_s_sleep(2);
            }
            UNROLL for (int r = 0; r < 4; ++r) UNROLL for (int a = 0; a < 4; ++a) he = max(he, hw[a][r] & 0x7fffu);
            he = min(wave_maxu(he) >> 7, 240u);
            const u32 msk = hc == 0 ? 0x0000ffffu : hc == 4 ? 0xffff0000u : ~0u, sg = hc > 4 ? 0x80000000u : 0u;
            u4 d;
            UNROLL for (int j = 0; j < 4; ++j) d[j] = hmul(hz[j], (lds[L_SC + 64 * q + 16 * j + n] & msk) | sg);
            f4 yr = mfma16(d, GR), yi = mfma16(d, GI);
            UNROLL for (int r = 0; r < 4; ++r) d[r] = pkh(yr[r] * T1[r].x + yi[r] * T1[r].y, yi[r] * T1[r].x - yr[r] * T1[r].y);
            const f4 xr = mfma16(FR, d), xi = mfma16(FI, d);
            const float sw = __builtin_bit_cast(float, (245u - he) << 23);
            UNROLL for (int f = 0; f < 2; ++f) {
                UNROLL for (int r = 0; r < 4; ++r) {
                    const float wr = bflo(hw[2 * f][r]) * sw, wi = bflo(hw[2 * f + 1][r]) * sw;
                    d[r] = pkh(xr[r] * wi + xi[r] * wr, xr[r] * wr - xi[r] * wi);
                }
                yr = mfma16(d, FR); yi = mfma16(d, FI);
                UNROLL for (int r = 0; r < 4; ++r) d[r] = pkh(yr[r] * T2[r].x + yi[r] * T2[r].y, yi[r] * T2[r].x - yr[r] * T2[r].y);
                o[f] = d;
            }
        } else {
            BAR();
        }
        STAMP(5);
        {
            const u4 e = *(const u4*)&lds[L_WW];
            flag_post(rws, WS_F3, b, lane, max(max(e.x, e.y), max(e.z, e.w)) | (he << 8), epoch);
        }
        flag_wait(rws, WS_F2, b, lane, epoch);
        STAMP(6);
        BAR();
        if (helper) {
            const f4 p0 = mfma16(HR, o[0]), p1 = mfma16(HI, o[0]), p2 = mfma16(HR, o[1]), p3 = mfma16(HI, o[1]);
            UNROLL for (int r = 0; r < 4; ++r)
                __builtin_amdgcn_raw_buffer_store_b64(u2{pkh(p0[r], p1[r]), pkh(p2[r], p3[r])}, rws, (64 * q + n) * 128, WS_VL + 16 * r * 128 + hc * 8, SC1);
        }
        {
            const u4 f = flag_wait(rws, WS_F3, b, lane, epoch);
            u32 m = 0;
            UNROLL for (int j = 0; j < 4; ++j) m = max3u(m, f[j] & 0xffu, (f[j] >> 8) & 0xffu);
            const u32 ewm = wave_maxu(m);
            UNROLL for (int j = 0; j < 4; ++j) {
                const int p = 4 * lane + j;
                lds[L_TS + cls_of(p) * 36 + tile_of(p)] = pow2h(ewm - (f[j] & 0xffu));
                if (is_helper(p)) lds[L_TS + helper_cls(p) * 36 + 32] = pow2h(ewm - ((f[j] >> 8) & 0xffu));
            }
            if (lane == 0) lds[L_GL] = (u32)min(max((int)emax + (int)ewm - 130, 1), 254) << 23;
            if (b == 0 && lane == 0) __builtin_amdgcn_raw_buffer_store_b32(epoch, rws, 0, WS_E, SC1);
        }
        STAMP(8);
        BAR();
        STAMP_OUT();
        return;
    }

    const bool packed = row == 0;
    const int rot = tile & 3;
    const int wvo = n * (128 * 1026) + min(q, 2) * 16, wso = ((cls + 32 * wv) * 513 + 16 * tile - cls) * 2;
    u4 wr[4][4];
    const rsrc_t rw0 = mkrs(war, 2048 * 1026), rw1 = mkrs(wai, 2048 * 1026), rw2 = mkrs(wbr, 2048 * 1026), rw3 = mkrs(wbi, 2048 * 1026);
#define LOAD_W(i) do { const int so_ = wso + (((i) + rot) & 3) * 8208; \
        wr[i][0] = __builtin_amdgcn_raw_buffer_load_b128(rw0, wvo, so_, WAUX); wr[i][1] = __builtin_amdgcn_raw_buffer_load_b128(rw1, wvo, so_, WAUX); \
        wr[i][2] = __builtin_amdgcn_raw_buffer_load_b128(rw2, wvo, so_, WAUX); wr[i][3] = __builtin_amdgcn_raw_buffer_load_b128(rw3, wvo, so_, WAUX); } while (0)
#define CLEAR_V(i) do { __builtin_amdgcn_raw_buffer_store_b128(ones, rws, t * 16, WS_V + b * 32768 + (i) * 8192, SC1); \
        __builtin_amdgcn_raw_buffer_store_b128(ones, rws, t * 16, WS_V + b * 32768 + (i) * 8192 + 4096, SC1); \
        if ((i) == 0) __builtin_amdgcn_raw_buffer_store_b128(ones, rws, t < 8 ? t * 16 : 0x7ffffff0, WS_VL + b * 128, SC1); } while (0)
#define SLOT(k) do { SB(); if (VC0 == k) CLEAR_V(0); if (VC1 == k) CLEAR_V(1); if (VC2 == k) CLEAR_V(2); if (VC3 == k) CLEAR_V(3); \
        if (WP0 == k) LOAD_W(0); if (WP1 == k) LOAD_W(1); if (WP2 == k) LOAD_W(2); if (WP3 == k) LOAD_W(3); SB(); } while (0)
    u2 xv[8];
    {
        const rsrc_t rx = mkrs(x, 2048 * 2048);
        UNROLL for (int s1 = 0; s1 < 8; ++s1) xv[s1] = __builtin_amdgcn_raw_buffer_load_b64(rx, t * 8, (256 * s1 + b) * 2048, XAUX);
    }
    const u2 gv = gw[t];
    SLOT(0);
    {
        u4 MR[4], MI[4]; f2 TW[16];
        core_setup(lane, MR, MI, TW);
        pin(MR); pin(MI); pin(TW);
        SLOT(1);
        STAMP(1);
        u32 mx = 0;
        UNROLL for (int s1 = 0; s1 < 8; ++s1) mx = absmax2(absmax2(mx, xv[s1].x), xv[s1].y);
        const int ew = min(max((int)(wave_maxu(fold16(mx)) >> 7), 8), 250);
        STAMP(2);
        SLOT(2);
        const float sc = __builtin_bit_cast(float, (u32)(253 - ew) << 23);
        const float rs = sc * 0.70710678118654752f;
        u32 o[8][2];
        UNROLL for (int j = 0; j < 2; ++j) {
            f2 v[8];
            UNROLL for (int s1 = 0; s1 < 8; ++s1) v[s1] = f2{bflo(xv[s1][j]), bfhi(xv[s1][j])};
            const f2 a0 = v[0] + v[4], a1 = v[1] + v[5], a2 = v[2] + v[6], a3 = v[3] + v[7];
            const f2 b0 = (v[0] - v[4]) * sc, b1 = v[1] - v[5], b2_ = (v[2] - v[6]) * sc, b3 = v[3] - v[7];
            const f2 e0 = a0 + a2, e1 = (a1 + a3) * sc, dm = b1 - b3, dp = b1 + b3;
            f2 r[8];
            r[0] = e0 * sc + e1; r[1] = e0 * sc - e1;
            r[2] = dm * rs + b0; r[3] = -(dp * rs) - b2_;
            r[4] = (a0 - a2) * sc; r[5] = (a3 - a1) * sc;
            r[6] = b0 - dm * rs; r[7] = b2_ - dp * rs;
            UNROLL for (int p = 0; p < 8; ++p) o[p][j] = pkh(r[p].x, r[p].y);
        }
        UNROLL for (int p = 0; p < 8; ++p) *(u2*)&lds[L_A + p * 512 + 2 * t] = u2{o[p][0], o[p][1]};
        if (lane == 0) lds[L_EW + wv] = ew;
        SLOT(3);
        BAR();
        STAMP(3);
        const u4 ews = *(const u4*)&lds[L_EW];
        const int emax = max(max((int)ews.x, (int)ews.y), max((int)ews.z, (int)ews.w));
        u4 in[4];
        {
            const int h = lane >> 5, blk = (lane >> 4) & 1, qq = (lane & 15) >> 2, p = lane & 3;
            const int off = L_A + wv * 1024 + (4 * h + qq) * 16 + blk * 8 + p * 2;
            UNROLL for (int s = 0; s < 4; ++s) {
                const u2 re = __builtin_bit_cast(u2, __builtin_amdgcn_ds_read_tr16_b64_v4i16((__attribute__((address_space(3))) s4*)&lds[off + s * 128]));
                const u2 im = __builtin_bit_cast(u2, __builtin_amdgcn_ds_read_tr16_b64_v4i16((__attribute__((address_space(3))) s4*)&lds[off + s * 128 + 512]));
                const u32 f = pow2h(emax - (int)ews[s]);
                in[s] = u4{hmul(re.x, f), hmul(re.y, f), hmul(im.x, f), hmul(im.y, f)};
            }
        }
        f16v zr, zi;
        core_fft<true>(in, MR, MI, TW, zr, zi);
        u32 z[16];
        UNROLL for (int r = 0; r < 16; ++r) z[r] = pkh(zr[r], zi[r]);
        pin(z);
        SLOT(4);
        STAMP(4);
        BAR();
        const int c = perm32(lane & 31) + (lane >> 5) * 256, cu = c + 511, c8 = lane == 0 ? 1023 : cu;
        const int zl = (c >> 4) * 16384 + (c & 15) * 4, zu = (cu >> 4) * 16384 + (cu & 15) * 4, z8 = (c8 >> 4) * 16384 + (c8 & 15) * 4;
        const int zs = WS_Z + wv * 64 * 16384 + (((b >> 2) & 3) * 64 + (b & 3) * 16 + ((b >> 4) & 3) * 4 + (b >> 6)) * 64;
        UNROLL for (int r = 0; r < 16; ++r) __builtin_amdgcn_raw_buffer_store_b32(z[r], rws, r < 8 ? zl : r == 8 ? z8 : zu, zs + (r & 7) * 32768, SC1);
    }
    STAMP(5);
    SLOT(5);
    UNROLL for (int i = 0; i < 4; ++i) *(u4*)&lds[L_X + i * 1024 + t * 4] = u4{xv[2 * i].x, xv[2 * i].y, xv[2 * i + 1].x, xv[2 * i + 1].y};
    u32 eprov;
    u4 SEL;
    {
        u32 mw = 0;
        UNROLL for (int a = 0; a < 4; ++a) UNROLL for (int j = 0; j < 4; ++j) mw = absmax2(mw, wr[0][a][j]);
        eprov = min(max(wave_maxu(fold16(mw)) >> 7, 40u), 240u);
        const u32 sw = (245u - eprov - (packed ? 1u : 0u)) << 7;
        UNROLL for (int j = 0; j < 4; ++j) SEL[j] = (8 * q + 2 * j == n + cls ? sw : 0u) | (8 * q + 2 * j + 1 == n + cls ? sw << 16 : 0u);
    }
    f4 Wf[4][4];
    float wmax = 0.0f;
#define WIDEN_W(i) do { UNROLL for (int a = 0; a < 4; ++a) { Wf[i][a] = mfma16b(wr[i][a], SEL); \
        if ((i) > 0) wmax = fmaxf(fmaxf(fmaxf(wmax, fabsf(Wf[i][a].x)), fabsf(Wf[i][a].y)), fmaxf(fabsf(Wf[i][a].z), fabsf(Wf[i][a].w))); } pin(Wf[i]); } while (0)
    WIDEN_W(0);
    if (WP1 < 5) WIDEN_W(1);
    if (WP2 < 5) WIDEN_W(2);
    if (WP3 < 5) WIDEN_W(3);
    u4 GR[4], GI[4], HR[4], HI[4], FR, FI;
    {
        const float ofs = cls == 4 ? 0.25f : 0.0f;
        const float gu = (float)(cls + 8 * n) * (1.0f / 2048.0f), gs0 = (float)(64 * q + 4 * wv);
        UNROLL for (int i = 0; i < 4; ++i) UNROLL for (int j = 0; j < 4; ++j) { u32 a, c; rot_pair((gs0 + (float)(16 * j + i)) * gu + ofs, a, c); GR[i][j] = a; GI[i][j] = c; }
    }
    pin(GR); pin(GI);
    STAMP(6);
    {
        constexpr int young = 4 * ((WP1 == 5) + (WP2 == 5) + (WP3 == 5)) + ((VC0 == 5 || VC1 == 5 || VC2 == 5 || VC3 == 5) ? 0 : 16);
        asm volatile("s_waitcnt vmcnt(%0)" :: "n"(young) : "memory");
    }
    BAR();
    STAMP(7);
    u32 z1[4][4], z2[4][4];
    const int zvm = q * 64 + ((tile == 0 && n == 0) ? 0 : (63 - tile) * 16384 + (15 - n) * 4);
    const int zv1 = cls <= 4 ? q * 64 + tile * 16384 + n * 4 : zvm, zso = WS_Z + row * 64 * 16384 + wv * 4096;
#define LOAD_Z() do { \
        if (packed) { int a_ = zvm; pin(a_); UNROLL for (int i = 0; i < 4; ++i) UNROLL for (int j = 0; j < 4; ++j) z2[i][j] = __builtin_amdgcn_raw_buffer_load_b32(rws, a_ + (4 * i + j) * 256, zso, SC1); } \
        else { UNROLL for (int i = 0; i < 4; ++i) UNROLL for (int j = 0; j < 4; ++j) z2[i][j] = 0u; } \
        int c_ = zv1; pin(c_); UNROLL for (int i = 0; i < 4; ++i) UNROLL for (int j = 0; j < 4; ++j) z1[i][j] = __builtin_amdgcn_raw_buffer_load_b32(rws, c_ + (4 * i + j) * 256, zso, SC1); } while (0)
    LOAD_Z();
    SLOT(6);
    if (WP1 == 5) WIDEN_W(1);
    if (WP2 == 5) WIDEN_W(2);
    if (WP3 == 5) WIDEN_W(3);
    u4 scl[4];
    UNROLL for (int j = 0; j < 4; ++j) scl[j] = *(const u4*)&lds[L_SC + 64 * q + 16 * j + 4 * wv];
    {
        const float hs0 = (float)(16 * n + 4 * wv) * (1.0f / 2048.0f);
        UNROLL for (int j = 0; j < 4; ++j) {
            const float hu = (float)(cls + 32 * q + 8 * ((j + rot) & 3));
            UNROLL for (int i = 0; i < 4; ++i) { u32 a, c; rot_pair((hs0 + (float)i * (1.0f / 2048.0f)) * hu, a, c); HR[i][j] = a; HI[i][j] = c; }
        }
        UNROLL for (int j = 0; j < 4; ++j) { u32 a, c; rot_pair((float)(n * (4 * q + j)) * (1.0f / 16.0f), a, c); FR[j] = a; FI[j] = c; }
        const u32 sg1 = cls > 4 ? 0x80000000u : 0u;
        UNROLL for (int j = 0; j < 4; ++j) scl[j] |= sg1;
    }
    pin(HR); pin(HI); pin(FR); pin(FI); pin(scl);
    STAMP(8);
    SB();
    _Pragma("unroll 1") for (int spin = 0; spin < SPIN_CAP; ++spin) {
        u32 m = 0;
        UNROLL for (int i = 0; i < 4; ++i) UNROLL for (int j = 0; j < 4; ++j) m = max3u(m, z1[i][j], z2[i][j]);
        if (__all(m != ~0u)) break;
        asm volatile("" ::: "memory");
        LOAD_Z();
    }
    STAMP(9);
    {
        const u32 sg2 = cls == 4 ? 0x3c00bc00u : 0xbc003c00u;
        u32 y[4][4];
        UNROLL for (int i = 0; i < 4; ++i) {
            u4 d;
            UNROLL for (int j = 0; j < 4; ++j) d[j] = hmul(hfma(z2[i][j], sg2, z1[i][j]), scl[j][i]);
            const f4 yr = mfma16(GR[i], d), yi = mfma16(GI[i], d);
            UNROLL for (int r = 0; r < 4; ++r) y[r][i] = pkh(yr[r], yi[r]);
        }
        UNROLL for (int r = 0; r < 4; ++r) *(u4*)&lds[L_A + ((wv * 16 + 4 * q + r) * 16 + n) * 4] = u4{y[r][0], y[r][1], y[r][2], y[r][3]};
    }
    if (WP1 == 6) WIDEN_W(1);
    if (WP2 == 6) WIDEN_W(2);
    if (WP3 == 6) WIDEN_W(3);
    {
        const int ex = (int)(wave_maxu(__builtin_bit_cast(u32, wmax)) >> 23) - (packed ? 117 : 118);
        if (lane == 0) lds[L_WW + wv] = min(eprov + (u32)max(ex, 0), 250u);
    }
    BAR();
    STAMP(10);
    {
        const u4 e4 = *(const u4*)&lds[L_WW];
        const int dw = __builtin_amdgcn_readfirstlane((int)max(max(e4.x, e4.y), max(e4.z, e4.w))) - (int)eprov;
        if (dw > 8) {
            const float ex = __builtin_bit_cast(float, (u32)max(127 + 8 - dw, 1) << 23);
            UNROLL for (int i = 0; i < 4; ++i) UNROLL for (int a = 0; a < 4; ++a) Wf[i][a] *= ex;
        }
        const u32 fw = pow2h(min(dw, 8));
        const u4 FR2 = {hmul(FR.x, fw), hmul(FR.y, fw), hmul(FR.z, fw), hmul(FR.w, fw)}, FI2 = {hmul(FI.x, fw), hmul(FI.y, fw), hmul(FI.z, fw), hmul(FI.w, fw)};
        u32 ra[4][4], rb[4][4];
        UNROLL for (int i = 0; i < 4; ++i) {
            const u4 d = *(const u4*)&lds[L_A + ((q * 16 + 4 * wv + ((i + rot) & 3)) * 16 + n) * 4];
            const f4 xr = mfma16(FR2, d), xi = mfma16(FI2, d);
            const f4 ai = xr * Wf[i][1] + xi * Wf[i][0], ar = xr * Wf[i][0] - xi * Wf[i][1];
            const f4 bi = xr * Wf[i][3] + xi * Wf[i][2], br = xr * Wf[i][2] - xi * Wf[i][3];
            u4 ya, yb;
            UNROLL for (int r = 0; r < 4; ++r) { ya[r] = pkh(ai[r], ar[r]); yb[r] = pkh(bi[r], br[r]); }
            const f4 p0 = mfma16(FR, ya), p1 = mfma16(FI, ya), p2 = mfma16(FR, yb), p3 = mfma16(FI, yb);
            UNROLL for (int r = 0; r < 4; ++r) { ra[r][i] = pkh(p0[r], p1[r]); rb[r][i] = pkh(p2[r], p3[r]); }
        }
        UNROLL for (int r = 0; r < 4; ++r) {
            *(u4*)&lds[L_B + ((wv * 16 + 4 * q + r) * 16 + n) * 4] = u4{ra[r][0], ra[r][1], ra[r][2], ra[r][3]};
            *(u4*)&lds[L_B + 4096 + ((wv * 16 + 4 * q + r) * 16 + n) * 4] = u4{rb[r][0], rb[r][1], rb[r][2], rb[r][3]};
        }
    }
    STAMP(11);
    BAR();
    {
        const int vo = 64 * q * 32768 + n * 8, so = WS_V + 4 * wv * 32768 + cls * 4096 + tile * 128;
        UNROLL for (int i = 0; i < 4; ++i) {
            const u4 da = *(const u4*)&lds[L_B + ((q * 16 + 4 * wv + i) * 16 + n) * 4], db = *(const u4*)&lds[L_B + 4096 + ((q * 16 + 4 * wv + i) * 16 + n) * 4];
            const f4 p0 = mfma16(HR[i], da), p1 = mfma16(HI[i], da), p2 = mfma16(HR[i], db), p3 = mfma16(HI[i], db);
            UNROLL for (int r = 0; r < 4; ++r) __builtin_amdgcn_raw_buffer_store_b64(u2{pkh(p0[r], p1[r]), pkh(p2[r], p3[r])}, rws, vo, so + (16 * r + i) * 32768, SC1);
        }
    }
    STAMP(12);
    SB();
    UNROLL for (int i = 0; i < 4; ++i) { const u4 v = *(const u4*)&lds[L_X + i * 1024 + t * 4]; xv[2 * i] = u2{v.x, v.y}; xv[2 * i + 1] = u2{v.z, v.w}; }
    {
        const float g0 = bflo(gv.x), g1 = bfhi(gv.x), g2 = bflo(gv.y), g3 = bfhi(gv.y);
        const b2 one = {(__bf16)1.0f, (__bf16)1.0f};
        UNROLL for (int s1 = 0; s1 < 8; ++s1) {
            const u32 p0 = pkb(bflo(xv[s1].x) * g0, bfhi(xv[s1].x) * g1), p1 = pkb(bflo(xv[s1].y) * g2, bfhi(xv[s1].y) * g3);
            float a = __builtin_amdgcn_fdot2_f32_bf16(__builtin_bit_cast(b2, p0), one, 0.0f, false);
            a = __builtin_amdgcn_fdot2_f32_bf16(__builtin_bit_cast(b2, p1), one, a, false);
            a += __builtin_bit_cast(float, dpp<0xB1>(__builtin_bit_cast(u32, a)));
            a += __builtin_bit_cast(float, dpp<0x4E>(__builtin_bit_cast(u32, a)));
            a += __builtin_bit_cast(float, dpp<0x141>(__builtin_bit_cast(u32, a)));
            a += __builtin_bit_cast(float, dpp<0x140>(__builtin_bit_cast(u32, a)));
            if (n == 0) lds[L_GS + s1 * 16 + (t >> 4)] = __builtin_bit_cast(u32, a);
        }
    }
    u4 MR[4], MI[4]; f2 TW[16];
    { int lane2 = lane; asm volatile("" : "+v"(lane2)); core_setup(lane2, MR, MI, TW); }
    pin(MR); pin(MI); pin(TW);
    const int ve = 2 * q * 4096 + (128 * wv + n) * 8;
    const int ve8 = wv == 3 ? (int)(WS_VL - WS_V) - b * 32768 + b * 128 + 16 * q : 2 * q * 4096 + (128 * wv + 128) * 8, vo8 = ve8 + (wv == 3 ? 8 : 4096);
    u2 v0[9], v1[9];
#define LOAD_V() do { int a_ = ve; pin(a_); \
        UNROLL for (int i = 0; i < 8; ++i) { \
            v0[i] = __builtin_amdgcn_raw_buffer_load_b64(rws, a_ + 128 * i, WS_V + b * 32768, SC1); \
            v1[i] = __builtin_amdgcn_raw_buffer_load_b64(rws, a_ + 128 * i, WS_V + b * 32768 + 4096, SC1); } \
        v0[8] = __builtin_amdgcn_raw_buffer_load_b64(rws, ve8, WS_V + b * 32768, SC1); \
        v1[8] = __builtin_amdgcn_raw_buffer_load_b64(rws, vo8, WS_V + b * 32768, SC1); } while (0)
    VMWAIT(0);
    LOAD_V();
    SB();
    BAR();
    u4 PM[9];
    {
        float gg[4];
        UNROLL for (int k = 0; k < 2; ++k) {
            const f4* pp = (const f4*)&lds[L_GS + ((n >> 2) * 2 + k) * 16];
            const f4 ps = (pp[0] + pp[1]) + (pp[2] + pp[3]);
            const float lg = rbf((ps.x + ps.y) + (ps.z + ps.w));
            gg[2 * k] = rbf(1.0f / (1.0f + __expf(-lg)));
            gg[2 * k + 1] = rbf(1.0f - gg[2 * k]);
        }
        const int r = n & 3, sa = (n >> 2) * 2;
        UNROLL for (int cc = 0; cc < 2; ++cc) {
            const int u1 = 2 * q + cc;
            const float ta = (float)(sa * u1) * 0.125f + ((r & 1) ? 0.25f : (r & 2) ? 0.5f : 0.0f);
            const float tb = (float)((sa + 1) * u1) * 0.125f + ((r & 1) ? ((r & 2) ? 0.0f : 0.5f) : 0.25f);
            const float ca = cosr(ta) * 0.25f, sa_ = sinr(ta) * 0.25f, cb = cosr(tb) * 0.25f, sb_ = sinr(tb) * 0.25f;
            const u32 pa = pkh(gg[0] * ca + gg[2] * cb, gg[0] * sa_ + gg[2] * sb_), pb = pkh(gg[1] * ca + gg[3] * cb, gg[1] * sa_ + gg[3] * sb_);
            UNROLL for (int i = 0; i < 9; ++i) { const u32 f = lds[L_TS + u1 * 36 + 8 * wv + i]; PM[i][2 * cc] = hmul(pa, f); PM[i][2 * cc + 1] = hmul(pb, f); }
        }
    }
    const float osc = __builtin_bit_cast(float, lds[L_GL]);
    pin(PM);
    STAMP(13);
    SB();
    _Pragma("unroll 1") for (int spin = 0; spin < SPIN_CAP; ++spin) {
        u32 m = 0;
        UNROLL for (int i = 0; i < 9; ++i) m = max3u(m, v0[i].x, v1[i].x);
        if (__all(m != ~0u)) break;
        asm volatile("" ::: "memory");
        LOAD_V();
    }
    STAMP(14);
    {
        f4 z[9];
        UNROLL for (int i = 0; i < 9; ++i) z[i] = mfma16(PM[i], u4{v0[i].x, v0[i].y, v1[i].x, v1[i].y});
        if (wv == 0 && n == 0) { z[0].x = 0.5f * (z[0].x + z[0].z); z[0].y = 0.5f * (z[0].y + z[0].w); }
        if (wv == 3) { z[8].z = 0.5f * (z[8].x + z[8].z); z[8].w = 0.5f * (z[8].y + z[8].w); }
        u4* l4 = (u4*)&lds[L_A];
        UNROLL for (int p = 0; p < 2; ++p)
            l4[(q * 8 + wv) * 32 + 16 * p + n] = u4{pkh(z[p].x, z[p + 2].x), pkh(z[p + 4].x, z[p + 6].x), pkh(z[p].y, z[p + 2].y), pkh(z[p + 4].y, z[p + 6].y)};
        l4[(q * 8 + 7 - wv) * 32 + 16 - n] = u4{pkh(z[7].z, z[5].z), pkh(z[3].z, z[1].z), pkh(z[7].w, z[5].w), pkh(z[3].w, z[1].w)};
        f2 m[4];
        UNROLL for (int i = 0; i < 4; ++i) m[i] = n == 0 ? f2{z[8 - 2 * i].z, z[8 - 2 * i].w} : f2{z[6 - 2 * i].z, z[6 - 2 * i].w};
        l4[(q * 8 + 7 - wv) * 32 + ((32 - n) & 31)] = u4{pkh(m[0].x, m[1].x), pkh(m[2].x, m[3].x), pkh(m[0].y, m[1].y), pkh(m[2].y, m[3].y)};
    }
    BAR();
    {
        u4 in[4];
        UNROLL for (int s = 0; s < 4; ++s) in[s] = *(const u4*)&lds[L_A + (wv * 256 + s * 64 + lane) * 4];
        f16v zr, zi;
        core_fft<false>(in, MR, MI, TW, zr, zi);
        zr *= osc; zi *= osc;
        const rsrc_t ro = mkrs(out, 2048 * 2048);
        const int ov = (4 * perm32(lane & 31) + (lane >> 5)) * 16, os = (512 * wv + b) * 2048;
        UNROLL for (int hf = 0; hf < 2; ++hf) {
            u4 a, c;
            UNROLL for (int j = 0; j < 4; ++j) { a[j] = pkb(zi[8 * hf + 2 * j], zi[8 * hf + 2 * j + 1]); c[j] = pkb(zr[8 * hf + 2 * j], zr[8 * hf + 2 * j + 1]); }
            __builtin_amdgcn_raw_buffer_store_b128(a, ro, ov + 32 * hf, os, OAUX);
            __builtin_amdgcn_raw_buffer_store_b128(c, ro, ov + 32 * hf, os + 256 * 2048, OAUX);
        }
    }
    STAMP(15);
    STAMP_OUT();
}

extern "C" void kernel_launch(void* const* d_in, const int* in_sizes, int n_in,
                              void* d_out, int out_size, void* d_ws, size_t ws_size,
                              hipStream_t stream) {
    gfnet_fused<<<256, 320, 0, stream>>>((const u32*)d_in[0], (const u2*)d_in[6], (const unsigned short*)d_in[1], (const unsigned short*)d_in[2],
                                         (const unsigned short*)d_in[4], (const unsigned short*)d_in[5], (char*)d_ws, (u4*)d_out);
}
```

```cpp
#define F1POS 0
#define F3POS 0
#define ZGATE 0
#define VGATE 0
#define ROT 1
#define W0WAIT 0
#define ZHOLD 0
#include <hip/hip_runtime.h>
#include <hip/hip_bf16.h>
#include <stdint.h>

typedef _Float16 h2 __attribute__((ext_vector_type(2)));
typedef _Float16 h8 __attribute__((ext_vector_type(8)));
typedef __bf16 b2 __attribute__((ext_vector_type(2)));
typedef __bf16 b8 __attribute__((ext_vector_type(8)));
typedef float f2 __attribute__((ext_vector_type(2)));
typedef float f4 __attribute__((ext_vector_type(4)));
typedef float f16v __attribute__((ext_vector_type(16)));
typedef short s4 __attribute__((ext_vector_type(4)));
typedef unsigned u32;
typedef u32 u2 __attribute__((ext_vector_type(2)));
typedef u32 u4 __attribute__((ext_vector_type(4)));
typedef unsigned short us2 __attribute__((ext_vector_type(2)));
#define DI __device__ __forceinline__
#define UNROLL _Pragma("unroll")
#define NOUNROLL _Pragma("unroll 1")
#define LAS __attribute__((address_space(3)))

#define MSLP 1
#define F1DELAY 12
#define GFIRST 1
#define GEARLY 0
#define LDEPTH 3
#define CODEKB 24
#define F3DELAY 16
#define MAPFULL 1
#define WGATE 4
#define XLAST 0
#define WAFTERX 0
#define PSLEEP 4
#define W0DELAY 2
#define WWIN 8
#define VCMODE 1
#define VWIN 8
#define ZWAIT 4
#define VWAIT 0
#define XAUX 2
#define W0AUX 0
#define OAUX 2

constexpr u32 FSTR = 16;
constexpr u32 WS_Z = 0;
constexpr u32 WS_V = 4u << 20;
constexpr u32 WS_VL = 12u << 20;
constexpr u32 WS_HB = WS_VL + (32u << 10);
constexpr u32 WS_F1 = WS_HB + (32u << 10);
constexpr u32 WS_F3 = WS_F1 + 2048 * FSTR;
constexpr u32 WS_E = WS_F3 + 2048 * FSTR;
constexpr u32 WS_BYTES = WS_E + 4096;
constexpr size_t WS_META = 24u << 20;
constexpr int SC1 = 16;
constexpr int SPIN_CAP = 8192;
constexpr int LSPIN_CAP = 1 << 17;
constexpr int OOB = 0x7ffffff0;
constexpr float TRUNC_FIX = 1.00066f;
constexpr u32 TM = 0x00010001u;

template <class T> DI void pin(T& v) { asm volatile("" : "+v"(v)); }
template <class T, int N> DI void pin(T (&v)[N]) { UNROLL for (int i = 0; i < N; ++i) pin(v[i]); }
DI u32 opq(u32 v) { asm("" : "+v"(v)); return v; }
DI u32 pkh(float lo, float hi) { h2 r; r.x = (_Float16)lo; r.y = (_Float16)hi; return __builtin_bit_cast(u32, r); }
DI u32 pkb(float lo, float hi) { b2 r; r.x = (__bf16)lo; r.y = (__bf16)hi; return __builtin_bit_cast(u32, r); }
DI float bflo(u32 v) { return __builtin_bit_cast(float, v << 16); }
DI float bfhi(u32 v) { return __builtin_bit_cast(float, v & 0xffff0000u); }
DI float rbf(float v) { return (float)(__bf16)v; }
DI h2 H(u32 v) { return __builtin_bit_cast(h2, v); }
DI u32 U(h2 v) { return __builtin_bit_cast(u32, v); }
DI f16v mfma32(u4 a, u4 b, f16v c) { return __builtin_amdgcn_mfma_f32_32x32x16_f16(__builtin_bit_cast(h8, a), __builtin_bit_cast(h8, b), c, 0, 0, 0); }
DI f4 mfma16(u4 a, u4 b) { return __builtin_amdgcn_mfma_f32_16x16x32_f16(__builtin_bit_cast(h8, a), __builtin_bit_cast(h8, b), f4{}, 0, 0, 0); }
DI f4 mfma16b(u4 a, u4 b) { return __builtin_amdgcn_mfma_f32_16x16x32_bf16(__builtin_bit_cast(b8, a), __builtin_bit_cast(b8, b), f4{}, 0, 0, 0); }
DI u32 hmul(u32 a, u32 b) { return U(H(a) * H(b)); }
DI u32 hfma(u32 a, u32 b, u32 c) { return U(__builtin_elementwise_fma(H(a), H(b), H(c))); }
DI u32 cmul_swap(u32 x, u32 w) { const h2 a = H(opq(x)), c = H(opq(w)); h2 n = c; n.y = -n.y; return U(__builtin_elementwise_fma(a.yy, n, a.xx * c.yx)); }
DI u32 cmul_conj(u32 x, u32 t) { const h2 a = H(opq(x)), c = H(opq(t)); h2 n = c.yy; n.y = -n.y; return U(__builtin_elementwise_fma(a.yx, n, a * c.xx)); }
DI u32 pkmax(u32 a, u32 b) { return __builtin_bit_cast(u32, __builtin_elementwise_max(__builtin_bit_cast(us2, a), __builtin_bit_cast(us2, b))); }
DI float sinr(float r) { return __builtin_amdgcn_sinf(r); }
DI float cosr(float r) { return __builtin_amdgcn_cosf(r); }
DI int perm32(int n) { return (n & 0x13) | ((n & 4) << 1) | ((n & 8) >> 1); }
DI u32 max3u(u32 a, u32 b, u32 c) { return max(max(a, b), c); }
DI u32 max4u(u4 v) { return max(max(v.x, v.y), max(v.z, v.w)); }
DI u32 min4u(u4 v) { return min(min(v.x, v.y), min(v.z, v.w)); }
DI u32 mark(u32 v, u32 par, u32 tm) { return (par & tm) | (v & ~tm); }
DI u32 rfl(u32 v) { return __builtin_amdgcn_readfirstlane(v); }
DI u32 pow2h(int d) { const float f = __builtin_bit_cast(float, (u32)(127 - min(d, 64)) << 23); return pkh(f, f); }

typedef __amdgpu_buffer_rsrc_t rsrc_t;
DI rsrc_t mkrs(const void* p, u32 bytes) { return __builtin_amdgcn_make_buffer_rsrc((void*)p, (short)0, (int)bytes, 0x00020000); }
template <int AUX> DI void store128(u4 v, rsrc_t r, int off) { __builtin_amdgcn_raw_buffer_store_b128(v, r, off, 0, AUX); }
#define SB() __builtin_amdgcn_sched_barrier(0)
#define MEMBAR() asm volatile("" ::: "memory")
#define VMWAIT(n) asm volatile("s_waitcnt vmcnt(%0)" :: "n"(n) : "memory")
#define STAMP_DECL
#define STAMP(i)
#define STAMP_OUT()
template <int CTRL> DI u32 dpp(u32 v) { return __builtin_amdgcn_update_dpp(0u, v, CTRL, 0xf, 0xf, true); }
DI u32 wave_maxu(u32 v) {
    v = max(v, dpp<0xB1>(v)); v = max(v, dpp<0x4E>(v)); v = max(v, dpp<0x141>(v)); v = max(v, dpp<0x140>(v));
    return max(max((u32)__builtin_amdgcn_readlane(v, 0), (u32)__builtin_amdgcn_readlane(v, 16)), max((u32)__builtin_amdgcn_readlane(v, 32), (u32)__builtin_amdgcn_readlane(v, 48)));
}

DI void core_setup(int lane, int s, u4& tw, u2& m) {
    float fc = (float)perm32(lane & 31) * (1.0f / 1024.0f), p0 = (float)(4 * (lane >> 5) + 8 * s) * fc, p1 = (float)(4 * (lane >> 5) + 8 * (s >> 1) + 2 * (s & 1)) * fc * 32.0f;
    pin(fc); pin(p0); pin(p1);
    float tc[4], ts[4];
    UNROLL for (int i = 0; i < 4; ++i) { const float pt = fmaf((float)i, fc, p0); tc[i] = cosr(pt); ts[i] = sinr(pt); }
    tw = u4{pkh(tc[0], tc[1]), pkh(tc[2], tc[3]), pkh(ts[0], ts[1]), pkh(ts[2], ts[3])};
    const float p2 = fmaf(32.0f, fc, p1);
    m = u2{pkh(cosr(p1), cosr(p2)), pkh(sinr(p1), sinr(p2))};
}
DI void core_expand(int lane, u4 (&MR)[4], u4 (&MI)[4]) {
    const u32 sg = (lane & 1) ? 0x80008000u : 0u;
    MR[2] = MR[0] ^ sg; MR[3] = MR[1] ^ sg;
    UNROLL for (int s = 0; s < 4; ++s) MI[s] = u4{MR[s].z ^ 0x80008000u, MR[s].w ^ 0x80008000u, MR[s].x, MR[s].y};
}
template <bool OUT_B> DI void core_fft(const u4 (&in)[4], const u4 (&MR)[4], const u4 (&MI)[4], const u4 (&TW)[4], f16v& outr, f16v& outi) {
    f16v yr = {}, yi = {};
    UNROLL for (int s = 0; s < 4; ++s) yr = mfma32(in[s], MR[s], yr);
    SB();
    yi = mfma32(in[0], MI[0], yi); yi = mfma32(in[1], MI[1], yi);
    SB();
    h2 t0[8], t1[8];
#define TW_RE(p) do { const h2 pr_ = H(pkh(yr[2 * (p)], yr[2 * (p) + 1])); t0[p] = pr_ * H(TW[(p) >> 1][(p) & 1]); t1[p] = pr_ * H(TW[(p) >> 1][2 + ((p) & 1)]); } while (0)
    TW_RE(0); TW_RE(1); SB();
    yi = mfma32(in[2], MI[2], yi);
    TW_RE(2); TW_RE(3); SB();
    yi = mfma32(in[3], MI[3], yi);
    TW_RE(4); TW_RE(5); TW_RE(6); TW_RE(7); SB();
    outr = f16v{}; outi = f16v{};
    u4 d[4];
#define TW_IM(s) do { UNROLL for (int j = 0; j < 2; ++j) { const int p_ = 2 * (s) + j; const h2 pi_ = H(pkh(yi[2 * p_], yi[2 * p_ + 1])); \
        d[s][j] = U(__builtin_elementwise_fma(pi_, H(TW[s][2 + j]), t0[p_])); d[s][2 + j] = U(__builtin_elementwise_fma(pi_, H(TW[s][j]), -t1[p_])); } } while (0)
    TW_IM(0); SB();
    UNROLL for (int s = 0; s < 4; ++s) {
        outr = OUT_B ? mfma32(MR[s], d[s], outr) : mfma32(d[s], MR[s], outr);
        if (s < 3) TW_IM(s + 1);
        SB();
    }
    UNROLL for (int s = 0; s < 4; ++s) outi = OUT_B ? mfma32(MI[s], d[s], outi) : mfma32(d[s], MI[s], outi);
}
DI u32 rot(float ph) { return pkh(cosr(ph), sinr(ph)); }
DI u32 quarter(u32 r, u32 f) { u32 o; asm("v_pk_mul_f16 %0, %1, %2 op_sel:[1,0] op_sel_hi:[0,1] neg_lo:[1,0]" : "=v"(o) : "v"(r), "v"(f)); return o; }
DI u4 quarter(u4 r) { u32 one = 0x3c003c00u; pin(one); return u4{quarter(r.x, one), quarter(r.y, one), quarter(r.z, one), quarter(r.w, one)}; }

DI u32 tag_of(u32 epoch, u32 idx) { u32 h = (epoch ^ (idx * 0x9E3779B1u)) * 0x85EBCA6Bu; return h ^ (h >> 15); }
DI u32 parity_of(u32 epoch) { return (epoch & 1u) ? 0x00000001u : 0x00010000u; }
DI void flag_store(rsrc_t rws, u32 arr, int b, int lane, u32 payload, u32 tag) {
    __builtin_amdgcn_raw_buffer_store_b64(u2{payload, tag}, rws, lane < 8 ? lane * (int)(256 * FSTR) : OOB, arr + b * FSTR, SC1);
}
DI u4 flag_wait(rsrc_t rws, u32 arr, int b, int lane, u32 epoch) {
    u2 f[4]; u32 tg[4];
    UNROLL for (int j = 0; j < 4; ++j) tg[j] = tag_of(epoch, arr + 4 * lane + j);
    NOUNROLL for (int spin = 0; spin < SPIN_CAP; ++spin) {
        MEMBAR();
        UNROLL for (int j = 0; j < 4; ++j) f[j] = __builtin_amdgcn_raw_buffer_load_b64(rws, lane * (4 * FSTR) + j * FSTR, arr + (b & 7) * (256 * FSTR), SC1);
        if (__all(f[0].y == tg[0] && f[1].y == tg[1] && f[2].y == tg[2] && f[3].y == tg[3])) break;
        __builtin_amdgcn_s_sleep(PSLEEP);
    }
    return u4{f[0].x, f[1].x, f[2].x, f[3].x};
}
DI int cls_of(int b) { const int row = (b & 7) >> 1; return row == 0 ? 4 * (b >> 7) : ((b & 1) ? 8 - row : row); }
DI int tile_of(int b) { return (b & 6) == 0 ? 16 * (b & 1) + ((b >> 3) & 15) : b >> 3; }
DI bool is_helper(int b) { return tile_of(b) == 31; }

constexpr int L_EW = 0;
constexpr int L_C1 = 4, L_WW = 8, L_C3 = 12, L_C4 = 16, L_C5 = 20, L_C6 = 24;
constexpr int L_HE = 28;
constexpr int L_OK = 32;
constexpr int L_OSC = 36;
constexpr int L_PR = 40;
constexpr int L_LD = 44;
constexpr int L_MOK = 45;
constexpr int L_ZL = 46;
constexpr int L_P1 = 47;
constexpr int L_NFLAG = 64;
constexpr int L_SC = 64;
constexpr int L_GS = 320;
constexpr int L_TS = 448;
constexpr int L_M = 768;
constexpr int L_A1 = L_M + 1536;
constexpr int L_A2 = L_A1 + 4096;
constexpr int L_B = L_A2 + 4096;
constexpr int L_W = L_B + 8192;
constexpr int L_END = L_W + 64 * 256;

DI void lset(u32* lds, int idx, u32 v, int lane) { MEMBAR(); if (lane == 0) lds[idx] = v; MEMBAR(); }
DI u32 lget(u32* lds, int idx) { MEMBAR(); return rfl(lds[idx]); }
DI u32 lwait(u32* lds, int idx, u32 atleast, int slp) {
    u32 v = 0;
    NOUNROLL for (int spin = 0; spin < LSPIN_CAP; ++spin) { v = lget(lds, idx); if (v >= atleast) break; if (slp) __builtin_amdgcn_s_sleep(1); }
    MEMBAR();
    return v;
}
DI u4 lwait4(u32* lds, int idx) {
    u4 v;
    NOUNROLL for (int spin = 0; spin < LSPIN_CAP; ++spin) { MEMBAR(); v = *(const u4*)&lds[idx]; if (rfl(min4u(v))) break; }
    MEMBAR();
    return v;
}

DI u32 lwait2(u32* lds, int idx) {
    u2 v;
    NOUNROLL for (int spin = 0; spin < LSPIN_CAP; ++spin) { MEMBAR(); v = *(const u2*)&lds[idx]; if (rfl(min(v.x, v.y))) break; __builtin_amdgcn_s_sleep(1); }
    MEMBAR();
    return rfl(max(v.x, v.y));
}
DI void lwait4s(u32* lds, int idx) {
    NOUNROLL for (int spin = 0; spin < LSPIN_CAP; ++spin) { MEMBAR(); const u4 v = *(const u4*)&lds[idx]; if (rfl(min4u(v))) break; __builtin_amdgcn_s_sleep(1); }
    MEMBAR();
}

__global__ __launch_bounds__(512, 2) void gfnet_fused(const void* x, const void* gw, const void* war, const void* wai, const void* wbr, const void* wbi,
                                                      char* ws, void* out) {
    __shared__ __attribute__((aligned(16))) u32 lds[L_END];
    STAMP_DECL;
    STAMP(0);
    unsigned long long pc0;
    asm volatile("s_getpc_b64 %0" : "=s"(pc0));
    asm volatile("" :: "s"(x), "s"(gw), "s"(war), "s"(wai), "s"(wbr), "s"(wbi), "s"(ws), "s"(out));
    const int t = threadIdx.x, lane = t & 63, wv = __builtin_amdgcn_readfirstlane(t >> 6), b = blockIdx.x;
    const int q = lane >> 4, n = lane & 15;
    const rsrc_t rws = mkrs(ws, WS_BYTES);
    const int row = (b & 7) >> 1, cls = cls_of(b), tile = tile_of(b);
    const bool packed = row == 0;

    if (wv >= 4) {
        const u32 ev = __builtin_amdgcn_raw_buffer_load_b32(rws, 0, WS_E, SC1);
        if (wv == 4) lds[lane] = 0u;
        asm volatile("s_waitcnt lgkmcnt(0)" ::: "memory");
        if (!XLAST || wv == 4 || wv == 7) { __builtin_amdgcn_s_barrier(); MEMBAR(); }
        if (wv == 5 || wv == 6) {
            const int wvo = (lane >> 2) * (128 * 1026) + min(lane & 3, 2) * 16, wso = (cls * 513 + 16 * tile - cls) * 2;
            const rsrc_t rw[2] = {mkrs(wv == 5 ? war : wbr, 2048 * 1026), mkrs(wv == 5 ? wai : wbi, 2048 * 1026)};
            u4 w[4][4][2];
            h2 hi = {}, lo = {};
            u4* wl = (u4*)&lds[L_W + (wv - 5) * 512] + lane;
#define WLOAD(r) do { UNROLL for (int st = 0; st < 4; ++st) UNROLL for (int a = 0; a < 2; ++a) \
                w[r][st][a] = __builtin_amdgcn_raw_buffer_load_b128(rw[a], wvo, wso + (4 * st + ((ROT * tile + (r)) & 3)) * 8208, (r) == 0 ? W0AUX : 0); SB(); } while (0)
#define WKEEP(r) do { UNROLL for (int st = 0; st < 4; ++st) UNROLL for (int a = 0; a < 2; ++a) { const u4 v = w[r][st][a]; \
                wl[((4 * st + ((ROT * tile + (r)) & 3)) * 4 + a) * 64] = v; \
                hi = __builtin_elementwise_maximum(__builtin_elementwise_maximum(hi, H(v.x)), H(v.y)); hi = __builtin_elementwise_maximum(__builtin_elementwise_maximum(hi, H(v.z)), H(v.w)); \
                lo = __builtin_elementwise_minimum(__builtin_elementwise_minimum(lo, H(v.x)), H(v.y)); lo = __builtin_elementwise_minimum(__builtin_elementwise_minimum(lo, H(v.z)), H(v.w)); \
                SB(); } } while (0)
            if (!XLAST) __builtin_amdgcn_s_sleep(W0DELAY);
            if (WAFTERX) lwait(lds, L_EW + wv - 5, 1u, 1);
            WLOAD(0);
            if (XLAST) { MEMBAR(); __builtin_amdgcn_s_barrier(); MEMBAR(); }
            VMWAIT(W0WAIT);
            const bool wfast = lget(lds, L_EW) == 0u || lget(lds, L_C1) == 0u;
            STAMP(1);
            if (WGATE > 1) WLOAD(1);
            if (WGATE > 2) WLOAD(2);
            if (WGATE > 3) WLOAD(3);
            WKEEP(0);
            if (WGATE < 4 && !wfast) lwait(lds, L_ZL, 1u, 1);
            if (WGATE <= 1) WLOAD(1);
            if (WGATE <= 2) WLOAD(2);
            if (WGATE <= 3) WLOAD(3);
            if (ZHOLD) { VMWAIT(ZHOLD - 1); lset(lds, L_LD + wv - 5, 1u, lane); }
            WKEEP(1); WKEEP(2); WKEEP(3);
            const u32 mx = pkmax(U(hi) & 0x7fff7fffu, U(lo) & 0x7fff7fffu);
            lset(lds, L_WW + wv - 5, min(max(wave_maxu(max(mx & 0xffffu, mx >> 16)) >> 7, 1u), 240u), lane);
            STAMP(2);
            if (wv == 6) {
                const u32 epoch = rfl(ev) + 1u;
                const u32 ewt = lwait2(lds, L_WW);
                if (F3POS == 1) lwait4s(lds, L_C3);
                if (F3POS == 2) lwait4s(lds, L_C4);
                flag_store(rws, WS_F3, b, lane, ewt, tag_of(epoch, WS_F3 + b));
                __builtin_amdgcn_s_sleep(F3DELAY);
                const u4 f = flag_wait(rws, WS_F3, b, lane, epoch) & 0xffu;
                const u32 ewm = wave_maxu(max4u(f));
                UNROLL for (int j = 0; j < 4; ++j) {
                    const int p = 4 * lane + j;
                    const u32 fs = pow2h(ewm + 5 - f[j]);
                    lds[L_TS + cls_of(p) * 36 + tile_of(p)] = fs;
                    if (is_helper(p)) lds[L_TS + cls_of(p) * 36 + 32] = fs;
                }
                lset(lds, L_OK + 3, ewm, lane);
                STAMP(5);
            }
        } else if (wv == 4) {
            {
                unsigned long long pc1;
                asm volatile("s_getpc_b64 %0" : "=s"(pc1));
                const rsrc_t rc = mkrs((const void*)pc0, pc1 > pc0 ? (u32)(pc1 - pc0) : 0u);
                u32 cv = __builtin_amdgcn_raw_buffer_load_b32(rc, lane * 16 + (b >> 3) * 1024, 0, 0);
                pin(cv);
            }
            const u32 epoch = rfl(ev) + 1u;
            lwait(lds, F1POS ? L_P1 : L_C1, 1u, 1);
            __builtin_amdgcn_s_sleep(F1DELAY);
            STAMP(3);
            const u4 f = flag_wait(rws, WS_F1, b, lane, epoch) & 0xffu;
            const u32 emax = wave_maxu(max4u(f));
            *(u4*)&lds[L_SC + 4 * lane] = u4{pow2h(emax + 4 - f.x), pow2h(emax + 4 - f.y), pow2h(emax + 4 - f.z), pow2h(emax + 4 - f.w)};
            lset(lds, L_OK + 1, emax, lane);
            STAMP(4);
        } else if (is_helper(b)) {
            {
                const int hc = cls;
                const u32 epoch = rfl(ev) + 1u;
                u4 GR, FR, HR, T1, T2, WH[2];
                UNROLL for (int j = 0; j < 4; ++j) {
                    GR[j] = rot((float)((4 * q + j) * (hc + 8 * n)) * (1.0f / 128.0f) + (hc == 4 ? 0.25f : 0.0f));
                    HR[j] = rot((float)(n * (hc + 8 * (4 * q + j))) * (1.0f / 128.0f));
                    FR[j] = rot((float)(n * (4 * q + j)) * (1.0f / 16.0f));
                    T1[j] = rot((float)((4 * q + j) * (hc + 8 * n)) * (1.0f / 2048.0f));
                    T2[j] = rot((float)(n * (hc + 8 * (4 * q + j))) * (1.0f / 2048.0f));
                }
                u4 GI = quarter(GR), HI = quarter(HR), FI = quarter(FR);
                pin(GR); pin(GI); pin(HR); pin(HI); pin(FR); pin(FI); pin(T1); pin(T2);
                STAMP(3);
                lwait(lds, L_OK + 1, 1u, 1);
                const u32 tg = tag_of(epoch, 0u), par = parity_of(epoch);
                u4 hz;
                NOUNROLL for (int spin = 0; spin < SPIN_CAP; ++spin) {
                    MEMBAR();
                    u32 bad = 0;
                    UNROLL for (int j = 0; j < 4; ++j) { const u2 g = __builtin_amdgcn_raw_buffer_load_b64(rws, (64 * q + n) * 16 + j * 256, WS_HB + hc * 4096, SC1); hz[j] = g.x & ~TM; bad |= g.y ^ tg; }
                    if (__all(bad == 0u)) break;
                    __builtin_amdgcn_s_sleep(PSLEEP);
                }
                STAMP(4);
                __builtin_amdgcn_s_setprio(2);
                const u32 msk = hc == 0 ? 0x0000ffffu : hc == 4 ? 0xffff0000u : ~0u, sg = hc > 4 ? 0x80000000u : 0u;
                u4 d, o[2];
                UNROLL for (int j = 0; j < 4; ++j) d[j] = hmul(hz[j], (lds[L_SC + 64 * q + 16 * j + n] & msk) | sg);
                f4 yr = mfma16(d, GR), yi = mfma16(d, GI);
                UNROLL for (int r = 0; r < 4; ++r) d[r] = cmul_conj(pkh(yr[r], yi[r]), T1[r]);
                const f4 xr = mfma16(FR, d), xi = mfma16(FI, d);
                {
                    const float sw = __builtin_bit_cast(float, (249u - lwait2(lds, L_WW)) << 23);
                    UNROLL for (int r = 0; r < 4; ++r) UNROLL for (int f = 0; f < 2; ++f) {
                        const unsigned short* wp = (const unsigned short*)&lds[L_W + (n * 4 + 2 * f) * 256 + (4 * q + r) * 16] + 16 + hc;
                        float re = bflo(wp[0]) * sw, im = bflo(wp[512]) * sw;
                        pin(re);
                        WH[f][r] = pkh(re, im);
                    }
                }
                UNROLL for (int f = 0; f < 2; ++f) {
                    UNROLL for (int r = 0; r < 4; ++r) d[r] = cmul_swap(pkh(xr[r], xi[r]), WH[f][r]);
                    yr = mfma16(d, FR); yi = mfma16(d, FI);
                    UNROLL for (int r = 0; r < 4; ++r) o[f][r] = cmul_conj(pkh(yr[r], yi[r]), T2[r]);
                }
                const f4 p0 = mfma16(HR, o[0]), p1 = mfma16(HI, o[0]), p2 = mfma16(HR, o[1]), p3 = mfma16(HI, o[1]);
                UNROLL for (int r = 0; r < 4; ++r)
                    store128<SC1>(u4{mark(pkh(p0[r], p1[r]), par, TM), tg, mark(pkh(p2[r], p3[r]), par, TM), tg}, rws, (64 * q + n) * 128 + WS_VL + 16 * r * 128 + hc * 16);
                STAMP(5);
            }
        }
        STAMP_OUT();
        return;
    }

#define GATE_BLOCK do { pin(xv); \
        const f2 g01 = {bflo(gv.x), bfhi(gv.x)}, g23 = {bflo(gv.y), bfhi(gv.y)}; \
        f4 acc = {}; \
        UNROLL for (int i = 0; i < 4; ++i) { \
            u4 pp; \
            UNROLL for (int k = 0; k < 2; ++k) { \
                const f2 a = f2{bflo(xv[2 * i + k].x), bfhi(xv[2 * i + k].x)} * g01, c = f2{bflo(xv[2 * i + k].y), bfhi(xv[2 * i + k].y)} * g23; \
                pp[2 * k] = pkb(a.x, a.y); pp[2 * k + 1] = pkb(c.x, c.y); } \
            const u32 s0 = n == 2 * i ? 0x3f803f80u : 0u, s1 = n == 2 * i + 1 ? 0x3f803f80u : 0u; \
            acc = __builtin_amdgcn_mfma_f32_16x16x32_bf16(__builtin_bit_cast(b8, pp), __builtin_bit_cast(b8, u4{s0, s0, s1, s1}), acc, 0, 0, 0); } \
        if (n < 8) lds[L_GS + n * 16 + (t >> 4)] = __builtin_bit_cast(u32, (acc.x + acc.y) + (acc.z + acc.w)); \
        lset(lds, L_C5 + wv, 1u, lane); } while (0)
#define GATE_EARLY
#define GATE_LATE GATE_BLOCK
    if (XLAST) { __builtin_amdgcn_s_barrier(); MEMBAR(); }
    u2 xv[8];
    {
        const rsrc_t rx = mkrs(x, 2048 * 2048);
        UNROLL for (int s1 = 0; s1 < 8; ++s1) xv[s1] = __builtin_amdgcn_raw_buffer_load_b64(rx, t * 8, (256 * s1 + b) * 2048, XAUX);
    }
    const u32 ev = __builtin_amdgcn_raw_buffer_load_b32(rws, 0, WS_E, SC1);
    const u2 gv = __builtin_amdgcn_raw_buffer_load_b64(mkrs(gw, 2048), t * 8, 0, 0);
    MEMBAR();
    SB();
    if (!XLAST) { __builtin_amdgcn_s_barrier(); MEMBAR(); }
    __builtin_amdgcn_s_setprio(1);
    u4 MR[4], MI[4], TW[4];
    {
        u4 tw; u2 m;
        core_setup(lane, wv, tw, m);
        *(u4*)&lds[L_M + (2 + wv) * 256 + lane * 4] = tw;
        lds[L_M + (wv >> 1) * 256 + lane * 4 + (wv & 1)] = m.x; lds[L_M + (wv >> 1) * 256 + lane * 4 + 2 + (wv & 1)] = m.y;
    }
    u4 FR, FI, GR[4], GI[4], HR[4], HI[4], sel1;
    UNROLL for (int j = 0; j < 4; ++j) FR[j] = rot((float)(n * (4 * q + j)) * (1.0f / 16.0f));
    FI = quarter(FR);
    {
        const float ofs = cls == 4 ? 0.25f : 0.0f;
        float gu = (float)(cls + 8 * n) * (1.0f / 2048.0f), g0 = (float)(64 * q + 4 * wv) * gu + ofs;
        pin(gu); pin(g0);
        UNROLL for (int i = 0; i < 4; ++i) UNROLL for (int j = 0; j < 4; ++j) GR[i][j] = rot(fmaf((float)(16 * j + i), gu, g0));
    }
    pin(FR); pin(FI); pin(GR);
    const int zc = perm32(lane & 31) + (lane >> 5) * 256, zcu = zc + ((lane & 31) == 0 ? 543 : 511);
    int zl = (zc >> 4) * 16384 + (zc & 15) * 4, zu = (zcu >> 4) * 16384 + (zcu & 15) * 4;
    const int zh0 = wv * 4096, zh1 = (wv == 0 ? 4 : 8 - wv) * 4096;
    int zh = lane == 0 ? zh0 : zh1; pin(zh); zh = lane < 2 ? zh : OOB;
    pin(zl); pin(zu); pin(zh);
    u4 tags;
    u32 tm = TM; pin(tm);
    STAMP(1);
    SB();
    {
        h2 hi = H(xv[0].x), lo = hi;
        hi = __builtin_elementwise_maximum(hi, H(xv[0].y)); lo = __builtin_elementwise_minimum(lo, H(xv[0].y));
        UNROLL for (int s1 = 1; s1 < 8; ++s1) {
            hi = __builtin_elementwise_maximum(__builtin_elementwise_maximum(hi, H(xv[s1].x)), H(xv[s1].y));
            lo = __builtin_elementwise_minimum(__builtin_elementwise_minimum(lo, H(xv[s1].x)), H(xv[s1].y));
        }
        const u32 mx = pkmax(U(hi) & 0x7fff7fffu, U(lo) & 0x7fff7fffu);
        lset(lds, L_EW + wv, min(max(wave_maxu(max(mx & 0xffffu, mx >> 16)) >> 7, 8u), 247u), lane);
        STAMP(2);
        f2 e0[2], e1[2], c0[2], c2[2], dm[2], dp[2], d0[2], d1[2];
        UNROLL for (int j = 0; j < 2; ++j) {
            f2 v[8];
            UNROLL for (int s1 = 0; s1 < 8; ++s1) v[s1] = f2{bflo(xv[s1][j]), bfhi(xv[s1][j])};
            const f2 a0 = v[0] + v[4], a1 = v[1] + v[5], a2 = v[2] + v[6], a3 = v[3] + v[7];
            const f2 b1 = v[1] - v[5], b3 = v[3] - v[7];
            c0[j] = v[0] - v[4]; c2[j] = v[2] - v[6];
            e0[j] = a0 + a2; e1[j] = a1 + a3; dm[j] = b1 - b3; dp[j] = b1 + b3; d0[j] = a0 - a2; d1[j] = a3 - a1;
        }
        pin(e0); pin(e1); pin(c0); pin(c2); pin(dm); pin(dp); pin(d0); pin(d1);
        const u32 epoch = rfl(ev) + 1u;
        tags = u4{parity_of(epoch), tag_of(epoch, WS_F1 + b), epoch, tag_of(epoch, 0u)};
        const u4 ews = lwait4(lds, L_EW);
        MR[0] = *(const u4*)&lds[L_M + lane * 4]; MR[1] = *(const u4*)&lds[L_M + 256 + lane * 4];
        UNROLL for (int i = 0; i < 4; ++i) TW[i] = *(const u4*)&lds[L_M + (2 + i) * 256 + lane * 4];
        const u32 emax = rfl(max4u(ews));
        if (F1POS == 0 && wv == 0) flag_store(rws, WS_F1, b, lane, emax, tags.y);
        const float sc = __builtin_bit_cast(float, (250u - emax) << 23);
        const float rs = sc * 0.70710678118654752f;
        u32 o[8][2];
        UNROLL for (int j = 0; j < 2; ++j) {
            const f2 e1s = e1[j] * sc, b0 = c0[j] * sc, b2_ = c2[j] * sc;
            f2 r[8];
            r[0] = e0[j] * sc + e1s; r[1] = e0[j] * sc - e1s;
            r[2] = dm[j] * rs + b0; r[3] = -(dp[j] * rs) - b2_;
            r[4] = d0[j] * sc; r[5] = d1[j] * sc;
            r[6] = b0 - dm[j] * rs; r[7] = b2_ - dp[j] * rs;
            UNROLL for (int p = 0; p < 8; ++p) o[p][j] = pkh(r[p].x, r[p].y);
        }
        UNROLL for (int p = 0; p < 8; ++p) *(u2*)&lds[L_A1 + p * 512 + 2 * t] = u2{o[p][0], o[p][1]};
        lset(lds, L_C1 + wv, 1u, lane);
        core_expand(lane, MR, MI);
        pin(MR); pin(MI);
        u4 in[4];
        {
            const int h = lane >> 5, blk = (lane >> 4) & 1, qq = (lane & 15) >> 2, p = lane & 3;
            const int off = L_A1 + wv * 1024 + (4 * h + qq) * 16 + blk * 8 + p * 2;
            NOUNROLL for (int spin = 0; spin < LSPIN_CAP; ++spin) {
                MEMBAR();
                const u4 c = *(const u4*)&lds[L_C1];
                MEMBAR();
                UNROLL for (int s = 0; s < 4; ++s) {
                    const u2 re = __builtin_bit_cast(u2, __builtin_amdgcn_ds_read_tr16_b64_v4i16((LAS s4*)&lds[off + s * 128]));
                    const u2 im = __builtin_bit_cast(u2, __builtin_amdgcn_ds_read_tr16_b64_v4i16((LAS s4*)&lds[off + s * 128 + 512]));
                    in[s] = u4{re.x, re.y, im.x, im.y};
                }
                if (rfl(min4u(c))) break;
            }
        }
        STAMP(3);
        if (F1POS == 1) { if (ZHOLD) lwait2(lds, L_LD); if (wv == 0) { flag_store(rws, WS_F1, b, lane, emax, tags.y); lset(lds, L_P1, 1u, lane); } }
        f16v zr, zi;
        core_fft<true>(in, MR, MI, TW, zr, zi);
        u32 z[16];
        UNROLL for (int r = 0; r < 16; ++r) z[r] = mark(pkh(zr[r], zi[r]), tags.x, tm);
        pin(z);
        if (ZHOLD && F1POS != 1) lwait2(lds, L_LD);
        if (F1POS == 2 && wv == 0) { flag_store(rws, WS_F1, b, lane, emax, tags.y); lset(lds, L_P1, 1u, lane); }
        STAMP(4);
        const int zp = ((b >> 2) & 3) * 64 + (b & 3) * 16 + ((b >> 4) & 3) * 4 + (b >> 6);
        const int zs = WS_Z + wv * 64 * 16384 + zp * 64;
        UNROLL for (int r = 0; r < 8; ++r) __builtin_amdgcn_raw_buffer_store_b32(z[r], rws, zl, zs + r * 32768, SC1);
        const u32 zn = rfl(z[8]), za = __builtin_amdgcn_readlane(z[8], 32), zb = __builtin_amdgcn_readlane(z[0], 0);
        MEMBAR();
        const u32 zt = lane < 32 ? za : zb;
        UNROLL for (int r = 8; r < 16; ++r) __builtin_amdgcn_raw_buffer_store_b32((lane & 31) == 0 ? (r < 15 ? z[r + 1] : zt) : z[r], rws, zu, zs + (r & 7) * 32768, SC1);
        __builtin_amdgcn_raw_buffer_store_b64(u2{zn, tags.w}, rws, zh, WS_HB + b * 16, SC1);
    }
    STAMP(5);
    SB();
    {
        float hs[4], hu[4];
        UNROLL for (int i = 0; i < 4; ++i) { hs[i] = (float)(16 * n + 4 * wv + i) * (1.0f / 2048.0f); hu[i] = (float)(cls + 32 * q + 8 * i); }
        pin(hs); pin(hu);
        UNROLL for (int i = 0; i < 4; ++i) { UNROLL for (int j = 0; j < 4; ++j) { float a = hs[i] * hu[j]; pin(a); HR[i][j] = rot(a); } HI[i] = quarter(HR[i]); }
    }
    pin(HR); pin(HI);
    {
        const int d = n + cls - 8 * q;
        UNROLL for (int j = 0; j < 4; ++j) sel1[j] = (d >> 1) == j ? ((d & 1) ? 0xffff0000u : 0xffffu) : 0u;
        pin(sel1);
    }
    GATE_EARLY;
    STAMP(6);
    u32 z1[4][4], z2[4][4];
    const int zvm = q * 64 + (63 - tile) * 16384 + (15 - n) * 4;
    const int zv1 = cls <= 4 ? q * 64 + tile * 16384 + n * 4 : zvm, zso = WS_Z + row * 64 * 16384 + wv * 4096;
#define LOAD_Z() do { \
        if (packed) { int a_ = zvm; pin(a_); UNROLL for (int i = 0; i < 4; ++i) UNROLL for (int j = 0; j < 4; ++j) z2[i][j] = __builtin_amdgcn_raw_buffer_load_b32(rws, a_ + (4 * i + j) * 256, zso, SC1); } \
        else { UNROLL for (int i = 0; i < 4; ++i) UNROLL for (int j = 0; j < 4; ++j) z2[i][j] = tags.x; } \
        int c_ = zv1; pin(c_); UNROLL for (int i = 0; i < 4; ++i) UNROLL for (int j = 0; j < 4; ++j) z1[i][j] = __builtin_amdgcn_raw_buffer_load_b32(rws, c_ + (4 * i + j) * 256, zso, SC1); } while (0)
    if (ZGATE) lwait(lds, L_OK + 1, 1u, MSLP);
    VMWAIT(ZWAIT);
    LOAD_Z();
    if (wv == 0) lset(lds, L_ZL, 1u, lane);
    SB();
    u32 emaxg;
    {
        u4 scl[4];
        emaxg = lwait(lds, L_OK + 1, 1u, MSLP);
        UNROLL for (int j = 0; j < 4; ++j) scl[j] = *(const u4*)&lds[L_SC + 64 * q + 16 * j + 4 * wv];
        SB();
        UNROLL for (int i = 0; i < 4; ++i) UNROLL for (int j = 0; j < 4; ++j) { GI[i][j] = quarter(GR[i][j], scl[j][i]); GR[i][j] = hmul(GR[i][j], scl[j][i]); }
    }
    pin(GR); pin(GI);
    STAMP(7);
    u4 WA[4], WB[4];
#define WIDEN(ewt) do { \
        u4 wr[4][4]; \
        int wl = L_W / 4 + wv * 1024 + n * 4 + min(q, 2); pin(wl); \
        UNROLL for (int i = 0; i < 4; ++i) UNROLL for (int a = 0; a < 4; ++a) wr[i][a] = ((const u4*)lds)[wl + (i * 4 + a) * 64]; \
        const u32 sw = ((249u - (ewt) - (packed ? 1u : 0u)) << 7) * 0x10001u;       \
        const u4 sels = sel1 & sw; \
        SB(); \
        f4 wf[4][4]; \
        UNROLL for (int k = 0; k < 10; ++k) { \
            if (k < 8) { wf[k >> 1][2 * (k & 1)] = mfma16b(wr[k >> 1][2 * (k & 1)], sels); wf[k >> 1][2 * (k & 1) + 1] = mfma16b(wr[k >> 1][2 * (k & 1) + 1], sels); } \
            if (k >= 2) { const int i_ = (k - 2) >> 1, f_ = k & 1; UNROLL for (int r = 0; r < 4; ++r) (f_ ? WB : WA)[i_][r] = pkh(wf[i_][2 * f_][r], wf[i_][2 * f_ + 1][r]); } \
            SB(); } \
        pin(WA); pin(WB); } while (0)
    MEMBAR();
    const u2 ww0 = *(const u2*)&lds[L_WW];
    const bool wearly = rfl(min(ww0.x, ww0.y)) != 0u;
    MEMBAR();
    if (wearly) WIDEN(rfl(max(ww0.x, ww0.y)));
    u32 x1 = tags.x ^ (cls > 4 ? 0x80000000u : 0u), x2 = tags.x ^ (cls == 4 ? 0x00008000u : cls == 0 ? 0x80000000u : 0u);
    const int lw = ((wv * 16 + 4 * q) * 16 + n) * 4;
    int lr = (q * 16 + 4 * wv) * 16 + n;
    const u4* l4 = (const u4*)lds;
    pin(GR); pin(GI); pin(x1); pin(x2); pin(lr);
    STAMP(8);
    SB();
    {
        f4 yr[4], yi[4];
        NOUNROLL for (int spin = 0; spin < SPIN_CAP; ++spin) {
            u32 m = 0;
            UNROLL for (int i = 0; i < 4; ++i) {
                u4 d;
                UNROLL for (int j = 0; j < 4; ++j) { z1[i][j] ^= x1; z2[i][j] ^= x2; }
                UNROLL for (int j = 0; j < 4; ++j) d[j] = U(H(z1[i][j]) + H(z2[i][j]));
                UNROLL for (int j = 0; j < 4; ++j) m |= z1[i][j] | z2[i][j];
                yr[i] = mfma16(GR[i], d); yi[i] = mfma16(GI[i], d);
                SB();
            }
            if (__all((m & TM) == 0u)) break;
            MEMBAR();
            LOAD_Z();
        }
        STAMP(9);
        u4 y[4];
        UNROLL for (int i = 0; i < 4; ++i) UNROLL for (int r = 0; r < 4; ++r) y[r][i] = pkh(yr[i][r], yi[i][r]);
        UNROLL for (int r = 0; r < 4; ++r) *(u4*)&lds[L_A2 + lw + r * 64] = y[r];
        lset(lds, L_C3 + wv, 1u, lane);
    }
    if (!wearly) { const u32 ewt = lwait2(lds, L_WW); WIDEN(ewt); }
    {
        u4 d[4];
        NOUNROLL for (int spin = 0; spin < LSPIN_CAP; ++spin) {
            MEMBAR();
            const u4 c = *(const u4*)&lds[L_C3];
            MEMBAR();
            UNROLL for (int i = 0; i < 4; ++i) d[i] = l4[L_A2 / 4 + lr + i * 16];
            pin(d[0]);
            if (rfl(min4u(c))) break;
        }
        STAMP(10);
        pin(WA); pin(WB);
        f4 xr[4], xi[4];
        u4 xx[4], ya[4], yb[4];
        UNROLL for (int i = 0; i < 4; ++i) {
            xr[i] = mfma16(FR, d[i]); xi[i] = mfma16(FI, d[i]);
            if (i >= 2) { UNROLL for (int r = 0; r < 4; ++r) xx[i - 2][r] = pkh(xr[i - 2][r], xi[i - 2][r]); pin(xx[i - 2]); }
            SB();
        }
        UNROLL for (int r = 0; r < 4; ++r) ya[0][r] = cmul_swap(xx[0][r], WA[0][r]);
        UNROLL for (int i = 2; i < 4; ++i) { UNROLL for (int r = 0; r < 4; ++r) xx[i][r] = pkh(xr[i][r], xi[i][r]); pin(xx[i]); }
        SB();
        f4 p0[4], p1[4], p2[4], p3[4];
        UNROLL for (int i = 0; i < 4; ++i) {
            p0[i] = mfma16(FR, ya[i]); p1[i] = mfma16(FI, ya[i]);
            if (i < 3) { UNROLL for (int r = 0; r < 4; ++r) ya[i + 1][r] = cmul_swap(xx[i + 1][r], WA[i + 1][r]); }
            else { UNROLL for (int r = 0; r < 4; ++r) yb[0][r] = cmul_swap(xx[0][r], WB[0][r]); }
            SB();
        }
        u4 ra[4], rb[4];
        UNROLL for (int i = 0; i < 4; ++i) {
            p2[i] = mfma16(FR, yb[i]); p3[i] = mfma16(FI, yb[i]);
            if (i < 3) { UNROLL for (int r = 0; r < 4; ++r) yb[i + 1][r] = cmul_swap(xx[i + 1][r], WB[i + 1][r]); }
            UNROLL for (int k = 0; k < 4; ++k) ra[i][k] = pkh(p0[k][i], p1[k][i]);
            *(u4*)&lds[L_B + lw + i * 64] = ra[i];
            SB();
        }
        UNROLL for (int i = 0; i < 3; ++i) UNROLL for (int r = 0; r < 4; ++r) rb[r][i] = pkh(p2[i][r], p3[i][r]);
        SB();
        UNROLL for (int r = 0; r < 4; ++r) { rb[r][3] = pkh(p2[3][r], p3[3][r]); *(u4*)&lds[L_B + 4096 + lw + r * 64] = rb[r]; }
        lset(lds, L_C4 + wv, 1u, lane);
    }
    STAMP(11);
    {
        u4 da[4], db[4];
        NOUNROLL for (int spin = 0; spin < LSPIN_CAP; ++spin) {
            MEMBAR();
            const u4 c = *(const u4*)&lds[L_C4];
            MEMBAR();
            UNROLL for (int i = 0; i < 4; ++i) { da[i] = l4[L_B / 4 + lr + i * 16]; db[i] = l4[L_B / 4 + 1024 + lr + i * 16]; }
            pin(da[0]);
            if (rfl(min4u(c))) break;
        }
        const int vo = 64 * q * 32768 + n * 8, so = WS_V + 4 * wv * 32768 + cls * 4096 + tile * 128;
        f4 p0[4], p1[4], p2[4], p3[4];
        UNROLL for (int i = 0; i < 5; ++i) {
            if (i < 4) { p0[i] = mfma16(HR[i], da[i]); p1[i] = mfma16(HI[i], da[i]); p2[i] = mfma16(HR[i], db[i]); p3[i] = mfma16(HI[i], db[i]); }
            if (i > 0) { UNROLL for (int r = 0; r < 4; ++r)
                __builtin_amdgcn_raw_buffer_store_b64(u2{mark(pkh(p0[i - 1][r], p1[i - 1][r]), tags.x, tm), mark(pkh(p2[i - 1][r], p3[i - 1][r]), tags.x, tm)}, rws, vo, so + (16 * r + i - 1) * 32768, SC1); }
            SB();
        }
    }
    STAMP(12);
    SB();
    GATE_LATE;
    MR[0] = *(const u4*)&lds[L_M + lane * 4]; MR[1] = *(const u4*)&lds[L_M + 256 + lane * 4];
    UNROLL for (int i = 0; i < 4; ++i) TW[i] = *(const u4*)&lds[L_M + (2 + i) * 256 + lane * 4];
    core_expand(lane, MR, MI);
    pin(MR); pin(MI); pin(TW);
    u4 P0;
    {
        lwait4(lds, L_C5);
        float gg[4];
        UNROLL for (int k = 0; k < 2; ++k) {
            const f4* pp = (const f4*)&lds[L_GS + ((n >> 2) * 2 + k) * 16];
            const f4 ps = (pp[0] + pp[1]) + (pp[2] + pp[3]);
            const float lg = rbf((ps.x + ps.y) + (ps.z + ps.w));
            gg[2 * k] = rbf(1.0f / (1.0f + __expf(-lg)));
            gg[2 * k + 1] = rbf(1.0f - gg[2 * k]);
        }
        const int r = n & 3, sa = (n >> 2) * 2;
        UNROLL for (int cc = 0; cc < 2; ++cc) {
            const int u1 = 2 * q + cc;
            const float ta = (float)(sa * u1) * 0.125f + ((r & 1) ? 0.25f : (r & 2) ? 0.5f : 0.0f);
            const float tb = (float)((sa + 1) * u1) * 0.125f + ((r & 1) ? ((r & 2) ? 0.0f : 0.5f) : 0.25f);
            const float ca = cosr(ta), sa_ = sinr(ta), cb = cosr(tb), sb_ = sinr(tb);
            float e[4] = {gg[2] * cb, gg[2] * sb_, gg[3] * cb, gg[3] * sb_};
            pin(e);
            e[0] = fmaf(gg[0], ca, e[0]); pin(e[0]); e[1] = fmaf(gg[0], sa_, e[1]); pin(e[1]); e[2] = fmaf(gg[1], ca, e[2]); pin(e[2]); e[3] = fmaf(gg[1], sa_, e[3]);
            P0[2 * cc] = pkh(e[0], e[1]); P0[2 * cc + 1] = pkh(e[2], e[3]);
        }
    }
    pin(P0);
    const int ve = 2 * q * 4096 + (128 * wv + n) * 8;
    u2 v0[8], v1[8];
    u4 ve8, vo8;
    int vs0 = WS_V + b * 32768, vs1 = vs0 + 4096, vsl = WS_VL + b * 128;
    asm volatile("" : "+s"(vs0), "+s"(vs1), "+s"(vsl));
#define LOAD_V() do { int a_ = ve; pin(a_); \
        if (wv == 3) { \
            ve8 = __builtin_amdgcn_raw_buffer_load_b128(rws, q * 32, vsl, SC1); vo8 = __builtin_amdgcn_raw_buffer_load_b128(rws, q * 32 + 16, vsl, SC1); \
        } else { \
            const u2 e2 = __builtin_amdgcn_raw_buffer_load_b64(rws, a_ + 1024, vs0, SC1), o2 = __builtin_amdgcn_raw_buffer_load_b64(rws, a_ + 1024, vs1, SC1); \
            ve8 = u4{e2.x, e2.y, tags.w, tags.w}; vo8 = u4{o2.x, o2.y, tags.w, tags.w}; \
        } \
        UNROLL for (int i = 0; i < 8; ++i) { \
            v0[i] = __builtin_amdgcn_raw_buffer_load_b64(rws, a_ + 128 * i, vs0, SC1); \
            v1[i] = __builtin_amdgcn_raw_buffer_load_b64(rws, a_ + 128 * i, vs1, SC1); } \
        SB(); } while (0)
    if (VGATE) lwait(lds, L_OK + 3, 1u, MSLP);
    VMWAIT(VWAIT);
    LOAD_V();
    u4 PM[9];
    const u32 ewm = lwait(lds, L_OK + 3, 1u, MSLP);
    UNROLL for (int cc = 0; cc < 2; ++cc) UNROLL for (int i = 0; i < 9; ++i) {
        const u32 f = lds[L_TS + (2 * q + cc) * 36 + 8 * wv + i]; PM[i][2 * cc] = hmul(P0[2 * cc], f); PM[i][2 * cc + 1] = hmul(P0[2 * cc + 1], f);
    }
    const float osc = __builtin_bit_cast(float, (u32)min(max((int)emaxg + (int)ewm - 130, 1), 254) << 23) * TRUNC_FIX;
    pin(PM);
    STAMP(13);
    SB();
    {
        f4 z[9];
        NOUNROLL for (int spin = 0; spin < SPIN_CAP; ++spin) {
            u32 m = (((wv == 3 ? ve8.y : ve8.z) ^ tags.w) | (ve8.w ^ tags.w) | ((wv == 3 ? vo8.y : vo8.z) ^ tags.w) | (vo8.w ^ tags.w)) ? TM : 0u;
            UNROLL for (int k = 0; k < 9; ++k) {
                const int i = (k + 8) % 9;
                const u4 d = (i == 8 ? u4{ve8.x, wv == 3 ? ve8.z : ve8.y, vo8.x, wv == 3 ? vo8.z : vo8.y} : u4{v0[i & 7].x, v0[i & 7].y, v1[i & 7].x, v1[i & 7].y}) ^ tags.x;
                m |= d.x | d.y; m |= d.z | d.w;
                z[i] = mfma16(PM[i], d);
                SB();
            }
            if (__all((m & TM) == 0u)) break;
            MEMBAR();
            LOAD_V();
        }
        STAMP(14);
        if (wv == 0 && n == 0) { z[0].x = 0.5f * (z[0].x + z[0].z); z[0].y = 0.5f * (z[0].y + z[0].w); }
        if (wv == 3) { z[8].z = 0.5f * (z[8].x + z[8].z); z[8].w = 0.5f * (z[8].y + z[8].w); }
        u4* l4 = (u4*)&lds[L_A1];
        UNROLL for (int p = 0; p < 2; ++p)
            l4[(q * 8 + wv) * 32 + 16 * p + n] = u4{pkh(z[p].x, z[p + 2].x), pkh(z[p + 4].x, z[p + 6].x), pkh(z[p].y, z[p + 2].y), pkh(z[p + 4].y, z[p + 6].y)};
        l4[(q * 8 + 7 - wv) * 32 + 16 - n] = u4{pkh(z[7].z, z[5].z), pkh(z[3].z, z[1].z), pkh(z[7].w, z[5].w), pkh(z[3].w, z[1].w)};
        f2 m[4];
        UNROLL for (int i = 0; i < 4; ++i) m[i] = n == 0 ? f2{z[8 - 2 * i].z, z[8 - 2 * i].w} : f2{z[6 - 2 * i].z, z[6 - 2 * i].w};
        l4[(q * 8 + 7 - wv) * 32 + ((32 - n) & 31)] = u4{pkh(m[0].x, m[1].x), pkh(m[2].x, m[3].x), pkh(m[0].y, m[1].y), pkh(m[2].y, m[3].y)};
        lset(lds, L_C6 + wv, 1u, lane);
    }
    {
        u4 in[4];
        NOUNROLL for (int spin = 0; spin < LSPIN_CAP; ++spin) {
            MEMBAR();
            const u4 c = *(const u4*)&lds[L_C6];
            MEMBAR();
            UNROLL for (int s = 0; s < 4; ++s) in[s] = *(const u4*)&lds[L_A1 + (wv * 256 + s * 64 + lane) * 4];
            pin(in[0]);
            if (rfl(min4u(c))) break;
        }
        if (b == 0 && t == 0) __builtin_amdgcn_raw_buffer_store_b32(tags.z, rws, 0, WS_E, SC1);
        f16v zr, zi;
        core_fft<false>(in, MR, MI, TW, zr, zi);
        zr *= osc; zi *= osc;
        const rsrc_t ro = mkrs(out, 2048 * 2048);
        const int ov = (4 * perm32(lane & 31) + (lane >> 5)) * 16, os = (512 * wv + b) * 2048;
        UNROLL for (int hf = 0; hf < 2; ++hf) {
            u4 a, c;
            UNROLL for (int j = 0; j < 4; ++j) { a[j] = pkb(zi[8 * hf + 2 * j], zi[8 * hf + 2 * j + 1]); c[j] = pkb(zr[8 * hf + 2 * j], zr[8 * hf + 2 * j + 1]); }
            store128<OAUX>(a, ro, ov + os + 32 * hf);
            store128<OAUX>(c, ro, ov + os + 256 * 2048 + 32 * hf);
        }
    }
    STAMP(15);
    STAMP_OUT();
}

extern "C" void kernel_launch(void* const* d_in, const int* in_sizes, int n_in,
                              void* d_out, int out_size, void* d_ws, size_t ws_size,
                              hipStream_t stream) {
    gfnet_fused<<<256, 512, 0, stream>>>(d_in[0], d_in[6], d_in[1], d_in[2], d_in[4], d_in[5], (char*)d_ws, d_out);
}
```

```cpp
#define XAUX 0
#include <hip/hip_runtime.h>
#include <hip/hip_bf16.h>
#include <stdint.h>

typedef _Float16 h2 __attribute__((ext_vector_type(2)));
typedef _Float16 h8 __attribute__((ext_vector_type(8)));
typedef __bf16 b2 __attribute__((ext_vector_type(2)));
typedef __bf16 b8 __attribute__((ext_vector_type(8)));
typedef float f2 __attribute__((ext_vector_type(2)));
typedef float f4 __attribute__((ext_vector_type(4)));
typedef float f16v __attribute__((ext_vector_type(16)));
typedef short s4 __attribute__((ext_vector_type(4)));
typedef unsigned u32;
typedef u32 u2 __attribute__((ext_vector_type(2)));
typedef u32 u4 __attribute__((ext_vector_type(4)));
typedef unsigned short us2 __attribute__((ext_vector_type(2)));
#define DI __device__ __forceinline__
#define UNROLL _Pragma("unroll")
#define NOUNROLL _Pragma("unroll 1")
#define LAS __attribute__((address_space(3)))

#define W0AUX 0
#define W1AUX 0
#define VDELAY 0
#define ZDELAY 0
#define PSLEEP 8
#define WGATE 4
#define W0DELAY 2
#define W0WAIT 0
#define WWIN 4
#define ZWAIT 4
#define VWAIT 0
#define OAUX 2
#define MARGIN 2

constexpr u32 FSTR = 16;
constexpr u32 WS_Z = 0;
constexpr u32 WS_V = 4u << 20;
constexpr u32 WS_VL = 12u << 20;
constexpr u32 WS_HB = WS_VL + (32u << 10);
constexpr u32 WS_F = WS_HB + (32u << 10);
constexpr u32 WS_E = WS_F + 2048 * FSTR;
constexpr u32 WS_BYTES = WS_E + 4096;
constexpr size_t WS_META = 24u << 20;
constexpr int SC1 = 16;
constexpr int SPIN_CAP = 8192;
constexpr int LSPIN_CAP = 1 << 17;
constexpr int OOB = 0x7ffffff0;
constexpr float TRUNC_FIX = 1.00066f;
constexpr u32 TM = 0x00010001u;

template <class T> DI void pin(T& v) { asm volatile("" : "+v"(v)); }
template <class T, int N> DI void pin(T (&v)[N]) { UNROLL for (int i = 0; i < N; ++i) pin(v[i]); }
DI u32 opq(u32 v) { asm("" : "+v"(v)); return v; }
DI u32 pkh(float lo, float hi) { h2 r; r.x = (_Float16)lo; r.y = (_Float16)hi; return __builtin_bit_cast(u32, r); }
DI u32 pkb(float lo, float hi) { b2 r; r.x = (__bf16)lo; r.y = (__bf16)hi; return __builtin_bit_cast(u32, r); }
DI float bflo(u32 v) { return __builtin_bit_cast(float, v << 16); }
DI float bfhi(u32 v) { return __builtin_bit_cast(float, v & 0xffff0000u); }
DI float rbf(float v) { return (float)(__bf16)v; }
DI h2 H(u32 v) { return __builtin_bit_cast(h2, v); }
DI u32 U(h2 v) { return __builtin_bit_cast(u32, v); }
DI f16v mfma32(u4 a, u4 b, f16v c) { return __builtin_amdgcn_mfma_f32_32x32x16_f16(__builtin_bit_cast(h8, a), __builtin_bit_cast(h8, b), c, 0, 0, 0); }
DI f4 mfma16(u4 a, u4 b) { return __builtin_amdgcn_mfma_f32_16x16x32_f16(__builtin_bit_cast(h8, a), __builtin_bit_cast(h8, b), f4{}, 0, 0, 0); }
DI f4 mfma16b(u4 a, u4 b) { return __builtin_amdgcn_mfma_f32_16x16x32_bf16(__builtin_bit_cast(b8, a), __builtin_bit_cast(b8, b), f4{}, 0, 0, 0); }
DI u32 hmul(u32 a, u32 b) { return U(H(a) * H(b)); }
DI u32 hfma(u32 a, u32 b, u32 c) { return U(__builtin_elementwise_fma(H(a), H(b), H(c))); }
DI u32 cmul_swap(u32 x, u32 w) { const h2 a = H(opq(x)), c = H(opq(w)); h2 n = c; n.y = -n.y; return U(__builtin_elementwise_fma(a.yy, n, a.xx * c.yx)); }
DI u32 cmul_conj(u32 x, u32 t) { const h2 a = H(opq(x)), c = H(opq(t)); h2 n = c.yy; n.y = -n.y; return U(__builtin_elementwise_fma(a.yx, n, a * c.xx)); }
DI u32 pkmax(u32 a, u32 b) { return __builtin_bit_cast(u32, __builtin_elementwise_max(__builtin_bit_cast(us2, a), __builtin_bit_cast(us2, b))); }
DI float sinr(float r) { return __builtin_amdgcn_sinf(r); }
DI float cosr(float r) { return __builtin_amdgcn_cosf(r); }
DI int perm32(int n) { return (n & 0x13) | ((n & 4) << 1) | ((n & 8) >> 1); }
DI u32 max3u(u32 a, u32 b, u32 c) { return max(max(a, b), c); }
DI u32 max4u(u4 v) { return max(max(v.x, v.y), max(v.z, v.w)); }
DI u32 min4u(u4 v) { return min(min(v.x, v.y), min(v.z, v.w)); }
DI u32 mark(u32 v, u32 par, u32 tm) { return (par & tm) | (v & ~tm); }
DI u32 rfl(u32 v) { return __builtin_amdgcn_readfirstlane(v); }
DI u32 pow2h(int d) { const float f = __builtin_bit_cast(float, (u32)(127 - min(d, 64)) << 23); return pkh(f, f); }

typedef __amdgpu_buffer_rsrc_t rsrc_t;
DI rsrc_t mkrs(const void* p, u32 bytes) { return __builtin_amdgcn_make_buffer_rsrc((void*)p, (short)0, (int)bytes, 0x00020000); }
template <int AUX> DI void store128(u4 v, rsrc_t r, int off) { __builtin_amdgcn_raw_buffer_store_b128(v, r, off, 0, AUX); }
#define SB() __builtin_amdgcn_sched_barrier(0)
#define MEMBAR() asm volatile("" ::: "memory")
#define VMWAIT(n) asm volatile("s_waitcnt vmcnt(%0)" :: "n"(n) : "memory")
#define STAMP_DECL
#define STAMP(i)
#define STAMP_OUT()
template <int CTRL> DI u32 dpp(u32 v) { return __builtin_amdgcn_update_dpp(0u, v, CTRL, 0xf, 0xf, true); }
DI u32 wave_maxu(u32 v) {
    v = max(v, dpp<0xB1>(v)); v = max(v, dpp<0x4E>(v)); v = max(v, dpp<0x141>(v)); v = max(v, dpp<0x140>(v));
    return max(max((u32)__builtin_amdgcn_readlane(v, 0), (u32)__builtin_amdgcn_readlane(v, 16)), max((u32)__builtin_amdgcn_readlane(v, 32), (u32)__builtin_amdgcn_readlane(v, 48)));
}

DI void core_setup(int lane, int s, u4& tw, u2& m) {
    float fc = (float)perm32(lane & 31) * (1.0f / 1024.0f), p0 = (float)(4 * (lane >> 5) + 8 * s) * fc, p1 = (float)(4 * (lane >> 5) + 8 * (s >> 1) + 2 * (s & 1)) * fc * 32.0f;
    pin(fc); pin(p0); pin(p1);
    float tc[4], ts[4];
    UNROLL for (int i = 0; i < 4; ++i) { const float pt = fmaf((float)i, fc, p0); tc[i] = cosr(pt); ts[i] = sinr(pt); }
    tw = u4{pkh(tc[0], tc[1]), pkh(tc[2], tc[3]), pkh(ts[0], ts[1]), pkh(ts[2], ts[3])};
    const float p2 = fmaf(32.0f, fc, p1);
    m = u2{pkh(cosr(p1), cosr(p2)), pkh(sinr(p1), sinr(p2))};
}
DI void core_expand(int lane, u4 (&MR)[4], u4 (&MI)[4]) {
    const u32 sg = (lane & 1) ? 0x80008000u : 0u;
    MR[2] = MR[0] ^ sg; MR[3] = MR[1] ^ sg;
    UNROLL for (int s = 0; s < 4; ++s) MI[s] = u4{MR[s].z ^ 0x80008000u, MR[s].w ^ 0x80008000u, MR[s].x, MR[s].y};
}
template <bool OUT_B> DI void core_fft(const u4 (&in)[4], const u4 (&MR)[4], const u4 (&MI)[4], const u4 (&TW)[4], f16v& outr, f16v& outi) {
    f16v yr = {}, yi = {};
    UNROLL for (int s = 0; s < 4; ++s) yr = mfma32(in[s], MR[s], yr);
    SB();
    yi = mfma32(in[0], MI[0], yi); yi = mfma32(in[1], MI[1], yi);
    SB();
    h2 t0[8], t1[8];
#define TW_RE(p) do { const h2 pr_ = H(pkh(yr[2 * (p)], yr[2 * (p) + 1])); t0[p] = pr_ * H(TW[(p) >> 1][(p) & 1]); t1[p] = pr_ * H(TW[(p) >> 1][2 + ((p) & 1)]); } while (0)
    TW_RE(0); TW_RE(1); SB();
    yi = mfma32(in[2], MI[2], yi);
    TW_RE(2); TW_RE(3); SB();
    yi = mfma32(in[3], MI[3], yi);
    TW_RE(4); TW_RE(5); TW_RE(6); TW_RE(7); SB();
    outr = f16v{}; outi = f16v{};
    u4 d[4];
#define TW_IM(s) do { UNROLL for (int j = 0; j < 2; ++j) { const int p_ = 2 * (s) + j; const h2 pi_ = H(pkh(yi[2 * p_], yi[2 * p_ + 1])); \
        d[s][j] = U(__builtin_elementwise_fma(pi_, H(TW[s][2 + j]), t0[p_])); d[s][2 + j] = U(__builtin_elementwise_fma(pi_, H(TW[s][j]), -t1[p_])); } } while (0)
    TW_IM(0); SB();
    UNROLL for (int s = 0; s < 4; ++s) {
        outr = OUT_B ? mfma32(MR[s], d[s], outr) : mfma32(d[s], MR[s], outr);
        if (s < 3) TW_IM(s + 1);
        SB();
    }
    UNROLL for (int s = 0; s < 4; ++s) outi = OUT_B ? mfma32(MI[s], d[s], outi) : mfma32(d[s], MI[s], outi);
}
DI u32 rot(float ph) { return pkh(cosr(ph), sinr(ph)); }
DI u32 quarter(u32 r, u32 f) { u32 o; asm("v_pk_mul_f16 %0, %1, %2 op_sel:[1,0] op_sel_hi:[0,1] neg_lo:[1,0]" : "=v"(o) : "v"(r), "v"(f)); return o; }
DI u4 quarter(u4 r) { u32 one = 0x3c003c00u; pin(one); return u4{quarter(r.x, one), quarter(r.y, one), quarter(r.z, one), quarter(r.w, one)}; }

DI u32 tag_of(u32 epoch, u32 idx) { u32 h = (epoch ^ (idx * 0x9E3779B1u)) * 0x85EBCA6Bu; return h ^ (h >> 15); }
DI u32 parity_of(u32 epoch) { return (epoch & 1u) ? 0x00000001u : 0x00010000u; }
DI void flag_store(rsrc_t rws, u32 arr, int b, int lane, u32 payload, u32 tag) {
    __builtin_amdgcn_raw_buffer_store_b64(u2{payload, tag}, rws, lane < 8 ? lane * (int)(256 * FSTR) : OOB, arr + b * FSTR, SC1);
}
DI u4 flag_wait(rsrc_t rws, u32 arr, int b, int lane, u32 epoch) {
    u2 f[4]; u32 tg[4];
    UNROLL for (int j = 0; j < 4; ++j) tg[j] = tag_of(epoch, arr + 4 * lane + j);
    NOUNROLL for (int spin = 0; spin < SPIN_CAP; ++spin) {
        MEMBAR();
        UNROLL for (int j = 0; j < 4; ++j) f[j] = __builtin_amdgcn_raw_buffer_load_b64(rws, lane * (4 * FSTR) + j * FSTR, arr + (b & 7) * (256 * FSTR), SC1);
        if (__all(f[0].y == tg[0] && f[1].y == tg[1] && f[2].y == tg[2] && f[3].y == tg[3])) break;
        __builtin_amdgcn_s_sleep(PSLEEP);
    }
    return u4{f[0].x, f[1].x, f[2].x, f[3].x};
}
DI int cls_of(int b) { const int row = (b & 7) >> 1; return row == 0 ? 4 * (b >> 7) : ((b & 1) ? 8 - row : row); }
DI int tile_of(int b) { return (b & 6) == 0 ? 16 * (b & 1) + ((b >> 3) & 15) : b >> 3; }
DI bool is_helper(int b) { return tile_of(b) == 31; }

constexpr int L_C1 = 0, L_C3 = 4, L_C4 = 8, L_C5 = 12, L_C6 = 16;
constexpr int L_EW = 20;
constexpr int L_WW = 24;
constexpr int L_SW = 28;
constexpr int L_PR = 32;
constexpr int L_SX = 36;
constexpr int L_ZL = 37;
constexpr int L_VD = 38;
constexpr int L_GS = 64;
constexpr int L_M = 192;
constexpr int L_A1 = L_M + 1536;
constexpr int L_A2 = L_A1 + 4096;
constexpr int L_B = L_A2 + 4096;
constexpr int L_W = L_B + 8192;
constexpr int L_END = L_W + 64 * 256;

DI void lset(u32* lds, int idx, u32 v, int lane) { MEMBAR(); if (lane == 0) lds[idx] = v; MEMBAR(); }
DI u32 lget(u32* lds, int idx) { MEMBAR(); return rfl(lds[idx]); }
DI u32 lwait(u32* lds, int idx, u32 atleast) {
    u32 v = 0;
    NOUNROLL for (int spin = 0; spin < LSPIN_CAP; ++spin) { v = lget(lds, idx); if (v >= atleast) break; __builtin_amdgcn_s_sleep(1); }
    MEMBAR();
    return v;
}
DI u32 lwait4(u32* lds, int idx, u32 atleast) {
    u4 v;
    NOUNROLL for (int spin = 0; spin < LSPIN_CAP; ++spin) { MEMBAR(); v = *(const u4*)&lds[idx]; if (rfl(min4u(v)) >= atleast) break; __builtin_amdgcn_s_sleep(1); }
    MEMBAR();
    return rfl(max4u(v));
}
struct MagMax {
    h2 hi = {}, lo = {};
    DI void add(u32 a, u32 c) {
        hi = __builtin_elementwise_maximum(__builtin_elementwise_maximum(hi, H(a)), H(c));
        lo = __builtin_elementwise_minimum(__builtin_elementwise_minimum(lo, H(a)), H(c));
    }
    DI u32 exponent() const {
        const u32 mx = pkmax(U(hi) & 0x7fff7fffu, U(lo) & 0x7fff7fffu);
        return wave_maxu(max(mx & 0xffffu, mx >> 16)) >> 7;
    }
};
DI u32 clampe(u32 e) { return min(max(e, 8u), 240u); }

__global__ __launch_bounds__(512, 2) void gfnet_fused(const void* x, const void* gw, const void* war, const void* wai, const void* wbr, const void* wbi,
                                                      char* ws, void* out) {
    __shared__ __attribute__((aligned(16))) u32 lds[L_END];
    STAMP_DECL;
    STAMP(0);
    asm volatile("" :: "s"(x), "s"(gw), "s"(war), "s"(wai), "s"(wbr), "s"(wbi), "s"(ws), "s"(out));
    const int wv0 = __builtin_amdgcn_readfirstlane(threadIdx.x >> 6);
    const rsrc_t rws = mkrs(ws, WS_BYTES);

    if (wv0 >= 4) {
        const int wv = wv0, b = blockIdx.x, cls = cls_of(b), tile = tile_of(b);
        const int lane = threadIdx.x & 63, q = lane >> 4, n = lane & 15;
        const u32 ev = __builtin_amdgcn_raw_buffer_load_b32(rws, 0, WS_E, SC1);
        const rsrc_t rw = mkrs(wv == 4 ? war : wv == 5 ? wai : wv == 6 ? wbr : wbi, 2048 * 1026);
        const u4 smp = __builtin_amdgcn_raw_buffer_load_b128(rw, (lane >> 2) * (131328 + 64) + (lane & 3) * 16, 0, 0);
        if (wv == 4) lds[lane] = 0u;
        asm volatile("s_waitcnt lgkmcnt(0)" ::: "memory");
        __builtin_amdgcn_s_barrier();
        MEMBAR();
        const bool helper = wv == 7 && is_helper(b);
        u4 GR, GI, FR, FI, HR, HI, T1, T2;
        {
            const int wvo = (lane >> 2) * (128 * 1026) + min(lane & 3, 2) * 16, wso = (cls * 513 + 16 * tile - cls) * 2;
#define WLOAD(r) do { UNROLL for (int st = 0; st < 4; ++st) { const int c_ = 4 * st + ((tile + (r)) & 3); \
                __builtin_amdgcn_raw_ptr_buffer_load_lds(rw, (LAS void*)&lds[L_W + (c_ * 4 + wv - 4) * 256], 16, wvo, wso + c_ * 8208, 0, (r) == 0 ? W0AUX : W1AUX); } VMWAIT(WWIN); SB(); } while (0)
            __builtin_amdgcn_s_sleep(W0DELAY);
            WLOAD(0);
            if (helper) {
            UNROLL for (int j = 0; j < 4; ++j) {
                GR[j] = rot((float)((4 * q + j) * (cls + 8 * n)) * (1.0f / 128.0f) + (cls == 4 ? 0.25f : 0.0f));
                HR[j] = rot((float)(n * (cls + 8 * (4 * q + j))) * (1.0f / 128.0f));
                FR[j] = rot((float)(n * (4 * q + j)) * (1.0f / 16.0f));
                T1[j] = rot((float)((4 * q + j) * (cls + 8 * n)) * (1.0f / 2048.0f));
                T2[j] = rot((float)(n * (cls + 8 * (4 * q + j))) * (1.0f / 2048.0f));
            }
            GI = quarter(GR); HI = quarter(HR); FI = quarter(FR);
            pin(GR); pin(GI); pin(HR); pin(HI); pin(FR); pin(FI); pin(T1); pin(T2);
            }
            VMWAIT(W0WAIT);
            STAMP(1);
            if (WGATE > 1) WLOAD(1);
            if (WGATE > 2) WLOAD(2);
            if (WGATE > 3) WLOAD(3);
            if (WGATE < 4 && lget(lds, L_C1) != 0u) lwait(lds, L_ZL, 1u);
            if (WGATE <= 1) WLOAD(1);
            if (WGATE <= 2) WLOAD(2);
            if (WGATE <= 3) WLOAD(3);
            MagMax ms; ms.add(smp.x, smp.y); ms.add(smp.z, smp.w);
            const u32 es = clampe(ms.exponent() + MARGIN);
            VMWAIT(0);
            lset(lds, L_PR + wv - 4, es, lane);
            STAMP(2);
        }
        const u32 ep0 = rfl(ev) + 1u;
        if (wv == 5 || wv == 6) {
            lwait4(lds, L_PR, 1u);
            MagMax m;
            UNROLL for (int c = 0; c < 32; ++c) { const u4 v = *(const u4*)&lds[L_W + ((wv - 5) * 32 + c) * 256 + lane * 4]; m.add(v.x, v.y); m.add(v.z, v.w); }
            const u32 e = clampe(m.exponent());
            MEMBAR(); if (lane == 0) *(u2*)&lds[L_WW + 2 * (wv - 5)] = u2{e, e}; MEMBAR();
        }
        if (wv == 4) {
            const u32 ex = lwait4(lds, L_EW, 1u), ew = lwait4(lds, L_WW, 1u);
            flag_store(rws, WS_F, b, lane, ex | (ew << 8), tag_of(ep0, WS_F + b));
            lwait4(lds, L_C5, 1u);
            const u4 f = flag_wait(rws, WS_F, b, lane, ep0);
            const u32 gx = wave_maxu(max4u(f & 0xffu)), gwm = wave_maxu(max4u((f >> 8) & 0xffu));
            const bool redo = gx > lget(lds, L_SX) || gwm > lwait4(lds, L_PR, 1u);
            lset(lds, L_VD, gx | (gwm << 8) | (redo ? 0x20000u : 0x10000u), lane);
            STAMP(5);
        } else if (helper) {
            __builtin_amdgcn_s_setprio(2);
            u32 ew = lwait4(lds, L_PR, 1u);
            NOUNROLL for (u32 pass = 0; pass < 2; ++pass) {
                u4 WH[2];
                const float sw = __builtin_bit_cast(float, (249u - ew) << 23);
                UNROLL for (int r = 0; r < 4; ++r) UNROLL for (int f = 0; f < 2; ++f) {
                    const unsigned short* wp = (const unsigned short*)&lds[L_W + (n * 4 + 2 * f) * 256 + (4 * q + r) * 16] + 16 + cls;
                    float re = bflo(wp[0]) * sw, im = bflo(wp[512]) * sw;
                    pin(re);
                    WH[f][r] = pkh(re, im);
                }
                pin(WH);
                STAMP(3);
                lwait(lds, L_ZL, pass + 1u);
                const u32 tg = tag_of(ep0 + pass, 0u), par = parity_of(ep0 + pass);
                u4 hz;
                NOUNROLL for (int spin = 0; spin < SPIN_CAP; ++spin) {
                    MEMBAR();
                    u32 bad = 0;
                    UNROLL for (int j = 0; j < 4; ++j) { const u2 g = __builtin_amdgcn_raw_buffer_load_b64(rws, (64 * q + n) * 16 + j * 256, WS_HB + cls * 4096, SC1); hz[j] = g.x & ~TM; bad |= g.y ^ tg; }
                    if (__all(bad == 0u)) break;
                    __builtin_amdgcn_s_sleep(4);
                }
                STAMP(4);
                const u32 f16 = cls == 0 ? 0x00002c00u : cls == 4 ? 0x2c000000u : cls > 4 ? 0xac002c00u : 0x2c002c00u;
                u4 d, o[2];
                UNROLL for (int j = 0; j < 4; ++j) d[j] = hmul(hz[j], f16);
                f4 yr = mfma16(d, GR), yi = mfma16(d, GI);
                UNROLL for (int r = 0; r < 4; ++r) d[r] = cmul_conj(pkh(yr[r], yi[r]), T1[r]);
                const f4 xr = mfma16(FR, d), xi = mfma16(FI, d);
                UNROLL for (int f = 0; f < 2; ++f) {
                    UNROLL for (int r = 0; r < 4; ++r) d[r] = cmul_swap(pkh(xr[r], xi[r]), WH[f][r]);
                    yr = mfma16(d, FR); yi = mfma16(d, FI);
                    UNROLL for (int r = 0; r < 4; ++r) o[f][r] = cmul_conj(pkh(yr[r], yi[r]), T2[r]);
                }
                const f4 p0 = mfma16(HR, o[0]), p1 = mfma16(HI, o[0]), p2 = mfma16(HR, o[1]), p3 = mfma16(HI, o[1]);
                UNROLL for (int r = 0; r < 4; ++r)
                    store128<SC1>(u4{mark(pkh(p0[r], p1[r]), par, TM), tg, mark(pkh(p2[r], p3[r]), par, TM), tg}, rws, (64 * q + n) * 128 + WS_VL + 16 * r * 128 + cls * 16);
                STAMP(5);
                const u32 vd = lwait(lds, L_VD, 1u);
                if (!(vd & 0x20000u)) break;
                ew = (vd >> 8) & 0xffu;
            }
        }
        STAMP_OUT();
        return;
    }

    u32 vd = 0;
    NOUNROLL for (u32 pass = 0; pass < 2; ++pass) {
    int t = threadIdx.x, wv = wv0, b = blockIdx.x;
    pin(t);
    asm volatile("" : "+s"(wv));
    asm volatile("" : "+s"(b));
    const int lane = t & 63, q = lane >> 4, n = lane & 15;
    const u32 pv = pass + 1u;
    u2 xv[8];
    const rsrc_t rx = mkrs(x, 2048 * 2048);
    const u4 smp = __builtin_amdgcn_raw_buffer_load_b128(rx, (lane >> 2) * (262144 + 2048 + 128) + (lane & 3) * 16, 0, 0);
    UNROLL for (int s1 = 0; s1 < 8; ++s1) xv[s1] = __builtin_amdgcn_raw_buffer_load_b64(rx, t * 8, (256 * s1 + b) * 2048, XAUX);
    const u32 ev = __builtin_amdgcn_raw_buffer_load_b32(rws, 0, WS_E, SC1);
    const u2 gv = __builtin_amdgcn_raw_buffer_load_b64(mkrs(gw, 2048), t * 8, 0, 0);
    MEMBAR();
    SB();
    if (pass == 0) { __builtin_amdgcn_s_barrier(); MEMBAR(); __builtin_amdgcn_s_setprio(1); }
    u4 MR[4], MI[4], TW[4];
    {
        u4 tw; u2 m;
        core_setup(lane, wv, tw, m);
        *(u4*)&lds[L_M + (2 + wv) * 256 + lane * 4] = tw;
        lds[L_M + (wv >> 1) * 256 + lane * 4 + (wv & 1)] = m.x; lds[L_M + (wv >> 1) * 256 + lane * 4 + 2 + (wv & 1)] = m.y;
    }
    const int zc = perm32(lane & 31) + (lane >> 5) * 256, zcu = zc + ((lane & 31) == 0 ? 543 : 511);
    int zl = (zc >> 4) * 16384 + (zc & 15) * 4, zu = (zcu >> 4) * 16384 + (zcu & 15) * 4;
    const int zh0 = wv * 4096, zh1 = (wv == 0 ? 4 : 8 - wv) * 4096;
    int zh = lane == 0 ? zh0 : zh1; pin(zh); zh = lane < 2 ? zh : OOB;
    int tro = L_A1 + wv * 1024 + (4 * (lane >> 5) + ((lane & 15) >> 2)) * 16 + ((lane >> 4) & 1) * 8 + (lane & 3) * 2;
    u32 tm = TM;
    pin(zl); pin(zu); pin(zh); pin(tro); pin(tm);
    STAMP(1);
    SB();
    u32 ex;
    {
        MagMax ms; ms.add(smp.x, smp.y); ms.add(smp.z, smp.w);
        ex = pass ? (vd & 0xffu) : clampe(ms.exponent() + MARGIN);
    }
    STAMP(2);
    u4 tags;
    {
        const float sc = __builtin_bit_cast(float, (250u - ex) << 23);
        const float rs = sc * 0.70710678118654752f;
        u32 o[8][2];
        UNROLL for (int j = 0; j < 2; ++j) {
            f2 v[8];
            UNROLL for (int s1 = 0; s1 < 8; ++s1) v[s1] = f2{bflo(xv[s1][j]), bfhi(xv[s1][j])};
            const f2 a0 = v[0] + v[4], a1 = v[1] + v[5], a2 = v[2] + v[6], a3 = v[3] + v[7];
            const f2 b0 = (v[0] - v[4]) * sc, b1 = v[1] - v[5], b2_ = (v[2] - v[6]) * sc, b3 = v[3] - v[7];
            const f2 e0 = a0 + a2, e1 = (a1 + a3) * sc, dm = b1 - b3, dp = b1 + b3;
            f2 r[8];
            r[0] = e0 * sc + e1; r[1] = e0 * sc - e1;
            r[2] = dm * rs + b0; r[3] = -(dp * rs) - b2_;
            r[4] = (a0 - a2) * sc; r[5] = (a3 - a1) * sc;
            r[6] = b0 - dm * rs; r[7] = b2_ - dp * rs;
            UNROLL for (int p = 0; p < 8; ++p) o[p][j] = pkh(r[p].x, r[p].y);
        }
        UNROLL for (int p = 0; p < 8; ++p) *(u2*)&lds[L_A1 + p * 512 + 2 * t] = u2{o[p][0], o[p][1]};
        lset(lds, L_C1 + wv, pv, lane);
        const u32 epoch = rfl(ev) + pv;
        tags = u4{parity_of(epoch), 0u, epoch, tag_of(epoch, 0u)};
        u4 in[4];
        NOUNROLL for (int spin = 0; spin < LSPIN_CAP; ++spin) {
            MEMBAR();
            const u4 c = *(const u4*)&lds[L_C1];
            MEMBAR();
            MR[0] = *(const u4*)&lds[L_M + lane * 4]; MR[1] = *(const u4*)&lds[L_M + 256 + lane * 4];
            UNROLL for (int s = 0; s < 4; ++s) {
                const u2 re = __builtin_bit_cast(u2, __builtin_amdgcn_ds_read_tr16_b64_v4i16((LAS s4*)&lds[tro + s * 128]));
                const u2 im = __builtin_bit_cast(u2, __builtin_amdgcn_ds_read_tr16_b64_v4i16((LAS s4*)&lds[tro + s * 128 + 512]));
                in[s] = u4{re.x, re.y, im.x, im.y};
            }
            pin(MR[0]); pin(MR[1]);
            if (rfl(min4u(c)) >= pv) break;
        }
        UNROLL for (int i = 0; i < 4; ++i) TW[i] = *(const u4*)&lds[L_M + (2 + i) * 256 + lane * 4];
        STAMP(3);
        core_expand(lane, MR, MI);
        f16v zr, zi;
        core_fft<true>(in, MR, MI, TW, zr, zi);
        u32 z[16];
        UNROLL for (int r = 0; r < 16; ++r) z[r] = mark(pkh(zr[r], zi[r]), tags.x, tm);
        STAMP(4);
        const int zp = ((b >> 2) & 3) * 64 + (b & 3) * 16 + ((b >> 4) & 3) * 4 + (b >> 6);
        const int zs = WS_Z + wv * 64 * 16384 + zp * 64;
        UNROLL for (int r = 0; r < 8; ++r) __builtin_amdgcn_raw_buffer_store_b32(z[r], rws, zl, zs + r * 32768, SC1);
        const u32 zn = rfl(z[8]), za = __builtin_amdgcn_readlane(z[8], 32), zb = __builtin_amdgcn_readlane(z[0], 0);
        MEMBAR();
        const u32 zt = lane < 32 ? za : zb;
        UNROLL for (int r = 8; r < 16; ++r) __builtin_amdgcn_raw_buffer_store_b32((lane & 31) == 0 ? (r < 15 ? z[r + 1] : zt) : z[r], rws, zu, zs + (r & 7) * 32768, SC1);
        __builtin_amdgcn_raw_buffer_store_b64(u2{zn, tags.w}, rws, zh, WS_HB + b * 16, SC1);
    }
    STAMP(5);
    SB();
    const int row = (b & 7) >> 1, cls = cls_of(b), tile = tile_of(b);
    const bool packed = row == 0;
    u4 FR, FI, GR[4], GI[4], HR[4], HI[4], sel1;
    {
        const float ofs = cls == 4 ? 0.25f : 0.0f;
        float gu = (float)(cls + 8 * n) * (1.0f / 2048.0f), g0 = (float)(64 * q + 4 * wv) * gu + ofs;
        u32 s16 = 0x2c002c00u;
        pin(gu); pin(g0); pin(s16);
        UNROLL for (int i = 0; i < 4; ++i) UNROLL for (int j = 0; j < 4; ++j) { const u32 g = rot(fmaf((float)(16 * j + i), gu, g0)); GR[i][j] = hmul(g, s16); GI[i][j] = quarter(g, s16); }
    }
    pin(GR); pin(GI);
    {
        MagMax m;
        UNROLL for (int s1 = 0; s1 < 8; ++s1) m.add(xv[s1].x, xv[s1].y);
        if (wv == 0) lset(lds, L_SX, ex, lane);
        lset(lds, L_EW + wv, clampe(m.exponent()), lane);
    }
    STAMP(6);
    u32 z1[4][4], z2[4][4];
    const int zvm = q * 64 + (63 - tile) * 16384 + (15 - n) * 4;
    const int zv1 = cls <= 4 ? q * 64 + tile * 16384 + n * 4 : zvm;
    int zso = WS_Z + row * 64 * 16384 + wv * 4096;
    asm volatile("" : "+s"(zso));
#define LOAD_Z() do { \
        if (packed) { int a_ = zvm; pin(a_); UNROLL for (int i = 0; i < 4; ++i) UNROLL for (int j = 0; j < 4; ++j) z2[i][j] = __builtin_amdgcn_raw_buffer_load_b32(rws, a_ + (4 * i + j) * 256, zso, SC1); } \
        else { UNROLL for (int i = 0; i < 4; ++i) UNROLL for (int j = 0; j < 4; ++j) z2[i][j] = tags.x; } \
        int c_ = zv1; pin(c_); UNROLL for (int i = 0; i < 4; ++i) UNROLL for (int j = 0; j < 4; ++j) z1[i][j] = __builtin_amdgcn_raw_buffer_load_b32(rws, c_ + (4 * i + j) * 256, zso, SC1); } while (0)
    VMWAIT(ZWAIT);
    if (ZDELAY) __builtin_amdgcn_s_sleep(ZDELAY);
    LOAD_Z();
    if (wv == 0) lset(lds, L_ZL, pv, lane);
    SB();
    UNROLL for (int j = 0; j < 4; ++j) FR[j] = rot((float)(n * (4 * q + j)) * (1.0f / 16.0f));
    FI = quarter(FR);
    {
        float hs[4], hu[4];
        UNROLL for (int i = 0; i < 4; ++i) { hs[i] = (float)(16 * n + 4 * wv + i) * (1.0f / 2048.0f); hu[i] = (float)(cls + 32 * q + 8 * i); }
        pin(hs); pin(hu);
        UNROLL for (int i = 0; i < 4; ++i) { UNROLL for (int j = 0; j < 4; ++j) { float a = hs[i] * hu[j]; pin(a); HR[i][j] = rot(a); } HI[i] = quarter(HR[i]); }
        const int d = n + cls - 8 * q;
        UNROLL for (int j = 0; j < 4; ++j) sel1[j] = (d >> 1) == j ? ((d & 1) ? 0xffff0000u : 0xffffu) : 0u;
    }
    pin(FR); pin(FI); pin(HR); pin(HI); pin(sel1);
    STAMP(7);
    u4 WA[4], WB[4];
    u32 ew = 0;
    UNROLL for (int i = 0; i < 4; ++i) asm volatile("" : "=v"(WA[i]), "=v"(WB[i]));
#define WIDEN(es) do { \
        u4 wr[4][4]; \
        int wl = L_W / 4 + wv * 1024 + n * 4 + min(q, 2); pin(wl); \
        UNROLL for (int i = 0; i < 4; ++i) UNROLL for (int a = 0; a < 4; ++a) wr[i][a] = ((const u4*)lds)[wl + (i * 4 + a) * 64]; \
        ew = pass ? (vd >> 8) & 0xffu : (es); \
        const u32 sw = ((249u - ew - (packed ? 1u : 0u)) << 7) * 0x10001u;       \
        const u4 sels = sel1 & sw; \
        SB(); \
        f4 wf[4][4]; \
        UNROLL for (int k = 0; k < 10; ++k) { \
            if (k < 8) { wf[k >> 1][2 * (k & 1)] = mfma16b(wr[k >> 1][2 * (k & 1)], sels); wf[k >> 1][2 * (k & 1) + 1] = mfma16b(wr[k >> 1][2 * (k & 1) + 1], sels); } \
            if (k >= 2) { const int i_ = (k - 2) >> 1, f_ = k & 1; UNROLL for (int r = 0; r < 4; ++r) (f_ ? WB : WA)[i_][r] = pkh(wf[i_][2 * f_][r], wf[i_][2 * f_ + 1][r]); } \
            SB(); } \
        pin(WA); pin(WB); } while (0)
    MEMBAR();
    const u4 pr0 = *(const u4*)&lds[L_PR];
    const bool wearly = rfl(min4u(pr0)) != 0u;
    MEMBAR();
    if (wearly) WIDEN(rfl(max4u(pr0)));
    u32 x1 = tags.x ^ (cls > 4 ? 0x80000000u : 0u), x2 = tags.x ^ (cls == 4 ? 0x00008000u : cls == 0 ? 0x80000000u : 0u);
    const int lw = ((wv * 16 + 4 * q) * 16 + n) * 4;
    int lr = (q * 16 + 4 * wv) * 16 + n;
    const u4* l4 = (const u4*)lds;
    pin(GR); pin(GI); pin(x1); pin(x2); pin(lr);
    STAMP(8);
    SB();
    {
        f4 yr[4], yi[4];
        NOUNROLL for (int spin = 0; spin < SPIN_CAP; ++spin) {
            u32 m = 0;
            UNROLL for (int i = 0; i < 4; ++i) {
                u4 d;
                UNROLL for (int j = 0; j < 4; ++j) { z1[i][j] ^= x1; z2[i][j] ^= x2; }
                UNROLL for (int j = 0; j < 4; ++j) d[j] = U(H(z1[i][j]) + H(z2[i][j]));
                UNROLL for (int j = 0; j < 4; ++j) m |= z1[i][j] | z2[i][j];
                yr[i] = mfma16(GR[i], d); yi[i] = mfma16(GI[i], d);
                SB();
            }
            if (__all((m & TM) == 0u)) break;
            MEMBAR();
            LOAD_Z();
        }
        STAMP(9);
        u4 y[4];
        UNROLL for (int i = 0; i < 4; ++i) UNROLL for (int r = 0; r < 4; ++r) y[r][i] = pkh(yr[i][r], yi[i][r]);
        UNROLL for (int r = 0; r < 4; ++r) *(u4*)&lds[L_A2 + lw + r * 64] = y[r];
        lset(lds, L_C3 + wv, pv, lane);
    }
    if (!wearly) { const u32 es = lwait4(lds, L_PR, 1u); WIDEN(es); }
    {
        u4 d[4];
        NOUNROLL for (int spin = 0; spin < LSPIN_CAP; ++spin) {
            MEMBAR();
            const u4 c = *(const u4*)&lds[L_C3];
            MEMBAR();
            UNROLL for (int i = 0; i < 4; ++i) d[i] = l4[L_A2 / 4 + lr + i * 16];
            pin(d[0]);
            if (rfl(min4u(c)) >= pv) break;
        }
        STAMP(10);
        pin(WA); pin(WB);
        f4 xr[4], xi[4];
        u4 xx[4], ya[4], yb[4];
        UNROLL for (int i = 0; i < 4; ++i) {
            xr[i] = mfma16(FR, d[i]); xi[i] = mfma16(FI, d[i]);
            if (i >= 2) { UNROLL for (int r = 0; r < 4; ++r) xx[i - 2][r] = pkh(xr[i - 2][r], xi[i - 2][r]); pin(xx[i - 2]); }
            SB();
        }
        UNROLL for (int r = 0; r < 4; ++r) ya[0][r] = cmul_swap(xx[0][r], WA[0][r]);
        UNROLL for (int i = 2; i < 4; ++i) { UNROLL for (int r = 0; r < 4; ++r) xx[i][r] = pkh(xr[i][r], xi[i][r]); pin(xx[i]); }
        SB();
        f4 p0[4], p1[4], p2[4], p3[4];
        UNROLL for (int i = 0; i < 4; ++i) {
            p0[i] = mfma16(FR, ya[i]); p1[i] = mfma16(FI, ya[i]);
            if (i < 3) { UNROLL for (int r = 0; r < 4; ++r) ya[i + 1][r] = cmul_swap(xx[i + 1][r], WA[i + 1][r]); }
            else { UNROLL for (int r = 0; r < 4; ++r) yb[0][r] = cmul_swap(xx[0][r], WB[0][r]); }
            SB();
        }
        u4 ra[4], rb[4];
        UNROLL for (int i = 0; i < 4; ++i) {
            p2[i] = mfma16(FR, yb[i]); p3[i] = mfma16(FI, yb[i]);
            if (i < 3) { UNROLL for (int r = 0; r < 4; ++r) yb[i + 1][r] = cmul_swap(xx[i + 1][r], WB[i + 1][r]); }
            UNROLL for (int k = 0; k < 4; ++k) ra[i][k] = pkh(p0[k][i], p1[k][i]);
            *(u4*)&lds[L_B + lw + i * 64] = ra[i];
            SB();
        }
        UNROLL for (int i = 0; i < 3; ++i) UNROLL for (int r = 0; r < 4; ++r) rb[r][i] = pkh(p2[i][r], p3[i][r]);
        SB();
        UNROLL for (int r = 0; r < 4; ++r) { rb[r][3] = pkh(p2[3][r], p3[3][r]); *(u4*)&lds[L_B + 4096 + lw + r * 64] = rb[r]; }
        lset(lds, L_C4 + wv, pv, lane);
    }
    STAMP(11);
    {
        u4 da[4], db[4];
        NOUNROLL for (int spin = 0; spin < LSPIN_CAP; ++spin) {
            MEMBAR();
            const u4 c = *(const u4*)&lds[L_C4];
            MEMBAR();
            UNROLL for (int i = 0; i < 4; ++i) { da[i] = l4[L_B / 4 + lr + i * 16]; db[i] = l4[L_B / 4 + 1024 + lr + i * 16]; }
            pin(da[0]);
            if (rfl(min4u(c)) >= pv) break;
        }
        const int vo = 64 * q * 32768 + n * 8, so = WS_V + 4 * wv * 32768 + cls * 4096 + tile * 128;
        f4 p0[4], p1[4], p2[4], p3[4];
        UNROLL for (int i = 0; i < 5; ++i) {
            if (i < 4) { p0[i] = mfma16(HR[i], da[i]); p1[i] = mfma16(HI[i], da[i]); p2[i] = mfma16(HR[i], db[i]); p3[i] = mfma16(HI[i], db[i]); }
            if (i > 0) { UNROLL for (int r = 0; r < 4; ++r)
                __builtin_amdgcn_raw_buffer_store_b64(u2{mark(pkh(p0[i - 1][r], p1[i - 1][r]), tags.x, tm), mark(pkh(p2[i - 1][r], p3[i - 1][r]), tags.x, tm)}, rws, vo, so + (16 * r + i - 1) * 32768, SC1); }
            SB();
        }
    }
    STAMP(12);
    SB();
    {
        pin(xv);
        const f2 g01 = {bflo(gv.x), bfhi(gv.x)}, g23 = {bflo(gv.y), bfhi(gv.y)};
        f4 acc = {};
        UNROLL for (int i = 0; i < 4; ++i) {
            u4 pp;
            UNROLL for (int k = 0; k < 2; ++k) {
                const f2 a = f2{bflo(xv[2 * i + k].x), bfhi(xv[2 * i + k].x)} * g01, c = f2{bflo(xv[2 * i + k].y), bfhi(xv[2 * i + k].y)} * g23;
                pp[2 * k] = pkb(a.x, a.y); pp[2 * k + 1] = pkb(c.x, c.y);
            }
            const u32 s0 = n == 2 * i ? 0x3f803f80u : 0u, s1 = n == 2 * i + 1 ? 0x3f803f80u : 0u;
            acc = __builtin_amdgcn_mfma_f32_16x16x32_bf16(__builtin_bit_cast(b8, pp), __builtin_bit_cast(b8, u4{s0, s0, s1, s1}), acc, 0, 0, 0);
        }
        if (n < 8) lds[L_GS + n * 16 + (t >> 4)] = __builtin_bit_cast(u32, (acc.x + acc.y) + (acc.z + acc.w));
        lset(lds, L_C5 + wv, pv, lane);
    }
    MR[0] = *(const u4*)&lds[L_M + lane * 4]; MR[1] = *(const u4*)&lds[L_M + 256 + lane * 4];
    UNROLL for (int i = 0; i < 4; ++i) TW[i] = *(const u4*)&lds[L_M + (2 + i) * 256 + lane * 4];
    core_expand(lane, MR, MI);
    pin(MR); pin(MI); pin(TW);
    u4 PM;
    {
        lwait4(lds, L_C5, pv);
        float gg[4];
        UNROLL for (int k = 0; k < 2; ++k) {
            const f4* pp = (const f4*)&lds[L_GS + ((n >> 2) * 2 + k) * 16];
            const f4 ps = (pp[0] + pp[1]) + (pp[2] + pp[3]);
            const float lg = rbf((ps.x + ps.y) + (ps.z + ps.w));
            gg[2 * k] = rbf(1.0f / (1.0f + __expf(-lg)));
            gg[2 * k + 1] = rbf(1.0f - gg[2 * k]);
        }
        UNROLL for (int k = 0; k < 4; ++k) { gg[k] *= 0.03125f; pin(gg[k]); }
        const int r = n & 3, sa = (n >> 2) * 2;
        UNROLL for (int cc = 0; cc < 2; ++cc) {
            const int u1 = 2 * q + cc;
            const float ta = (float)(sa * u1) * 0.125f + ((r & 1) ? 0.25f : (r & 2) ? 0.5f : 0.0f);
            const float tb = (float)((sa + 1) * u1) * 0.125f + ((r & 1) ? ((r & 2) ? 0.0f : 0.5f) : 0.25f);
            const float ca = cosr(ta), sa_ = sinr(ta), cb = cosr(tb), sb_ = sinr(tb);
            float e[4] = {gg[2] * cb, gg[2] * sb_, gg[3] * cb, gg[3] * sb_};
            pin(e);
            e[0] = fmaf(gg[0], ca, e[0]); pin(e[0]); e[1] = fmaf(gg[0], sa_, e[1]); pin(e[1]); e[2] = fmaf(gg[1], ca, e[2]); pin(e[2]); e[3] = fmaf(gg[1], sa_, e[3]);
            PM[2 * cc] = pkh(e[0], e[1]); PM[2 * cc + 1] = pkh(e[2], e[3]);
        }
    }
    const float osc = __builtin_bit_cast(float, (u32)min(max((int)ex + (int)ew - 130, 1), 254) << 23) * TRUNC_FIX;
    const int ve = 2 * q * 4096 + (128 * wv + n) * 8;
    int vs0 = WS_V + b * 32768, vs1 = vs0 + 4096, vsl = WS_VL + b * 128;
    asm volatile("" : "+s"(vs0), "+s"(vs1), "+s"(vsl));
    int ov = (4 * perm32(lane & 31) + (lane >> 5)) * 16 + (512 * wv + b) * 2048;
    pin(PM); pin(ov);
    STAMP(13);
    VMWAIT(VWAIT);
    if (VDELAY) __builtin_amdgcn_s_sleep(VDELAY);
    {
        f4 z[9];
        NOUNROLL for (int spin = 0; spin < SPIN_CAP; ++spin) {
            MEMBAR();
            u2 v0[8], v1[8];
            u4 ve8, vo8;
            int a_ = ve; pin(a_);
            if (wv == 3) {
                ve8 = __builtin_amdgcn_raw_buffer_load_b128(rws, q * 32, vsl, SC1); vo8 = __builtin_amdgcn_raw_buffer_load_b128(rws, q * 32 + 16, vsl, SC1);
            } else {
                const u2 e2 = __builtin_amdgcn_raw_buffer_load_b64(rws, a_ + 1024, vs0, SC1), o2 = __builtin_amdgcn_raw_buffer_load_b64(rws, a_ + 1024, vs1, SC1);
                ve8 = u4{e2.x, e2.y, tags.w, tags.w}; vo8 = u4{o2.x, o2.y, tags.w, tags.w};
            }
            UNROLL for (int i = 0; i < 8; ++i) {
                v0[i] = __builtin_amdgcn_raw_buffer_load_b64(rws, a_ + 128 * i, vs0, SC1);
                v1[i] = __builtin_amdgcn_raw_buffer_load_b64(rws, a_ + 128 * i, vs1, SC1);
            }
            SB();
            u32 m = (((wv == 3 ? ve8.y : ve8.z) ^ tags.w) | (ve8.w ^ tags.w) | ((wv == 3 ? vo8.y : vo8.z) ^ tags.w) | (vo8.w ^ tags.w)) ? TM : 0u;
            UNROLL for (int k = 0; k < 9; ++k) {
                const int i = (k + 8) % 9;
                const u4 d = (i == 8 ? u4{ve8.x, wv == 3 ? ve8.z : ve8.y, vo8.x, wv == 3 ? vo8.z : vo8.y} : u4{v0[i & 7].x, v0[i & 7].y, v1[i & 7].x, v1[i & 7].y}) ^ tags.x;
                m |= d.x | d.y; m |= d.z | d.w;
                z[i] = mfma16(PM, d);
                SB();
            }
            if (__all((m & TM) == 0u)) break;
        }
        STAMP(14);
        if (wv == 0 && n == 0) { z[0].x = 0.5f * (z[0].x + z[0].z); z[0].y = 0.5f * (z[0].y + z[0].w); }
        if (wv == 3) { z[8].z = 0.5f * (z[8].x + z[8].z); z[8].w = 0.5f * (z[8].y + z[8].w); }
        u4* l4w = (u4*)&lds[L_A1];
        UNROLL for (int p = 0; p < 2; ++p)
            l4w[(q * 8 + wv) * 32 + 16 * p + n] = u4{pkh(z[p].x, z[p + 2].x), pkh(z[p + 4].x, z[p + 6].x), pkh(z[p].y, z[p + 2].y), pkh(z[p + 4].y, z[p + 6].y)};
        l4w[(q * 8 + 7 - wv) * 32 + 16 - n] = u4{pkh(z[7].z, z[5].z), pkh(z[3].z, z[1].z), pkh(z[7].w, z[5].w), pkh(z[3].w, z[1].w)};
        f2 m[4];
        UNROLL for (int i = 0; i < 4; ++i) m[i] = n == 0 ? f2{z[8 - 2 * i].z, z[8 - 2 * i].w} : f2{z[6 - 2 * i].z, z[6 - 2 * i].w};
        l4w[(q * 8 + 7 - wv) * 32 + ((32 - n) & 31)] = u4{pkh(m[0].x, m[1].x), pkh(m[2].x, m[3].x), pkh(m[0].y, m[1].y), pkh(m[2].y, m[3].y)};
        lset(lds, L_C6 + wv, 2u * pv - 1u, lane);
    }
    {
        u4 in[4];
        NOUNROLL for (int spin = 0; spin < LSPIN_CAP; ++spin) {
            MEMBAR();
            const u4 c = *(const u4*)&lds[L_C6];
            MEMBAR();
            UNROLL for (int s = 0; s < 4; ++s) in[s] = *(const u4*)&lds[L_A1 + (wv * 256 + s * 64 + lane) * 4];
            pin(in[0]);
            if (rfl(min4u(c)) >= 2u * pv - 1u) break;
        }
        f16v zr, zi;
        core_fft<false>(in, MR, MI, TW, zr, zi);
        zr *= osc; zi *= osc;
        const rsrc_t ro = mkrs(out, 2048 * 2048);
        UNROLL for (int hf = 0; hf < 2; ++hf) {
            u4 c;
            UNROLL for (int j = 0; j < 4; ++j) c[j] = pkb(zr[8 * hf + 2 * j], zr[8 * hf + 2 * j + 1]);
            store128<OAUX>(c, ro, ov + 256 * 2048 + 32 * hf);
        }
        UNROLL for (int hf = 0; hf < 2; ++hf) {
            u4 a;
            UNROLL for (int j = 0; j < 4; ++j) a[j] = pkb(zi[8 * hf + 2 * j], zi[8 * hf + 2 * j + 1]);
            store128<OAUX>(a, ro, ov + 32 * hf);
        }
    }
    STAMP(15);
    vd = lwait(lds, L_VD, 1u);
    if (pass == 1 || !(vd & 0x20000u)) {
        if (b == 0 && t == 0) __builtin_amdgcn_raw_buffer_store_b32(tags.z, rws, 0, WS_E, SC1);
        break;
    }
    lset(lds, L_C6 + wv, 2u, lane);
    lwait4(lds, L_C6, 2u);
    VMWAIT(0);
    }
    STAMP_OUT();
}

extern "C" void kernel_launch(void* const* d_in, const int* in_sizes, int n_in,
                              void* d_out, int out_size, void* d_ws, size_t ws_size,
                              hipStream_t stream) {
    gfnet_fused<<<256, 512, 0, stream>>>(d_in[0], d_in[6], d_in[1], d_in[2], d_in[4], d_in[5], (char*)d_ws, d_out);
}
```

```cpp
#include <hip/hip_runtime.h>
#include <hip/hip_bf16.h>
#include <stdint.h>

typedef _Float16 h2 __attribute__((ext_vector_type(2)));
typedef _Float16 h8 __attribute__((ext_vector_type(8)));
typedef __bf16 b2 __attribute__((ext_vector_type(2)));
typedef __bf16 b8 __attribute__((ext_vector_type(8)));
typedef float f2 __attribute__((ext_vector_type(2)));
typedef float f4 __attribute__((ext_vector_type(4)));
typedef float f16v __attribute__((ext_vector_type(16)));
typedef short s4 __attribute__((ext_vector_type(4)));
typedef unsigned u32;
typedef u32 u2 __attribute__((ext_vector_type(2)));
typedef u32 u4 __attribute__((ext_vector_type(4)));
typedef unsigned short us2 __attribute__((ext_vector_type(2)));
#define DI __device__ __forceinline__
#define UNROLL _Pragma("unroll")
#define NOUNROLL _Pragma("unroll 1")
#define LAS __attribute__((address_space(3)))

#define W0AUX 0
#define W1AUX 0
#define VDELAY 0
#define ZDELAY 0
#define WFIRST 0
#define VSTAG 0
#define GEARLY 0
#define ZGATE 0
#define VGATE 0
#define G2DELAY 50
#define MAXLATE 0
#define FLATE 0
#define FDELAY 24
#define PSLEEP 8
#define WGATE 4
#define W0DELAY 2
#define W0WAIT 0
#define WWIN 4
#define ZWAIT 4
#define VWAIT 0
#define XAUX 2
#define OAUX 2
#define MARGIN 2

#define FSTRIDE 16
constexpr u32 FSTR = FSTRIDE;
constexpr u32 WS_Z = 0;
constexpr u32 WS_V = 4u << 20;
constexpr u32 WS_VL = 12u << 20;
#define HSTR 32
constexpr u32 WS_HB = WS_VL + (32u << 10);
constexpr u32 WS_F = WS_HB + (128u << 10);
constexpr u32 WS_G1 = WS_F + 2048 * FSTR;
constexpr u32 WS_G2 = WS_G1 + 2048 * FSTR;
constexpr u32 WS_E = WS_G2 + 2048 * FSTR;
constexpr u32 WS_BYTES = WS_E + 4096;
constexpr size_t WS_META = 24u << 20;
constexpr int SC1 = 16;
constexpr int SPIN_CAP = 8192;
constexpr int LSPIN_CAP = 1 << 17;
constexpr int OOB = 0x7ffffff0;
constexpr float TRUNC_FIX = 1.00066f;
constexpr u32 TM = 0x00010001u;

template <class T> DI void pin(T& v) { asm volatile("" : "+v"(v)); }
template <class T, int N> DI void pin(T (&v)[N]) { UNROLL for (int i = 0; i < N; ++i) pin(v[i]); }
DI u32 opq(u32 v) { asm("" : "+v"(v)); return v; }
DI u32 pkh(float lo, float hi) { h2 r; r.x = (_Float16)lo; r.y = (_Float16)hi; return __builtin_bit_cast(u32, r); }
DI u32 pkb(float lo, float hi) { b2 r; r.x = (__bf16)lo; r.y = (__bf16)hi; return __builtin_bit_cast(u32, r); }
DI float bflo(u32 v) { return __builtin_bit_cast(float, v << 16); }
DI float bfhi(u32 v) { return __builtin_bit_cast(float, v & 0xffff0000u); }
DI float rbf(float v) { return (float)(__bf16)v; }
DI h2 H(u32 v) { return __builtin_bit_cast(h2, v); }
DI u32 U(h2 v) { return __builtin_bit_cast(u32, v); }
DI f16v mfma32(u4 a, u4 b, f16v c) { return __builtin_amdgcn_mfma_f32_32x32x16_f16(__builtin_bit_cast(h8, a), __builtin_bit_cast(h8, b), c, 0, 0, 0); }
DI f4 mfma16(u4 a, u4 b) { return __builtin_amdgcn_mfma_f32_16x16x32_f16(__builtin_bit_cast(h8, a), __builtin_bit_cast(h8, b), f4{}, 0, 0, 0); }
DI f4 mfma16b(u4 a, u4 b) { return __builtin_amdgcn_mfma_f32_16x16x32_bf16(__builtin_bit_cast(b8, a), __builtin_bit_cast(b8, b), f4{}, 0, 0, 0); }
DI u32 hmul(u32 a, u32 b) { return U(H(a) * H(b)); }
DI u32 hfma(u32 a, u32 b, u32 c) { return U(__builtin_elementwise_fma(H(a), H(b), H(c))); }
DI u32 cmul_swap(u32 x, u32 w) { const h2 a = H(opq(x)), c = H(opq(w)); h2 n = c; n.y = -n.y; return U(__builtin_elementwise_fma(a.yy, n, a.xx * c.yx)); }
DI u32 cmul_conj(u32 x, u32 t) { const h2 a = H(opq(x)), c = H(opq(t)); h2 n = c.yy; n.y = -n.y; return U(__builtin_elementwise_fma(a.yx, n, a * c.xx)); }
DI u32 pkmax(u32 a, u32 b) { return __builtin_bit_cast(u32, __builtin_elementwise_max(__builtin_bit_cast(us2, a), __builtin_bit_cast(us2, b))); }
DI float sinr(float r) { return __builtin_amdgcn_sinf(r); }
DI float cosr(float r) { return __builtin_amdgcn_cosf(r); }
DI int perm32(int n) { return (n & 0x13) | ((n & 4) << 1) | ((n & 8) >> 1); }
DI u32 max3u(u32 a, u32 b, u32 c) { return max(max(a, b), c); }
DI u32 max4u(u4 v) { return max(max(v.x, v.y), max(v.z, v.w)); }
DI u32 min4u(u4 v) { return min(min(v.x, v.y), min(v.z, v.w)); }
DI u32 mark(u32 v, u32 par, u32 tm) { return (par & tm) | (v & ~tm); }
DI u32 rfl(u32 v) { return __builtin_amdgcn_readfirstlane(v); }
DI u32 pow2h(int d) { const float f = __builtin_bit_cast(float, (u32)(127 - min(d, 64)) << 23); return pkh(f, f); }

typedef __amdgpu_buffer_rsrc_t rsrc_t;
DI rsrc_t mkrs(const void* p, u32 bytes) { return __builtin_amdgcn_make_buffer_rsrc((void*)p, (short)0, (int)bytes, 0x00020000); }
template <int AUX> DI void store128(u4 v, rsrc_t r, int off) { __builtin_amdgcn_raw_buffer_store_b128(v, r, off, 0, AUX); }
#define SB() __builtin_amdgcn_sched_barrier(0)
#define MEMBAR() asm volatile("" ::: "memory")
#define VMWAIT(n) asm volatile("s_waitcnt vmcnt(%0)" :: "n"(n) : "memory")
#define STAMP_DECL
#define STAMP(i)
#define STAMP_OUT()
template <int CTRL> DI u32 dpp(u32 v) { return __builtin_amdgcn_update_dpp(0u, v, CTRL, 0xf, 0xf, true); }
DI u32 wave_maxu(u32 v) {
    v = max(v, dpp<0xB1>(v)); v = max(v, dpp<0x4E>(v)); v = max(v, dpp<0x141>(v)); v = max(v, dpp<0x140>(v));
    return max(max((u32)__builtin_amdgcn_readlane(v, 0), (u32)__builtin_amdgcn_readlane(v, 16)), max((u32)__builtin_amdgcn_readlane(v, 32), (u32)__builtin_amdgcn_readlane(v, 48)));
}

DI void core_setup(int lane, int s, u4& tw, u2& m) {
    float fc = (float)perm32(lane & 31) * (1.0f / 1024.0f), p0 = (float)(4 * (lane >> 5) + 8 * s) * fc, p1 = (float)(4 * (lane >> 5) + 8 * (s >> 1) + 2 * (s & 1)) * fc * 32.0f;
    pin(fc); pin(p0); pin(p1);
    float tc[4], ts[4];
    UNROLL for (int i = 0; i < 4; ++i) { const float pt = fmaf((float)i, fc, p0); tc[i] = cosr(pt); ts[i] = sinr(pt); }
    tw = u4{pkh(tc[0], tc[1]), pkh(tc[2], tc[3]), pkh(ts[0], ts[1]), pkh(ts[2], ts[3])};
    const float p2 = fmaf(32.0f, fc, p1);
    m = u2{pkh(cosr(p1), cosr(p2)), pkh(sinr(p1), sinr(p2))};
}
DI void core_expand(int lane, u4 (&MR)[4], u4 (&MI)[4]) {
    const u32 sg = (lane & 1) ? 0x80008000u : 0u;
    MR[2] = MR[0] ^ sg; MR[3] = MR[1] ^ sg;
    UNROLL for (int s = 0; s < 4; ++s) MI[s] = u4{MR[s].z ^ 0x80008000u, MR[s].w ^ 0x80008000u, MR[s].x, MR[s].y};
}
template <bool OUT_B> DI void core_fft(const u4 (&in)[4], const u4 (&MR)[4], const u4 (&MI)[4], const u4 (&TW)[4], f16v& outr, f16v& outi) {
    f16v yr = {}, yi = {};
    UNROLL for (int s = 0; s < 4; ++s) yr = mfma32(in[s], MR[s], yr);
    SB();
    yi = mfma32(in[0], MI[0], yi); yi = mfma32(in[1], MI[1], yi);
    SB();
    h2 t0[8], t1[8];
#define TW_RE(p) do { const h2 pr_ = H(pkh(yr[2 * (p)], yr[2 * (p) + 1])); t0[p] = pr_ * H(TW[(p) >> 1][(p) & 1]); t1[p] = pr_ * H(TW[(p) >> 1][2 + ((p) & 1)]); } while (0)
    TW_RE(0); TW_RE(1); SB();
    yi = mfma32(in[2], MI[2], yi);
    TW_RE(2); TW_RE(3); SB();
    yi = mfma32(in[3], MI[3], yi);
    TW_RE(4); TW_RE(5); TW_RE(6); TW_RE(7); SB();
    outr = f16v{}; outi = f16v{};
    u4 d[4];
#define TW_IM(s) do { UNROLL for (int j = 0; j < 2; ++j) { const int p_ = 2 * (s) + j; const h2 pi_ = H(pkh(yi[2 * p_], yi[2 * p_ + 1])); \
        d[s][j] = U(__builtin_elementwise_fma(pi_, H(TW[s][2 + j]), t0[p_])); d[s][2 + j] = U(__builtin_elementwise_fma(pi_, H(TW[s][j]), -t1[p_])); } } while (0)
    TW_IM(0); SB();
    UNROLL for (int s = 0; s < 4; ++s) {
        outr = OUT_B ? mfma32(MR[s], d[s], outr) : mfma32(d[s], MR[s], outr);
        if (s < 3) TW_IM(s + 1);
        SB();
    }
    UNROLL for (int s = 0; s < 4; ++s) outi = OUT_B ? mfma32(MI[s], d[s], outi) : mfma32(d[s], MI[s], outi);
}
DI u32 rot(float ph) { return pkh(cosr(ph), sinr(ph)); }
DI u32 quarter(u32 r, u32 f) { u32 o; asm("v_pk_mul_f16 %0, %1, %2 op_sel:[1,0] op_sel_hi:[0,1] neg_lo:[1,0]" : "=v"(o) : "v"(r), "v"(f)); return o; }
DI u4 quarter(u4 r) { u32 one = 0x3c003c00u; pin(one); return u4{quarter(r.x, one), quarter(r.y, one), quarter(r.z, one), quarter(r.w, one)}; }

DI u32 tag_of(u32 epoch, u32 idx) { u32 h = (epoch ^ (idx * 0x9E3779B1u)) * 0x85EBCA6Bu; return h ^ (h >> 15); }
DI u32 parity_of(u32 epoch) { return (epoch & 1u) ? 0x00000001u : 0x00010000u; }
DI void flag_store(rsrc_t rws, u32 arr, int b, int lane, u32 payload, u32 tag) {
    __builtin_amdgcn_raw_buffer_store_b64(u2{payload, tag}, rws, lane < 8 ? lane * (int)(256 * FSTR) : OOB, arr + b * FSTR, SC1);
}
DI u4 flag_wait(rsrc_t rws, u32 arr, int b, int lane, u32 epoch) {
    u2 f[4]; u32 tg[4];
    UNROLL for (int j = 0; j < 4; ++j) tg[j] = tag_of(epoch, arr + 4 * lane + j);
    NOUNROLL for (int spin = 0; spin < SPIN_CAP; ++spin) {
        MEMBAR();
        UNROLL for (int j = 0; j < 4; ++j) f[j] = __builtin_amdgcn_raw_buffer_load_b64(rws, lane * (4 * FSTR) + j * FSTR, arr + (b & 7) * (256 * FSTR), SC1);
        if (__all(f[0].y == tg[0] && f[1].y == tg[1] && f[2].y == tg[2] && f[3].y == tg[3])) break;
        __builtin_amdgcn_s_sleep(PSLEEP);
    }
    return u4{f[0].x, f[1].x, f[2].x, f[3].x};
}
DI int cls_of(int b) { const int row = (b & 7) >> 1; return row == 0 ? 4 * (b >> 7) : ((b & 1) ? 8 - row : row); }
DI int tile_of(int b) { return (b & 6) == 0 ? 16 * (b & 1) + ((b >> 3) & 15) : b >> 3; }
DI bool is_helper(int b) { return tile_of(b) == 31; }

constexpr int L_C1 = 0, L_C3 = 4, L_C4 = 8, L_C5 = 12, L_C6 = 16;
constexpr int L_EW = 20;
constexpr int L_WW = 24;
constexpr int L_SW = 28;
constexpr int L_PR = 32;
constexpr int L_SX = 36;
constexpr int L_ZL = 37;
constexpr int L_VD = 38;
constexpr int L_G1 = 39, L_G2 = 40, L_P2 = 41;
constexpr int L_GS = 64;
constexpr int L_M = 192;
constexpr int L_A1 = L_M + 1536;
constexpr int L_A2 = L_A1 + 4096;
constexpr int L_B = L_A2 + 4096;
constexpr int L_W = L_B + 8192;
constexpr int L_END = L_W + 64 * 256;

DI void lset(u32* lds, int idx, u32 v, int lane) { MEMBAR(); if (lane == 0) lds[idx] = v; MEMBAR(); }
DI u32 lget(u32* lds, int idx) { MEMBAR(); return rfl(lds[idx]); }
DI u32 lwait(u32* lds, int idx, u32 atleast) {
    u32 v = 0;
    NOUNROLL for (int spin = 0; spin < LSPIN_CAP; ++spin) { v = lget(lds, idx); if (v >= atleast) break; __builtin_amdgcn_s_sleep(1); }
    MEMBAR();
    return v;
}
DI u32 lwait4(u32* lds, int idx, u32 atleast) {
    u4 v;
    NOUNROLL for (int spin = 0; spin < LSPIN_CAP; ++spin) { MEMBAR(); v = *(const u4*)&lds[idx]; if (rfl(min4u(v)) >= atleast) break; __builtin_amdgcn_s_sleep(1); }
    MEMBAR();
    return rfl(max4u(v));
}
struct MagMax {
    h2 hi = {}, lo = {};
    DI void add(u32 a, u32 c) {
        hi = __builtin_elementwise_maximum(__builtin_elementwise_maximum(hi, H(a)), H(c));
        lo = __builtin_elementwise_minimum(__builtin_elementwise_minimum(lo, H(a)), H(c));
    }
    DI u32 exponent() const {
        const u32 mx = pkmax(U(hi) & 0x7fff7fffu, U(lo) & 0x7fff7fffu);
        return wave_maxu(max(mx & 0xffffu, mx >> 16)) >> 7;
    }
};
DI u32 clampe(u32 e) { return min(max(e, 8u), 240u); }

__global__ __launch_bounds__(512, 2) void gfnet_fused(const void* x, const void* gw, const void* war, const void* wai, const void* wbr, const void* wbi,
                                                      char* ws, void* out) {
    __shared__ __attribute__((aligned(16))) u32 lds[L_END];
    STAMP_DECL;
    STAMP(0);
    asm volatile("" :: "s"(x), "s"(gw), "s"(war), "s"(wai), "s"(wbr), "s"(wbi), "s"(ws), "s"(out));
    const int wv0 = __builtin_amdgcn_readfirstlane(threadIdx.x >> 6);
    const rsrc_t rws = mkrs(ws, WS_BYTES);

    if (wv0 >= 4) {
        const int wv = wv0, b = blockIdx.x, cls = cls_of(b), tile = tile_of(b);
        const int lane = threadIdx.x & 63, q = lane >> 4, n = lane & 15;
        const rsrc_t rw = mkrs(wv == 4 ? war : wv == 5 ? wai : wv == 6 ? wbr : wbi, 2048 * 1026);
        const int wvo = (lane >> 2) * (128 * 1026) + min(lane & 3, 2) * 16, wso = (cls * 513 + 16 * tile - cls) * 2;
#define WLOAD(r) do { UNROLL for (int st = 0; st < 4; ++st) { const int c_ = 4 * st + ((tile + (r)) & 3); \
                __builtin_amdgcn_raw_ptr_buffer_load_lds(rw, (LAS void*)&lds[L_W + (c_ * 4 + wv - 4) * 256], 16, wvo, wso + c_ * 8208, 0, 0); } SB(); } while (0)
        if (wv == 4) { lds[lane] = 0u; asm volatile("s_waitcnt lgkmcnt(0)" ::: "memory"); }
        if (WFIRST) { __builtin_amdgcn_s_sleep(W0DELAY); WLOAD(0); }
        MEMBAR();
        __builtin_amdgcn_s_barrier();
        MEMBAR();
        const bool helper = wv == 7 && is_helper(b);
        u4 GR, GI, FR, FI, HR, HI, T1, T2;
        u32 ev;
        {
            if (!WFIRST) { __builtin_amdgcn_s_sleep(W0DELAY); WLOAD(0); }
            ev = __builtin_amdgcn_raw_buffer_load_b32(rws, 0, WS_E, SC1);
            const u4 smp = __builtin_amdgcn_raw_buffer_load_b128(rw, (lane >> 2) * (131328 + 64) + (lane & 3) * 16, 0, 0);
            MEMBAR();
            if (helper) {
            UNROLL for (int j = 0; j < 4; ++j) {
                GR[j] = rot((float)((4 * q + j) * (cls + 8 * n)) * (1.0f / 128.0f) + (cls == 4 ? 0.25f : 0.0f));
                HR[j] = rot((float)(n * (cls + 8 * (4 * q + j))) * (1.0f / 128.0f));
                FR[j] = rot((float)(n * (4 * q + j)) * (1.0f / 16.0f));
                T1[j] = rot((float)((4 * q + j) * (cls + 8 * n)) * (1.0f / 2048.0f));
                T2[j] = rot((float)(n * (cls + 8 * (4 * q + j))) * (1.0f / 2048.0f));
            }
            GI = quarter(GR); HI = quarter(HR); FI = quarter(FR);
            pin(GR); pin(GI); pin(HR); pin(HI); pin(FR); pin(FI); pin(T1); pin(T2);
            }
            VMWAIT(W0WAIT);
            STAMP(1);
            WLOAD(1); VMWAIT(WWIN); WLOAD(2); VMWAIT(WWIN); WLOAD(3);
            MagMax ms; ms.add(smp.x, smp.y); ms.add(smp.z, smp.w);
            const u32 es = clampe(ms.exponent() + MARGIN);
            VMWAIT(0);
            lset(lds, L_PR + wv - 4, es, lane);
            STAMP(2);
        }
        const u32 ep0 = rfl(ev) + 1u;
        if (wv == 5 || wv == 6) {
            lwait4(lds, L_PR, 1u);
            if (MAXLATE) lwait(lds, L_P2, 1u);
            MagMax m;
            UNROLL for (int c = 0; c < 32; ++c) { const u4 v = *(const u4*)&lds[L_W + ((wv - 5) * 32 + c) * 256 + lane * 4]; m.add(v.x, v.y); m.add(v.z, v.w); }
            const u32 e = clampe(m.exponent());
            MEMBAR(); if (lane == 0) *(u2*)&lds[L_WW + 2 * (wv - 5)] = u2{e, e}; MEMBAR();
        }
        if (VGATE && wv == 6) {
            __builtin_amdgcn_s_setprio(2);
            lwait(lds, L_P2, 1u);
            __builtin_amdgcn_s_sleep(G2DELAY);
            flag_wait(rws, WS_G2, b, lane, ep0);
            lset(lds, L_G2, 1u, lane);
            STAMP(5);
        }
        if (wv == 4) {
            if (ZGATE) { flag_wait(rws, WS_G1, b, lane, ep0); lset(lds, L_G1, 1u, lane); }
            const u32 ex = lwait4(lds, L_EW, 1u), ew = lwait4(lds, L_WW, 1u);
            if (FLATE) lwait(lds, L_P2, 1u);
            flag_store(rws, WS_F, b, lane, ex | (ew << 8), tag_of(ep0, WS_F + b));
            lwait(lds, L_P2, 1u);
            __builtin_amdgcn_s_sleep(FDELAY);
            const u4 f = flag_wait(rws, WS_F, b, lane, ep0);
            const u32 gx = wave_maxu(max4u(f & 0xffu)), gwm = wave_maxu(max4u((f >> 8) & 0xffu));
            const bool redo = gx > lget(lds, L_SX) || gwm > lwait4(lds, L_PR, 1u);
            lset(lds, L_VD, gx | (gwm << 8) | (redo ? 0x20000u : 0x10000u), lane);
            STAMP(5);
        } else if (helper) {
            __builtin_amdgcn_s_setprio(2);
            u32 ew = lwait4(lds, L_PR, 1u);
            NOUNROLL for (u32 pass = 0; pass < 2; ++pass) {
                u4 WH[2];
                const float sw = __builtin_bit_cast(float, (249u - ew) << 23);
                UNROLL for (int r = 0; r < 4; ++r) UNROLL for (int f = 0; f < 2; ++f) {
                    const unsigned short* wp = (const unsigned short*)&lds[L_W + (n * 4 + 2 * f) * 256 + (4 * q + r) * 16] + 16 + cls;
                    float re = bflo(wp[0]) * sw, im = bflo(wp[512]) * sw;
                    pin(re);
                    WH[f][r] = pkh(re, im);
                }
                pin(WH);
                STAMP(3);
                lwait(lds, L_ZL, pass + 1u);
                const u32 tg = tag_of(ep0 + pass, 0u), par = parity_of(ep0 + pass);
                u4 hz;
                NOUNROLL for (int spin = 0; spin < SPIN_CAP; ++spin) {
                    MEMBAR();
                    u32 bad = 0;
                    UNROLL for (int j = 0; j < 4; ++j) { const u2 g = __builtin_amdgcn_raw_buffer_load_b64(rws, (64 * q + n + 16 * j) * HSTR, WS_HB + ((b & 7) >> 1) * (256 * HSTR), SC1); hz[j] = g.x & ~TM; bad |= g.y ^ tg; }
                    if (__all(bad == 0u)) break;
                    __builtin_amdgcn_s_sleep(4);
                }
                STAMP(4);
                const u32 f16 = cls == 0 ? 0x00002c00u : cls == 4 ? 0x2c000000u : cls > 4 ? 0xac002c00u : 0x2c002c00u;
                u4 d, o[2];
                UNROLL for (int j = 0; j < 4; ++j) d[j] = hmul(hz[j], f16);
                f4 yr = mfma16(d, GR), yi = mfma16(d, GI);
                UNROLL for (int r = 0; r < 4; ++r) d[r] = cmul_conj(pkh(yr[r], yi[r]), T1[r]);
                const f4 xr = mfma16(FR, d), xi = mfma16(FI, d);
                UNROLL for (int f = 0; f < 2; ++f) {
                    UNROLL for (int r = 0; r < 4; ++r) d[r] = cmul_swap(pkh(xr[r], xi[r]), WH[f][r]);
                    yr = mfma16(d, FR); yi = mfma16(d, FI);
                    UNROLL for (int r = 0; r < 4; ++r) o[f][r] = cmul_conj(pkh(yr[r], yi[r]), T2[r]);
                }
                const f4 p0 = mfma16(HR, o[0]), p1 = mfma16(HI, o[0]), p2 = mfma16(HR, o[1]), p3 = mfma16(HI, o[1]);
                UNROLL for (int r = 0; r < 4; ++r)
                    store128<SC1>(u4{mark(pkh(p0[r], p1[r]), par, TM), tg, mark(pkh(p2[r], p3[r]), par, TM), tg}, rws, (64 * q + n) * 16 + WS_VL + r * 256 + cls * 4096);
                STAMP(5);
                const u32 vd = lwait(lds, L_VD, 1u);
                if (!(vd & 0x20000u)) break;
                ew = (vd >> 8) & 0xffu;
            }
        }
        STAMP_OUT();
        return;
    }

    u32 vd = 0;
    NOUNROLL for (u32 pass = 0; pass < 2; ++pass) {
    int t = threadIdx.x, wv = wv0, b = blockIdx.x;
    pin(t);
    asm volatile("" : "+s"(wv));
    asm volatile("" : "+s"(b));
    const int lane = t & 63, q = lane >> 4, n = lane & 15;
    const u32 pv = pass + 1u;
    u2 xv[8];
    const rsrc_t rx = mkrs(x, 2048 * 2048);
    const u4 smp = __builtin_amdgcn_raw_buffer_load_b128(rx, (lane >> 2) * (262144 + 2048 + 128) + (lane & 3) * 16, 0, 0);
    UNROLL for (int s1 = 0; s1 < 8; ++s1) xv[s1] = __builtin_amdgcn_raw_buffer_load_b64(rx, t * 8, (256 * s1 + b) * 2048, XAUX);
    const u32 ev = __builtin_amdgcn_raw_buffer_load_b32(rws, 0, WS_E, SC1);
    const u2 gv = __builtin_amdgcn_raw_buffer_load_b64(mkrs(gw, 2048), t * 8, 0, 0);
    MEMBAR();
    SB();
    if (pass == 0) { __builtin_amdgcn_s_barrier(); MEMBAR(); __builtin_amdgcn_s_setprio(1); }
    u4 MR[4], MI[4], TW[4];
    {
        u4 tw; u2 m;
        core_setup(lane, wv, tw, m);
        *(u4*)&lds[L_M + (2 + wv) * 256 + lane * 4] = tw;
        lds[L_M + (wv >> 1) * 256 + lane * 4 + (wv & 1)] = m.x; lds[L_M + (wv >> 1) * 256 + lane * 4 + 2 + (wv & 1)] = m.y;
    }
    const int zc = perm32(lane & 31) + (lane >> 5) * 256, zcu = zc + ((lane & 31) == 0 ? 543 : 511);
    int zl = (zc >> 4) * 16384 + (zc & 15) * 4, zu = (zcu >> 4) * 16384 + (zcu & 15) * 4;
    int zh = lane == 0 ? 0 : OOB;
    int tro = L_A1 + wv * 1024 + (4 * (lane >> 5) + ((lane & 15) >> 2)) * 16 + ((lane >> 4) & 1) * 8 + (lane & 3) * 2;
    u32 tm = TM;
    pin(zl); pin(zu); pin(zh); pin(tro); pin(tm);
    STAMP(1);
    SB();
    u32 ex;
    {
        MagMax ms; ms.add(smp.x, smp.y); ms.add(smp.z, smp.w);
        ex = pass ? (vd & 0xffu) : clampe(ms.exponent() + MARGIN);
    }
    STAMP(2);
    u4 tags;
    {
        const float sc = __builtin_bit_cast(float, (250u - ex) << 23);
        const float rs = sc * 0.70710678118654752f;
        u32 o[8][2];
        UNROLL for (int j = 0; j < 2; ++j) {
            f2 v[8];
            UNROLL for (int s1 = 0; s1 < 8; ++s1) v[s1] = f2{bflo(xv[s1][j]), bfhi(xv[s1][j])};
            const f2 a0 = v[0] + v[4], a1 = v[1] + v[5], a2 = v[2] + v[6], a3 = v[3] + v[7];
            const f2 b0 = (v[0] - v[4]) * sc, b1 = v[1] - v[5], b2_ = (v[2] - v[6]) * sc, b3 = v[3] - v[7];
            const f2 e0 = a0 + a2, e1 = (a1 + a3) * sc, dm = b1 - b3, dp = b1 + b3;
            f2 r[8];
            r[0] = e0 * sc + e1; r[1] = e0 * sc - e1;
            r[2] = dm * rs + b0; r[3] = -(dp * rs) - b2_;
            r[4] = (a0 - a2) * sc; r[5] = (a3 - a1) * sc;
            r[6] = b0 - dm * rs; r[7] = b2_ - dp * rs;
            UNROLL for (int p = 0; p < 8; ++p) o[p][j] = pkh(r[p].x, r[p].y);
        }
        UNROLL for (int p = 0; p < 8; ++p) *(u2*)&lds[L_A1 + p * 512 + 2 * t] = u2{o[p][0], o[p][1]};
        lset(lds, L_C1 + wv, pv, lane);
        const u32 epoch = rfl(ev) + pv;
        tags = u4{parity_of(epoch), 0u, epoch, tag_of(epoch, 0u)};
        u4 in[4];
        NOUNROLL for (int spin = 0; spin < LSPIN_CAP; ++spin) {
            MEMBAR();
            const u4 c = *(const u4*)&lds[L_C1];
            MEMBAR();
            MR[0] = *(const u4*)&lds[L_M + lane * 4]; MR[1] = *(const u4*)&lds[L_M + 256 + lane * 4];
            UNROLL for (int s = 0; s < 4; ++s) {
                const u2 re = __builtin_bit_cast(u2, __builtin_amdgcn_ds_read_tr16_b64_v4i16((LAS s4*)&lds[tro + s * 128]));
                const u2 im = __builtin_bit_cast(u2, __builtin_amdgcn_ds_read_tr16_b64_v4i16((LAS s4*)&lds[tro + s * 128 + 512]));
                in[s] = u4{re.x, re.y, im.x, im.y};
            }
            pin(MR[0]); pin(MR[1]);
            if (rfl(min4u(c)) >= pv) break;
        }
        UNROLL for (int i = 0; i < 4; ++i) TW[i] = *(const u4*)&lds[L_M + (2 + i) * 256 + lane * 4];
        STAMP(3);
        if (ZGATE && pass == 0 && wv == 0) flag_store(rws, WS_G1, b, lane, 0u, tag_of(tags.z, WS_G1 + b));
        core_expand(lane, MR, MI);
        f16v zr, zi;
        core_fft<true>(in, MR, MI, TW, zr, zi);
        u32 z[16];
        UNROLL for (int r = 0; r < 16; ++r) z[r] = mark(pkh(zr[r], zi[r]), tags.x, tm);
        STAMP(4);
        const int zp = ((b >> 2) & 3) * 64 + (b & 3) * 16 + ((b >> 4) & 3) * 4 + (b >> 6);
        const int zs = WS_Z + wv * 64 * 16384 + zp * 64;
        UNROLL for (int r = 0; r < 8; ++r) __builtin_amdgcn_raw_buffer_store_b32(z[r], rws, zl, zs + r * 32768, SC1);
        const u32 za = __builtin_amdgcn_readlane(z[8], 32), zb = __builtin_amdgcn_readlane(z[0], 0);
        MEMBAR();
        const u32 zt = lane < 32 ? za : zb;
        UNROLL for (int r = 8; r < 16; ++r) __builtin_amdgcn_raw_buffer_store_b32((lane & 31) == 0 ? (r < 15 ? z[r + 1] : zt) : z[r], rws, zu, zs + (r & 7) * 32768, SC1);
        __builtin_amdgcn_raw_buffer_store_b64(u2{z[8], tags.w}, rws, zh, WS_HB + (wv * 256 + b) * HSTR, SC1);
    }
    STAMP(5);
    SB();
    const int row = (b & 7) >> 1, cls = cls_of(b), tile = tile_of(b);
    const bool packed = row == 0;
    u4 FR, FI, GR[4], GI[4], HR[4], HI[4], sel1;
    {
        const float ofs = cls == 4 ? 0.25f : 0.0f;
        float gu = (float)(cls + 8 * n) * (1.0f / 2048.0f), g0 = (float)(64 * q + 4 * wv) * gu + ofs;
        u32 s16 = 0x2c002c00u;
        pin(gu); pin(g0); pin(s16);
        UNROLL for (int i = 0; i < 4; ++i) UNROLL for (int j = 0; j < 4; ++j) { const u32 g = rot(fmaf((float)(16 * j + i), gu, g0)); GR[i][j] = hmul(g, s16); GI[i][j] = quarter(g, s16); }
    }
    pin(GR); pin(GI);
    {
        MagMax m;
        UNROLL for (int s1 = 0; s1 < 8; ++s1) m.add(xv[s1].x, xv[s1].y);
        if (wv == 0) lset(lds, L_SX, ex, lane);
        lset(lds, L_EW + wv, clampe(m.exponent()), lane);
    }
    STAMP(6);
    u32 z1[4][4], z2[4][4];
    const int zvm = q * 64 + (63 - tile) * 16384 + (15 - n) * 4;
    const int zv1 = cls <= 4 ? q * 64 + tile * 16384 + n * 4 : zvm;
    int zso = WS_Z + row * 64 * 16384 + wv * 4096;
    asm volatile("" : "+s"(zso));
#define LOAD_Z() do { \
        if (packed) { int a_ = zvm; pin(a_); UNROLL for (int i = 0; i < 4; ++i) UNROLL for (int j = 0; j < 4; ++j) z2[i][j] = __builtin_amdgcn_raw_buffer_load_b32(rws, a_ + (4 * i + j) * 256, zso, SC1); } \
        else { UNROLL for (int i = 0; i < 4; ++i) UNROLL for (int j = 0; j < 4; ++j) z2[i][j] = tags.x; } \
        int c_ = zv1; pin(c_); UNROLL for (int i = 0; i < 4; ++i) UNROLL for (int j = 0; j < 4; ++j) z1[i][j] = __builtin_amdgcn_raw_buffer_load_b32(rws, c_ + (4 * i + j) * 256, zso, SC1); } while (0)
    if (ZGATE) lwait(lds, L_G1, 1u);
    VMWAIT(ZWAIT);
    if (ZDELAY) __builtin_amdgcn_s_sleep(ZDELAY);
    LOAD_Z();
    if (wv == 0) lset(lds, L_ZL, pv, lane);
    SB();
    UNROLL for (int j = 0; j < 4; ++j) FR[j] = rot((float)(n * (4 * q + j)) * (1.0f / 16.0f));
    FI = quarter(FR);
    {
        float hs[4], hu[4];
        UNROLL for (int i = 0; i < 4; ++i) { hs[i] = (float)(16 * n + 4 * wv + i) * (1.0f / 2048.0f); hu[i] = (float)(cls + 32 * q + 8 * i); }
        pin(hs); pin(hu);
        UNROLL for (int i = 0; i < 4; ++i) { UNROLL for (int j = 0; j < 4; ++j) { float a = hs[i] * hu[j]; pin(a); HR[i][j] = rot(a); } HI[i] = quarter(HR[i]); }
        const int d = n + cls - 8 * q;
        UNROLL for (int j = 0; j < 4; ++j) sel1[j] = (d >> 1) == j ? ((d & 1) ? 0xffff0000u : 0xffffu) : 0u;
    }
    pin(FR); pin(FI); pin(HR); pin(HI); pin(sel1);
    STAMP(7);
    u4 WA[4], WB[4];
    u32 ew = 0;
    UNROLL for (int i = 0; i < 4; ++i) asm volatile("" : "=v"(WA[i]), "=v"(WB[i]));
#define WIDEN(es) do { \
        u4 wr[4][4]; \
        int wl = L_W / 4 + wv * 1024 + n * 4 + min(q, 2); pin(wl); \
        UNROLL for (int i = 0; i < 4; ++i) UNROLL for (int a = 0; a < 4; ++a) wr[i][a] = ((const u4*)lds)[wl + (i * 4 + a) * 64]; \
        ew = pass ? (vd >> 8) & 0xffu : (es); \
        const u32 sw = ((249u - ew - (packed ? 1u : 0u)) << 7) * 0x10001u;       \
        const u4 sels = sel1 & sw; \
        SB(); \
        f4 wf[4][4]; \
        UNROLL for (int k = 0; k < 10; ++k) { \
            if (k < 8) { wf[k >> 1][2 * (k & 1)] = mfma16b(wr[k >> 1][2 * (k & 1)], sels); wf[k >> 1][2 * (k & 1) + 1] = mfma16b(wr[k >> 1][2 * (k & 1) + 1], sels); } \
            if (k >= 2) { const int i_ = (k - 2) >> 1, f_ = k & 1; UNROLL for (int r = 0; r < 4; ++r) (f_ ? WB : WA)[i_][r] = pkh(wf[i_][2 * f_][r], wf[i_][2 * f_ + 1][r]); } \
            SB(); } \
        pin(WA); pin(WB); } while (0)
    MEMBAR();
    const u4 pr0 = *(const u4*)&lds[L_PR];
    const bool wearly = rfl(min4u(pr0)) != 0u;
    MEMBAR();
    if (wearly) WIDEN(rfl(max4u(pr0)));
    u32 x1 = tags.x ^ (cls > 4 ? 0x80000000u : 0u), x2 = tags.x ^ (cls == 4 ? 0x00008000u : cls == 0 ? 0x80000000u : 0u);
    const int lw = ((wv * 16 + 4 * q) * 16 + n) * 4;
    int lr = (q * 16 + 4 * wv) * 16 + n;
    const u4* l4 = (const u4*)lds;
    pin(GR); pin(GI); pin(x1); pin(x2); pin(lr);
    STAMP(8);
    SB();
    {
        f4 yr[4], yi[4];
        NOUNROLL for (int spin = 0; spin < SPIN_CAP; ++spin) {
            u32 m = 0;
            UNROLL for (int i = 0; i < 4; ++i) {
                u4 d;
                UNROLL for (int j = 0; j < 4; ++j) { z1[i][j] ^= x1; z2[i][j] ^= x2; }
                UNROLL for (int j = 0; j < 4; ++j) d[j] = U(H(z1[i][j]) + H(z2[i][j]));
                UNROLL for (int j = 0; j < 4; ++j) m |= z1[i][j] | z2[i][j];
                yr[i] = mfma16(GR[i], d); yi[i] = mfma16(GI[i], d);
                SB();
            }
            if (__all((m & TM) == 0u)) break;
            MEMBAR();
            LOAD_Z();
        }
        STAMP(9);
        u4 y[4];
        UNROLL for (int i = 0; i < 4; ++i) UNROLL for (int r = 0; r < 4; ++r) y[r][i] = pkh(yr[i][r], yi[i][r]);
        UNROLL for (int r = 0; r < 4; ++r) *(u4*)&lds[L_A2 + lw + r * 64] = y[r];
        lset(lds, L_C3 + wv, pv, lane);
    }
    if (!wearly) { const u32 es = lwait4(lds, L_PR, 1u); WIDEN(es); }
    {
        u4 d[4];
        NOUNROLL for (int spin = 0; spin < LSPIN_CAP; ++spin) {
            MEMBAR();
            const u4 c = *(const u4*)&lds[L_C3];
            MEMBAR();
            UNROLL for (int i = 0; i < 4; ++i) d[i] = l4[L_A2 / 4 + lr + i * 16];
            pin(d[0]);
            if (rfl(min4u(c)) >= pv) break;
        }
        STAMP(10);
        if (VGATE == 1 && pass == 0 && wv == 0) { flag_store(rws, WS_G2, b, lane, 0u, tag_of(tags.z, WS_G2 + b)); lset(lds, L_P2, 1u, lane); }
        pin(WA); pin(WB);
        f4 xr[4], xi[4];
        u4 xx[4], ya[4], yb[4];
        UNROLL for (int i = 0; i < 4; ++i) {
            xr[i] = mfma16(FR, d[i]); xi[i] = mfma16(FI, d[i]);
            if (i >= 2) { UNROLL for (int r = 0; r < 4; ++r) xx[i - 2][r] = pkh(xr[i - 2][r], xi[i - 2][r]); pin(xx[i - 2]); }
            SB();
        }
        UNROLL for (int r = 0; r < 4; ++r) ya[0][r] = cmul_swap(xx[0][r], WA[0][r]);
        UNROLL for (int i = 2; i < 4; ++i) { UNROLL for (int r = 0; r < 4; ++r) xx[i][r] = pkh(xr[i][r], xi[i][r]); pin(xx[i]); }
        SB();
        f4 p0[4], p1[4], p2[4], p3[4];
        UNROLL for (int i = 0; i < 4; ++i) {
            p0[i] = mfma16(FR, ya[i]); p1[i] = mfma16(FI, ya[i]);
            if (i < 3) { UNROLL for (int r = 0; r < 4; ++r) ya[i + 1][r] = cmul_swap(xx[i + 1][r], WA[i + 1][r]); }
            else { UNROLL for (int r = 0; r < 4; ++r) yb[0][r] = cmul_swap(xx[0][r], WB[0][r]); }
            SB();
        }
        u4 ra[4], rb[4];
        UNROLL for (int i = 0; i < 4; ++i) {
            p2[i] = mfma16(FR, yb[i]); p3[i] = mfma16(FI, yb[i]);
            if (i < 3) { UNROLL for (int r = 0; r < 4; ++r) yb[i + 1][r] = cmul_swap(xx[i + 1][r], WB[i + 1][r]); }
            UNROLL for (int k = 0; k < 4; ++k) ra[i][k] = pkh(p0[k][i], p1[k][i]);
            *(u4*)&lds[L_B + lw + i * 64] = ra[i];
            SB();
        }
        UNROLL for (int i = 0; i < 3; ++i) UNROLL for (int r = 0; r < 4; ++r) rb[r][i] = pkh(p2[i][r], p3[i][r]);
        SB();
        UNROLL for (int r = 0; r < 4; ++r) { rb[r][3] = pkh(p2[3][r], p3[3][r]); *(u4*)&lds[L_B + 4096 + lw + r * 64] = rb[r]; }
        lset(lds, L_C4 + wv, pv, lane);
    }
    STAMP(11);
    if (VGATE == 2 && pass == 0 && wv == 0) { flag_store(rws, WS_G2, b, lane, 0u, tag_of(tags.z, WS_G2 + b)); lset(lds, L_P2, 1u, lane); }
    {
        u4 da[4], db[4];
        NOUNROLL for (int spin = 0; spin < LSPIN_CAP; ++spin) {
            MEMBAR();
            const u4 c = *(const u4*)&lds[L_C4];
            MEMBAR();
            UNROLL for (int i = 0; i < 4; ++i) { da[i] = l4[L_B / 4 + lr + i * 16]; db[i] = l4[L_B / 4 + 1024 + lr + i * 16]; }
            pin(da[0]);
            if (rfl(min4u(c)) >= pv) break;
        }
        const int vo = 64 * q * 32768 + n * 8, so = WS_V + 4 * wv * 32768 + cls * 4096 + tile * 128;
        f4 p0[4], p1[4], p2[4], p3[4];
        UNROLL for (int i = 0; i < 5; ++i) {
            if (i < 4) { p0[i] = mfma16(HR[i], da[i]); p1[i] = mfma16(HI[i], da[i]); p2[i] = mfma16(HR[i], db[i]); p3[i] = mfma16(HI[i], db[i]); }
            if (i > 0) { UNROLL for (int r = 0; r < 4; ++r)
                __builtin_amdgcn_raw_buffer_store_b64(u2{mark(pkh(p0[i - 1][r], p1[i - 1][r]), tags.x, tm), mark(pkh(p2[i - 1][r], p3[i - 1][r]), tags.x, tm)}, rws, vo, so + (16 * r + i - 1) * 32768, SC1); }
            SB();
        }
    }
    if (wv == 0) lset(lds, L_P2, 1u, lane);
    STAMP(12);
    SB();
    {
        pin(xv);
        const f2 g01 = {bflo(gv.x), bfhi(gv.x)}, g23 = {bflo(gv.y), bfhi(gv.y)};
        f4 acc = {};
        UNROLL for (int i = 0; i < 4; ++i) {
            u4 pp;
            UNROLL for (int k = 0; k < 2; ++k) {
                const f2 a = f2{bflo(xv[2 * i + k].x), bfhi(xv[2 * i + k].x)} * g01, c = f2{bflo(xv[2 * i + k].y), bfhi(xv[2 * i + k].y)} * g23;
                pp[2 * k] = pkb(a.x, a.y); pp[2 * k + 1] = pkb(c.x, c.y);
            }
            const u32 s0 = n == 2 * i ? 0x3f803f80u : 0u, s1 = n == 2 * i + 1 ? 0x3f803f80u : 0u;
            acc = __builtin_amdgcn_mfma_f32_16x16x32_bf16(__builtin_bit_cast(b8, pp), __builtin_bit_cast(b8, u4{s0, s0, s1, s1}), acc, 0, 0, 0);
        }
        if (n < 8) lds[L_GS + n * 16 + (t >> 4)] = __builtin_bit_cast(u32, (acc.x + acc.y) + (acc.z + acc.w));
        lset(lds, L_C5 + wv, pv, lane);
    }
    MR[0] = *(const u4*)&lds[L_M + lane * 4]; MR[1] = *(const u4*)&lds[L_M + 256 + lane * 4];
    UNROLL for (int i = 0; i < 4; ++i) TW[i] = *(const u4*)&lds[L_M + (2 + i) * 256 + lane * 4];
    core_expand(lane, MR, MI);
    pin(MR); pin(MI); pin(TW);
    u4 PM;
    {
        lwait4(lds, L_C5, pv);
        float gg[4];
        UNROLL for (int k = 0; k < 2; ++k) {
            const f4* pp = (const f4*)&lds[L_GS + ((n >> 2) * 2 + k) * 16];
            const f4 ps = (pp[0] + pp[1]) + (pp[2] + pp[3]);
            const float lg = rbf((ps.x + ps.y) + (ps.z + ps.w));
            gg[2 * k] = rbf(1.0f / (1.0f + __expf(-lg)));
            gg[2 * k + 1] = rbf(1.0f - gg[2 * k]);
        }
        UNROLL for (int k = 0; k < 4; ++k) { gg[k] *= 0.03125f; pin(gg[k]); }
        const int r = n & 3, sa = (n >> 2) * 2;
        UNROLL for (int cc = 0; cc < 2; ++cc) {
            const int u1 = 2 * q + cc;
            const float ta = (float)(sa * u1) * 0.125f + ((r & 1) ? 0.25f : (r & 2) ? 0.5f : 0.0f);
            const float tb = (float)((sa + 1) * u1) * 0.125f + ((r & 1) ? ((r & 2) ? 0.0f : 0.5f) : 0.25f);
            const float ca = cosr(ta), sa_ = sinr(ta), cb = cosr(tb), sb_ = sinr(tb);
            float e[4] = {gg[2] * cb, gg[2] * sb_, gg[3] * cb, gg[3] * sb_};
            pin(e);
            e[0] = fmaf(gg[0], ca, e[0]); pin(e[0]); e[1] = fmaf(gg[0], sa_, e[1]); pin(e[1]); e[2] = fmaf(gg[1], ca, e[2]); pin(e[2]); e[3] = fmaf(gg[1], sa_, e[3]);
            PM[2 * cc] = pkh(e[0], e[1]); PM[2 * cc + 1] = pkh(e[2], e[3]);
        }
    }
    const float osc = __builtin_bit_cast(float, (u32)min(max((int)ex + (int)ew - 130, 1), 254) << 23) * TRUNC_FIX;
    const int ve = 2 * q * 4096 + (128 * wv + n) * 8;
    int vs0 = WS_V + b * 32768, vs1 = vs0 + 4096, vsl = WS_VL + b * 16;
    asm volatile("" : "+s"(vs0), "+s"(vs1), "+s"(vsl));
    int ov = (4 * perm32(lane & 31) + (lane >> 5)) * 16 + (512 * wv + b) * 2048;
    pin(PM); pin(ov);
    STAMP(13);
    if (VGATE) lwait(lds, L_G2, 1u);
    if (VSTAG) { const int st_ = b & 3; if (st_ == 0) VMWAIT(12); else if (st_ == 1) VMWAIT(8); else if (st_ == 2) VMWAIT(4); else VMWAIT(0); }
    else VMWAIT(VWAIT);
    if (VDELAY) __builtin_amdgcn_s_sleep(VDELAY);
    {
        f4 z[9];
        NOUNROLL for (int spin = 0; spin < SPIN_CAP; ++spin) {
            MEMBAR();
            u2 v0[8], v1[8];
            u4 ve8, vo8;
            int a_ = ve; pin(a_);
            if (wv == 3) {
                ve8 = __builtin_amdgcn_raw_buffer_load_b128(rws, q * 8192, vsl, SC1); vo8 = __builtin_amdgcn_raw_buffer_load_b128(rws, q * 8192 + 4096, vsl, SC1);
            } else {
                const u2 e2 = __builtin_amdgcn_raw_buffer_load_b64(rws, a_ + 1024, vs0, SC1), o2 = __builtin_amdgcn_raw_buffer_load_b64(rws, a_ + 1024, vs1, SC1);
                ve8 = u4{e2.x, e2.y, tags.w, tags.w}; vo8 = u4{o2.x, o2.y, tags.w, tags.w};
            }
            UNROLL for (int i = 0; i < 8; ++i) {
                v0[i] = __builtin_amdgcn_raw_buffer_load_b64(rws, a_ + 128 * i, vs0, SC1);
                v1[i] = __builtin_amdgcn_raw_buffer_load_b64(rws, a_ + 128 * i, vs1, SC1);
            }
            SB();
            u32 m = (((wv == 3 ? ve8.y : ve8.z) ^ tags.w) | (ve8.w ^ tags.w) | ((wv == 3 ? vo8.y : vo8.z) ^ tags.w) | (vo8.w ^ tags.w)) ? TM : 0u;
            UNROLL for (int k = 0; k < 9; ++k) {
                const int i = (k + 8) % 9;
                const u4 d = (i == 8 ? u4{ve8.x, wv == 3 ? ve8.z : ve8.y, vo8.x, wv == 3 ? vo8.z : vo8.y} : u4{v0[i & 7].x, v0[i & 7].y, v1[i & 7].x, v1[i & 7].y}) ^ tags.x;
                m |= d.x | d.y; m |= d.z | d.w;
                z[i] = mfma16(PM, d);
                SB();
            }
            if (__all((m & TM) == 0u)) break;
        }
        STAMP(14);
        if (wv == 0 && n == 0) { z[0].x = 0.5f * (z[0].x + z[0].z); z[0].y = 0.5f * (z[0].y + z[0].w); }
        if (wv == 3) { z[8].z = 0.5f * (z[8].x + z[8].z); z[8].w = 0.5f * (z[8].y + z[8].w); }
        u4* l4w = (u4*)&lds[L_A1];
        UNROLL for (int p = 0; p < 2; ++p)
            l4w[(q * 8 + wv) * 32 + 16 * p + n] = u4{pkh(z[p].x, z[p + 2].x), pkh(z[p + 4].x, z[p + 6].x), pkh(z[p].y, z[p + 2].y), pkh(z[p + 4].y, z[p + 6].y)};
        l4w[(q * 8 + 7 - wv) * 32 + 16 - n] = u4{pkh(z[7].z, z[5].z), pkh(z[3].z, z[1].z), pkh(z[7].w, z[5].w), pkh(z[3].w, z[1].w)};
        f2 m[4];
        UNROLL for (int i = 0; i < 4; ++i) m[i] = n == 0 ? f2{z[8 - 2 * i].z, z[8 - 2 * i].w} : f2{z[6 - 2 * i].z, z[6 - 2 * i].w};
        l4w[(q * 8 + 7 - wv) * 32 + ((32 - n) & 31)] = u4{pkh(m[0].x, m[1].x), pkh(m[2].x, m[3].x), pkh(m[0].y, m[1].y), pkh(m[2].y, m[3].y)};
        lset(lds, L_C6 + wv, 2u * pv - 1u, lane);
    }
    {
        u4 in[4];
        NOUNROLL for (int spin = 0; spin < LSPIN_CAP; ++spin) {
            MEMBAR();
            const u4 c = *(const u4*)&lds[L_C6];
            MEMBAR();
            UNROLL for (int s = 0; s < 4; ++s) in[s] = *(const u4*)&lds[L_A1 + (wv * 256 + s * 64 + lane) * 4];
            pin(in[0]);
            if (rfl(min4u(c)) >= 2u * pv - 1u) break;
        }
        f16v zr, zi;
        core_fft<false>(in, MR, MI, TW, zr, zi);
        zr *= osc; zi *= osc;
        const rsrc_t ro = mkrs(out, 2048 * 2048);
        UNROLL for (int hf = 0; hf < 2; ++hf) {
            u4 c;
            UNROLL for (int j = 0; j < 4; ++j) c[j] = pkb(zr[8 * hf + 2 * j], zr[8 * hf + 2 * j + 1]);
            store128<OAUX>(c, ro, ov + 256 * 2048 + 32 * hf);
        }
        UNROLL for (int hf = 0; hf < 2; ++hf) {
            u4 a;
            UNROLL for (int j = 0; j < 4; ++j) a[j] = pkb(zi[8 * hf + 2 * j], zi[8 * hf + 2 * j + 1]);
            store128<OAUX>(a, ro, ov + 32 * hf);
        }
    }
    STAMP(15);
    vd = lwait(lds, L_VD, 1u);
    if (pass == 1 || !(vd & 0x20000u)) {
        if (b == 0 && t == 0) __builtin_amdgcn_raw_buffer_store_b32(tags.z, rws, 0, WS_E, SC1);
        break;
    }
    lset(lds, L_C6 + wv, 2u, lane);
    lwait4(lds, L_C6, 2u);
    VMWAIT(0);
    }
    STAMP_OUT();
}

extern "C" void kernel_launch(void* const* d_in, const int* in_sizes, int n_in,
                              void* d_out, int out_size, void* d_ws, size_t ws_size,
                              hipStream_t stream) {
    gfnet_fused<<<256, 512, 0, stream>>>(d_in[0], d_in[6], d_in[1], d_in[2], d_in[4], d_in[5], (char*)d_ws, d_out);
}
```

```cpp
#define W0AUX 0
#include <hip/hip_runtime.h>
#include <hip/hip_bf16.h>
#include <stdint.h>

typedef _Float16 h2 __attribute__((ext_vector_type(2)));
typedef _Float16 h8 __attribute__((ext_vector_type(8)));
typedef __bf16 b2 __attribute__((ext_vector_type(2)));
typedef __bf16 b8 __attribute__((ext_vector_type(8)));
typedef float f2 __attribute__((ext_vector_type(2)));
typedef float f4 __attribute__((ext_vector_type(4)));
typedef float f16v __attribute__((ext_vector_type(16)));
typedef short s4 __attribute__((ext_vector_type(4)));
typedef unsigned u32;
typedef u32 u2 __attribute__((ext_vector_type(2)));
typedef u32 u4 __attribute__((ext_vector_type(4)));
typedef unsigned short us2 __attribute__((ext_vector_type(2)));
#define DI __device__ __forceinline__
#define UNROLL _Pragma("unroll")
#define NOUNROLL _Pragma("unroll 1")
#define LAS __attribute__((address_space(3)))

#define W1AUX 0
#define VDELAY 0
#define ZDELAY 0
#define WFIRST 0
#define VSTAG 0
#define GEARLY 0
#define ZGATE 0
#define VGATE 0
#define G2DELAY 50
#define MAXLATE 0
#define FLATE 0
#define FDELAY 24
#define EDGE 1
#define SCAN 0
#define ZHOLD 4
#define ZSLEEP 0
#define XLAST 0
#define XDELAY 0
#define G0 0
#define WWAIT 4
#define PSLEEP 8
#define WGATE 4
#define W0DELAY 2
#define W0WAIT 0
#define WWIN 12
#define ZWAIT 0
#define VWAIT 0
#define XAUX 2
#define OAUX 2
#define MARGIN 2

#define FSTRIDE 16
constexpr u32 FSTR = FSTRIDE;
constexpr u32 WS_Z = 0;
constexpr u32 WS_V = 4u << 20;
constexpr u32 WS_VL = 12u << 20;
#define HSTR 32
constexpr u32 WS_HB = WS_VL + (32u << 10);
constexpr u32 WS_F = WS_HB + (128u << 10);
constexpr u32 WS_G1 = WS_F + 2048 * FSTR;
constexpr u32 WS_G2 = WS_G1 + 2048 * FSTR;
constexpr u32 WS_E = WS_G2 + 2048 * FSTR;
constexpr u32 WS_BYTES = WS_E + 4096;
constexpr size_t WS_META = 24u << 20;
constexpr int SC1 = 16;
constexpr int SPIN_CAP = 8192;
constexpr int LSPIN_CAP = 1 << 17;
constexpr int OOB = 0x7ffffff0;
constexpr float TRUNC_FIX = 1.00066f;
constexpr u32 TM = 0x00010001u;

template <class T> DI void pin(T& v) { asm volatile("" : "+v"(v)); }
template <class T, int N> DI void pin(T (&v)[N]) { UNROLL for (int i = 0; i < N; ++i) pin(v[i]); }
DI u32 opq(u32 v) { asm("" : "+v"(v)); return v; }
DI u32 pkh(float lo, float hi) { h2 r; r.x = (_Float16)lo; r.y = (_Float16)hi; return __builtin_bit_cast(u32, r); }
DI u32 pkb(float lo, float hi) { b2 r; r.x = (__bf16)lo; r.y = (__bf16)hi; return __builtin_bit_cast(u32, r); }
DI float bflo(u32 v) { return __builtin_bit_cast(float, v << 16); }
DI float bfhi(u32 v) { return __builtin_bit_cast(float, v & 0xffff0000u); }
DI float rbf(float v) { return (float)(__bf16)v; }
DI h2 H(u32 v) { return __builtin_bit_cast(h2, v); }
DI u32 U(h2 v) { return __builtin_bit_cast(u32, v); }
DI f16v mfma32(u4 a, u4 b, f16v c) { return __builtin_amdgcn_mfma_f32_32x32x16_f16(__builtin_bit_cast(h8, a), __builtin_bit_cast(h8, b), c, 0, 0, 0); }
DI f4 mfma16(u4 a, u4 b) { return __builtin_amdgcn_mfma_f32_16x16x32_f16(__builtin_bit_cast(h8, a), __builtin_bit_cast(h8, b), f4{}, 0, 0, 0); }
DI f4 mfma16b(u4 a, u4 b) { return __builtin_amdgcn_mfma_f32_16x16x32_bf16(__builtin_bit_cast(b8, a), __builtin_bit_cast(b8, b), f4{}, 0, 0, 0); }
DI u32 hmul(u32 a, u32 b) { return U(H(a) * H(b)); }
DI u32 hfma(u32 a, u32 b, u32 c) { return U(__builtin_elementwise_fma(H(a), H(b), H(c))); }
DI u32 cmul_swap(u32 x, u32 w) { const h2 a = H(opq(x)), c = H(opq(w)); h2 n = c; n.y = -n.y; return U(__builtin_elementwise_fma(a.yy, n, a.xx * c.yx)); }
DI u32 cmul_conj(u32 x, u32 t) { const h2 a = H(opq(x)), c = H(opq(t)); h2 n = c.yy; n.y = -n.y; return U(__builtin_elementwise_fma(a.yx, n, a * c.xx)); }
DI u32 pkmax(u32 a, u32 b) { return __builtin_bit_cast(u32, __builtin_elementwise_max(__builtin_bit_cast(us2, a), __builtin_bit_cast(us2, b))); }
DI float sinr(float r) { return __builtin_amdgcn_sinf(r); }
DI float cosr(float r) { return __builtin_amdgcn_cosf(r); }
DI int perm32(int n) { return (n & 0x13) | ((n & 4) << 1) | ((n & 8) >> 1); }
DI u32 max3u(u32 a, u32 b, u32 c) { return max(max(a, b), c); }
DI u32 max4u(u4 v) { return max(max(v.x, v.y), max(v.z, v.w)); }
DI u32 min4u(u4 v) { return min(min(v.x, v.y), min(v.z, v.w)); }
DI u32 mark(u32 v, u32 par, u32 tm) { return (par & tm) | (v & ~tm); }
DI u32 rfl(u32 v) { return __builtin_amdgcn_readfirstlane(v); }
DI u32 pow2h(int d) { const float f = __builtin_bit_cast(float, (u32)(127 - min(d, 64)) << 23); return pkh(f, f); }

typedef __amdgpu_buffer_rsrc_t rsrc_t;
DI rsrc_t mkrs(const void* p, u32 bytes) { return __builtin_amdgcn_make_buffer_rsrc((void*)p, (short)0, (int)bytes, 0x00020000); }
template <int AUX> DI void store128(u4 v, rsrc_t r, int off) { __builtin_amdgcn_raw_buffer_store_b128(v, r, off, 0, AUX); }
#define SB() __builtin_amdgcn_sched_barrier(0)
#define MEMBAR() asm volatile("" ::: "memory")
#define VMWAIT(n) asm volatile("s_waitcnt vmcnt(%0)" :: "n"(n) : "memory")
#define STAMP_DECL
#define STAMP(i)
#define STAMP_OUT()
template <int CTRL> DI u32 dpp(u32 v) { return __builtin_amdgcn_update_dpp(0u, v, CTRL, 0xf, 0xf, true); }
DI u32 wave_maxu(u32 v) {
    v = max(v, dpp<0xB1>(v)); v = max(v, dpp<0x4E>(v)); v = max(v, dpp<0x141>(v)); v = max(v, dpp<0x140>(v));
    return max(max((u32)__builtin_amdgcn_readlane(v, 0), (u32)__builtin_amdgcn_readlane(v, 16)), max((u32)__builtin_amdgcn_readlane(v, 32), (u32)__builtin_amdgcn_readlane(v, 48)));
}

DI void core_setup(int lane, int s, u4& tw, u2& m) {
    float fc = (float)perm32(lane & 31) * (1.0f / 1024.0f), p0 = (float)(4 * (lane >> 5) + 8 * s) * fc, p1 = (float)(4 * (lane >> 5) + 8 * (s >> 1) + 2 * (s & 1)) * fc * 32.0f;
    pin(fc); pin(p0); pin(p1);
    float tc[4], ts[4];
    UNROLL for (int i = 0; i < 4; ++i) { const float pt = fmaf((float)i, fc, p0); tc[i] = cosr(pt); ts[i] = sinr(pt); }
    tw = u4{pkh(tc[0], tc[1]), pkh(tc[2], tc[3]), pkh(ts[0], ts[1]), pkh(ts[2], ts[3])};
    const float p2 = fmaf(32.0f, fc, p1);
    m = u2{pkh(cosr(p1), cosr(p2)), pkh(sinr(p1), sinr(p2))};
}
DI void core_expand(int lane, u4 (&MR)[4], u4 (&MI)[4]) {
    const u32 sg = (lane & 1) ? 0x80008000u : 0u;
    MR[2] = MR[0] ^ sg; MR[3] = MR[1] ^ sg;
    UNROLL for (int s = 0; s < 4; ++s) MI[s] = u4{MR[s].z ^ 0x80008000u, MR[s].w ^ 0x80008000u, MR[s].x, MR[s].y};
}
template <bool OUT_B> DI void core_fft(const u4 (&in)[4], const u4 (&MR)[4], const u4 (&MI)[4], const u4 (&TW)[4], f16v& outr, f16v& outi) {
    f16v yr = {}, yi = {};
    UNROLL for (int s = 0; s < 4; ++s) yr = mfma32(in[s], MR[s], yr);
    SB();
    yi = mfma32(in[0], MI[0], yi); yi = mfma32(in[1], MI[1], yi);
    SB();
    h2 t0[8], t1[8];
#define TW_RE(p) do { const h2 pr_ = H(pkh(yr[2 * (p)], yr[2 * (p) + 1])); t0[p] = pr_ * H(TW[(p) >> 1][(p) & 1]); t1[p] = pr_ * H(TW[(p) >> 1][2 + ((p) & 1)]); } while (0)
    TW_RE(0); TW_RE(1); SB();
    yi = mfma32(in[2], MI[2], yi);
    TW_RE(2); TW_RE(3); SB();
    yi = mfma32(in[3], MI[3], yi);
    TW_RE(4); TW_RE(5); TW_RE(6); TW_RE(7); SB();
    outr = f16v{}; outi = f16v{};
    u4 d[4];
#define TW_IM(s) do { UNROLL for (int j = 0; j < 2; ++j) { const int p_ = 2 * (s) + j; const h2 pi_ = H(pkh(yi[2 * p_], yi[2 * p_ + 1])); \
        d[s][j] = U(__builtin_elementwise_fma(pi_, H(TW[s][2 + j]), t0[p_])); d[s][2 + j] = U(__builtin_elementwise_fma(pi_, H(TW[s][j]), -t1[p_])); } } while (0)
    TW_IM(0); SB();
    UNROLL for (int s = 0; s < 4; ++s) {
        outr = OUT_B ? mfma32(MR[s], d[s], outr) : mfma32(d[s], MR[s], outr);
        if (s < 3) TW_IM(s + 1);
        SB();
    }
    UNROLL for (int s = 0; s < 4; ++s) outi = OUT_B ? mfma32(MI[s], d[s], outi) : mfma32(d[s], MI[s], outi);
}
DI u32 rot(float ph) { return pkh(cosr(ph), sinr(ph)); }
DI u32 quarter(u32 r, u32 f) { u32 o; asm("v_pk_mul_f16 %0, %1, %2 op_sel:[1,0] op_sel_hi:[0,1] neg_lo:[1,0]" : "=v"(o) : "v"(r), "v"(f)); return o; }
DI u4 quarter(u4 r) { u32 one = 0x3c003c00u; pin(one); return u4{quarter(r.x, one), quarter(r.y, one), quarter(r.z, one), quarter(r.w, one)}; }

DI u32 tag_of(u32 epoch, u32 idx) { u32 h = (epoch ^ (idx * 0x9E3779B1u)) * 0x85EBCA6Bu; return h ^ (h >> 15); }
DI u32 parity_of(u32 epoch) { return (epoch & 1u) ? 0x00000001u : 0x00010000u; }
DI void flag_store(rsrc_t rws, u32 arr, int b, int lane, u32 payload, u32 tag) {
    __builtin_amdgcn_raw_buffer_store_b64(u2{payload, tag}, rws, lane < 8 ? lane * (int)(256 * FSTR) : OOB, arr + b * FSTR, SC1);
}
DI u4 flag_wait(rsrc_t rws, u32 arr, int b, int lane, u32 epoch) {
    u2 f[4]; u32 tg[4];
    UNROLL for (int j = 0; j < 4; ++j) tg[j] = tag_of(epoch, arr + 4 * lane + j);
    NOUNROLL for (int spin = 0; spin < SPIN_CAP; ++spin) {
        MEMBAR();
        UNROLL for (int j = 0; j < 4; ++j) f[j] = __builtin_amdgcn_raw_buffer_load_b64(rws, lane * (4 * FSTR) + j * FSTR, arr + (b & 7) * (256 * FSTR), SC1);
        if (__all(f[0].y == tg[0] && f[1].y == tg[1] && f[2].y == tg[2] && f[3].y == tg[3])) break;
        __builtin_amdgcn_s_sleep(PSLEEP);
    }
    return u4{f[0].x, f[1].x, f[2].x, f[3].x};
}
DI int cls_of(int b) { const int row = (b & 7) >> 1; return row == 0 ? 4 * (b >> 7) : ((b & 1) ? 8 - row : row); }
DI int tile_of(int b) { return (b & 6) == 0 ? 16 * (b & 1) + ((b >> 3) & 15) : b >> 3; }
DI bool is_helper(int b) { return tile_of(b) == 31; }

constexpr int L_C1 = 0, L_C3 = 4, L_C4 = 8, L_C5 = 12, L_C6 = 16;
constexpr int L_EW = 20;
constexpr int L_WW = 24;
constexpr int L_SW = 28;
constexpr int L_PR = 32;
constexpr int L_SX = 36;
constexpr int L_ZL = 37;
constexpr int L_VD = 38;
constexpr int L_G1 = 39, L_G2 = 40, L_P2 = 41;
constexpr int L_LD = 44;
constexpr int L_GS = 64;
constexpr int L_M = 192;
constexpr int L_A1 = L_M + 1536;
constexpr int L_A2 = L_A1 + 4096;
constexpr int L_B = L_A2 + 4096;
constexpr int L_W = L_B + 8192;
constexpr int L_END = L_W + 64 * 256;

DI void lset(u32* lds, int idx, u32 v, int lane) { MEMBAR(); if (lane == 0) lds[idx] = v; MEMBAR(); }
DI u32 lget(u32* lds, int idx) { MEMBAR(); return rfl(lds[idx]); }
DI u32 lwait(u32* lds, int idx, u32 atleast) {
    u32 v = 0;
    NOUNROLL for (int spin = 0; spin < LSPIN_CAP; ++spin) { v = lget(lds, idx); if (v >= atleast) break; __builtin_amdgcn_s_sleep(1); }
    MEMBAR();
    return v;
}
DI u32 lwait4(u32* lds, int idx, u32 atleast) {
    u4 v;
    NOUNROLL for (int spin = 0; spin < LSPIN_CAP; ++spin) { MEMBAR(); v = *(const u4*)&lds[idx]; if (rfl(min4u(v)) >= atleast) break; __builtin_amdgcn_s_sleep(1); }
    MEMBAR();
    return rfl(max4u(v));
}
struct MagMax {
    h2 hi = {}, lo = {};
    DI void add(u32 a, u32 c) {
        hi = __builtin_elementwise_maximum(__builtin_elementwise_maximum(hi, H(a)), H(c));
        lo = __builtin_elementwise_minimum(__builtin_elementwise_minimum(lo, H(a)), H(c));
    }
    DI u32 exponent() const {
        const u32 mx = pkmax(U(hi) & 0x7fff7fffu, U(lo) & 0x7fff7fffu);
        return wave_maxu(max(mx & 0xffffu, mx >> 16)) >> 7;
    }
};
DI u32 clampe(u32 e) { return min(max(e, 8u), 240u); }

__global__ __launch_bounds__(512, 2) void gfnet_fused(const void* x, const void* gw, const void* war, const void* wai, const void* wbr, const void* wbi,
                                                      char* ws, void* out) {
    __shared__ __attribute__((aligned(16))) u32 lds[L_END];
    STAMP_DECL;
    STAMP(0);
    asm volatile("" :: "s"(x), "s"(gw), "s"(war), "s"(wai), "s"(wbr), "s"(wbi), "s"(ws), "s"(out));
    const int wv0 = __builtin_amdgcn_readfirstlane(threadIdx.x >> 6);
    const rsrc_t rws = mkrs(ws, WS_BYTES);

    if (wv0 >= 4) {
        const int wv = wv0, b = blockIdx.x, cls = cls_of(b), tile = tile_of(b);
        const int lane = threadIdx.x & 63, q = lane >> 4, n = lane & 15;
        const rsrc_t rw = mkrs(wv == 4 ? war : wv == 5 ? wai : wv == 6 ? wbr : wbi, 2048 * 1026);
        const int wvo = (lane >> 2) * (128 * 1026) + min(lane & 3, 2) * 16, wso = (cls * 513 + 16 * tile - cls) * 2;
#define WLOAD(r) do { UNROLL for (int st = 0; st < 4; ++st) { const int c_ = 4 * st + ((tile + (r)) & 3); \
                __builtin_amdgcn_raw_ptr_buffer_load_lds(rw, (LAS void*)&lds[L_W + (c_ * 4 + wv - 4) * 256], 16, wvo, wso + c_ * 8208, 0, (r) == 0 ? W0AUX : W1AUX); } SB(); } while (0)
        if (wv == 4) { lds[lane] = 0u; asm volatile("s_waitcnt lgkmcnt(0)" ::: "memory"); }
        if (WFIRST) { __builtin_amdgcn_s_sleep(W0DELAY); WLOAD(0); }
        MEMBAR();
        __builtin_amdgcn_s_barrier();
        MEMBAR();
        const bool helper = wv == 7 && is_helper(b);
        u4 GR, GI, FR, FI, HR, HI, T1, T2;
        u32 ev;
        {
            if (!WFIRST) { __builtin_amdgcn_s_sleep(W0DELAY); if (!SCAN) WLOAD(0); }
            const bool split = (b & 6) == 0;
            const int er = split ? 16 * (tile & 15) + ((lane >> 1) & 15) : 8 * tile + ((lane >> 1) & 7);
            const int ec = (split ? (tile & 16) * 16 : 0) - cls + ((lane & 1) ? (split ? 262 : 518) : SCAN ? (split ? 256 : 512) : 0);
            const int sr = split ? 16 * (tile & 15) + (lane >> 2) : 8 * tile + (lane >> 3);
            const int scn = (split ? (tile & 16) * 16 + (lane & 3) * 64 : (lane & 7) * 64) - cls;
            u32 scan = SCAN ? __builtin_amdgcn_raw_buffer_load_b32(rw, ((cls + 8 * sr) * 513 + scn) * 2, 0, W0AUX) : 0u;
            u32 edge = EDGE ? __builtin_amdgcn_raw_buffer_load_b32(rw, ((cls + 8 * er) * 513 + ec) * 2, 0, W0AUX) : 0u;
            ev = __builtin_amdgcn_raw_buffer_load_b32(rws, 0, WS_E, SC1);
            const u4 smp = __builtin_amdgcn_raw_buffer_load_b128(rw, (lane >> 3) * (2 * 131328 + 128) + (lane & 7) * 16, 0, 0);
            MEMBAR();
            if (helper) {
            UNROLL for (int j = 0; j < 4; ++j) {
                GR[j] = rot((float)((4 * q + j) * (cls + 8 * n)) * (1.0f / 128.0f) + (cls == 4 ? 0.25f : 0.0f));
                HR[j] = rot((float)(n * (cls + 8 * (4 * q + j))) * (1.0f / 128.0f));
                FR[j] = rot((float)(n * (4 * q + j)) * (1.0f / 16.0f));
                T1[j] = rot((float)((4 * q + j) * (cls + 8 * n)) * (1.0f / 2048.0f));
                T2[j] = rot((float)(n * (cls + 8 * (4 * q + j))) * (1.0f / 2048.0f));
            }
            GI = quarter(GR); HI = quarter(HR); FI = quarter(FR);
            pin(GR); pin(GI); pin(HR); pin(HI); pin(FR); pin(FI); pin(T1); pin(T2);
            }
            VMWAIT(W0WAIT);
            STAMP(1);
            if (SCAN) { WLOAD(4); VMWAIT(WWIN); }
            const u32 lda = (u32)(size_t)(LAS u32*)&lds[L_LD + wv - 4];
            if (ZHOLD == 1) asm volatile("ds_write_b32 %0, %1" :: "v"(lda), "v"(1u) : "memory");
            WLOAD(1); VMWAIT(WWIN);
            if (ZHOLD == 2) asm volatile("ds_write_b32 %0, %1" :: "v"(lda), "v"(1u) : "memory");
            WLOAD(2); VMWAIT(WWIN);
            if (ZHOLD == 3) asm volatile("ds_write_b32 %0, %1" :: "v"(lda), "v"(1u) : "memory");
            WLOAD(3);
            if (ZHOLD == 4) asm volatile("ds_write_b32 %0, %1" :: "v"(lda), "v"(1u) : "memory");
            pin(edge); pin(scan);
            MagMax ms; ms.add(smp.x, smp.y); ms.add(smp.z, smp.w);
            const u32 es = clampe(ms.exponent() + MARGIN);
            VMWAIT(0);
            lset(lds, L_PR + wv - 4, es, lane);
            STAMP(2);
        }
        const u32 ep0 = rfl(ev) + 1u;
        if (wv == 5 || wv == 6) {
            lwait4(lds, L_PR, 1u);
            if (MAXLATE) lwait(lds, L_P2, 1u);
            MagMax m;
            UNROLL for (int c = 0; c < 32; ++c) { const u4 v = *(const u4*)&lds[L_W + ((wv - 5) * 32 + c) * 256 + lane * 4]; m.add(v.x, v.y); m.add(v.z, v.w); }
            const u32 e = clampe(m.exponent());
            MEMBAR(); if (lane == 0) *(u2*)&lds[L_WW + 2 * (wv - 5)] = u2{e, e}; MEMBAR();
        }
        if (VGATE && wv == 6) {
            __builtin_amdgcn_s_setprio(2);
            lwait(lds, L_P2, 1u);
            __builtin_amdgcn_s_sleep(G2DELAY);
            flag_wait(rws, WS_G2, b, lane, ep0);
            lset(lds, L_G2, 1u, lane);
            STAMP(5);
        }
        if (wv == 4) {
            if (ZGATE) { flag_wait(rws, WS_G1, b, lane, ep0); lset(lds, L_G1, 1u, lane); }
            const u32 ex = lwait4(lds, L_EW, 1u), ew = lwait4(lds, L_WW, 1u);
            if (FLATE) lwait(lds, L_P2, 1u);
            flag_store(rws, WS_F, b, lane, ex | (ew << 8), tag_of(ep0, WS_F + b));
            lwait(lds, L_P2, 1u);
            __builtin_amdgcn_s_sleep(FDELAY);
            const u4 f = flag_wait(rws, WS_F, b, lane, ep0);
            const u32 gx = wave_maxu(max4u(f & 0xffu)), gwm = wave_maxu(max4u((f >> 8) & 0xffu));
            const bool redo = gx > lget(lds, L_SX) || gwm > lwait4(lds, L_PR, 1u);
            lset(lds, L_VD, gx | (gwm << 8) | (redo ? 0x20000u : 0x10000u), lane);
            STAMP(5);
        } else if (helper) {
            __builtin_amdgcn_s_setprio(2);
            u32 ew = lwait4(lds, L_PR, 1u);
            NOUNROLL for (u32 pass = 0; pass < 2; ++pass) {
                u4 WH[2];
                const float sw = __builtin_bit_cast(float, (249u - ew) << 23);
                UNROLL for (int r = 0; r < 4; ++r) UNROLL for (int f = 0; f < 2; ++f) {
                    const unsigned short* wp = (const unsigned short*)&lds[L_W + (n * 4 + 2 * f) * 256 + (4 * q + r) * 16] + 16 + cls;
                    float re = bflo(wp[0]) * sw, im = bflo(wp[512]) * sw;
                    pin(re);
                    WH[f][r] = pkh(re, im);
                }
                pin(WH);
                STAMP(3);
                lwait(lds, L_ZL, pass + 1u);
                const u32 tg = tag_of(ep0 + pass, 0u), par = parity_of(ep0 + pass);
                u4 hz;
                NOUNROLL for (int spin = 0; spin < SPIN_CAP; ++spin) {
                    MEMBAR();
                    u32 bad = 0;
                    UNROLL for (int j = 0; j < 4; ++j) { const u2 g = __builtin_amdgcn_raw_buffer_load_b64(rws, (64 * q + n + 16 * j) * HSTR, WS_HB + ((b & 7) >> 1) * (256 * HSTR), SC1); hz[j] = g.x & ~TM; bad |= g.y ^ tg; }
                    if (__all(bad == 0u)) break;
                    __builtin_amdgcn_s_sleep(4);
                }
                STAMP(4);
                const u32 f16 = cls == 0 ? 0x00002c00u : cls == 4 ? 0x2c000000u : cls > 4 ? 0xac002c00u : 0x2c002c00u;
                u4 d, o[2];
                UNROLL for (int j = 0; j < 4; ++j) d[j] = hmul(hz[j], f16);
                f4 yr = mfma16(d, GR), yi = mfma16(d, GI);
                UNROLL for (int r = 0; r < 4; ++r) d[r] = cmul_conj(pkh(yr[r], yi[r]), T1[r]);
                const f4 xr = mfma16(FR, d), xi = mfma16(FI, d);
                UNROLL for (int f = 0; f < 2; ++f) {
                    UNROLL for (int r = 0; r < 4; ++r) d[r] = cmul_swap(pkh(xr[r], xi[r]), WH[f][r]);
                    yr = mfma16(d, FR); yi = mfma16(d, FI);
                    UNROLL for (int r = 0; r < 4; ++r) o[f][r] = cmul_conj(pkh(yr[r], yi[r]), T2[r]);
                }
                const f4 p0 = mfma16(HR, o[0]), p1 = mfma16(HI, o[0]), p2 = mfma16(HR, o[1]), p3 = mfma16(HI, o[1]);
                UNROLL for (int r = 0; r < 4; ++r)
                    store128<SC1>(u4{mark(pkh(p0[r], p1[r]), par, TM), tg, mark(pkh(p2[r], p3[r]), par, TM), tg}, rws, (64 * q + n) * 16 + WS_VL + r * 256 + cls * 4096);
                STAMP(5);
                const u32 vd = lwait(lds, L_VD, 1u);
                if (!(vd & 0x20000u)) break;
                ew = (vd >> 8) & 0xffu;
            }
        }
        STAMP_OUT();
        return;
    }

    u32 vd = 0;
    NOUNROLL for (u32 pass = 0; pass < 2; ++pass) {
    int t = threadIdx.x, wv = wv0, b = blockIdx.x;
    pin(t);
    asm volatile("" : "+s"(wv));
    asm volatile("" : "+s"(b));
    const int lane = t & 63, q = lane >> 4, n = lane & 15;
    const u32 pv = pass + 1u;
    if (XLAST && pass == 0) { __builtin_amdgcn_s_barrier(); MEMBAR(); __builtin_amdgcn_s_setprio(1); if (XDELAY) __builtin_amdgcn_s_sleep(XDELAY); }
    u2 xv[8];
    const rsrc_t rx = mkrs(x, 2048 * 2048);
    const u4 smp = __builtin_amdgcn_raw_buffer_load_b128(rx, (lane >> 3) * (524288 + 2048 + 256) + (lane & 7) * 16, 0, 0);
    UNROLL for (int s1 = 0; s1 < 8; ++s1) xv[s1] = __builtin_amdgcn_raw_buffer_load_b64(rx, t * 8, (256 * s1 + b) * 2048, XAUX);
    const u32 ev = __builtin_amdgcn_raw_buffer_load_b32(rws, 0, WS_E, SC1);
    const u2 gv = __builtin_amdgcn_raw_buffer_load_b64(mkrs(gw, 2048), t * 8, 0, 0);
    MEMBAR();
    SB();
    if (!XLAST && pass == 0) { __builtin_amdgcn_s_barrier(); MEMBAR(); __builtin_amdgcn_s_setprio(1); }
    u4 MR[4], MI[4], TW[4];
    {
        u4 tw; u2 m;
        core_setup(lane, wv, tw, m);
        *(u4*)&lds[L_M + (2 + wv) * 256 + lane * 4] = tw;
        lds[L_M + (wv >> 1) * 256 + lane * 4 + (wv & 1)] = m.x; lds[L_M + (wv >> 1) * 256 + lane * 4 + 2 + (wv & 1)] = m.y;
    }
    const int zc = perm32(lane & 31) + (lane >> 5) * 256, zcu = zc + ((lane & 31) == 0 ? 543 : 511);
    int zl = (zc >> 4) * 16384 + (zc & 15) * 4, zu = (zcu >> 4) * 16384 + (zcu & 15) * 4;
    int zh = lane == 0 ? 0 : OOB;
    int tro = L_A1 + wv * 1024 + (4 * (lane >> 5) + ((lane & 15) >> 2)) * 16 + ((lane >> 4) & 1) * 8 + (lane & 3) * 2;
    u32 tm = TM;
    pin(zl); pin(zu); pin(zh); pin(tro); pin(tm);
    STAMP(1);
    SB();
    u32 ex;
    {
        MagMax ms; ms.add(smp.x, smp.y); ms.add(smp.z, smp.w);
        ex = pass ? (vd & 0xffu) : clampe(ms.exponent() + MARGIN);
    }
    STAMP(2);
    u4 tags;
    u32 z[16], z512;
    {
        const float sc = __builtin_bit_cast(float, (250u - ex) << 23);
        const float rs = sc * 0.70710678118654752f;
        u32 o[8][2];
        UNROLL for (int j = 0; j < 2; ++j) {
            f2 v[8];
            UNROLL for (int s1 = 0; s1 < 8; ++s1) v[s1] = f2{bflo(xv[s1][j]), bfhi(xv[s1][j])};
            const f2 a0 = v[0] + v[4], a1 = v[1] + v[5], a2 = v[2] + v[6], a3 = v[3] + v[7];
            const f2 b0 = (v[0] - v[4]) * sc, b1 = v[1] - v[5], b2_ = (v[2] - v[6]) * sc, b3 = v[3] - v[7];
            const f2 e0 = a0 + a2, e1 = (a1 + a3) * sc, dm = b1 - b3, dp = b1 + b3;
            f2 r[8];
            r[0] = e0 * sc + e1; r[1] = e0 * sc - e1;
            r[2] = dm * rs + b0; r[3] = -(dp * rs) - b2_;
            r[4] = (a0 - a2) * sc; r[5] = (a3 - a1) * sc;
            r[6] = b0 - dm * rs; r[7] = b2_ - dp * rs;
            UNROLL for (int p = 0; p < 8; ++p) o[p][j] = pkh(r[p].x, r[p].y);
        }
        UNROLL for (int p = 0; p < 8; ++p) *(u2*)&lds[L_A1 + p * 512 + 2 * t] = u2{o[p][0], o[p][1]};
        lset(lds, L_C1 + wv, pv, lane);
        const u32 epoch = rfl(ev) + pv;
        tags = u4{parity_of(epoch), 0u, epoch, tag_of(epoch, 0u)};
        u4 in[4];
        NOUNROLL for (int spin = 0; spin < LSPIN_CAP; ++spin) {
            MEMBAR();
            const u4 c = *(const u4*)&lds[L_C1];
            MEMBAR();
            MR[0] = *(const u4*)&lds[L_M + lane * 4]; MR[1] = *(const u4*)&lds[L_M + 256 + lane * 4];
            UNROLL for (int s = 0; s < 4; ++s) {
                const u2 re = __builtin_bit_cast(u2, __builtin_amdgcn_ds_read_tr16_b64_v4i16((LAS s4*)&lds[tro + s * 128]));
                const u2 im = __builtin_bit_cast(u2, __builtin_amdgcn_ds_read_tr16_b64_v4i16((LAS s4*)&lds[tro + s * 128 + 512]));
                in[s] = u4{re.x, re.y, im.x, im.y};
            }
            pin(MR[0]); pin(MR[1]);
            if (rfl(min4u(c)) >= pv) break;
        }
        UNROLL for (int i = 0; i < 4; ++i) TW[i] = *(const u4*)&lds[L_M + (2 + i) * 256 + lane * 4];
        STAMP(3);
        if (ZGATE && pass == 0 && wv == 0) flag_store(rws, WS_G1, b, lane, 0u, tag_of(tags.z, WS_G1 + b));
        core_expand(lane, MR, MI);
        f16v zr, zi;
        core_fft<true>(in, MR, MI, TW, zr, zi);
        UNROLL for (int r = 0; r < 16; ++r) z[r] = mark(pkh(zr[r], zi[r]), tags.x, tm);
        z512 = z[8];
        const u32 za = __builtin_amdgcn_readlane(z[8], 32), zb = __builtin_amdgcn_readlane(z[0], 0);
        MEMBAR();
        const u32 zt = lane < 32 ? za : zb;
        UNROLL for (int r = 8; r < 16; ++r) z[r] = (lane & 31) == 0 ? (r < 15 ? z[r + 1] : zt) : z[r];
    }
    pin(z);
    STAMP(4);
    const int zs = WS_Z + wv * 64 * 16384 + (((b >> 2) & 3) * 64 + (b & 3) * 16 + ((b >> 4) & 3) * 4 + (b >> 6)) * 64;
    bool zsent = false;
#define TRY_STORE_Z(force) do { if (!zsent) { \
        bool go = true; \
        if (force) lwait4(lds, L_LD, 1u); else { MEMBAR(); go = rfl(min4u(*(const u4*)&lds[L_LD])) != 0u; } \
        if (go) { \
            UNROLL for (int r = 0; r < 16; ++r) __builtin_amdgcn_raw_buffer_store_b32(z[r], rws, r < 8 ? zl : zu, zs + (r & 7) * 32768, SC1); \
            __builtin_amdgcn_raw_buffer_store_b64(u2{z512, tags.w}, rws, zh, WS_HB + (wv * 256 + b) * HSTR, SC1); \
            zsent = true; } } SB(); } while (0)
    TRY_STORE_Z(false);
    const int row = (b & 7) >> 1, cls = cls_of(b), tile = tile_of(b);
    const bool packed = row == 0;
    u4 FR, FI, GR[4], GI[4], HR[4], HI[4], sel1;
    {
        const float ofs = cls == 4 ? 0.25f : 0.0f;
        float gu = (float)(cls + 8 * n) * (1.0f / 2048.0f), g0 = (float)(64 * q + 4 * wv) * gu + ofs;
        u32 s16 = 0x2c002c00u;
        pin(gu); pin(g0); pin(s16);
        UNROLL for (int i = 0; i < 4; ++i) UNROLL for (int j = 0; j < 4; ++j) { const u32 g = rot(fmaf((float)(16 * j + i), gu, g0)); GR[i][j] = hmul(g, s16); GI[i][j] = quarter(g, s16); }
    }
    pin(GR); pin(GI);
    {
        MagMax m;
        UNROLL for (int s1 = 0; s1 < 8; ++s1) m.add(xv[s1].x, xv[s1].y);
        if (wv == 0) lset(lds, L_SX, ex, lane);
        lset(lds, L_EW + wv, clampe(m.exponent()), lane);
    }
    TRY_STORE_Z(false);
    UNROLL for (int j = 0; j < 4; ++j) FR[j] = rot((float)(n * (4 * q + j)) * (1.0f / 16.0f));
    FI = quarter(FR);
    {
        float hs[4], hu[4];
        UNROLL for (int i = 0; i < 4; ++i) { hs[i] = (float)(16 * n + 4 * wv + i) * (1.0f / 2048.0f); hu[i] = (float)(cls + 32 * q + 8 * i); }
        pin(hs); pin(hu);
        UNROLL for (int i = 0; i < 4; ++i) { UNROLL for (int j = 0; j < 4; ++j) { float a = hs[i] * hu[j]; pin(a); HR[i][j] = rot(a); } HI[i] = quarter(HR[i]); }
        const int d = n + cls - 8 * q;
        UNROLL for (int j = 0; j < 4; ++j) sel1[j] = (d >> 1) == j ? ((d & 1) ? 0xffff0000u : 0xffffu) : 0u;
    }
    pin(FR); pin(FI); pin(HR); pin(HI); pin(sel1);
    TRY_STORE_Z(true);
    STAMP(5);
    VMWAIT(WWAIT);
    u4 WA[4], WB[4];
    u32 ew = 0;
    UNROLL for (int i = 0; i < 4; ++i) asm volatile("" : "=v"(WA[i]), "=v"(WB[i]));
#define WIDEN(es) do { \
        u4 wr[4][4]; \
        int wl = L_W / 4 + wv * 1024 + n * 4 + min(q, 2); pin(wl); \
        UNROLL for (int i = 0; i < 4; ++i) UNROLL for (int a = 0; a < 4; ++a) wr[i][a] = ((const u4*)lds)[wl + (i * 4 + a) * 64]; \
        ew = pass ? (vd >> 8) & 0xffu : (es); \
        const u32 sw = ((249u - ew - (packed ? 1u : 0u)) << 7) * 0x10001u;       \
        const u4 sels = sel1 & sw; \
        SB(); \
        f4 wf[4][4]; \
        UNROLL for (int k = 0; k < 10; ++k) { \
            if (k < 8) { wf[k >> 1][2 * (k & 1)] = mfma16b(wr[k >> 1][2 * (k & 1)], sels); wf[k >> 1][2 * (k & 1) + 1] = mfma16b(wr[k >> 1][2 * (k & 1) + 1], sels); } \
            if (k >= 2) { const int i_ = (k - 2) >> 1, f_ = k & 1; UNROLL for (int r = 0; r < 4; ++r) (f_ ? WB : WA)[i_][r] = pkh(wf[i_][2 * f_][r], wf[i_][2 * f_ + 1][r]); } \
            SB(); } \
        pin(WA); pin(WB); } while (0)
    MEMBAR();
    const u4 pr0 = *(const u4*)&lds[L_PR];
    const bool wearly = rfl(min4u(pr0)) != 0u;
    MEMBAR();
    if (wearly) WIDEN(rfl(max4u(pr0)));
    STAMP(6);
    u32 z1[4][4], z2[4][4];
    const int zvm = q * 64 + (63 - tile) * 16384 + (15 - n) * 4;
    const int zv1 = cls <= 4 ? q * 64 + tile * 16384 + n * 4 : zvm;
    int zso = WS_Z + row * 64 * 16384 + wv * 4096;
    asm volatile("" : "+s"(zso));
#define LOAD_Z() do { \
        if (packed) { int a_ = zvm; pin(a_); UNROLL for (int i = 0; i < 4; ++i) UNROLL for (int j = 0; j < 4; ++j) z2[i][j] = __builtin_amdgcn_raw_buffer_load_b32(rws, a_ + (4 * i + j) * 256, zso, SC1); } \
        else { UNROLL for (int i = 0; i < 4; ++i) UNROLL for (int j = 0; j < 4; ++j) z2[i][j] = tags.x; } \
        int c_ = zv1; pin(c_); UNROLL for (int i = 0; i < 4; ++i) UNROLL for (int j = 0; j < 4; ++j) z1[i][j] = __builtin_amdgcn_raw_buffer_load_b32(rws, c_ + (4 * i + j) * 256, zso, SC1); } while (0)
    if (ZGATE) lwait(lds, L_G1, 1u);
    VMWAIT(ZWAIT);
    if (ZDELAY) __builtin_amdgcn_s_sleep(ZDELAY);
    LOAD_Z();
    if (wv == 0) lset(lds, L_ZL, pv, lane);
    SB();
    STAMP(7);
    u32 x1 = tags.x ^ (cls > 4 ? 0x80000000u : 0u), x2 = tags.x ^ (cls == 4 ? 0x00008000u : cls == 0 ? 0x80000000u : 0u);
    const int lw = ((wv * 16 + 4 * q) * 16 + n) * 4;
    int lr = (q * 16 + 4 * wv) * 16 + n;
    const u4* l4 = (const u4*)lds;
    pin(GR); pin(GI); pin(x1); pin(x2); pin(lr);
    STAMP(8);
    SB();
    {
        f4 yr[4], yi[4];
        NOUNROLL for (int spin = 0; spin < SPIN_CAP; ++spin) {
            u32 m = 0;
            UNROLL for (int i = 0; i < 4; ++i) {
                u4 d;
                UNROLL for (int j = 0; j < 4; ++j) { z1[i][j] ^= x1; z2[i][j] ^= x2; }
                UNROLL for (int j = 0; j < 4; ++j) d[j] = U(H(z1[i][j]) + H(z2[i][j]));
                UNROLL for (int j = 0; j < 4; ++j) m |= z1[i][j] | z2[i][j];
                yr[i] = mfma16(GR[i], d); yi[i] = mfma16(GI[i], d);
                SB();
            }
            if (__all((m & TM) == 0u)) break;
            MEMBAR();
            LOAD_Z();
        }
        STAMP(9);
        u4 y[4];
        UNROLL for (int i = 0; i < 4; ++i) UNROLL for (int r = 0; r < 4; ++r) y[r][i] = pkh(yr[i][r], yi[i][r]);
        UNROLL for (int r = 0; r < 4; ++r) *(u4*)&lds[L_A2 + lw + r * 64] = y[r];
        lset(lds, L_C3 + wv, pv, lane);
    }
    if (!wearly) { const u32 es = lwait4(lds, L_PR, 1u); WIDEN(es); }
    {
        u4 d[4];
        NOUNROLL for (int spin = 0; spin < LSPIN_CAP; ++spin) {
            MEMBAR();
            const u4 c = *(const u4*)&lds[L_C3];
            MEMBAR();
            UNROLL for (int i = 0; i < 4; ++i) d[i] = l4[L_A2 / 4 + lr + i * 16];
            pin(d[0]);
            if (rfl(min4u(c)) >= pv) break;
        }
        STAMP(10);
        if (VGATE == 1 && pass == 0 && wv == 0) { flag_store(rws, WS_G2, b, lane, 0u, tag_of(tags.z, WS_G2 + b)); lset(lds, L_P2, 1u, lane); }
        pin(WA); pin(WB);
        f4 xr[4], xi[4];
        u4 xx[4], ya[4], yb[4];
        UNROLL for (int i = 0; i < 4; ++i) {
            xr[i] = mfma16(FR, d[i]); xi[i] = mfma16(FI, d[i]);
            if (i >= 2) { UNROLL for (int r = 0; r < 4; ++r) xx[i - 2][r] = pkh(xr[i - 2][r], xi[i - 2][r]); pin(xx[i - 2]); }
            SB();
        }
        UNROLL for (int r = 0; r < 4; ++r) ya[0][r] = cmul_swap(xx[0][r], WA[0][r]);
        UNROLL for (int i = 2; i < 4; ++i) { UNROLL for (int r = 0; r < 4; ++r) xx[i][r] = pkh(xr[i][r], xi[i][r]); pin(xx[i]); }
        SB();
        f4 p0[4], p1[4], p2[4], p3[4];
        UNROLL for (int i = 0; i < 4; ++i) {
            p0[i] = mfma16(FR, ya[i]); p1[i] = mfma16(FI, ya[i]);
            if (i < 3) { UNROLL for (int r = 0; r < 4; ++r) ya[i + 1][r] = cmul_swap(xx[i + 1][r], WA[i + 1][r]); }
            else { UNROLL for (int r = 0; r < 4; ++r) yb[0][r] = cmul_swap(xx[0][r], WB[0][r]); }
            SB();
        }
        u4 ra[4], rb[4];
        UNROLL for (int i = 0; i < 4; ++i) {
            p2[i] = mfma16(FR, yb[i]); p3[i] = mfma16(FI, yb[i]);
            if (i < 3) { UNROLL for (int r = 0; r < 4; ++r) yb[i + 1][r] = cmul_swap(xx[i + 1][r], WB[i + 1][r]); }
            UNROLL for (int k = 0; k < 4; ++k) ra[i][k] = pkh(p0[k][i], p1[k][i]);
            *(u4*)&lds[L_B + lw + i * 64] = ra[i];
            SB();
        }
        UNROLL for (int i = 0; i < 3; ++i) UNROLL for (int r = 0; r < 4; ++r) rb[r][i] = pkh(p2[i][r], p3[i][r]);
        SB();
        UNROLL for (int r = 0; r < 4; ++r) { rb[r][3] = pkh(p2[3][r], p3[3][r]); *(u4*)&lds[L_B + 4096 + lw + r * 64] = rb[r]; }
        lset(lds, L_C4 + wv, pv, lane);
    }
    STAMP(11);
    if (VGATE == 2 && pass == 0 && wv == 0) { flag_store(rws, WS_G2, b, lane, 0u, tag_of(tags.z, WS_G2 + b)); lset(lds, L_P2, 1u, lane); }
    {
        u4 da[4], db[4];
        NOUNROLL for (int spin = 0; spin < LSPIN_CAP; ++spin) {
            MEMBAR();
            const u4 c = *(const u4*)&lds[L_C4];
            MEMBAR();
            UNROLL for (int i = 0; i < 4; ++i) { da[i] = l4[L_B / 4 + lr + i * 16]; db[i] = l4[L_B / 4 + 1024 + lr + i * 16]; }
            pin(da[0]);
            if (rfl(min4u(c)) >= pv) break;
        }
        const int vo = 64 * q * 32768 + n * 8, so = WS_V + 4 * wv * 32768 + cls * 4096 + tile * 128;
        f4 p0[4], p1[4], p2[4], p3[4];
        UNROLL for (int i = 0; i < 5; ++i) {
            if (i < 4) { p0[i] = mfma16(HR[i], da[i]); p1[i] = mfma16(HI[i], da[i]); p2[i] = mfma16(HR[i], db[i]); p3[i] = mfma16(HI[i], db[i]); }
            if (i > 0) { UNROLL for (int r = 0; r < 4; ++r)
                __builtin_amdgcn_raw_buffer_store_b64(u2{mark(pkh(p0[i - 1][r], p1[i - 1][r]), tags.x, tm), mark(pkh(p2[i - 1][r], p3[i - 1][r]), tags.x, tm)}, rws, vo, so + (16 * r + i - 1) * 32768, SC1); }
            SB();
        }
    }
    if (wv == 0) lset(lds, L_P2, 1u, lane);
    STAMP(12);
    SB();
    {
        pin(xv);
        const f2 g01 = {bflo(gv.x), bfhi(gv.x)}, g23 = {bflo(gv.y), bfhi(gv.y)};
        f4 acc = {};
        UNROLL for (int i = 0; i < 4; ++i) {
            u4 pp;
            UNROLL for (int k = 0; k < 2; ++k) {
                const f2 a = f2{bflo(xv[2 * i + k].x), bfhi(xv[2 * i + k].x)} * g01, c = f2{bflo(xv[2 * i + k].y), bfhi(xv[2 * i + k].y)} * g23;
                pp[2 * k] = pkb(a.x, a.y); pp[2 * k + 1] = pkb(c.x, c.y);
            }
            const u32 s0 = n == 2 * i ? 0x3f803f80u : 0u, s1 = n == 2 * i + 1 ? 0x3f803f80u : 0u;
            acc = __builtin_amdgcn_mfma_f32_16x16x32_bf16(__builtin_bit_cast(b8, pp), __builtin_bit_cast(b8, u4{s0, s0, s1, s1}), acc, 0, 0, 0);
        }
        if (n < 8) lds[L_GS + n * 16 + (t >> 4)] = __builtin_bit_cast(u32, (acc.x + acc.y) + (acc.z + acc.w));
        lset(lds, L_C5 + wv, pv, lane);
    }
    MR[0] = *(const u4*)&lds[L_M + lane * 4]; MR[1] = *(const u4*)&lds[L_M + 256 + lane * 4];
    UNROLL for (int i = 0; i < 4; ++i) TW[i] = *(const u4*)&lds[L_M + (2 + i) * 256 + lane * 4];
    core_expand(lane, MR, MI);
    pin(MR); pin(MI); pin(TW);
    u4 PM;
    {
        lwait4(lds, L_C5, pv);
        float gg[4];
        UNROLL for (int k = 0; k < 2; ++k) {
            const f4* pp = (const f4*)&lds[L_GS + ((n >> 2) * 2 + k) * 16];
            const f4 ps = (pp[0] + pp[1]) + (pp[2] + pp[3]);
            const float lg = rbf((ps.x + ps.y) + (ps.z + ps.w));
            gg[2 * k] = rbf(1.0f / (1.0f + __expf(-lg)));
            gg[2 * k + 1] = rbf(1.0f - gg[2 * k]);
        }
        UNROLL for (int k = 0; k < 4; ++k) { gg[k] *= 0.03125f; pin(gg[k]); }
        const int r = n & 3, sa = (n >> 2) * 2;
        UNROLL for (int cc = 0; cc < 2; ++cc) {
            const int u1 = 2 * q + cc;
            const float ta = (float)(sa * u1) * 0.125f + ((r & 1) ? 0.25f : (r & 2) ? 0.5f : 0.0f);
            const float tb = (float)((sa + 1) * u1) * 0.125f + ((r & 1) ? ((r & 2) ? 0.0f : 0.5f) : 0.25f);
            const float ca = cosr(ta), sa_ = sinr(ta), cb = cosr(tb), sb_ = sinr(tb);
            float e[4] = {gg[2] * cb, gg[2] * sb_, gg[3] * cb, gg[3] * sb_};
            pin(e);
            e[0] = fmaf(gg[0], ca, e[0]); pin(e[0]); e[1] = fmaf(gg[0], sa_, e[1]); pin(e[1]); e[2] = fmaf(gg[1], ca, e[2]); pin(e[2]); e[3] = fmaf(gg[1], sa_, e[3]);
            PM[2 * cc] = pkh(e[0], e[1]); PM[2 * cc + 1] = pkh(e[2], e[3]);
        }
    }
    const float osc = __builtin_bit_cast(float, (u32)min(max((int)ex + (int)ew - 130, 1), 254) << 23) * TRUNC_FIX;
    const int ve = 2 * q * 4096 + (128 * wv + n) * 8;
    int vs0 = WS_V + b * 32768, vs1 = vs0 + 4096, vsl = WS_VL + b * 16;
    asm volatile("" : "+s"(vs0), "+s"(vs1), "+s"(vsl));
    int ov = (4 * perm32(lane & 31) + (lane >> 5)) * 16 + (512 * wv + b) * 2048;
    pin(PM); pin(ov);
    STAMP(13);
    if (VGATE) lwait(lds, L_G2, 1u);
    if (VSTAG) { const int st_ = b & 3; if (st_ == 0) VMWAIT(12); else if (st_ == 1) VMWAIT(8); else if (st_ == 2) VMWAIT(4); else VMWAIT(0); }
    else VMWAIT(VWAIT);
    if (VDELAY) __builtin_amdgcn_s_sleep(VDELAY);
    {
        f4 z[9];
        NOUNROLL for (int spin = 0; spin < SPIN_CAP; ++spin) {
            MEMBAR();
            u2 v0[8], v1[8];
            u4 ve8, vo8;
            int a_ = ve; pin(a_);
            if (wv == 3) {
                ve8 = __builtin_amdgcn_raw_buffer_load_b128(rws, q * 8192, vsl, SC1); vo8 = __builtin_amdgcn_raw_buffer_load_b128(rws, q * 8192 + 4096, vsl, SC1);
            } else {
                const u2 e2 = __builtin_amdgcn_raw_buffer_load_b64(rws, a_ + 1024, vs0, SC1), o2 = __builtin_amdgcn_raw_buffer_load_b64(rws, a_ + 1024, vs1, SC1);
                ve8 = u4{e2.x, e2.y, tags.w, tags.w}; vo8 = u4{o2.x, o2.y, tags.w, tags.w};
            }
            UNROLL for (int i = 0; i < 8; ++i) {
                v0[i] = __builtin_amdgcn_raw_buffer_load_b64(rws, a_ + 128 * i, vs0, SC1);
                v1[i] = __builtin_amdgcn_raw_buffer_load_b64(rws, a_ + 128 * i, vs1, SC1);
            }
            SB();
            u32 m = (((wv == 3 ? ve8.y : ve8.z) ^ tags.w) | (ve8.w ^ tags.w) | ((wv == 3 ? vo8.y : vo8.z) ^ tags.w) | (vo8.w ^ tags.w)) ? TM : 0u;
            UNROLL for (int k = 0; k < 9; ++k) {
                const int i = (k + 8) % 9;
                const u4 d = (i == 8 ? u4{ve8.x, wv == 3 ? ve8.z : ve8.y, vo8.x, wv == 3 ? vo8.z : vo8.y} : u4{v0[i & 7].x, v0[i & 7].y, v1[i & 7].x, v1[i & 7].y}) ^ tags.x;
                m |= d.x | d.y; m |= d.z | d.w;
                z[i] = mfma16(PM, d);
                SB();
            }
            if (__all((m & TM) == 0u)) break;
        }
        STAMP(14);
        if (wv == 0 && n == 0) { z[0].x = 0.5f * (z[0].x + z[0].z); z[0].y = 0.5f * (z[0].y + z[0].w); }
        if (wv == 3) { z[8].z = 0.5f * (z[8].x + z[8].z); z[8].w = 0.5f * (z[8].y + z[8].w); }
        u4* l4w = (u4*)&lds[L_A1];
        UNROLL for (int p = 0; p < 2; ++p)
            l4w[(q * 8 + wv) * 32 + 16 * p + n] = u4{pkh(z[p].x, z[p + 2].x), pkh(z[p + 4].x, z[p + 6].x), pkh(z[p].y, z[p + 2].y), pkh(z[p + 4].y, z[p + 6].y)};
        l4w[(q * 8 + 7 - wv) * 32 + 16 - n] = u4{pkh(z[7].z, z[5].z), pkh(z[3].z, z[1].z), pkh(z[7].w, z[5].w), pkh(z[3].w, z[1].w)};
        f2 m[4];
        UNROLL for (int i = 0; i < 4; ++i) m[i] = n == 0 ? f2{z[8 - 2 * i].z, z[8 - 2 * i].w} : f2{z[6 - 2 * i].z, z[6 - 2 * i].w};
        l4w[(q * 8 + 7 - wv) * 32 + ((32 - n) & 31)] = u4{pkh(m[0].x, m[1].x), pkh(m[2].x, m[3].x), pkh(m[0].y, m[1].y), pkh(m[2].y, m[3].y)};
        lset(lds, L_C6 + wv, 2u * pv - 1u, lane);
    }
    {
        u4 in[4];
        NOUNROLL for (int spin = 0; spin < LSPIN_CAP; ++spin) {
            MEMBAR();
            const u4 c = *(const u4*)&lds[L_C6];
            MEMBAR();
            UNROLL for (int s = 0; s < 4; ++s) in[s] = *(const u4*)&lds[L_A1 + (wv * 256 + s * 64 + lane) * 4];
            pin(in[0]);
            if (rfl(min4u(c)) >= 2u * pv - 1u) break;
        }
        f16v zr, zi;
        core_fft<false>(in, MR, MI, TW, zr, zi);
        zr *= osc; zi *= osc;
        const rsrc_t ro = mkrs(out, 2048 * 2048);
        UNROLL for (int hf = 0; hf < 2; ++hf) {
            u4 c;
            UNROLL for (int j = 0; j < 4; ++j) c[j] = pkb(zr[8 * hf + 2 * j], zr[8 * hf + 2 * j + 1]);
            store128<OAUX>(c, ro, ov + 256 * 2048 + 32 * hf);
        }
        UNROLL for (int hf = 0; hf < 2; ++hf) {
            u4 a;
            UNROLL for (int j = 0; j < 4; ++j) a[j] = pkb(zi[8 * hf + 2 * j], zi[8 * hf + 2 * j + 1]);
            store128<OAUX>(a, ro, ov + 32 * hf);
        }
    }
    STAMP(15);
    vd = lwait(lds, L_VD, 1u);
    if (pass == 1 || !(vd & 0x20000u)) {
        if (b == 0 && t == 0) __builtin_amdgcn_raw_buffer_store_b32(tags.z, rws, 0, WS_E, SC1);
        break;
    }
    lset(lds, L_C6 + wv, 2u, lane);
    lwait4(lds, L_C6, 2u);
    VMWAIT(0);
    }
    STAMP_OUT();
}

extern "C" void kernel_launch(void* const* d_in, const int* in_sizes, int n_in,
                              void* d_out, int out_size, void* d_ws, size_t ws_size,
                              hipStream_t stream) {
    gfnet_fused<<<256, 512, 0, stream>>>(d_in[0], d_in[6], d_in[1], d_in[2], d_in[4], d_in[5], (char*)d_ws, d_out);
}
```

```cpp
#define DIAG 4
#include <hip/hip_runtime.h>
#include <hip/hip_bf16.h>
#include <stdint.h>

typedef _Float16 h2 __attribute__((ext_vector_type(2)));
typedef _Float16 h8 __attribute__((ext_vector_type(8)));
typedef __bf16 b2 __attribute__((ext_vector_type(2)));
typedef __bf16 b8 __attribute__((ext_vector_type(8)));
typedef float f2 __attribute__((ext_vector_type(2)));
typedef float f4 __attribute__((ext_vector_type(4)));
typedef float f16v __attribute__((ext_vector_type(16)));
typedef short s4 __attribute__((ext_vector_type(4)));
typedef unsigned u32;
typedef u32 u2 __attribute__((ext_vector_type(2)));
typedef u32 u4 __attribute__((ext_vector_type(4)));
typedef unsigned short us2 __attribute__((ext_vector_type(2)));
#define DI __device__ __forceinline__
#define UNROLL _Pragma("unroll")
#define NOUNROLL _Pragma("unroll 1")
#define LAS __attribute__((address_space(3)))

#define W0AUX 0
#define W1AUX 0
#define VDELAY 0
#define ZDELAY 0
#define WFIRST 0
#define VSTAG 0
#define GEARLY 0
#define ZGATE 0
#define VGATE 0
#define G2DELAY 50
#define MAXLATE 0
#define FLATE 0
#define FDELAY 24
#define EDGE 1
#define SCAN 0
#define ZHOLD 4
#define ZSLEEP 0
#define XLAST 0
#define XDELAY 0
#define G0 0
#define WWAIT 4
#define DIAGB -1
#define PSLEEP 8
#define WGATE 4
#define W0DELAY 2
#define W0WAIT 0
#define WWIN 12
#define ZWAIT 0
#define VWAIT 0
#define XAUX 2
#define OAUX 2
#define MARGIN 2

#define FSTRIDE 16
constexpr u32 FSTR = FSTRIDE;
constexpr u32 WS_Z = 0;
constexpr u32 WS_V = 4u << 20;
constexpr u32 WS_VL = 12u << 20;
#define HSTR 32
constexpr u32 WS_HB = WS_VL + (32u << 10);
constexpr u32 WS_F = WS_HB + (128u << 10);
constexpr u32 WS_G1 = WS_F + 2048 * FSTR;
constexpr u32 WS_G2 = WS_G1 + 2048 * FSTR;
constexpr u32 WS_E = WS_G2 + 2048 * FSTR;
constexpr u32 WS_BYTES = WS_E + 4096;
constexpr size_t WS_META = 24u << 20;
constexpr int SC1 = 16;
constexpr int SPIN_CAP = 8192;
constexpr int LSPIN_CAP = 1 << 17;
constexpr int OOB = 0x7ffffff0;
constexpr float TRUNC_FIX = 1.00066f;
constexpr u32 TM = 0x00010001u;

template <class T> DI void pin(T& v) { asm volatile("" : "+v"(v)); }
template <class T, int N> DI void pin(T (&v)[N]) { UNROLL for (int i = 0; i < N; ++i) pin(v[i]); }
DI u32 opq(u32 v) { asm("" : "+v"(v)); return v; }
DI u32 pkh(float lo, float hi) { h2 r; r.x = (_Float16)lo; r.y = (_Float16)hi; return __builtin_bit_cast(u32, r); }
DI u32 pkb(float lo, float hi) { b2 r; r.x = (__bf16)lo; r.y = (__bf16)hi; return __builtin_bit_cast(u32, r); }
DI float bflo(u32 v) { return __builtin_bit_cast(float, v << 16); }
DI float bfhi(u32 v) { return __builtin_bit_cast(float, v & 0xffff0000u); }
DI float rbf(float v) { return (float)(__bf16)v; }
DI h2 H(u32 v) { return __builtin_bit_cast(h2, v); }
DI u32 U(h2 v) { return __builtin_bit_cast(u32, v); }
DI f16v mfma32(u4 a, u4 b, f16v c) { return __builtin_amdgcn_mfma_f32_32x32x16_f16(__builtin_bit_cast(h8, a), __builtin_bit_cast(h8, b), c, 0, 0, 0); }
DI f4 mfma16(u4 a, u4 b) { return __builtin_amdgcn_mfma_f32_16x16x32_f16(__builtin_bit_cast(h8, a), __builtin_bit_cast(h8, b), f4{}, 0, 0, 0); }
DI f4 mfma16b(u4 a, u4 b) { return __builtin_amdgcn_mfma_f32_16x16x32_bf16(__builtin_bit_cast(b8, a), __builtin_bit_cast(b8, b), f4{}, 0, 0, 0); }
DI u32 hmul(u32 a, u32 b) { return U(H(a) * H(b)); }
DI u32 hfma(u32 a, u32 b, u32 c) { return U(__builtin_elementwise_fma(H(a), H(b), H(c))); }
DI u32 cmul_swap(u32 x, u32 w) { const h2 a = H(opq(x)), c = H(opq(w)); h2 n = c; n.y = -n.y; return U(__builtin_elementwise_fma(a.yy, n, a.xx * c.yx)); }
DI u32 cmul_conj(u32 x, u32 t) { const h2 a = H(opq(x)), c = H(opq(t)); h2 n = c.yy; n.y = -n.y; return U(__builtin_elementwise_fma(a.yx, n, a * c.xx)); }
DI u32 pkmax(u32 a, u32 b) { return __builtin_bit_cast(u32, __builtin_elementwise_max(__builtin_bit_cast(us2, a), __builtin_bit_cast(us2, b))); }
DI float sinr(float r) { return __builtin_amdgcn_sinf(r); }
DI float cosr(float r) { return __builtin_amdgcn_cosf(r); }
DI int perm32(int n) { return (n & 0x13) | ((n & 4) << 1) | ((n & 8) >> 1); }
DI u32 max3u(u32 a, u32 b, u32 c) { return max(max(a, b), c); }
DI u32 max4u(u4 v) { return max(max(v.x, v.y), max(v.z, v.w)); }
DI u32 min4u(u4 v) { return min(min(v.x, v.y), min(v.z, v.w)); }
DI u32 mark(u32 v, u32 par, u32 tm) { return (par & tm) | (v & ~tm); }
DI u32 rfl(u32 v) { return __builtin_amdgcn_readfirstlane(v); }
DI u32 pow2h(int d) { const float f = __builtin_bit_cast(float, (u32)(127 - min(d, 64)) << 23); return pkh(f, f); }

typedef __amdgpu_buffer_rsrc_t rsrc_t;
DI rsrc_t mkrs(const void* p, u32 bytes) { return __builtin_amdgcn_make_buffer_rsrc((void*)p, (short)0, (int)bytes, 0x00020000); }
template <int AUX> DI void store128(u4 v, rsrc_t r, int off) { __builtin_amdgcn_raw_buffer_store_b128(v, r, off, 0, AUX); }
#define SB() __builtin_amdgcn_sched_barrier(0)
#define MEMBAR() asm volatile("" ::: "memory")
#define VMWAIT(n) asm volatile("s_waitcnt vmcnt(%0)" :: "n"(n) : "memory")
#define STAMP_DECL unsigned long long st_[16] = {}
#define STAMP(i) do { if (i == 0 || i == DIAG % 20) { SB(); asm volatile("s_memrealtime %0\n\ts_waitcnt lgkmcnt(0)" : "=s"(st_[i]) :: "memory"); SB(); } } while (0)
#define STAMP_OUT() do { if ((DIAG < 20) == (wv0 < 4) && (DIAGB < 0 || (int)blockIdx.x == DIAGB)) { while (__builtin_amdgcn_s_memrealtime() - st_[0] < 3000 + 4 * (st_[DIAG % 20] - st_[0])) __builtin_amdgcn_s_sleep(1); } } while (0)
template <int CTRL> DI u32 dpp(u32 v) { return __builtin_amdgcn_update_dpp(0u, v, CTRL, 0xf, 0xf, true); }
DI u32 wave_maxu(u32 v) {
    v = max(v, dpp<0xB1>(v)); v = max(v, dpp<0x4E>(v)); v = max(v, dpp<0x141>(v)); v = max(v, dpp<0x140>(v));
    return max(max((u32)__builtin_amdgcn_readlane(v, 0), (u32)__builtin_amdgcn_readlane(v, 16)), max((u32)__builtin_amdgcn_readlane(v, 32), (u32)__builtin_amdgcn_readlane(v, 48)));
}

DI void core_setup(int lane, int s, u4& tw, u2& m) {
    float fc = (float)perm32(lane & 31) * (1.0f / 1024.0f), p0 = (float)(4 * (lane >> 5) + 8 * s) * fc, p1 = (float)(4 * (lane >> 5) + 8 * (s >> 1) + 2 * (s & 1)) * fc * 32.0f;
    pin(fc); pin(p0); pin(p1);
    float tc[4], ts[4];
    UNROLL for (int i = 0; i < 4; ++i) { const float pt = fmaf((float)i, fc, p0); tc[i] = cosr(pt); ts[i] = sinr(pt); }
    tw = u4{pkh(tc[0], tc[1]), pkh(tc[2], tc[3]), pkh(ts[0], ts[1]), pkh(ts[2], ts[3])};
    const float p2 = fmaf(32.0f, fc, p1);
    m = u2{pkh(cosr(p1), cosr(p2)), pkh(sinr(p1), sinr(p2))};
}
DI void core_expand(int lane, u4 (&MR)[4], u4 (&MI)[4]) {
    const u32 sg = (lane & 1) ? 0x80008000u : 0u;
    MR[2] = MR[0] ^ sg; MR[3] = MR[1] ^ sg;
    UNROLL for (int s = 0; s < 4; ++s) MI[s] = u4{MR[s].z ^ 0x80008000u, MR[s].w ^ 0x80008000u, MR[s].x, MR[s].y};
}
template <bool OUT_B> DI void core_fft(const u4 (&in)[4], const u4 (&MR)[4], const u4 (&MI)[4], const u4 (&TW)[4], f16v& outr, f16v& outi) {
    f16v yr = {}, yi = {};
    UNROLL for (int s = 0; s < 4; ++s) yr = mfma32(in[s], MR[s], yr);
    SB();
    yi = mfma32(in[0], MI[0], yi); yi = mfma32(in[1], MI[1], yi);
    SB();
    h2 t0[8], t1[8];
#define TW_RE(p) do { const h2 pr_ = H(pkh(yr[2 * (p)], yr[2 * (p) + 1])); t0[p] = pr_ * H(TW[(p) >> 1][(p) & 1]); t1[p] = pr_ * H(TW[(p) >> 1][2 + ((p) & 1)]); } while (0)
    TW_RE(0); TW_RE(1); SB();
    yi = mfma32(in[2], MI[2], yi);
    TW_RE(2); TW_RE(3); SB();
    yi = mfma32(in[3], MI[3], yi);
    TW_RE(4); TW_RE(5); TW_RE(6); TW_RE(7); SB();
    outr = f16v{}; outi = f16v{};
    u4 d[4];
#define TW_IM(s) do { UNROLL for (int j = 0; j < 2; ++j) { const int p_ = 2 * (s) + j; const h2 pi_ = H(pkh(yi[2 * p_], yi[2 * p_ + 1])); \
        d[s][j] = U(__builtin_elementwise_fma(pi_, H(TW[s][2 + j]), t0[p_])); d[s][2 + j] = U(__builtin_elementwise_fma(pi_, H(TW[s][j]), -t1[p_])); } } while (0)
    TW_IM(0); SB();
    UNROLL for (int s = 0; s < 4; ++s) {
        outr = OUT_B ? mfma32(MR[s], d[s], outr) : mfma32(d[s], MR[s], outr);
        if (s < 3) TW_IM(s + 1);
        SB();
    }
    UNROLL for (int s = 0; s < 4; ++s) outi = OUT_B ? mfma32(MI[s], d[s], outi) : mfma32(d[s], MI[s], outi);
}
DI u32 rot(float ph) { return pkh(cosr(ph), sinr(ph)); }
DI u32 quarter(u32 r, u32 f) { u32 o; asm("v_pk_mul_f16 %0, %1, %2 op_sel:[1,0] op_sel_hi:[0,1] neg_lo:[1,0]" : "=v"(o) : "v"(r), "v"(f)); return o; }
DI u4 quarter(u4 r) { u32 one = 0x3c003c00u; pin(one); return u4{quarter(r.x, one), quarter(r.y, one), quarter(r.z, one), quarter(r.w, one)}; }

DI u32 tag_of(u32 epoch, u32 idx) { u32 h = (epoch ^ (idx * 0x9E3779B1u)) * 0x85EBCA6Bu; return h ^ (h >> 15); }
DI u32 parity_of(u32 epoch) { return (epoch & 1u) ? 0x00000001u : 0x00010000u; }
DI void flag_store(rsrc_t rws, u32 arr, int b, int lane, u32 payload, u32 tag) {
    __builtin_amdgcn_raw_buffer_store_b64(u2{payload, tag}, rws, lane < 8 ? lane * (int)(256 * FSTR) : OOB, arr + b * FSTR, SC1);
}
DI u4 flag_wait(rsrc_t rws, u32 arr, int b, int lane, u32 epoch) {
    u2 f[4]; u32 tg[4];
    UNROLL for (int j = 0; j < 4; ++j) tg[j] = tag_of(epoch, arr + 4 * lane + j);
    NOUNROLL for (int spin = 0; spin < SPIN_CAP; ++spin) {
        MEMBAR();
        UNROLL for (int j = 0; j < 4; ++j) f[j] = __builtin_amdgcn_raw_buffer_load_b64(rws, lane * (4 * FSTR) + j * FSTR, arr + (b & 7) * (256 * FSTR), SC1);
        if (__all(f[0].y == tg[0] && f[1].y == tg[1] && f[2].y == tg[2] && f[3].y == tg[3])) break;
        __builtin_amdgcn_s_sleep(PSLEEP);
    }
    return u4{f[0].x, f[1].x, f[2].x, f[3].x};
}
DI int cls_of(int b) { const int row = (b & 7) >> 1; return row == 0 ? 4 * (b >> 7) : ((b & 1) ? 8 - row : row); }
DI int tile_of(int b) { return (b & 6) == 0 ? 16 * (b & 1) + ((b >> 3) & 15) : b >> 3; }
DI bool is_helper(int b) { return tile_of(b) == 31; }

constexpr int L_C1 = 0, L_C3 = 4, L_C4 = 8, L_C5 = 12, L_C6 = 16;
constexpr int L_EW = 20;
constexpr int L_WW = 24;
constexpr int L_SW = 28;
constexpr int L_PR = 32;
constexpr int L_SX = 36;
constexpr int L_ZL = 37;
constexpr int L_VD = 38;
constexpr int L_G1 = 39, L_G2 = 40, L_P2 = 41;
constexpr int L_LD = 44;
constexpr int L_GS = 64;
constexpr int L_M = 192;
constexpr int L_A1 = L_M + 1536;
constexpr int L_A2 = L_A1 + 4096;
constexpr int L_B = L_A2 + 4096;
constexpr int L_W = L_B + 8192;
constexpr int L_END = L_W + 64 * 256;

DI void lset(u32* lds, int idx, u32 v, int lane) { MEMBAR(); if (lane == 0) lds[idx] = v; MEMBAR(); }
DI u32 lget(u32* lds, int idx) { MEMBAR(); return rfl(lds[idx]); }
DI u32 lwait(u32* lds, int idx, u32 atleast) {
    u32 v = 0;
    NOUNROLL for (int spin = 0; spin < LSPIN_CAP; ++spin) { v = lget(lds, idx); if (v >= atleast) break; __builtin_amdgcn_s_sleep(1); }
    MEMBAR();
    return v;
}
DI u32 lwait4(u32* lds, int idx, u32 atleast) {
    u4 v;
    NOUNROLL for (int spin = 0; spin < LSPIN_CAP; ++spin) { MEMBAR(); v = *(const u4*)&lds[idx]; if (rfl(min4u(v)) >= atleast) break; __builtin_amdgcn_s_sleep(1); }
    MEMBAR();
    return rfl(max4u(v));
}
struct MagMax {
    h2 hi = {}, lo = {};
    DI void add(u32 a, u32 c) {
        hi = __builtin_elementwise_maximum(__builtin_elementwise_maximum(hi, H(a)), H(c));
        lo = __builtin_elementwise_minimum(__builtin_elementwise_minimum(lo, H(a)), H(c));
    }
    DI u32 exponent() const {
        const u32 mx = pkmax(U(hi) & 0x7fff7fffu, U(lo) & 0x7fff7fffu);
        return wave_maxu(max(mx & 0xffffu, mx >> 16)) >> 7;
    }
};
DI u32 clampe(u32 e) { return min(max(e, 8u), 240u); }

__global__ __launch_bounds__(512, 2) void gfnet_fused(const void* x, const void* gw, const void* war, const void* wai, const void* wbr, const void* wbi,
                                                      char* ws, void* out) {
    __shared__ __attribute__((aligned(16))) u32 lds[L_END];
    STAMP_DECL;
    STAMP(0);
    asm volatile("" :: "s"(x), "s"(gw), "s"(war), "s"(wai), "s"(wbr), "s"(wbi), "s"(ws), "s"(out));
    const int wv0 = __builtin_amdgcn_readfirstlane(threadIdx.x >> 6);
    const rsrc_t rws = mkrs(ws, WS_BYTES);

    if (wv0 >= 4) {
        const int wv = wv0, b = blockIdx.x, cls = cls_of(b), tile = tile_of(b);
        const int lane = threadIdx.x & 63, q = lane >> 4, n = lane & 15;
        const rsrc_t rw = mkrs(wv == 4 ? war : wv == 5 ? wai : wv == 6 ? wbr : wbi, 2048 * 1026);
        const int wvo = (lane >> 2) * (128 * 1026) + min(lane & 3, 2) * 16, wso = (cls * 513 + 16 * tile - cls) * 2;
#define WLOAD(r) do { UNROLL for (int st = 0; st < 4; ++st) { const int c_ = 4 * st + ((tile + (r)) & 3); \
                __builtin_amdgcn_raw_ptr_buffer_load_lds(rw, (LAS void*)&lds[L_W + (c_ * 4 + wv - 4) * 256], 16, wvo, wso + c_ * 8208, 0, (r) == 0 ? W0AUX : W1AUX); } SB(); } while (0)
        if (wv == 4) { lds[lane] = 0u; asm volatile("s_waitcnt lgkmcnt(0)" ::: "memory"); }
        if (WFIRST) { __builtin_amdgcn_s_sleep(W0DELAY); WLOAD(0); }
        MEMBAR();
        __builtin_amdgcn_s_barrier();
        MEMBAR();
        const bool helper = wv == 7 && is_helper(b);
        u4 GR, GI, FR, FI, HR, HI, T1, T2;
        u32 ev;
        {
            if (!WFIRST) { __builtin_amdgcn_s_sleep(W0DELAY); if (!SCAN) WLOAD(0); }
            const bool split = (b & 6) == 0;
            const int er = split ? 16 * (tile & 15) + ((lane >> 1) & 15) : 8 * tile + ((lane >> 1) & 7);
            const int ec = (split ? (tile & 16) * 16 : 0) - cls + ((lane & 1) ? (split ? 262 : 518) : SCAN ? (split ? 256 : 512) : 0);
            const int sr = split ? 16 * (tile & 15) + (lane >> 2) : 8 * tile + (lane >> 3);
            const int scn = (split ? (tile & 16) * 16 + (lane & 3) * 64 : (lane & 7) * 64) - cls;
            u32 scan = SCAN ? __builtin_amdgcn_raw_buffer_load_b32(rw, ((cls + 8 * sr) * 513 + scn) * 2, 0, W0AUX) : 0u;
            u32 edge = EDGE ? __builtin_amdgcn_raw_buffer_load_b32(rw, ((cls + 8 * er) * 513 + ec) * 2, 0, W0AUX) : 0u;
            ev = __builtin_amdgcn_raw_buffer_load_b32(rws, 0, WS_E, SC1);
            const u4 smp = __builtin_amdgcn_raw_buffer_load_b128(rw, (lane >> 3) * (2 * 131328 + 128) + (lane & 7) * 16, 0, 0);
            MEMBAR();
            if (helper) {
            UNROLL for (int j = 0; j < 4; ++j) {
                GR[j] = rot((float)((4 * q + j) * (cls + 8 * n)) * (1.0f / 128.0f) + (cls == 4 ? 0.25f : 0.0f));
                HR[j] = rot((float)(n * (cls + 8 * (4 * q + j))) * (1.0f / 128.0f));
                FR[j] = rot((float)(n * (4 * q + j)) * (1.0f / 16.0f));
                T1[j] = rot((float)((4 * q + j) * (cls + 8 * n)) * (1.0f / 2048.0f));
                T2[j] = rot((float)(n * (cls + 8 * (4 * q + j))) * (1.0f / 2048.0f));
            }
            GI = quarter(GR); HI = quarter(HR); FI = quarter(FR);
            pin(GR); pin(GI); pin(HR); pin(HI); pin(FR); pin(FI); pin(T1); pin(T2);
            }
            VMWAIT(W0WAIT);
            STAMP(1);
            if (SCAN) { WLOAD(4); VMWAIT(WWIN); }
            const u32 lda = (u32)(size_t)(LAS u32*)&lds[L_LD + wv - 4];
            if (ZHOLD == 1) asm volatile("ds_write_b32 %0, %1" :: "v"(lda), "v"(1u) : "memory");
            WLOAD(1); VMWAIT(WWIN);
            if (ZHOLD == 2) asm volatile("ds_write_b32 %0, %1" :: "v"(lda), "v"(1u) : "memory");
            WLOAD(2); VMWAIT(WWIN);
            if (ZHOLD == 3) asm volatile("ds_write_b32 %0, %1" :: "v"(lda), "v"(1u) : "memory");
            WLOAD(3);
            if (ZHOLD == 4) asm volatile("ds_write_b32 %0, %1" :: "v"(lda), "v"(1u) : "memory");
            pin(edge); pin(scan);
            MagMax ms; ms.add(smp.x, smp.y); ms.add(smp.z, smp.w);
            const u32 es = clampe(ms.exponent() + MARGIN);
            VMWAIT(0);
            lset(lds, L_PR + wv - 4, es, lane);
            STAMP(2);
        }
        const u32 ep0 = rfl(ev) + 1u;
        if (wv == 5 || wv == 6) {
            lwait4(lds, L_PR, 1u);
            if (MAXLATE) lwait(lds, L_P2, 1u);
            MagMax m;
            UNROLL for (int c = 0; c < 32; ++c) { const u4 v = *(const u4*)&lds[L_W + ((wv - 5) * 32 + c) * 256 + lane * 4]; m.add(v.x, v.y); m.add(v.z, v.w); }
            const u32 e = clampe(m.exponent());
            MEMBAR(); if (lane == 0) *(u2*)&lds[L_WW + 2 * (wv - 5)] = u2{e, e}; MEMBAR();
        }
        if (VGATE && wv == 6) {
            __builtin_amdgcn_s_setprio(2);
            lwait(lds, L_P2, 1u);
            __builtin_amdgcn_s_sleep(G2DELAY);
            flag_wait(rws, WS_G2, b, lane, ep0);
            lset(lds, L_G2, 1u, lane);
            STAMP(5);
        }
        if (wv == 4) {
            if (ZGATE) { flag_wait(rws, WS_G1, b, lane, ep0); lset(lds, L_G1, 1u, lane); }
            const u32 ex = lwait4(lds, L_EW, 1u), ew = lwait4(lds, L_WW, 1u);
            if (FLATE) lwait(lds, L_P2, 1u);
            flag_store(rws, WS_F, b, lane, ex | (ew << 8), tag_of(ep0, WS_F + b));
            lwait(lds, L_P2, 1u);
            __builtin_amdgcn_s_sleep(FDELAY);
            const u4 f = flag_wait(rws, WS_F, b, lane, ep0);
            const u32 gx = wave_maxu(max4u(f & 0xffu)), gwm = wave_maxu(max4u((f >> 8) & 0xffu));
            const bool redo = gx > lget(lds, L_SX) || gwm > lwait4(lds, L_PR, 1u);
            lset(lds, L_VD, gx | (gwm << 8) | (redo ? 0x20000u : 0x10000u), lane);
            STAMP(5);
        } else if (helper) {
            __builtin_amdgcn_s_setprio(2);
            u32 ew = lwait4(lds, L_PR, 1u);
            NOUNROLL for (u32 pass = 0; pass < 2; ++pass) {
                u4 WH[2];
                const float sw = __builtin_bit_cast(float, (249u - ew) << 23);
                UNROLL for (int r = 0; r < 4; ++r) UNROLL for (int f = 0; f < 2; ++f) {
                    const unsigned short* wp = (const unsigned short*)&lds[L_W + (n * 4 + 2 * f) * 256 + (4 * q + r) * 16] + 16 + cls;
                    float re = bflo(wp[0]) * sw, im = bflo(wp[512]) * sw;
                    pin(re);
                    WH[f][r] = pkh(re, im);
                }
                pin(WH);
                STAMP(3);
                lwait(lds, L_ZL, pass + 1u);
                const u32 tg = tag_of(ep0 + pass, 0u), par = parity_of(ep0 + pass);
                u4 hz;
                NOUNROLL for (int spin = 0; spin < SPIN_CAP; ++spin) {
                    MEMBAR();
                    u32 bad = 0;
                    UNROLL for (int j = 0; j < 4; ++j) { const u2 g = __builtin_amdgcn_raw_buffer_load_b64(rws, (64 * q + n + 16 * j) * HSTR, WS_HB + ((b & 7) >> 1) * (256 * HSTR), SC1); hz[j] = g.x & ~TM; bad |= g.y ^ tg; }
                    if (__all(bad == 0u)) break;
                    __builtin_amdgcn_s_sleep(4);
                }
                STAMP(4);
                const u32 f16 = cls == 0 ? 0x00002c00u : cls == 4 ? 0x2c000000u : cls > 4 ? 0xac002c00u : 0x2c002c00u;
                u4 d, o[2];
                UNROLL for (int j = 0; j < 4; ++j) d[j] = hmul(hz[j], f16);
                f4 yr = mfma16(d, GR), yi = mfma16(d, GI);
                UNROLL for (int r = 0; r < 4; ++r) d[r] = cmul_conj(pkh(yr[r], yi[r]), T1[r]);
                const f4 xr = mfma16(FR, d), xi = mfma16(FI, d);
                UNROLL for (int f = 0; f < 2; ++f) {
                    UNROLL for (int r = 0; r < 4; ++r) d[r] = cmul_swap(pkh(xr[r], xi[r]), WH[f][r]);
                    yr = mfma16(d, FR); yi = mfma16(d, FI);
                    UNROLL for (int r = 0; r < 4; ++r) o[f][r] = cmul_conj(pkh(yr[r], yi[r]), T2[r]);
                }
                const f4 p0 = mfma16(HR, o[0]), p1 = mfma16(HI, o[0]), p2 = mfma16(HR, o[1]), p3 = mfma16(HI, o[1]);
                UNROLL for (int r = 0; r < 4; ++r)
                    store128<SC1>(u4{mark(pkh(p0[r], p1[r]), par, TM), tg, mark(pkh(p2[r], p3[r]), par, TM), tg}, rws, (64 * q + n) * 16 + WS_VL + r * 256 + cls * 4096);
                STAMP(5);
                const u32 vd = lwait(lds, L_VD, 1u);
                if (!(vd & 0x20000u)) break;
                ew = (vd >> 8) & 0xffu;
            }
        }
        STAMP_OUT();
        return;
    }

    u32 vd = 0;
    NOUNROLL for (u32 pass = 0; pass < 2; ++pass) {
    int t = threadIdx.x, wv = wv0, b = blockIdx.x;
    pin(t);
    asm volatile("" : "+s"(wv));
    asm volatile("" : "+s"(b));
    const int lane = t & 63, q = lane >> 4, n = lane & 15;
    const u32 pv = pass + 1u;
    if (XLAST && pass == 0) { __builtin_amdgcn_s_barrier(); MEMBAR(); __builtin_amdgcn_s_setprio(1); if (XDELAY) __builtin_amdgcn_s_sleep(XDELAY); }
    u2 xv[8];
    const rsrc_t rx = mkrs(x, 2048 * 2048);
    const u4 smp = __builtin_amdgcn_raw_buffer_load_b128(rx, (lane >> 3) * (524288 + 2048 + 256) + (lane & 7) * 16, 0, 0);
    UNROLL for (int s1 = 0; s1 < 8; ++s1) xv[s1] = __builtin_amdgcn_raw_buffer_load_b64(rx, t * 8, (256 * s1 + b) * 2048, XAUX);
    const u32 ev = __builtin_amdgcn_raw_buffer_load_b32(rws, 0, WS_E, SC1);
    const u2 gv = __builtin_amdgcn_raw_buffer_load_b64(mkrs(gw, 2048), t * 8, 0, 0);
    MEMBAR();
    SB();
    if (!XLAST && pass == 0) { __builtin_amdgcn_s_barrier(); MEMBAR(); __builtin_amdgcn_s_setprio(1); }
    u4 MR[4], MI[4], TW[4];
    {
        u4 tw; u2 m;
        core_setup(lane, wv, tw, m);
        *(u4*)&lds[L_M + (2 + wv) * 256 + lane * 4] = tw;
        lds[L_M + (wv >> 1) * 256 + lane * 4 + (wv & 1)] = m.x; lds[L_M + (wv >> 1) * 256 + lane * 4 + 2 + (wv & 1)] = m.y;
    }
    const int zc = perm32(lane & 31) + (lane >> 5) * 256, zcu = zc + ((lane & 31) == 0 ? 543 : 511);
    int zl = (zc >> 4) * 16384 + (zc & 15) * 4, zu = (zcu >> 4) * 16384 + (zcu & 15) * 4;
    int zh = lane == 0 ? 0 : OOB;
    int tro = L_A1 + wv * 1024 + (4 * (lane >> 5) + ((lane & 15) >> 2)) * 16 + ((lane >> 4) & 1) * 8 + (lane & 3) * 2;
    u32 tm = TM;
    pin(zl); pin(zu); pin(zh); pin(tro); pin(tm);
    STAMP(1);
    SB();
    u32 ex;
    {
        MagMax ms; ms.add(smp.x, smp.y); ms.add(smp.z, smp.w);
        ex = pass ? (vd & 0xffu) : clampe(ms.exponent() + MARGIN);
    }
    STAMP(2);
    u4 tags;
    u32 z[16], z512;
    {
        const float sc = __builtin_bit_cast(float, (250u - ex) << 23);
        const float rs = sc * 0.70710678118654752f;
        u32 o[8][2];
        UNROLL for (int j = 0; j < 2; ++j) {
            f2 v[8];
            UNROLL for (int s1 = 0; s1 < 8; ++s1) v[s1] = f2{bflo(xv[s1][j]), bfhi(xv[s1][j])};
            const f2 a0 = v[0] + v[4], a1 = v[1] + v[5], a2 = v[2] + v[6], a3 = v[3] + v[7];
            const f2 b0 = (v[0] - v[4]) * sc, b1 = v[1] - v[5], b2_ = (v[2] - v[6]) * sc, b3 = v[3] - v[7];
            const f2 e0 = a0 + a2, e1 = (a1 + a3) * sc, dm = b1 - b3, dp = b1 + b3;
            f2 r[8];
            r[0] = e0 * sc + e1; r[1] = e0 * sc - e1;
            r[2] = dm * rs + b0; r[3] = -(dp * rs) - b2_;
            r[4] = (a0 - a2) * sc; r[5] = (a3 - a1) * sc;
            r[6] = b0 - dm * rs; r[7] = b2_ - dp * rs;
            UNROLL for (int p = 0; p < 8; ++p) o[p][j] = pkh(r[p].x, r[p].y);
        }
        UNROLL for (int p = 0; p < 8; ++p) *(u2*)&lds[L_A1 + p * 512 + 2 * t] = u2{o[p][0], o[p][1]};
        lset(lds, L_C1 + wv, pv, lane);
        const u32 epoch = rfl(ev) + pv;
        tags = u4{parity_of(epoch), 0u, epoch, tag_of(epoch, 0u)};
        u4 in[4];
        NOUNROLL for (int spin = 0; spin < LSPIN_CAP; ++spin) {
            MEMBAR();
            const u4 c = *(const u4*)&lds[L_C1];
            MEMBAR();
            MR[0] = *(const u4*)&lds[L_M + lane * 4]; MR[1] = *(const u4*)&lds[L_M + 256 + lane * 4];
            UNROLL for (int s = 0; s < 4; ++s) {
                const u2 re = __builtin_bit_cast(u2, __builtin_amdgcn_ds_read_tr16_b64_v4i16((LAS s4*)&lds[tro + s * 128]));
                const u2 im = __builtin_bit_cast(u2, __builtin_amdgcn_ds_read_tr16_b64_v4i16((LAS s4*)&lds[tro + s * 128 + 512]));
                in[s] = u4{re.x, re.y, im.x, im.y};
            }
            pin(MR[0]); pin(MR[1]);
            if (rfl(min4u(c)) >= pv) break;
        }
        UNROLL for (int i = 0; i < 4; ++i) TW[i] = *(const u4*)&lds[L_M + (2 + i) * 256 + lane * 4];
        STAMP(3);
        if (ZGATE && pass == 0 && wv == 0) flag_store(rws, WS_G1, b, lane, 0u, tag_of(tags.z, WS_G1 + b));
        core_expand(lane, MR, MI);
        f16v zr, zi;
        core_fft<true>(in, MR, MI, TW, zr, zi);
        UNROLL for (int r = 0; r < 16; ++r) z[r] = mark(pkh(zr[r], zi[r]), tags.x, tm);
        z512 = z[8];
        const u32 za = __builtin_amdgcn_readlane(z[8], 32), zb = __builtin_amdgcn_readlane(z[0], 0);
        MEMBAR();
        const u32 zt = lane < 32 ? za : zb;
        UNROLL for (int r = 8; r < 16; ++r) z[r] = (lane & 31) == 0 ? (r < 15 ? z[r + 1] : zt) : z[r];
    }
    pin(z);
    STAMP(4);
    const int zs = WS_Z + wv * 64 * 16384 + (((b >> 2) & 3) * 64 + (b & 3) * 16 + ((b >> 4) & 3) * 4 + (b >> 6)) * 64;
    bool zsent = false;
#define TRY_STORE_Z(force) do { if (!zsent) { \
        bool go = true; \
        if (force) lwait4(lds, L_LD, 1u); else { MEMBAR(); go = rfl(min4u(*(const u4*)&lds[L_LD])) != 0u; } \
        if (go) { \
            UNROLL for (int r = 0; r < 16; ++r) __builtin_amdgcn_raw_buffer_store_b32(z[r], rws, r < 8 ? zl : zu, zs + (r & 7) * 32768, SC1); \
            __builtin_amdgcn_raw_buffer_store_b64(u2{z512, tags.w}, rws, zh, WS_HB + (wv * 256 + b) * HSTR, SC1); \
            zsent = true; } } SB(); } while (0)
    TRY_STORE_Z(false);
    const int row = (b & 7) >> 1, cls = cls_of(b), tile = tile_of(b);
    const bool packed = row == 0;
    u4 FR, FI, GR[4], GI[4], HR[4], HI[4], sel1;
    {
        const float ofs = cls == 4 ? 0.25f : 0.0f;
        float gu = (float)(cls + 8 * n) * (1.0f / 2048.0f), g0 = (float)(64 * q + 4 * wv) * gu + ofs;
        u32 s16 = 0x2c002c00u;
        pin(gu); pin(g0); pin(s16);
        UNROLL for (int i = 0; i < 4; ++i) UNROLL for (int j = 0; j < 4; ++j) { const u32 g = rot(fmaf((float)(16 * j + i), gu, g0)); GR[i][j] = hmul(g, s16); GI[i][j] = quarter(g, s16); }
    }
    pin(GR); pin(GI);
    {
        MagMax m;
        UNROLL for (int s1 = 0; s1 < 8; ++s1) m.add(xv[s1].x, xv[s1].y);
        if (wv == 0) lset(lds, L_SX, ex, lane);
        lset(lds, L_EW + wv, clampe(m.exponent()), lane);
    }
    TRY_STORE_Z(false);
    UNROLL for (int j = 0; j < 4; ++j) FR[j] = rot((float)(n * (4 * q + j)) * (1.0f / 16.0f));
    FI = quarter(FR);
    {
        float hs[4], hu[4];
        UNROLL for (int i = 0; i < 4; ++i) { hs[i] = (float)(16 * n + 4 * wv + i) * (1.0f / 2048.0f); hu[i] = (float)(cls + 32 * q + 8 * i); }
        pin(hs); pin(hu);
        UNROLL for (int i = 0; i < 4; ++i) { UNROLL for (int j = 0; j < 4; ++j) { float a = hs[i] * hu[j]; pin(a); HR[i][j] = rot(a); } HI[i] = quarter(HR[i]); }
        const int d = n + cls - 8 * q;
        UNROLL for (int j = 0; j < 4; ++j) sel1[j] = (d >> 1) == j ? ((d & 1) ? 0xffff0000u : 0xffffu) : 0u;
    }
    pin(FR); pin(FI); pin(HR); pin(HI); pin(sel1);
    TRY_STORE_Z(true);
    STAMP(5);
    VMWAIT(WWAIT);
    u4 WA[4], WB[4];
    u32 ew = 0;
    UNROLL for (int i = 0; i < 4; ++i) asm volatile("" : "=v"(WA[i]), "=v"(WB[i]));
#define WIDEN(es) do { \
        u4 wr[4][4]; \
        int wl = L_W / 4 + wv * 1024 + n * 4 + min(q, 2); pin(wl); \
        UNROLL for (int i = 0; i < 4; ++i) UNROLL for (int a = 0; a < 4; ++a) wr[i][a] = ((const u4*)lds)[wl + (i * 4 + a) * 64]; \
        ew = pass ? (vd >> 8) & 0xffu : (es); \
        const u32 sw = ((249u - ew - (packed ? 1u : 0u)) << 7) * 0x10001u;       \
        const u4 sels = sel1 & sw; \
        SB(); \
        f4 wf[4][4]; \
        UNROLL for (int k = 0; k < 10; ++k) { \
            if (k < 8) { wf[k >> 1][2 * (k & 1)] = mfma16b(wr[k >> 1][2 * (k & 1)], sels); wf[k >> 1][2 * (k & 1) + 1] = mfma16b(wr[k >> 1][2 * (k & 1) + 1], sels); } \
            if (k >= 2) { const int i_ = (k - 2) >> 1, f_ = k & 1; UNROLL for (int r = 0; r < 4; ++r) (f_ ? WB : WA)[i_][r] = pkh(wf[i_][2 * f_][r], wf[i_][2 * f_ + 1][r]); } \
            SB(); } \
        pin(WA); pin(WB); } while (0)
    MEMBAR();
    const u4 pr0 = *(const u4*)&lds[L_PR];
    const bool wearly = rfl(min4u(pr0)) != 0u;
    MEMBAR();
    if (wearly) WIDEN(rfl(max4u(pr0)));
    STAMP(6);
    u32 z1[4][4], z2[4][4];
    const int zvm = q * 64 + (63 - tile) * 16384 + (15 - n) * 4;
    const int zv1 = cls <= 4 ? q * 64 + tile * 16384 + n * 4 : zvm;
    int zso = WS_Z + row * 64 * 16384 + wv * 4096;
    asm volatile("" : "+s"(zso));
#define LOAD_Z() do { \
        if (packed) { int a_ = zvm; pin(a_); UNROLL for (int i = 0; i < 4; ++i) UNROLL for (int j = 0; j < 4; ++j) z2[i][j] = __builtin_amdgcn_raw_buffer_load_b32(rws, a_ + (4 * i + j) * 256, zso, SC1); } \
        else { UNROLL for (int i = 0; i < 4; ++i) UNROLL for (int j = 0; j < 4; ++j) z2[i][j] = tags.x; } \
        int c_ = zv1; pin(c_); UNROLL for (int i = 0; i < 4; ++i) UNROLL for (int j = 0; j < 4; ++j) z1[i][j] = __builtin_amdgcn_raw_buffer_load_b32(rws, c_ + (4 * i + j) * 256, zso, SC1); } while (0)
    if (ZGATE) lwait(lds, L_G1, 1u);
    VMWAIT(ZWAIT);
    if (ZDELAY) __builtin_amdgcn_s_sleep(ZDELAY);
    LOAD_Z();
    if (wv == 0) lset(lds, L_ZL, pv, lane);
    SB();
    STAMP(7);
    u32 x1 = tags.x ^ (cls > 4 ? 0x80000000u : 0u), x2 = tags.x ^ (cls == 4 ? 0x00008000u : cls == 0 ? 0x80000000u : 0u);
    const int lw = ((wv * 16 + 4 * q) * 16 + n) * 4;
    int lr = (q * 16 + 4 * wv) * 16 + n;
    const u4* l4 = (const u4*)lds;
    pin(GR); pin(GI); pin(x1); pin(x2); pin(lr);
    STAMP(8);
    SB();
    {
        f4 yr[4], yi[4];
        NOUNROLL for (int spin = 0; spin < SPIN_CAP; ++spin) {
            u32 m = 0;
            UNROLL for (int i = 0; i < 4; ++i) {
                u4 d;
                UNROLL for (int j = 0; j < 4; ++j) { z1[i][j] ^= x1; z2[i][j] ^= x2; }
                UNROLL for (int j = 0; j < 4; ++j) d[j] = U(H(z1[i][j]) + H(z2[i][j]));
                UNROLL for (int j = 0; j < 4; ++j) m |= z1[i][j] | z2[i][j];
                yr[i] = mfma16(GR[i], d); yi[i] = mfma16(GI[i], d);
                SB();
            }
            if (__all((m & TM) == 0u)) break;
            MEMBAR();
            LOAD_Z();
        }
        STAMP(9);
        u4 y[4];
        UNROLL for (int i = 0; i < 4; ++i) UNROLL for (int r = 0; r < 4; ++r) y[r][i] = pkh(yr[i][r], yi[i][r]);
        UNROLL for (int r = 0; r < 4; ++r) *(u4*)&lds[L_A2 + lw + r * 64] = y[r];
        lset(lds, L_C3 + wv, pv, lane);
    }
    if (!wearly) { const u32 es = lwait4(lds, L_PR, 1u); WIDEN(es); }
    {
        u4 d[4];
        NOUNROLL for (int spin = 0; spin < LSPIN_CAP; ++spin) {
            MEMBAR();
            const u4 c = *(const u4*)&lds[L_C3];
            MEMBAR();
            UNROLL for (int i = 0; i < 4; ++i) d[i] = l4[L_A2 / 4 + lr + i * 16];
            pin(d[0]);
            if (rfl(min4u(c)) >= pv) break;
        }
        STAMP(10);
        if (VGATE == 1 && pass == 0 && wv == 0) { flag_store(rws, WS_G2, b, lane, 0u, tag_of(tags.z, WS_G2 + b)); lset(lds, L_P2, 1u, lane); }
        pin(WA); pin(WB);
        f4 xr[4], xi[4];
        u4 xx[4], ya[4], yb[4];
        UNROLL for (int i = 0; i < 4; ++i) {
            xr[i] = mfma16(FR, d[i]); xi[i] = mfma16(FI, d[i]);
            if (i >= 2) { UNROLL for (int r = 0; r < 4; ++r) xx[i - 2][r] = pkh(xr[i - 2][r], xi[i - 2][r]); pin(xx[i - 2]); }
            SB();
        }
        UNROLL for (int r = 0; r < 4; ++r) ya[0][r] = cmul_swap(xx[0][r], WA[0][r]);
        UNROLL for (int i = 2; i < 4; ++i) { UNROLL for (int r = 0; r < 4; ++r) xx[i][r] = pkh(xr[i][r], xi[i][r]); pin(xx[i]); }
        SB();
        f4 p0[4], p1[4], p2[4], p3[4];
        UNROLL for (int i = 0; i < 4; ++i) {
            p0[i] = mfma16(FR, ya[i]); p1[i] = mfma16(FI, ya[i]);
            if (i < 3) { UNROLL for (int r = 0; r < 4; ++r) ya[i + 1][r] = cmul_swap(xx[i + 1][r], WA[i + 1][r]); }
            else { UNROLL for (int r = 0; r < 4; ++r) yb[0][r] = cmul_swap(xx[0][r], WB[0][r]); }
            SB();
        }
        u4 ra[4], rb[4];
        UNROLL for (int i = 0; i < 4; ++i) {
            p2[i] = mfma16(FR, yb[i]); p3[i] = mfma16(FI, yb[i]);
            if (i < 3) { UNROLL for (int r = 0; r < 4; ++r) yb[i + 1][r] = cmul_swap(xx[i + 1][r], WB[i + 1][r]); }
            UNROLL for (int k = 0; k < 4; ++k) ra[i][k] = pkh(p0[k][i], p1[k][i]);
            *(u4*)&lds[L_B + lw + i * 64] = ra[i];
            SB();
        }
        UNROLL for (int i = 0; i < 3; ++i) UNROLL for (int r = 0; r < 4; ++r) rb[r][i] = pkh(p2[i][r], p3[i][r]);
        SB();
        UNROLL for (int r = 0; r < 4; ++r) { rb[r][3] = pkh(p2[3][r], p3[3][r]); *(u4*)&lds[L_B + 4096 + lw + r * 64] = rb[r]; }
        lset(lds, L_C4 + wv, pv, lane);
    }
    STAMP(11);
    if (VGATE == 2 && pass == 0 && wv == 0) { flag_store(rws, WS_G2, b, lane, 0u, tag_of(tags.z, WS_G2 + b)); lset(lds, L_P2, 1u, lane); }
    {
        u4 da[4], db[4];
        NOUNROLL for (int spin = 0; spin < LSPIN_CAP; ++spin) {
            MEMBAR();
            const u4 c = *(const u4*)&lds[L_C4];
            MEMBAR();
            UNROLL for (int i = 0; i < 4; ++i) { da[i] = l4[L_B / 4 + lr + i * 16]; db[i] = l4[L_B / 4 + 1024 + lr + i * 16]; }
            pin(da[0]);
            if (rfl(min4u(c)) >= pv) break;
        }
        const int vo = 64 * q * 32768 + n * 8, so = WS_V + 4 * wv * 32768 + cls * 4096 + tile * 128;
        f4 p0[4], p1[4], p2[4], p3[4];
        UNROLL for (int i = 0; i < 5; ++i) {
            if (i < 4) { p0[i] = mfma16(HR[i], da[i]); p1[i] = mfma16(HI[i], da[i]); p2[i] = mfma16(HR[i], db[i]); p3[i] = mfma16(HI[i], db[i]); }
            if (i > 0) { UNROLL for (int r = 0; r < 4; ++r)
                __builtin_amdgcn_raw_buffer_store_b64(u2{mark(pkh(p0[i - 1][r], p1[i - 1][r]), tags.x, tm), mark(pkh(p2[i - 1][r], p3[i - 1][r]), tags.x, tm)}, rws, vo, so + (16 * r + i - 1) * 32768, SC1); }
            SB();
        }
    }
    if (wv == 0) lset(lds, L_P2, 1u, lane);
    STAMP(12);
    SB();
    {
        pin(xv);
        const f2 g01 = {bflo(gv.x), bfhi(gv.x)}, g23 = {bflo(gv.y), bfhi(gv.y)};
        f4 acc = {};
        UNROLL for (int i = 0; i < 4; ++i) {
            u4 pp;
            UNROLL for (int k = 0; k < 2; ++k) {
                const f2 a = f2{bflo(xv[2 * i + k].x), bfhi(xv[2 * i + k].x)} * g01, c = f2{bflo(xv[2 * i + k].y), bfhi(xv[2 * i + k].y)} * g23;
                pp[2 * k] = pkb(a.x, a.y); pp[2 * k + 1] = pkb(c.x, c.y);
            }
            const u32 s0 = n == 2 * i ? 0x3f803f80u : 0u, s1 = n == 2 * i + 1 ? 0x3f803f80u : 0u;
            acc = __builtin_amdgcn_mfma_f32_16x16x32_bf16(__builtin_bit_cast(b8, pp), __builtin_bit_cast(b8, u4{s0, s0, s1, s1}), acc, 0, 0, 0);
        }
        if (n < 8) lds[L_GS + n * 16 + (t >> 4)] = __builtin_bit_cast(u32, (acc.x + acc.y) + (acc.z + acc.w));
        lset(lds, L_C5 + wv, pv, lane);
    }
    MR[0] = *(const u4*)&lds[L_M + lane * 4]; MR[1] = *(const u4*)&lds[L_M + 256 + lane * 4];
    UNROLL for (int i = 0; i < 4; ++i) TW[i] = *(const u4*)&lds[L_M + (2 + i) * 256 + lane * 4];
    core_expand(lane, MR, MI);
    pin(MR); pin(MI); pin(TW);
    u4 PM;
    {
        lwait4(lds, L_C5, pv);
        float gg[4];
        UNROLL for (int k = 0; k < 2; ++k) {
            const f4* pp = (const f4*)&lds[L_GS + ((n >> 2) * 2 + k) * 16];
            const f4 ps = (pp[0] + pp[1]) + (pp[2] + pp[3]);
            const float lg = rbf((ps.x + ps.y) + (ps.z + ps.w));
            gg[2 * k] = rbf(1.0f / (1.0f + __expf(-lg)));
            gg[2 * k + 1] = rbf(1.0f - gg[2 * k]);
        }
        UNROLL for (int k = 0; k < 4; ++k) { gg[k] *= 0.03125f; pin(gg[k]); }
        const int r = n & 3, sa = (n >> 2) * 2;
        UNROLL for (int cc = 0; cc < 2; ++cc) {
            const int u1 = 2 * q + cc;
            const float ta = (float)(sa * u1) * 0.125f + ((r & 1) ? 0.25f : (r & 2) ? 0.5f : 0.0f);
            const float tb = (float)((sa + 1) * u1) * 0.125f + ((r & 1) ? ((r & 2) ? 0.0f : 0.5f) : 0.25f);
            const float ca = cosr(ta), sa_ = sinr(ta), cb = cosr(tb), sb_ = sinr(tb);
            float e[4] = {gg[2] * cb, gg[2] * sb_, gg[3] * cb, gg[3] * sb_};
            pin(e);
            e[0] = fmaf(gg[0], ca, e[0]); pin(e[0]); e[1] = fmaf(gg[0], sa_, e[1]); pin(e[1]); e[2] = fmaf(gg[1], ca, e[2]); pin(e[2]); e[3] = fmaf(gg[1], sa_, e[3]);
            PM[2 * cc] = pkh(e[0], e[1]); PM[2 * cc + 1] = pkh(e[2], e[3]);
        }
    }
    const float osc = __builtin_bit_cast(float, (u32)min(max((int)ex + (int)ew - 130, 1), 254) << 23) * TRUNC_FIX;
    const int ve = 2 * q * 4096 + (128 * wv + n) * 8;
    int vs0 = WS_V + b * 32768, vs1 = vs0 + 4096, vsl = WS_VL + b * 16;
    asm volatile("" : "+s"(vs0), "+s"(vs1), "+s"(vsl));
    int ov = (4 * perm32(lane & 31) + (lane >> 5)) * 16 + (512 * wv + b) * 2048;
    pin(PM); pin(ov);
    STAMP(13);
    if (VGATE) lwait(lds, L_G2, 1u);
    if (VSTAG) { const int st_ = b & 3; if (st_ == 0) VMWAIT(12); else if (st_ == 1) VMWAIT(8); else if (st_ == 2) VMWAIT(4); else VMWAIT(0); }
    else VMWAIT(VWAIT);
    if (VDELAY) __builtin_amdgcn_s_sleep(VDELAY);
    {
        f4 z[9];
        NOUNROLL for (int spin = 0; spin < SPIN_CAP; ++spin) {
            MEMBAR();
            u2 v0[8], v1[8];
            u4 ve8, vo8;
            int a_ = ve; pin(a_);
            if (wv == 3) {
                ve8 = __builtin_amdgcn_raw_buffer_load_b128(rws, q * 8192, vsl, SC1); vo8 = __builtin_amdgcn_raw_buffer_load_b128(rws, q * 8192 + 4096, vsl, SC1);
            } else {
                const u2 e2 = __builtin_amdgcn_raw_buffer_load_b64(rws, a_ + 1024, vs0, SC1), o2 = __builtin_amdgcn_raw_buffer_load_b64(rws, a_ + 1024, vs1, SC1);
                ve8 = u4{e2.x, e2.y, tags.w, tags.w}; vo8 = u4{o2.x, o2.y, tags.w, tags.w};
            }
            UNROLL for (int i = 0; i < 8; ++i) {
                v0[i] = __builtin_amdgcn_raw_buffer_load_b64(rws, a_ + 128 * i, vs0, SC1);
                v1[i] = __builtin_amdgcn_raw_buffer_load_b64(rws, a_ + 128 * i, vs1, SC1);
            }
            SB();
            u32 m = (((wv == 3 ? ve8.y : ve8.z) ^ tags.w) | (ve8.w ^ tags.w) | ((wv == 3 ? vo8.y : vo8.z) ^ tags.w) | (vo8.w ^ tags.w)) ? TM : 0u;
            UNROLL for (int k = 0; k < 9; ++k) {
                const int i = (k + 8) % 9;
                const u4 d = (i == 8 ? u4{ve8.x, wv == 3 ? ve8.z : ve8.y, vo8.x, wv == 3 ? vo8.z : vo8.y} : u4{v0[i & 7].x, v0[i & 7].y, v1[i & 7].x, v1[i & 7].y}) ^ tags.x;
                m |= d.x | d.y; m |= d.z | d.w;
                z[i] = mfma16(PM, d);
                SB();
            }
            if (__all((m & TM) == 0u)) break;
        }
        STAMP(14);
        if (wv == 0 && n == 0) { z[0].x = 0.5f * (z[0].x + z[0].z); z[0].y = 0.5f * (z[0].y + z[0].w); }
        if (wv == 3) { z[8].z = 0.5f * (z[8].x + z[8].z); z[8].w = 0.5f * (z[8].y + z[8].w); }
        u4* l4w = (u4*)&lds[L_A1];
        UNROLL for (int p = 0; p < 2; ++p)
            l4w[(q * 8 + wv) * 32 + 16 * p + n] = u4{pkh(z[p].x, z[p + 2].x), pkh(z[p + 4].x, z[p + 6].x), pkh(z[p].y, z[p + 2].y), pkh(z[p + 4].y, z[p + 6].y)};
        l4w[(q * 8 + 7 - wv) * 32 + 16 - n] = u4{pkh(z[7].z, z[5].z), pkh(z[3].z, z[1].z), pkh(z[7].w, z[5].w), pkh(z[3].w, z[1].w)};
        f2 m[4];
        UNROLL for (int i = 0; i < 4; ++i) m[i] = n == 0 ? f2{z[8 - 2 * i].z, z[8 - 2 * i].w} : f2{z[6 - 2 * i].z, z[6 - 2 * i].w};
        l4w[(q * 8 + 7 - wv) * 32 + ((32 - n) & 31)] = u4{pkh(m[0].x, m[1].x), pkh(m[2].x, m[3].x), pkh(m[0].y, m[1].y), pkh(m[2].y, m[3].y)};
        lset(lds, L_C6 + wv, 2u * pv - 1u, lane);
    }
    {
        u4 in[4];
        NOUNROLL for (int spin = 0; spin < LSPIN_CAP; ++spin) {
            MEMBAR();
            const u4 c = *(const u4*)&lds[L_C6];
            MEMBAR();
            UNROLL for (int s = 0; s < 4; ++s) in[s] = *(const u4*)&lds[L_A1 + (wv * 256 + s * 64 + lane) * 4];
            pin(in[0]);
            if (rfl(min4u(c)) >= 2u * pv - 1u) break;
        }
        f16v zr, zi;
        core_fft<false>(in, MR, MI, TW, zr, zi);
        zr *= osc; zi *= osc;
        const rsrc_t ro = mkrs(out, 2048 * 2048);
        UNROLL for (int hf = 0; hf < 2; ++hf) {
            u4 c;
            UNROLL for (int j = 0; j < 4; ++j) c[j] = pkb(zr[8 * hf + 2 * j], zr[8 * hf + 2 * j + 1]);
            store128<OAUX>(c, ro, ov + 256 * 2048 + 32 * hf);
        }
        UNROLL for (int hf = 0; hf < 2; ++hf) {
            u4 a;
            UNROLL for (int j = 0; j < 4; ++j) a[j] = pkb(zi[8 * hf + 2 * j], zi[8 * hf + 2 * j + 1]);
            store128<OAUX>(a, ro, ov + 32 * hf);
        }
    }
    STAMP(15);
    vd = lwait(lds, L_VD, 1u);
    if (pass == 1 || !(vd & 0x20000u)) {
        if (b == 0 && t == 0) __builtin_amdgcn_raw_buffer_store_b32(tags.z, rws, 0, WS_E, SC1);
        break;
    }
    lset(lds, L_C6 + wv, 2u, lane);
    lwait4(lds, L_C6, 2u);
    VMWAIT(0);
    }
    STAMP_OUT();
}

extern "C" void kernel_launch(void* const* d_in, const int* in_sizes, int n_in,
                              void* d_out, int out_size, void* d_ws, size_t ws_size,
                              hipStream_t stream) {
    gfnet_fused<<<256, 512, 0, stream>>>(d_in[0], d_in[6], d_in[1], d_in[2], d_in[4], d_in[5], (char*)d_ws, d_out);
}
```

```cpp
#define GEARLY 1
#include <hip/hip_runtime.h>
#include <hip/hip_bf16.h>
#include <stdint.h>

typedef _Float16 h2 __attribute__((ext_vector_type(2)));
typedef _Float16 h8 __attribute__((ext_vector_type(8)));
typedef __bf16 b2 __attribute__((ext_vector_type(2)));
typedef __bf16 b8 __attribute__((ext_vector_type(8)));
typedef float f2 __attribute__((ext_vector_type(2)));
typedef float f4 __attribute__((ext_vector_type(4)));
typedef float f16v __attribute__((ext_vector_type(16)));
typedef short s4 __attribute__((ext_vector_type(4)));
typedef unsigned u32;
typedef u32 u2 __attribute__((ext_vector_type(2)));
typedef u32 u4 __attribute__((ext_vector_type(4)));
typedef unsigned short us2 __attribute__((ext_vector_type(2)));
#define DI __device__ __forceinline__
#define UNROLL _Pragma("unroll")
#define NOUNROLL _Pragma("unroll 1")
#define LAS __attribute__((address_space(3)))

#define W0AUX 0
#define W1AUX 0
#define VDELAY 0
#define ZDELAY 0
#define WFIRST 0
#define VSTAG 0
#define ZGATE 0
#define VGATE 0
#define G2DELAY 50
#define MAXLATE 0
#define FLATE 0
#define FDELAY 24
#define EDGE 1
#define SCAN 0
#define ZHOLD 4
#define ZSLEEP 0
#define XLAST 0
#define XDELAY 0
#define G0 0
#define WWAIT 4
#define DIAGB -1
#define CODEPF 1
#define PSLEEP 8
#define WGATE 4
#define W0DELAY 2
#define W0WAIT 0
#define WWIN 12
#define ZWAIT 0
#define VWAIT 0
#define XAUX 2
#define OAUX 2
#define MARGIN 2

#define FSTRIDE 16
constexpr u32 FSTR = FSTRIDE;
constexpr u32 WS_Z = 0;
constexpr u32 WS_V = 4u << 20;
constexpr u32 WS_VL = 12u << 20;
#define HSTR 32
constexpr u32 WS_HB = WS_VL + (32u << 10);
constexpr u32 WS_F = WS_HB + (128u << 10);
constexpr u32 WS_G1 = WS_F + 2048 * FSTR;
constexpr u32 WS_G2 = WS_G1 + 2048 * FSTR;
constexpr u32 WS_E = WS_G2 + 2048 * FSTR;
constexpr u32 WS_BYTES = WS_E + 4096;
constexpr size_t WS_META = 24u << 20;
constexpr int SC1 = 16;
constexpr int SPIN_CAP = 8192;
constexpr int LSPIN_CAP = 1 << 17;
constexpr int OOB = 0x7ffffff0;
constexpr float TRUNC_FIX = 1.00066f;
constexpr u32 TM = 0x00010001u;

template <class T> DI void pin(T& v) { asm volatile("" : "+v"(v)); }
template <class T, int N> DI void pin(T (&v)[N]) { UNROLL for (int i = 0; i < N; ++i) pin(v[i]); }
DI u32 opq(u32 v) { asm("" : "+v"(v)); return v; }
DI u32 pkh(float lo, float hi) { h2 r; r.x = (_Float16)lo; r.y = (_Float16)hi; return __builtin_bit_cast(u32, r); }
DI u32 pkb(float lo, float hi) { b2 r; r.x = (__bf16)lo; r.y = (__bf16)hi; return __builtin_bit_cast(u32, r); }
DI float bflo(u32 v) { return __builtin_bit_cast(float, v << 16); }
DI float bfhi(u32 v) { return __builtin_bit_cast(float, v & 0xffff0000u); }
DI float rbf(float v) { return (float)(__bf16)v; }
DI h2 H(u32 v) { return __builtin_bit_cast(h2, v); }
DI u32 U(h2 v) { return __builtin_bit_cast(u32, v); }
DI f16v mfma32(u4 a, u4 b, f16v c) { return __builtin_amdgcn_mfma_f32_32x32x16_f16(__builtin_bit_cast(h8, a), __builtin_bit_cast(h8, b), c, 0, 0, 0); }
DI f4 mfma16(u4 a, u4 b) { return __builtin_amdgcn_mfma_f32_16x16x32_f16(__builtin_bit_cast(h8, a), __builtin_bit_cast(h8, b), f4{}, 0, 0, 0); }
DI f4 mfma16b(u4 a, u4 b) { return __builtin_amdgcn_mfma_f32_16x16x32_bf16(__builtin_bit_cast(b8, a), __builtin_bit_cast(b8, b), f4{}, 0, 0, 0); }
DI u32 hmul(u32 a, u32 b) { return U(H(a) * H(b)); }
DI u32 hfma(u32 a, u32 b, u32 c) { return U(__builtin_elementwise_fma(H(a), H(b), H(c))); }
DI u32 cmul_swap(u32 x, u32 w) { const h2 a = H(opq(x)), c = H(opq(w)); h2 n = c; n.y = -n.y; return U(__builtin_elementwise_fma(a.yy, n, a.xx * c.yx)); }
DI u32 cmul_conj(u32 x, u32 t) { const h2 a = H(opq(x)), c = H(opq(t)); h2 n = c.yy; n.y = -n.y; return U(__builtin_elementwise_fma(a.yx, n, a * c.xx)); }
DI u32 pkmax(u32 a, u32 b) { return __builtin_bit_cast(u32, __builtin_elementwise_max(__builtin_bit_cast(us2, a), __builtin_bit_cast(us2, b))); }
DI float sinr(float r) { return __builtin_amdgcn_sinf(r); }
DI float cosr(float r) { return __builtin_amdgcn_cosf(r); }
DI int perm32(int n) { return (n & 0x13) | ((n & 4) << 1) | ((n & 8) >> 1); }
DI u32 max3u(u32 a, u32 b, u32 c) { return max(max(a, b), c); }
DI u32 max4u(u4 v) { return max(max(v.x, v.y), max(v.z, v.w)); }
DI u32 min4u(u4 v) { return min(min(v.x, v.y), min(v.z, v.w)); }
DI u32 mark(u32 v, u32 par, u32 tm) { return (par & tm) | (v & ~tm); }
DI u32 rfl(u32 v) { return __builtin_amdgcn_readfirstlane(v); }
DI u32 pow2h(int d) { const float f = __builtin_bit_cast(float, (u32)(127 - min(d, 64)) << 23); return pkh(f, f); }

typedef __amdgpu_buffer_rsrc_t rsrc_t;
DI rsrc_t mkrs(const void* p, u32 bytes) { return __builtin_amdgcn_make_buffer_rsrc((void*)p, (short)0, (int)bytes, 0x00020000); }
template <int AUX> DI void store128(u4 v, rsrc_t r, int off) { __builtin_amdgcn_raw_buffer_store_b128(v, r, off, 0, AUX); }
#define SB() __builtin_amdgcn_sched_barrier(0)
#define MEMBAR() asm volatile("" ::: "memory")
#define VMWAIT(n) asm volatile("s_waitcnt vmcnt(%0)" :: "n"(n) : "memory")
#define STAMP_DECL
#define STAMP(i)
#define STAMP_OUT()
template <int CTRL> DI u32 dpp(u32 v) { return __builtin_amdgcn_update_dpp(0u, v, CTRL, 0xf, 0xf, true); }
DI u32 wave_maxu(u32 v) {
    v = max(v, dpp<0xB1>(v)); v = max(v, dpp<0x4E>(v)); v = max(v, dpp<0x141>(v)); v = max(v, dpp<0x140>(v));
    return max(max((u32)__builtin_amdgcn_readlane(v, 0), (u32)__builtin_amdgcn_readlane(v, 16)), max((u32)__builtin_amdgcn_readlane(v, 32), (u32)__builtin_amdgcn_readlane(v, 48)));
}

DI void core_setup(int lane, int s, u4& tw, u2& m) {
    float fc = (float)perm32(lane & 31) * (1.0f / 1024.0f), p0 = (float)(4 * (lane >> 5) + 8 * s) * fc, p1 = (float)(4 * (lane >> 5) + 8 * (s >> 1) + 2 * (s & 1)) * fc * 32.0f;
    pin(fc); pin(p0); pin(p1);
    float tc[4], ts[4];
    UNROLL for (int i = 0; i < 4; ++i) { const float pt = fmaf((float)i, fc, p0); tc[i] = cosr(pt); ts[i] = sinr(pt); }
    tw = u4{pkh(tc[0], tc[1]), pkh(tc[2], tc[3]), pkh(ts[0], ts[1]), pkh(ts[2], ts[3])};
    const float p2 = fmaf(32.0f, fc, p1);
    m = u2{pkh(cosr(p1), cosr(p2)), pkh(sinr(p1), sinr(p2))};
}
DI void core_expand(int lane, u4 (&MR)[4], u4 (&MI)[4]) {
    const u32 sg = (lane & 1) ? 0x80008000u : 0u;
    MR[2] = MR[0] ^ sg; MR[3] = MR[1] ^ sg;
    UNROLL for (int s = 0; s < 4; ++s) MI[s] = u4{MR[s].z ^ 0x80008000u, MR[s].w ^ 0x80008000u, MR[s].x, MR[s].y};
}
template <bool OUT_B> DI void core_fft(const u4 (&in)[4], const u4 (&MR)[4], const u4 (&MI)[4], const u4 (&TW)[4], f16v& outr, f16v& outi) {
    f16v yr = {}, yi = {};
    UNROLL for (int s = 0; s < 4; ++s) yr = mfma32(in[s], MR[s], yr);
    SB();
    yi = mfma32(in[0], MI[0], yi); yi = mfma32(in[1], MI[1], yi);
    SB();
    h2 t0[8], t1[8];
#define TW_RE(p) do { const h2 pr_ = H(pkh(yr[2 * (p)], yr[2 * (p) + 1])); t0[p] = pr_ * H(TW[(p) >> 1][(p) & 1]); t1[p] = pr_ * H(TW[(p) >> 1][2 + ((p) & 1)]); } while (0)
    TW_RE(0); TW_RE(1); SB();
    yi = mfma32(in[2], MI[2], yi);
    TW_RE(2); TW_RE(3); SB();
    yi = mfma32(in[3], MI[3], yi);
    TW_RE(4); TW_RE(5); TW_RE(6); TW_RE(7); SB();
    outr = f16v{}; outi = f16v{};
    u4 d[4];
#define TW_IM(s) do { UNROLL for (int j = 0; j < 2; ++j) { const int p_ = 2 * (s) + j; const h2 pi_ = H(pkh(yi[2 * p_], yi[2 * p_ + 1])); \
        d[s][j] = U(__builtin_elementwise_fma(pi_, H(TW[s][2 + j]), t0[p_])); d[s][2 + j] = U(__builtin_elementwise_fma(pi_, H(TW[s][j]), -t1[p_])); } } while (0)
    TW_IM(0); SB();
    UNROLL for (int s = 0; s < 4; ++s) {
        outr = OUT_B ? mfma32(MR[s], d[s], outr) : mfma32(d[s], MR[s], outr);
        if (s < 3) TW_IM(s + 1);
        SB();
    }
    UNROLL for (int s = 0; s < 4; ++s) outi = OUT_B ? mfma32(MI[s], d[s], outi) : mfma32(d[s], MI[s], outi);
}
DI u32 rot(float ph) { return pkh(cosr(ph), sinr(ph)); }
DI u32 quarter(u32 r, u32 f) { u32 o; asm("v_pk_mul_f16 %0, %1, %2 op_sel:[1,0] op_sel_hi:[0,1] neg_lo:[1,0]" : "=v"(o) : "v"(r), "v"(f)); return o; }
DI u4 quarter(u4 r) { u32 one = 0x3c003c00u; pin(one); return u4{quarter(r.x, one), quarter(r.y, one), quarter(r.z, one), quarter(r.w, one)}; }

DI u32 tag_of(u32 epoch, u32 idx) { u32 h = (epoch ^ (idx * 0x9E3779B1u)) * 0x85EBCA6Bu; return h ^ (h >> 15); }
DI u32 parity_of(u32 epoch) { return (epoch & 1u) ? 0x00000001u : 0x00010000u; }
DI void flag_store(rsrc_t rws, u32 arr, int b, int lane, u32 payload, u32 tag) {
    __builtin_amdgcn_raw_buffer_store_b64(u2{payload, tag}, rws, lane < 8 ? lane * (int)(256 * FSTR) : OOB, arr + b * FSTR, SC1);
}
DI u4 flag_wait(rsrc_t rws, u32 arr, int b, int lane, u32 epoch) {
    u2 f[4]; u32 tg[4];
    UNROLL for (int j = 0; j < 4; ++j) tg[j] = tag_of(epoch, arr + 4 * lane + j);
    NOUNROLL for (int spin = 0; spin < SPIN_CAP; ++spin) {
        MEMBAR();
        UNROLL for (int j = 0; j < 4; ++j) f[j] = __builtin_amdgcn_raw_buffer_load_b64(rws, lane * (4 * FSTR) + j * FSTR, arr + (b & 7) * (256 * FSTR), SC1);
        if (__all(f[0].y == tg[0] && f[1].y == tg[1] && f[2].y == tg[2] && f[3].y == tg[3])) break;
        __builtin_amdgcn_s_sleep(PSLEEP);
    }
    return u4{f[0].x, f[1].x, f[2].x, f[3].x};
}
DI int cls_of(int b) { const int row = (b & 7) >> 1; return row == 0 ? 4 * (b >> 7) : ((b & 1) ? 8 - row : row); }
DI int tile_of(int b) { return (b & 6) == 0 ? 16 * (b & 1) + ((b >> 3) & 15) : b >> 3; }
DI bool is_helper(int b) { return tile_of(b) == 31; }

constexpr int L_C1 = 0, L_C3 = 4, L_C4 = 8, L_C5 = 12, L_C6 = 16;
constexpr int L_EW = 20;
constexpr int L_WW = 24;
constexpr int L_SW = 28;
constexpr int L_PR = 32;
constexpr int L_SX = 36;
constexpr int L_ZL = 37;
constexpr int L_VD = 38;
constexpr int L_G1 = 39, L_G2 = 40, L_P2 = 41;
constexpr int L_LD = 44;
constexpr int L_GS = 64;
constexpr int L_M = 192;
constexpr int L_A1 = L_M + 1536;
constexpr int L_A2 = L_A1 + 4096;
constexpr int L_B = L_A2 + 4096;
constexpr int L_W = L_B + 8192;
constexpr int L_END = L_W + 64 * 256;

DI void lset(u32* lds, int idx, u32 v, int lane) { MEMBAR(); if (lane == 0) lds[idx] = v; MEMBAR(); }
DI u32 lget(u32* lds, int idx) { MEMBAR(); return rfl(lds[idx]); }
DI u32 lwait(u32* lds, int idx, u32 atleast) {
    u32 v = 0;
    NOUNROLL for (int spin = 0; spin < LSPIN_CAP; ++spin) { v = lget(lds, idx); if (v >= atleast) break; __builtin_amdgcn_s_sleep(1); }
    MEMBAR();
    return v;
}
DI u32 lwait4(u32* lds, int idx, u32 atleast) {
    u4 v;
    NOUNROLL for (int spin = 0; spin < LSPIN_CAP; ++spin) { MEMBAR(); v = *(const u4*)&lds[idx]; if (rfl(min4u(v)) >= atleast) break; __builtin_amdgcn_s_sleep(1); }
    MEMBAR();
    return rfl(max4u(v));
}
struct MagMax {
    h2 hi = {}, lo = {};
    DI void add(u32 a, u32 c) {
        hi = __builtin_elementwise_maximum(__builtin_elementwise_maximum(hi, H(a)), H(c));
        lo = __builtin_elementwise_minimum(__builtin_elementwise_minimum(lo, H(a)), H(c));
    }
    DI u32 exponent() const {
        const u32 mx = pkmax(U(hi) & 0x7fff7fffu, U(lo) & 0x7fff7fffu);
        return wave_maxu(max(mx & 0xffffu, mx >> 16)) >> 7;
    }
};
DI u32 clampe(u32 e) { return min(max(e, 8u), 240u); }

__global__ __launch_bounds__(512, 2) void gfnet_fused(const void* x, const void* gw, const void* war, const void* wai, const void* wbr, const void* wbi,
                                                      char* ws, void* out) {
    __shared__ __attribute__((aligned(16))) u32 lds[L_END];
    STAMP_DECL;
    STAMP(0);
    unsigned long long pc0;
    asm volatile("s_getpc_b64 %0" : "=s"(pc0));
    asm volatile("" :: "s"(x), "s"(gw), "s"(war), "s"(wai), "s"(wbr), "s"(wbi), "s"(ws), "s"(out));
    const int wv0 = __builtin_amdgcn_readfirstlane(threadIdx.x >> 6);
    const rsrc_t rws = mkrs(ws, WS_BYTES);

    if (wv0 >= 4) {
        const int wv = wv0, b = blockIdx.x, cls = cls_of(b), tile = tile_of(b);
        const int lane = threadIdx.x & 63, q = lane >> 4, n = lane & 15;
        const rsrc_t rw = mkrs(wv == 4 ? war : wv == 5 ? wai : wv == 6 ? wbr : wbi, 2048 * 1026);
        const int wvo = (lane >> 2) * (128 * 1026) + min(lane & 3, 2) * 16, wso = (cls * 513 + 16 * tile - cls) * 2;
#define WLOAD(r) do { UNROLL for (int st = 0; st < 4; ++st) { const int c_ = 4 * st + ((tile + (r)) & 3); \
                __builtin_amdgcn_raw_ptr_buffer_load_lds(rw, (LAS void*)&lds[L_W + (c_ * 4 + wv - 4) * 256], 16, wvo, wso + c_ * 8208, 0, (r) == 0 ? W0AUX : W1AUX); } SB(); } while (0)
        if (wv == 4) { lds[lane] = 0u; asm volatile("s_waitcnt lgkmcnt(0)" ::: "memory"); }
        if (WFIRST) { __builtin_amdgcn_s_sleep(W0DELAY); WLOAD(0); }
        MEMBAR();
        __builtin_amdgcn_s_barrier();
        MEMBAR();
        const bool helper = wv == 7 && is_helper(b);
        u4 GR, GI, FR, FI, HR, HI, T1, T2;
        u32 ev;
        {
            if (!WFIRST) { __builtin_amdgcn_s_sleep(W0DELAY); if (!SCAN) WLOAD(0); }
            const bool split = (b & 6) == 0;
            const int er = split ? 16 * (tile & 15) + ((lane >> 1) & 15) : 8 * tile + ((lane >> 1) & 7);
            const int ec = (split ? (tile & 16) * 16 : 0) - cls + ((lane & 1) ? (split ? 262 : 518) : SCAN ? (split ? 256 : 512) : 0);
            const int sr = split ? 16 * (tile & 15) + (lane >> 2) : 8 * tile + (lane >> 3);
            const int scn = (split ? (tile & 16) * 16 + (lane & 3) * 64 : (lane & 7) * 64) - cls;
            u32 scan = SCAN ? __builtin_amdgcn_raw_buffer_load_b32(rw, ((cls + 8 * sr) * 513 + scn) * 2, 0, W0AUX) : 0u;
            u32 edge = EDGE ? __builtin_amdgcn_raw_buffer_load_b32(rw, ((cls + 8 * er) * 513 + ec) * 2, 0, W0AUX) : 0u;
            u32 code = 0;
            if (CODEPF) {
                unsigned long long pc1;
                asm volatile("s_getpc_b64 %0" : "=s"(pc1));
                const rsrc_t rc = mkrs((const void*)pc0, pc1 > pc0 ? (u32)(pc1 - pc0) : 0u);
                code = __builtin_amdgcn_raw_buffer_load_b32(rc, (lane & 3) * 64 + ((b >> 3) * 4 + wv - 4) * 256, 0, 0);
            }
            ev = __builtin_amdgcn_raw_buffer_load_b32(rws, 0, WS_E, SC1);
            const u4 smp = __builtin_amdgcn_raw_buffer_load_b128(rw, (lane >> 3) * (2 * 131328 + 128) + (lane & 7) * 16, 0, 0);
            MEMBAR();
            if (helper) {
            UNROLL for (int j = 0; j < 4; ++j) {
                GR[j] = rot((float)((4 * q + j) * (cls + 8 * n)) * (1.0f / 128.0f) + (cls == 4 ? 0.25f : 0.0f));
                HR[j] = rot((float)(n * (cls + 8 * (4 * q + j))) * (1.0f / 128.0f));
                FR[j] = rot((float)(n * (4 * q + j)) * (1.0f / 16.0f));
                T1[j] = rot((float)((4 * q + j) * (cls + 8 * n)) * (1.0f / 2048.0f));
                T2[j] = rot((float)(n * (cls + 8 * (4 * q + j))) * (1.0f / 2048.0f));
            }
            GI = quarter(GR); HI = quarter(HR); FI = quarter(FR);
            pin(GR); pin(GI); pin(HR); pin(HI); pin(FR); pin(FI); pin(T1); pin(T2);
            }
            VMWAIT(W0WAIT);
            STAMP(1);
            if (SCAN) { WLOAD(4); VMWAIT(WWIN); }
            const u32 lda = (u32)(size_t)(LAS u32*)&lds[L_LD + wv - 4];
            if (ZHOLD == 1) asm volatile("ds_write_b32 %0, %1" :: "v"(lda), "v"(1u) : "memory");
            WLOAD(1); VMWAIT(WWIN);
            if (ZHOLD == 2) asm volatile("ds_write_b32 %0, %1" :: "v"(lda), "v"(1u) : "memory");
            WLOAD(2); VMWAIT(WWIN);
            if (ZHOLD == 3) asm volatile("ds_write_b32 %0, %1" :: "v"(lda), "v"(1u) : "memory");
            WLOAD(3);
            if (ZHOLD == 4) asm volatile("ds_write_b32 %0, %1" :: "v"(lda), "v"(1u) : "memory");
            pin(edge); pin(scan); pin(code);
            MagMax ms; ms.add(smp.x, smp.y); ms.add(smp.z, smp.w);
            const u32 es = clampe(ms.exponent() + MARGIN);
            VMWAIT(0);
            lset(lds, L_PR + wv - 4, es, lane);
            STAMP(2);
        }
        const u32 ep0 = rfl(ev) + 1u;
        if (wv == 5 || wv == 6) {
            lwait4(lds, L_PR, 1u);
            if (MAXLATE) lwait(lds, L_P2, 1u);
            MagMax m;
            UNROLL for (int c = 0; c < 32; ++c) { const u4 v = *(const u4*)&lds[L_W + ((wv - 5) * 32 + c) * 256 + lane * 4]; m.add(v.x, v.y); m.add(v.z, v.w); }
            const u32 e = clampe(m.exponent());
            MEMBAR(); if (lane == 0) *(u2*)&lds[L_WW + 2 * (wv - 5)] = u2{e, e}; MEMBAR();
        }
        if (VGATE && wv == 6) {
            __builtin_amdgcn_s_setprio(2);
            lwait(lds, L_P2, 1u);
            __builtin_amdgcn_s_sleep(G2DELAY);
            flag_wait(rws, WS_G2, b, lane, ep0);
            lset(lds, L_G2, 1u, lane);
            STAMP(5);
        }
        if (wv == 4) {
            if (ZGATE) { flag_wait(rws, WS_G1, b, lane, ep0); lset(lds, L_G1, 1u, lane); }
            const u32 ex = lwait4(lds, L_EW, 1u), ew = lwait4(lds, L_WW, 1u);
            if (FLATE) lwait(lds, L_P2, 1u);
            flag_store(rws, WS_F, b, lane, ex | (ew << 8), tag_of(ep0, WS_F + b));
            lwait(lds, L_P2, 1u);
            __builtin_amdgcn_s_sleep(FDELAY);
            const u4 f = flag_wait(rws, WS_F, b, lane, ep0);
            const u32 gx = wave_maxu(max4u(f & 0xffu)), gwm = wave_maxu(max4u((f >> 8) & 0xffu));
            const bool redo = gx > lget(lds, L_SX) || gwm > lwait4(lds, L_PR, 1u);
            lset(lds, L_VD, gx | (gwm << 8) | (redo ? 0x20000u : 0x10000u), lane);
            STAMP(5);
        } else if (helper) {
            __builtin_amdgcn_s_setprio(2);
            u32 ew = lwait4(lds, L_PR, 1u);
            NOUNROLL for (u32 pass = 0; pass < 2; ++pass) {
                u4 WH[2];
                const float sw = __builtin_bit_cast(float, (249u - ew) << 23);
                UNROLL for (int r = 0; r < 4; ++r) UNROLL for (int f = 0; f < 2; ++f) {
                    const unsigned short* wp = (const unsigned short*)&lds[L_W + (n * 4 + 2 * f) * 256 + (4 * q + r) * 16] + 16 + cls;
                    float re = bflo(wp[0]) * sw, im = bflo(wp[512]) * sw;
                    pin(re);
                    WH[f][r] = pkh(re, im);
                }
                pin(WH);
                STAMP(3);
                lwait(lds, L_ZL, pass + 1u);
                const u32 tg = tag_of(ep0 + pass, 0u), par = parity_of(ep0 + pass);
                u4 hz;
                NOUNROLL for (int spin = 0; spin < SPIN_CAP; ++spin) {
                    MEMBAR();
                    u32 bad = 0;
                    UNROLL for (int j = 0; j < 4; ++j) { const u2 g = __builtin_amdgcn_raw_buffer_load_b64(rws, (64 * q + n + 16 * j) * HSTR, WS_HB + ((b & 7) >> 1) * (256 * HSTR), SC1); hz[j] = g.x & ~TM; bad |= g.y ^ tg; }
                    if (__all(bad == 0u)) break;
                    __builtin_amdgcn_s_sleep(4);
                }
                STAMP(4);
                const u32 f16 = cls == 0 ? 0x00002c00u : cls == 4 ? 0x2c000000u : cls > 4 ? 0xac002c00u : 0x2c002c00u;
                u4 d, o[2];
                UNROLL for (int j = 0; j < 4; ++j) d[j] = hmul(hz[j], f16);
                f4 yr = mfma16(d, GR), yi = mfma16(d, GI);
                UNROLL for (int r = 0; r < 4; ++r) d[r] = cmul_conj(pkh(yr[r], yi[r]), T1[r]);
                const f4 xr = mfma16(FR, d), xi = mfma16(FI, d);
                UNROLL for (int f = 0; f < 2; ++f) {
                    UNROLL for (int r = 0; r < 4; ++r) d[r] = cmul_swap(pkh(xr[r], xi[r]), WH[f][r]);
                    yr = mfma16(d, FR); yi = mfma16(d, FI);
                    UNROLL for (int r = 0; r < 4; ++r) o[f][r] = cmul_conj(pkh(yr[r], yi[r]), T2[r]);
                }
                const f4 p0 = mfma16(HR, o[0]), p1 = mfma16(HI, o[0]), p2 = mfma16(HR, o[1]), p3 = mfma16(HI, o[1]);
                UNROLL for (int r = 0; r < 4; ++r)
                    store128<SC1>(u4{mark(pkh(p0[r], p1[r]), par, TM), tg, mark(pkh(p2[r], p3[r]), par, TM), tg}, rws, (64 * q + n) * 16 + WS_VL + r * 256 + cls * 4096);
                STAMP(5);
                const u32 vd = lwait(lds, L_VD, 1u);
                if (!(vd & 0x20000u)) break;
                ew = (vd >> 8) & 0xffu;
            }
        }
        STAMP_OUT();
        return;
    }

    u32 vd = 0;
    NOUNROLL for (u32 pass = 0; pass < 2; ++pass) {
    int t = threadIdx.x, wv = wv0, b = blockIdx.x;
    pin(t);
    asm volatile("" : "+s"(wv));
    asm volatile("" : "+s"(b));
    const int lane = t & 63, q = lane >> 4, n = lane & 15;
    const u32 pv = pass + 1u;
    if (XLAST && pass == 0) { __builtin_amdgcn_s_barrier(); MEMBAR(); __builtin_amdgcn_s_setprio(1); if (XDELAY) __builtin_amdgcn_s_sleep(XDELAY); }
    u2 xv[8];
    const rsrc_t rx = mkrs(x, 2048 * 2048);
    const u4 smp = __builtin_amdgcn_raw_buffer_load_b128(rx, (lane >> 3) * (524288 + 2048 + 256) + (lane & 7) * 16, 0, 0);
    UNROLL for (int s1 = 0; s1 < 8; ++s1) xv[s1] = __builtin_amdgcn_raw_buffer_load_b64(rx, t * 8, (256 * s1 + b) * 2048, XAUX);
    const u32 ev = __builtin_amdgcn_raw_buffer_load_b32(rws, 0, WS_E, SC1);
    const u2 gv = __builtin_amdgcn_raw_buffer_load_b64(mkrs(gw, 2048), t * 8, 0, 0);
    MEMBAR();
    SB();
    if (!XLAST && pass == 0) { __builtin_amdgcn_s_barrier(); MEMBAR(); __builtin_amdgcn_s_setprio(1); }
    u4 MR[4], MI[4], TW[4];
    {
        u4 tw; u2 m;
        core_setup(lane, wv, tw, m);
        *(u4*)&lds[L_M + (2 + wv) * 256 + lane * 4] = tw;
        lds[L_M + (wv >> 1) * 256 + lane * 4 + (wv & 1)] = m.x; lds[L_M + (wv >> 1) * 256 + lane * 4 + 2 + (wv & 1)] = m.y;
    }
    const int zc = perm32(lane & 31) + (lane >> 5) * 256, zcu = zc + ((lane & 31) == 0 ? 543 : 511);
    int zl = (zc >> 4) * 16384 + (zc & 15) * 4, zu = (zcu >> 4) * 16384 + (zcu & 15) * 4;
    int zh = lane == 0 ? 0 : OOB;
    int tro = L_A1 + wv * 1024 + (4 * (lane >> 5) + ((lane & 15) >> 2)) * 16 + ((lane >> 4) & 1) * 8 + (lane & 3) * 2;
    u32 tm = TM;
    pin(zl); pin(zu); pin(zh); pin(tro); pin(tm);
    STAMP(1);
    SB();
    u32 ex;
    {
        MagMax ms; ms.add(smp.x, smp.y); ms.add(smp.z, smp.w);
        ex = pass ? (vd & 0xffu) : clampe(ms.exponent() + MARGIN);
    }
    STAMP(2);
    u4 tags;
    u32 z[16], z512;
    {
        const float sc = __builtin_bit_cast(float, (250u - ex) << 23);
        const float rs = sc * 0.70710678118654752f;
        u32 o[8][2];
        UNROLL for (int j = 0; j < 2; ++j) {
            f2 v[8];
            UNROLL for (int s1 = 0; s1 < 8; ++s1) v[s1] = f2{bflo(xv[s1][j]), bfhi(xv[s1][j])};
            const f2 a0 = v[0] + v[4], a1 = v[1] + v[5], a2 = v[2] + v[6], a3 = v[3] + v[7];
            const f2 b0 = (v[0] - v[4]) * sc, b1 = v[1] - v[5], b2_ = (v[2] - v[6]) * sc, b3 = v[3] - v[7];
            const f2 e0 = a0 + a2, e1 = (a1 + a3) * sc, dm = b1 - b3, dp = b1 + b3;
            f2 r[8];
            r[0] = e0 * sc + e1; r[1] = e0 * sc - e1;
            r[2] = dm * rs + b0; r[3] = -(dp * rs) - b2_;
            r[4] = (a0 - a2) * sc; r[5] = (a3 - a1) * sc;
            r[6] = b0 - dm * rs; r[7] = b2_ - dp * rs;
            UNROLL for (int p = 0; p < 8; ++p) o[p][j] = pkh(r[p].x, r[p].y);
        }
        UNROLL for (int p = 0; p < 8; ++p) *(u2*)&lds[L_A1 + p * 512 + 2 * t] = u2{o[p][0], o[p][1]};
        lset(lds, L_C1 + wv, pv, lane);
        const u32 epoch = rfl(ev) + pv;
        tags = u4{parity_of(epoch), 0u, epoch, tag_of(epoch, 0u)};
        u4 in[4];
        NOUNROLL for (int spin = 0; spin < LSPIN_CAP; ++spin) {
            MEMBAR();
            const u4 c = *(const u4*)&lds[L_C1];
            MEMBAR();
            MR[0] = *(const u4*)&lds[L_M + lane * 4]; MR[1] = *(const u4*)&lds[L_M + 256 + lane * 4];
            UNROLL for (int s = 0; s < 4; ++s) {
                const u2 re = __builtin_bit_cast(u2, __builtin_amdgcn_ds_read_tr16_b64_v4i16((LAS s4*)&lds[tro + s * 128]));
                const u2 im = __builtin_bit_cast(u2, __builtin_amdgcn_ds_read_tr16_b64_v4i16((LAS s4*)&lds[tro + s * 128 + 512]));
                in[s] = u4{re.x, re.y, im.x, im.y};
            }
            pin(MR[0]); pin(MR[1]);
            if (rfl(min4u(c)) >= pv) break;
        }
        UNROLL for (int i = 0; i < 4; ++i) TW[i] = *(const u4*)&lds[L_M + (2 + i) * 256 + lane * 4];
        STAMP(3);
        if (ZGATE && pass == 0 && wv == 0) flag_store(rws, WS_G1, b, lane, 0u, tag_of(tags.z, WS_G1 + b));
        core_expand(lane, MR, MI);
        f16v zr, zi;
        core_fft<true>(in, MR, MI, TW, zr, zi);
        UNROLL for (int r = 0; r < 16; ++r) z[r] = mark(pkh(zr[r], zi[r]), tags.x, tm);
        z512 = z[8];
        const u32 za = __builtin_amdgcn_readlane(z[8], 32), zb = __builtin_amdgcn_readlane(z[0], 0);
        MEMBAR();
        const u32 zt = lane < 32 ? za : zb;
        UNROLL for (int r = 8; r < 16; ++r) z[r] = (lane & 31) == 0 ? (r < 15 ? z[r + 1] : zt) : z[r];
    }
    pin(z);
    STAMP(4);
    const int zs = WS_Z + wv * 64 * 16384 + (((b >> 2) & 3) * 64 + (b & 3) * 16 + ((b >> 4) & 3) * 4 + (b >> 6)) * 64;
    bool zsent = false;
#define TRY_STORE_Z(force) do { if (!zsent) { \
        bool go = true; \
        if (ZHOLD) { if (force) lwait4(lds, L_LD, 1u); else { MEMBAR(); go = rfl(min4u(*(const u4*)&lds[L_LD])) != 0u; } } \
        if (go) { \
            UNROLL for (int r = 0; r < 16; ++r) __builtin_amdgcn_raw_buffer_store_b32(z[r], rws, r < 8 ? zl : zu, zs + (r & 7) * 32768, SC1); \
            __builtin_amdgcn_raw_buffer_store_b64(u2{z512, tags.w}, rws, zh, WS_HB + (wv * 256 + b) * HSTR, SC1); \
            zsent = true; } } SB(); } while (0)
    TRY_STORE_Z(false);
    const int row = (b & 7) >> 1, cls = cls_of(b), tile = tile_of(b);
    const bool packed = row == 0;
    u4 FR, FI, GR[4], GI[4], HR[4], HI[4], sel1;
    {
        const float ofs = cls == 4 ? 0.25f : 0.0f;
        float gu = (float)(cls + 8 * n) * (1.0f / 2048.0f), g0 = (float)(64 * q + 4 * wv) * gu + ofs;
        u32 s16 = 0x2c002c00u;
        pin(gu); pin(g0); pin(s16);
        UNROLL for (int i = 0; i < 4; ++i) UNROLL for (int j = 0; j < 4; ++j) { const u32 g = rot(fmaf((float)(16 * j + i), gu, g0)); GR[i][j] = hmul(g, s16); GI[i][j] = quarter(g, s16); }
    }
    pin(GR); pin(GI);
    {
        MagMax m;
        UNROLL for (int s1 = 0; s1 < 8; ++s1) m.add(xv[s1].x, xv[s1].y);
        if (wv == 0) lset(lds, L_SX, ex, lane);
        lset(lds, L_EW + wv, clampe(m.exponent()), lane);
    }
    TRY_STORE_Z(false);
    UNROLL for (int j = 0; j < 4; ++j) FR[j] = rot((float)(n * (4 * q + j)) * (1.0f / 16.0f));
    FI = quarter(FR);
    {
        float hs[4], hu[4];
        UNROLL for (int i = 0; i < 4; ++i) { hs[i] = (float)(16 * n + 4 * wv + i) * (1.0f / 2048.0f); hu[i] = (float)(cls + 32 * q + 8 * i); }
        pin(hs); pin(hu);
        UNROLL for (int i = 0; i < 4; ++i) { UNROLL for (int j = 0; j < 4; ++j) { float a = hs[i] * hu[j]; pin(a); HR[i][j] = rot(a); } HI[i] = quarter(HR[i]); }
        const int d = n + cls - 8 * q;
        UNROLL for (int j = 0; j < 4; ++j) sel1[j] = (d >> 1) == j ? ((d & 1) ? 0xffff0000u : 0xffffu) : 0u;
    }
    pin(FR); pin(FI); pin(HR); pin(HI); pin(sel1);
    TRY_STORE_Z(false);
    {
        pin(xv);
        const f2 g01 = {bflo(gv.x), bfhi(gv.x)}, g23 = {bflo(gv.y), bfhi(gv.y)};
        f4 acc = {};
        UNROLL for (int i = 0; i < 4; ++i) {
            u4 pp;
            UNROLL for (int k = 0; k < 2; ++k) {
                const f2 a = f2{bflo(xv[2 * i + k].x), bfhi(xv[2 * i + k].x)} * g01, c = f2{bflo(xv[2 * i + k].y), bfhi(xv[2 * i + k].y)} * g23;
                pp[2 * k] = pkb(a.x, a.y); pp[2 * k + 1] = pkb(c.x, c.y);
            }
            const u32 s0 = n == 2 * i ? 0x3f803f80u : 0u, s1 = n == 2 * i + 1 ? 0x3f803f80u : 0u;
            acc = __builtin_amdgcn_mfma_f32_16x16x32_bf16(__builtin_bit_cast(b8, pp), __builtin_bit_cast(b8, u4{s0, s0, s1, s1}), acc, 0, 0, 0);
        }
        if (n < 8) lds[L_GS + n * 16 + (t >> 4)] = __builtin_bit_cast(u32, (acc.x + acc.y) + (acc.z + acc.w));
        lset(lds, L_C5 + wv, pv, lane);
    }
    TRY_STORE_Z(true);
    STAMP(5);
    VMWAIT(WWAIT);
    u4 WA[4], WB[4];
    u32 ew = 0;
    UNROLL for (int i = 0; i < 4; ++i) asm volatile("" : "=v"(WA[i]), "=v"(WB[i]));
#define WIDEN(es) do { \
        u4 wr[4][4]; \
        int wl = L_W / 4 + wv * 1024 + n * 4 + min(q, 2); pin(wl); \
        UNROLL for (int i = 0; i < 4; ++i) UNROLL for (int a = 0; a < 4; ++a) wr[i][a] = ((const u4*)lds)[wl + (i * 4 + a) * 64]; \
        ew = pass ? (vd >> 8) & 0xffu : (es); \
        const u32 sw = ((249u - ew - (packed ? 1u : 0u)) << 7) * 0x10001u;       \
        const u4 sels = sel1 & sw; \
        SB(); \
        f4 wf[4][4]; \
        UNROLL for (int k = 0; k < 10; ++k) { \
            if (k < 8) { wf[k >> 1][2 * (k & 1)] = mfma16b(wr[k >> 1][2 * (k & 1)], sels); wf[k >> 1][2 * (k & 1) + 1] = mfma16b(wr[k >> 1][2 * (k & 1) + 1], sels); } \
            if (k >= 2) { const int i_ = (k - 2) >> 1, f_ = k & 1; UNROLL for (int r = 0; r < 4; ++r) (f_ ? WB : WA)[i_][r] = pkh(wf[i_][2 * f_][r], wf[i_][2 * f_ + 1][r]); } \
            SB(); } \
        pin(WA); pin(WB); } while (0)
    MEMBAR();
    const u4 pr0 = *(const u4*)&lds[L_PR];
    const bool wearly = rfl(min4u(pr0)) != 0u;
    MEMBAR();
    if (wearly) WIDEN(rfl(max4u(pr0)));
    STAMP(6);
    u32 z1[4][4], z2[4][4];
    const int zvm = q * 64 + (63 - tile) * 16384 + (15 - n) * 4;
    const int zv1 = cls <= 4 ? q * 64 + tile * 16384 + n * 4 : zvm;
    int zso = WS_Z + row * 64 * 16384 + wv * 4096;
    asm volatile("" : "+s"(zso));
#define LOAD_Z() do { \
        if (packed) { int a_ = zvm; pin(a_); UNROLL for (int i = 0; i < 4; ++i) UNROLL for (int j = 0; j < 4; ++j) z2[i][j] = __builtin_amdgcn_raw_buffer_load_b32(rws, a_ + (4 * i + j) * 256, zso, SC1); } \
        else { UNROLL for (int i = 0; i < 4; ++i) UNROLL for (int j = 0; j < 4; ++j) z2[i][j] = tags.x; } \
        int c_ = zv1; pin(c_); UNROLL for (int i = 0; i < 4; ++i) UNROLL for (int j = 0; j < 4; ++j) z1[i][j] = __builtin_amdgcn_raw_buffer_load_b32(rws, c_ + (4 * i + j) * 256, zso, SC1); } while (0)
    if (ZGATE) lwait(lds, L_G1, 1u);
    VMWAIT(ZWAIT);
    if (ZDELAY) __builtin_amdgcn_s_sleep(ZDELAY);
    LOAD_Z();
    if (wv == 0) lset(lds, L_ZL, pv, lane);
    SB();
    STAMP(7);
    u32 x1 = tags.x ^ (cls > 4 ? 0x80000000u : 0u), x2 = tags.x ^ (cls == 4 ? 0x00008000u : cls == 0 ? 0x80000000u : 0u);
    const int lw = ((wv * 16 + 4 * q) * 16 + n) * 4;
    int lr = (q * 16 + 4 * wv) * 16 + n;
    const u4* l4 = (const u4*)lds;
    pin(GR); pin(GI); pin(x1); pin(x2); pin(lr);
    STAMP(8);
    SB();
    {
        f4 yr[4], yi[4];
        NOUNROLL for (int spin = 0; spin < SPIN_CAP; ++spin) {
            u32 m = 0;
            UNROLL for (int i = 0; i < 4; ++i) {
                u4 d;
                UNROLL for (int j = 0; j < 4; ++j) { z1[i][j] ^= x1; z2[i][j] ^= x2; }
                UNROLL for (int j = 0; j < 4; ++j) d[j] = U(H(z1[i][j]) + H(z2[i][j]));
                UNROLL for (int j = 0; j < 4; ++j) m |= z1[i][j] | z2[i][j];
                yr[i] = mfma16(GR[i], d); yi[i] = mfma16(GI[i], d);
                SB();
            }
            if (__all((m & TM) == 0u)) break;
            MEMBAR();
            LOAD_Z();
        }
        STAMP(9);
        u4 y[4];
        UNROLL for (int i = 0; i < 4; ++i) UNROLL for (int r = 0; r < 4; ++r) y[r][i] = pkh(yr[i][r], yi[i][r]);
        UNROLL for (int r = 0; r < 4; ++r) *(u4*)&lds[L_A2 + lw + r * 64] = y[r];
        lset(lds, L_C3 + wv, pv, lane);
    }
    if (!wearly) { const u32 es = lwait4(lds, L_PR, 1u); WIDEN(es); }
    {
        u4 d[4];
        NOUNROLL for (int spin = 0; spin < LSPIN_CAP; ++spin) {
            MEMBAR();
            const u4 c = *(const u4*)&lds[L_C3];
            MEMBAR();
            UNROLL for (int i = 0; i < 4; ++i) d[i] = l4[L_A2 / 4 + lr + i * 16];
            pin(d[0]);
            if (rfl(min4u(c)) >= pv) break;
        }
        STAMP(10);
        if (VGATE == 1 && pass == 0 && wv == 0) { flag_store(rws, WS_G2, b, lane, 0u, tag_of(tags.z, WS_G2 + b)); lset(lds, L_P2, 1u, lane); }
        pin(WA); pin(WB);
        f4 xr[4], xi[4];
        u4 xx[4], ya[4], yb[4];
        UNROLL for (int i = 0; i < 4; ++i) {
            xr[i] = mfma16(FR, d[i]); xi[i] = mfma16(FI, d[i]);
            if (i >= 2) { UNROLL for (int r = 0; r < 4; ++r) xx[i - 2][r] = pkh(xr[i - 2][r], xi[i - 2][r]); pin(xx[i - 2]); }
            SB();
        }
        UNROLL for (int r = 0; r < 4; ++r) ya[0][r] = cmul_swap(xx[0][r], WA[0][r]);
        UNROLL for (int i = 2; i < 4; ++i) { UNROLL for (int r = 0; r < 4; ++r) xx[i][r] = pkh(xr[i][r], xi[i][r]); pin(xx[i]); }
        SB();
        f4 p0[4], p1[4], p2[4], p3[4];
        UNROLL for (int i = 0; i < 4; ++i) {
            p0[i] = mfma16(FR, ya[i]); p1[i] = mfma16(FI, ya[i]);
            if (i < 3) { UNROLL for (int r = 0; r < 4; ++r) ya[i + 1][r] = cmul_swap(xx[i + 1][r], WA[i + 1][r]); }
            else { UNROLL for (int r = 0; r < 4; ++r) yb[0][r] = cmul_swap(xx[0][r], WB[0][r]); }
            SB();
        }
        u4 ra[4], rb[4];
        UNROLL for (int i = 0; i < 4; ++i) {
            p2[i] = mfma16(FR, yb[i]); p3[i] = mfma16(FI, yb[i]);
            if (i < 3) { UNROLL for (int r = 0; r < 4; ++r) yb[i + 1][r] = cmul_swap(xx[i + 1][r], WB[i + 1][r]); }
            UNROLL for (int k = 0; k < 4; ++k) ra[i][k] = pkh(p0[k][i], p1[k][i]);
            *(u4*)&lds[L_B + lw + i * 64] = ra[i];
            SB();
        }
        UNROLL for (int i = 0; i < 3; ++i) UNROLL for (int r = 0; r < 4; ++r) rb[r][i] = pkh(p2[i][r], p3[i][r]);
        SB();
        UNROLL for (int r = 0; r < 4; ++r) { rb[r][3] = pkh(p2[3][r], p3[3][r]); *(u4*)&lds[L_B + 4096 + lw + r * 64] = rb[r]; }
        lset(lds, L_C4 + wv, pv, lane);
    }
    STAMP(11);
    if (VGATE == 2 && pass == 0 && wv == 0) { flag_store(rws, WS_G2, b, lane, 0u, tag_of(tags.z, WS_G2 + b)); lset(lds, L_P2, 1u, lane); }
    {
        u4 da[4], db[4];
        NOUNROLL for (int spin = 0; spin < LSPIN_CAP; ++spin) {
            MEMBAR();
            const u4 c = *(const u4*)&lds[L_C4];
            MEMBAR();
            UNROLL for (int i = 0; i < 4; ++i) { da[i] = l4[L_B / 4 + lr + i * 16]; db[i] = l4[L_B / 4 + 1024 + lr + i * 16]; }
            pin(da[0]);
            if (rfl(min4u(c)) >= pv) break;
        }
        const int vo = 64 * q * 32768 + n * 8, so = WS_V + 4 * wv * 32768 + cls * 4096 + tile * 128;
        f4 p0[4], p1[4], p2[4], p3[4];
        UNROLL for (int i = 0; i < 5; ++i) {
            if (i < 4) { p0[i] = mfma16(HR[i], da[i]); p1[i] = mfma16(HI[i], da[i]); p2[i] = mfma16(HR[i], db[i]); p3[i] = mfma16(HI[i], db[i]); }
            if (i > 0) { UNROLL for (int r = 0; r < 4; ++r)
                __builtin_amdgcn_raw_buffer_store_b64(u2{mark(pkh(p0[i - 1][r], p1[i - 1][r]), tags.x, tm), mark(pkh(p2[i - 1][r], p3[i - 1][r]), tags.x, tm)}, rws, vo, so + (16 * r + i - 1) * 32768, SC1); }
            SB();
        }
    }
    if (wv == 0) lset(lds, L_P2, 1u, lane);
    STAMP(12);
    SB();
    MR[0] = *(const u4*)&lds[L_M + lane * 4]; MR[1] = *(const u4*)&lds[L_M + 256 + lane * 4];
    UNROLL for (int i = 0; i < 4; ++i) TW[i] = *(const u4*)&lds[L_M + (2 + i) * 256 + lane * 4];
    core_expand(lane, MR, MI);
    pin(MR); pin(MI); pin(TW);
    u4 PM;
    {
        lwait4(lds, L_C5, pv);
        float gg[4];
        UNROLL for (int k = 0; k < 2; ++k) {
            const f4* pp = (const f4*)&lds[L_GS + ((n >> 2) * 2 + k) * 16];
            const f4 ps = (pp[0] + pp[1]) + (pp[2] + pp[3]);
            const float lg = rbf((ps.x + ps.y) + (ps.z + ps.w));
            gg[2 * k] = rbf(1.0f / (1.0f + __expf(-lg)));
            gg[2 * k + 1] = rbf(1.0f - gg[2 * k]);
        }
        UNROLL for (int k = 0; k < 4; ++k) { gg[k] *= 0.03125f; pin(gg[k]); }
        const int r = n & 3, sa = (n >> 2) * 2;
        UNROLL for (int cc = 0; cc < 2; ++cc) {
            const int u1 = 2 * q + cc;
            const float ta = (float)(sa * u1) * 0.125f + ((r & 1) ? 0.25f : (r & 2) ? 0.5f : 0.0f);
            const float tb = (float)((sa + 1) * u1) * 0.125f + ((r & 1) ? ((r & 2) ? 0.0f : 0.5f) : 0.25f);
            const float ca = cosr(ta), sa_ = sinr(ta), cb = cosr(tb), sb_ = sinr(tb);
            float e[4] = {gg[2] * cb, gg[2] * sb_, gg[3] * cb, gg[3] * sb_};
            pin(e);
            e[0] = fmaf(gg[0], ca, e[0]); pin(e[0]); e[1] = fmaf(gg[0], sa_, e[1]); pin(e[1]); e[2] = fmaf(gg[1], ca, e[2]); pin(e[2]); e[3] = fmaf(gg[1], sa_, e[3]);
            PM[2 * cc] = pkh(e[0], e[1]); PM[2 * cc + 1] = pkh(e[2], e[3]);
        }
    }
    const float osc = __builtin_bit_cast(float, (u32)min(max((int)ex + (int)ew - 130, 1), 254) << 23) * TRUNC_FIX;
    const int ve = 2 * q * 4096 + (128 * wv + n) * 8;
    int vs0 = WS_V + b * 32768, vs1 = vs0 + 4096, vsl = WS_VL + b * 16;
    asm volatile("" : "+s"(vs0), "+s"(vs1), "+s"(vsl));
    int ov = (4 * perm32(lane & 31) + (lane >> 5)) * 16 + (512 * wv + b) * 2048;
    pin(PM); pin(ov);
    STAMP(13);
    if (VGATE) lwait(lds, L_G2, 1u);
    if (VSTAG) { const int st_ = b & 3; if (st_ == 0) VMWAIT(12); else if (st_ == 1) VMWAIT(8); else if (st_ == 2) VMWAIT(4); else VMWAIT(0); }
    else VMWAIT(VWAIT);
    if (VDELAY) __builtin_amdgcn_s_sleep(VDELAY);
    {
        f4 z[9];
        NOUNROLL for (int spin = 0; spin < SPIN_CAP; ++spin) {
            MEMBAR();
            u2 v0[8], v1[8];
            u4 ve8, vo8;
            int a_ = ve; pin(a_);
            if (wv == 3) {
                ve8 = __builtin_amdgcn_raw_buffer_load_b128(rws, q * 8192, vsl, SC1); vo8 = __builtin_amdgcn_raw_buffer_load_b128(rws, q * 8192 + 4096, vsl, SC1);
            } else {
                const u2 e2 = __builtin_amdgcn_raw_buffer_load_b64(rws, a_ + 1024, vs0, SC1), o2 = __builtin_amdgcn_raw_buffer_load_b64(rws, a_ + 1024, vs1, SC1);
                ve8 = u4{e2.x, e2.y, tags.w, tags.w}; vo8 = u4{o2.x, o2.y, tags.w, tags.w};
            }
            UNROLL for (int i = 0; i < 8; ++i) {
                v0[i] = __builtin_amdgcn_raw_buffer_load_b64(rws, a_ + 128 * i, vs0, SC1);
                v1[i] = __builtin_amdgcn_raw_buffer_load_b64(rws, a_ + 128 * i, vs1, SC1);
            }
            SB();
            u32 m = (((wv == 3 ? ve8.y : ve8.z) ^ tags.w) | (ve8.w ^ tags.w) | ((wv == 3 ? vo8.y : vo8.z) ^ tags.w) | (vo8.w ^ tags.w)) ? TM : 0u;
            UNROLL for (int k = 0; k < 9; ++k) {
                const int i = (k + 8) % 9;
                const u4 d = (i == 8 ? u4{ve8.x, wv == 3 ? ve8.z : ve8.y, vo8.x, wv == 3 ? vo8.z : vo8.y} : u4{v0[i & 7].x, v0[i & 7].y, v1[i & 7].x, v1[i & 7].y}) ^ tags.x;
                m |= d.x | d.y; m |= d.z | d.w;
                z[i] = mfma16(PM, d);
                SB();
            }
            if (__all((m & TM) == 0u)) break;
        }
        STAMP(14);
        if (wv == 0 && n == 0) { z[0].x = 0.5f * (z[0].x + z[0].z); z[0].y = 0.5f * (z[0].y + z[0].w); }
        if (wv == 3) { z[8].z = 0.5f * (z[8].x + z[8].z); z[8].w = 0.5f * (z[8].y + z[8].w); }
        u4* l4w = (u4*)&lds[L_A1];
        UNROLL for (int p = 0; p < 2; ++p)
            l4w[(q * 8 + wv) * 32 + 16 * p + n] = u4{pkh(z[p].x, z[p + 2].x), pkh(z[p + 4].x, z[p + 6].x), pkh(z[p].y, z[p + 2].y), pkh(z[p + 4].y, z[p + 6].y)};
        l4w[(q * 8 + 7 - wv) * 32 + 16 - n] = u4{pkh(z[7].z, z[5].z), pkh(z[3].z, z[1].z), pkh(z[7].w, z[5].w), pkh(z[3].w, z[1].w)};
        f2 m[4];
        UNROLL for (int i = 0; i < 4; ++i) m[i] = n == 0 ? f2{z[8 - 2 * i].z, z[8 - 2 * i].w} : f2{z[6 - 2 * i].z, z[6 - 2 * i].w};
        l4w[(q * 8 + 7 - wv) * 32 + ((32 - n) & 31)] = u4{pkh(m[0].x, m[1].x), pkh(m[2].x, m[3].x), pkh(m[0].y, m[1].y), pkh(m[2].y, m[3].y)};
        lset(lds, L_C6 + wv, 2u * pv - 1u, lane);
    }
    {
        u4 in[4];
        NOUNROLL for (int spin = 0; spin < LSPIN_CAP; ++spin) {
            MEMBAR();
            const u4 c = *(const u4*)&lds[L_C6];
            MEMBAR();
            UNROLL for (int s = 0; s < 4; ++s) in[s] = *(const u4*)&lds[L_A1 + (wv * 256 + s * 64 + lane) * 4];
            pin(in[0]);
            if (rfl(min4u(c)) >= 2u * pv - 1u) break;
        }
        f16v zr, zi;
        core_fft<false>(in, MR, MI, TW, zr, zi);
        zr *= osc; zi *= osc;
        const rsrc_t ro = mkrs(out, 2048 * 2048);
        UNROLL for (int hf = 0; hf < 2; ++hf) {
            u4 c;
            UNROLL for (int j = 0; j < 4; ++j) c[j] = pkb(zr[8 * hf + 2 * j], zr[8 * hf + 2 * j + 1]);
            store128<OAUX>(c, ro, ov + 256 * 2048 + 32 * hf);
        }
        UNROLL for (int hf = 0; hf < 2; ++hf) {
            u4 a;
            UNROLL for (int j = 0; j < 4; ++j) a[j] = pkb(zi[8 * hf + 2 * j], zi[8 * hf + 2 * j + 1]);
            store128<OAUX>(a, ro, ov + 32 * hf);
        }
    }
    STAMP(15);
    vd = lwait(lds, L_VD, 1u);
    if (pass == 1 || !(vd & 0x20000u)) {
        if (b == 0 && t == 0) __builtin_amdgcn_raw_buffer_store_b32(tags.z, rws, 0, WS_E, SC1);
        break;
    }
    lset(lds, L_C6 + wv, 2u, lane);
    lwait4(lds, L_C6, 2u);
    VMWAIT(0);
    }
    STAMP_OUT();
}

extern "C" void kernel_launch(void* const* d_in, const int* in_sizes, int n_in,
                              void* d_out, int out_size, void* d_ws, size_t ws_size,
                              hipStream_t stream) {
    gfnet_fused<<<256, 512, 0, stream>>>(d_in[0], d_in[6], d_in[1], d_in[2], d_in[4], d_in[5], (char*)d_ws, d_out);
}
```

```cpp
#include <hip/hip_runtime.h>
#include <hip/hip_bf16.h>
#include <stdint.h>

typedef _Float16 h2 __attribute__((ext_vector_type(2)));
typedef _Float16 h8 __attribute__((ext_vector_type(8)));
typedef __bf16 b2 __attribute__((ext_vector_type(2)));
typedef __bf16 b8 __attribute__((ext_vector_type(8)));
typedef float f2 __attribute__((ext_vector_type(2)));
typedef float f4 __attribute__((ext_vector_type(4)));
typedef float f16v __attribute__((ext_vector_type(16)));
typedef short s4 __attribute__((ext_vector_type(4)));
typedef unsigned u32;
typedef u32 u2 __attribute__((ext_vector_type(2)));
typedef u32 u4 __attribute__((ext_vector_type(4)));
typedef unsigned short us2 __attribute__((ext_vector_type(2)));
#define DI __device__ __forceinline__
#define UNROLL _Pragma("unroll")
#define NOUNROLL _Pragma("unroll 1")
#define LAS __attribute__((address_space(3)))

#define W0AUX 0
#define W1AUX 0
#define VDELAY 0
#define ZDELAY 0
#define WFIRST 0
#define VSTAG 0
#define GEARLY 0
#define ZGATE 0
#define VGATE 0
#define G2DELAY 50
#define MAXLATE 0
#define FLATE 0
#define FDELAY 24
#define EDGE 1
#define SCAN 0
#define ZHOLD 4
#define ZSLEEP 0
#define XLAST 0
#define XDELAY 0
#define G0 0
#define WWAIT 4
#define DIAGB -1
#define CODEPF 4
#define PSLEEP 8
#define WGATE 4
#define W0DELAY 2
#define W0WAIT 0
#define WWIN 12
#define ZWAIT 0
#define VWAIT 0
#define XAUX 2
#define OAUX 2
#define MARGIN 2

#define FSTRIDE 16
constexpr u32 FSTR = FSTRIDE;
constexpr u32 WS_Z = 0;
constexpr u32 WS_V = 4u << 20;
constexpr u32 WS_VL = 12u << 20;
#define HSTR 32
constexpr u32 WS_HB = WS_VL + (32u << 10);
constexpr u32 WS_F = WS_HB + (128u << 10);
constexpr u32 WS_G1 = WS_F + 2048 * FSTR;
constexpr u32 WS_G2 = WS_G1 + 2048 * FSTR;
constexpr u32 WS_E = WS_G2 + 2048 * FSTR;
constexpr u32 WS_BYTES = WS_E + 4096;
constexpr size_t WS_META = 24u << 20;
constexpr int SC1 = 16;
constexpr int SPIN_CAP = 8192;
constexpr int LSPIN_CAP = 1 << 17;
constexpr int OOB = 0x7ffffff0;
constexpr float TRUNC_FIX = 1.00066f;
constexpr u32 TM = 0x00010001u;

template <class T> DI void pin(T& v) { asm volatile("" : "+v"(v)); }
template <class T, int N> DI void pin(T (&v)[N]) { UNROLL for (int i = 0; i < N; ++i) pin(v[i]); }
DI u32 opq(u32 v) { asm("" : "+v"(v)); return v; }
DI u32 pkh(float lo, float hi) { h2 r; r.x = (_Float16)lo; r.y = (_Float16)hi; return __builtin_bit_cast(u32, r); }
DI u32 pkb(float lo, float hi) { b2 r; r.x = (__bf16)lo; r.y = (__bf16)hi; return __builtin_bit_cast(u32, r); }
DI float bflo(u32 v) { return __builtin_bit_cast(float, v << 16); }
DI float bfhi(u32 v) { return __builtin_bit_cast(float, v & 0xffff0000u); }
DI float rbf(float v) { return (float)(__bf16)v; }
DI h2 H(u32 v) { return __builtin_bit_cast(h2, v); }
DI u32 U(h2 v) { return __builtin_bit_cast(u32, v); }
DI f16v mfma32(u4 a, u4 b, f16v c) { return __builtin_amdgcn_mfma_f32_32x32x16_f16(__builtin_bit_cast(h8, a), __builtin_bit_cast(h8, b), c, 0, 0, 0); }
DI f4 mfma16(u4 a, u4 b) { return __builtin_amdgcn_mfma_f32_16x16x32_f16(__builtin_bit_cast(h8, a), __builtin_bit_cast(h8, b), f4{}, 0, 0, 0); }
DI f4 mfma16b(u4 a, u4 b) { return __builtin_amdgcn_mfma_f32_16x16x32_bf16(__builtin_bit_cast(b8, a), __builtin_bit_cast(b8, b), f4{}, 0, 0, 0); }
DI u32 hmul(u32 a, u32 b) { return U(H(a) * H(b)); }
DI u32 hfma(u32 a, u32 b, u32 c) { return U(__builtin_elementwise_fma(H(a), H(b), H(c))); }
DI u32 cmul_swap(u32 x, u32 w) { const h2 a = H(opq(x)), c = H(opq(w)); h2 n = c; n.y = -n.y; return U(__builtin_elementwise_fma(a.yy, n, a.xx * c.yx)); }
DI u32 cmul_conj(u32 x, u32 t) { const h2 a = H(opq(x)), c = H(opq(t)); h2 n = c.yy; n.y = -n.y; return U(__builtin_elementwise_fma(a.yx, n, a * c.xx)); }
DI u32 pkmax(u32 a, u32 b) { return __builtin_bit_cast(u32, __builtin_elementwise_max(__builtin_bit_cast(us2, a), __builtin_bit_cast(us2, b))); }
DI float sinr(float r) { return __builtin_amdgcn_sinf(r); }
DI float cosr(float r) { return __builtin_amdgcn_cosf(r); }
DI int perm32(int n) { return (n & 0x13) | ((n & 4) << 1) | ((n & 8) >> 1); }
DI u32 max3u(u32 a, u32 b, u32 c) { return max(max(a, b), c); }
DI u32 max4u(u4 v) { return max(max(v.x, v.y), max(v.z, v.w)); }
DI u32 min4u(u4 v) { return min(min(v.x, v.y), min(v.z, v.w)); }
DI u32 mark(u32 v, u32 par, u32 tm) { return (par & tm) | (v & ~tm); }
DI u32 rfl(u32 v) { return __builtin_amdgcn_readfirstlane(v); }
DI u32 pow2h(int d) { const float f = __builtin_bit_cast(float, (u32)(127 - min(d, 64)) << 23); return pkh(f, f); }

typedef __amdgpu_buffer_rsrc_t rsrc_t;
DI rsrc_t mkrs(const void* p, u32 bytes) { return __builtin_amdgcn_make_buffer_rsrc((void*)p, (short)0, (int)bytes, 0x00020000); }
template <int AUX> DI void store128(u4 v, rsrc_t r, int off) { __builtin_amdgcn_raw_buffer_store_b128(v, r, off, 0, AUX); }
#define SB() __builtin_amdgcn_sched_barrier(0)
#define MEMBAR() asm volatile("" ::: "memory")
#define VMWAIT(n) asm volatile("s_waitcnt vmcnt(%0)" :: "n"(n) : "memory")
#define STAMP_DECL
#define STAMP(i)
#define STAMP_OUT()
template <int CTRL> DI u32 dpp(u32 v) { return __builtin_amdgcn_update_dpp(0u, v, CTRL, 0xf, 0xf, true); }
DI u32 wave_maxu(u32 v) {
    v = max(v, dpp<0xB1>(v)); v = max(v, dpp<0x4E>(v)); v = max(v, dpp<0x141>(v)); v = max(v, dpp<0x140>(v));
    return max(max((u32)__builtin_amdgcn_readlane(v, 0), (u32)__builtin_amdgcn_readlane(v, 16)), max((u32)__builtin_amdgcn_readlane(v, 32), (u32)__builtin_amdgcn_readlane(v, 48)));
}

DI void core_setup(int lane, int s, u4& tw, u2& m) {
    float fc = (float)perm32(lane & 31) * (1.0f / 1024.0f), p0 = (float)(4 * (lane >> 5) + 8 * s) * fc, p1 = (float)(4 * (lane >> 5) + 8 * (s >> 1) + 2 * (s & 1)) * fc * 32.0f;
    pin(fc); pin(p0); pin(p1);
    float tc[4], ts[4];
    UNROLL for (int i = 0; i < 4; ++i) { const float pt = fmaf((float)i, fc, p0); tc[i] = cosr(pt); ts[i] = sinr(pt); }
    tw = u4{pkh(tc[0], tc[1]), pkh(tc[2], tc[3]), pkh(ts[0], ts[1]), pkh(ts[2], ts[3])};
    const float p2 = fmaf(32.0f, fc, p1);
    m = u2{pkh(cosr(p1), cosr(p2)), pkh(sinr(p1), sinr(p2))};
}
DI void core_expand(int lane, u4 (&MR)[4], u4 (&MI)[4]) {
    const u32 sg = (lane & 1) ? 0x80008000u : 0u;
    MR[2] = MR[0] ^ sg; MR[3] = MR[1] ^ sg;
    UNROLL for (int s = 0; s < 4; ++s) MI[s] = u4{MR[s].z ^ 0x80008000u, MR[s].w ^ 0x80008000u, MR[s].x, MR[s].y};
}
template <bool OUT_B> DI void core_fft(const u4 (&in)[4], const u4 (&MR)[4], const u4 (&MI)[4], const u4 (&TW)[4], f16v& outr, f16v& outi) {
    f16v yr = {}, yi = {};
    UNROLL for (int s = 0; s < 4; ++s) yr = mfma32(in[s], MR[s], yr);
    SB();
    yi = mfma32(in[0], MI[0], yi); yi = mfma32(in[1], MI[1], yi);
    SB();
    h2 t0[8], t1[8];
#define TW_RE(p) do { const h2 pr_ = H(pkh(yr[2 * (p)], yr[2 * (p) + 1])); t0[p] = pr_ * H(TW[(p) >> 1][(p) & 1]); t1[p] = pr_ * H(TW[(p) >> 1][2 + ((p) & 1)]); } while (0)
    TW_RE(0); TW_RE(1); SB();
    yi = mfma32(in[2], MI[2], yi);
    TW_RE(2); TW_RE(3); SB();
    yi = mfma32(in[3], MI[3], yi);
    TW_RE(4); TW_RE(5); TW_RE(6); TW_RE(7); SB();
    outr = f16v{}; outi = f16v{};
    u4 d[4];
#define TW_IM(s) do { UNROLL for (int j = 0; j < 2; ++j) { const int p_ = 2 * (s) + j; const h2 pi_ = H(pkh(yi[2 * p_], yi[2 * p_ + 1])); \
        d[s][j] = U(__builtin_elementwise_fma(pi_, H(TW[s][2 + j]), t0[p_])); d[s][2 + j] = U(__builtin_elementwise_fma(pi_, H(TW[s][j]), -t1[p_])); } } while (0)
    TW_IM(0); SB();
    UNROLL for (int s = 0; s < 4; ++s) {
        outr = OUT_B ? mfma32(MR[s], d[s], outr) : mfma32(d[s], MR[s], outr);
        if (s < 3) TW_IM(s + 1);
        SB();
    }
    UNROLL for (int s = 0; s < 4; ++s) outi = OUT_B ? mfma32(MI[s], d[s], outi) : mfma32(d[s], MI[s], outi);
}
DI u32 rot(float ph) { return pkh(cosr(ph), sinr(ph)); }
DI u32 quarter(u32 r, u32 f) { u32 o; asm("v_pk_mul_f16 %0, %1, %2 op_sel:[1,0] op_sel_hi:[0,1] neg_lo:[1,0]" : "=v"(o) : "v"(r), "v"(f)); return o; }
DI u4 quarter(u4 r) { u32 one = 0x3c003c00u; pin(one); return u4{quarter(r.x, one), quarter(r.y, one), quarter(r.z, one), quarter(r.w, one)}; }

DI u32 tag_of(u32 epoch, u32 idx) { u32 h = (epoch ^ (idx * 0x9E3779B1u)) * 0x85EBCA6Bu; return h ^ (h >> 15); }
DI u32 parity_of(u32 epoch) { return (epoch & 1u) ? 0x00000001u : 0x00010000u; }
DI void flag_store(rsrc_t rws, u32 arr, int b, int lane, u32 payload, u32 tag) {
    __builtin_amdgcn_raw_buffer_store_b64(u2{payload, tag}, rws, lane < 8 ? lane * (int)(256 * FSTR) : OOB, arr + b * FSTR, SC1);
}
DI u4 flag_wait(rsrc_t rws, u32 arr, int b, int lane, u32 epoch) {
    u2 f[4]; u32 tg[4];
    UNROLL for (int j = 0; j < 4; ++j) tg[j] = tag_of(epoch, arr + 4 * lane + j);
    NOUNROLL for (int spin = 0; spin < SPIN_CAP; ++spin) {
        MEMBAR();
        UNROLL for (int j = 0; j < 4; ++j) f[j] = __builtin_amdgcn_raw_buffer_load_b64(rws, lane * (4 * FSTR) + j * FSTR, arr + (b & 7) * (256 * FSTR), SC1);
        if (__all(f[0].y == tg[0] && f[1].y == tg[1] && f[2].y == tg[2] && f[3].y == tg[3])) break;
        __builtin_amdgcn_s_sleep(PSLEEP);
    }
    return u4{f[0].x, f[1].x, f[2].x, f[3].x};
}
DI int cls_of(int b) { const int row = (b & 7) >> 1; return row == 0 ? 4 * (b >> 7) : ((b & 1) ? 8 - row : row); }
DI int tile_of(int b) { return (b & 6) == 0 ? 16 * (b & 1) + ((b >> 3) & 15) : b >> 3; }
DI bool is_helper(int b) { return tile_of(b) == 31; }

constexpr int L_C1 = 0, L_C3 = 4, L_C4 = 8, L_C5 = 12, L_C6 = 16;
constexpr int L_EW = 20;
constexpr int L_WW = 24;
constexpr int L_SW = 28;
constexpr int L_PR = 32;
constexpr int L_SX = 36;
constexpr int L_ZL = 37;
constexpr int L_VD = 38;
constexpr int L_G1 = 39, L_G2 = 40, L_P2 = 41;
constexpr int L_LD = 44;
constexpr int L_GS = 64;
constexpr int L_M = 192;
constexpr int L_A1 = L_M + 1536;
constexpr int L_A2 = L_A1 + 4096;
constexpr int L_B = L_A2 + 4096;
constexpr int L_W = L_B + 8192;
constexpr int L_END = L_W + 64 * 256;

DI void lset(u32* lds, int idx, u32 v, int lane) { MEMBAR(); if (lane == 0) lds[idx] = v; MEMBAR(); }
DI u32 lget(u32* lds, int idx) { MEMBAR(); return rfl(lds[idx]); }
DI u32 lwait(u32* lds, int idx, u32 atleast) {
    u32 v = 0;
    NOUNROLL for (int spin = 0; spin < LSPIN_CAP; ++spin) { v = lget(lds, idx); if (v >= atleast) break; __builtin_amdgcn_s_sleep(1); }
    MEMBAR();
    return v;
}
DI u32 lwait4(u32* lds, int idx, u32 atleast) {
    u4 v;
    NOUNROLL for (int spin = 0; spin < LSPIN_CAP; ++spin) { MEMBAR(); v = *(const u4*)&lds[idx]; if (rfl(min4u(v)) >= atleast) break; __builtin_amdgcn_s_sleep(1); }
    MEMBAR();
    return rfl(max4u(v));
}
struct MagMax {
    h2 hi = {}, lo = {};
    DI void add(u32 a, u32 c) {
        hi = __builtin_elementwise_maximum(__builtin_elementwise_maximum(hi, H(a)), H(c));
        lo = __builtin_elementwise_minimum(__builtin_elementwise_minimum(lo, H(a)), H(c));
    }
    DI u32 exponent() const {
        const u32 mx = pkmax(U(hi) & 0x7fff7fffu, U(lo) & 0x7fff7fffu);
        return wave_maxu(max(mx & 0xffffu, mx >> 16)) >> 7;
    }
};
DI u32 clampe(u32 e) { return min(max(e, 8u), 240u); }

__global__ __launch_bounds__(512, 2) void gfnet_fused(const void* x, const void* gw, const void* war, const void* wai, const void* wbr, const void* wbi,
                                                      char* ws, void* out) {
    __shared__ __attribute__((aligned(16))) u32 lds[L_END];
    STAMP_DECL;
    STAMP(0);
    u32 code = 0;
    if (CODEPF) {
        unsigned long long pc0; int len;
        asm volatile("s_getpc_b64 %0\n.Lgf_begin:\n\ts_mov_b32 %1, .Lgf_end-.Lgf_begin" : "=s"(pc0), "=s"(len));
        code = __builtin_amdgcn_raw_buffer_load_b32(mkrs((const void*)pc0, (u32)max(len, 0)), (threadIdx.x - 256u) < 16u * CODEPF ? ((blockIdx.x >> 3) / CODEPF * CODEPF * 16 + threadIdx.x - 256) * 64 : OOB, 0, 0);
        SB();
    }
    asm volatile("" :: "s"(x), "s"(gw), "s"(war), "s"(wai), "s"(wbr), "s"(wbi), "s"(ws), "s"(out));
    const int wv0 = __builtin_amdgcn_readfirstlane(threadIdx.x >> 6);
    const rsrc_t rws = mkrs(ws, WS_BYTES);

    if (wv0 >= 4) {
        const int wv = wv0, b = blockIdx.x, cls = cls_of(b), tile = tile_of(b);
        const int lane = threadIdx.x & 63, q = lane >> 4, n = lane & 15;
        const rsrc_t rw = mkrs(wv == 4 ? war : wv == 5 ? wai : wv == 6 ? wbr : wbi, 2048 * 1026);
        const int wvo = (lane >> 2) * (128 * 1026) + min(lane & 3, 2) * 16, wso = (cls * 513 + 16 * tile - cls) * 2;
#define WLOAD(r) do { UNROLL for (int st = 0; st < 4; ++st) { const int c_ = 4 * st + ((tile + (r)) & 3); \
                __builtin_amdgcn_raw_ptr_buffer_load_lds(rw, (LAS void*)&lds[L_W + (c_ * 4 + wv - 4) * 256], 16, wvo, wso + c_ * 8208, 0, (r) == 0 ? W0AUX : W1AUX); } SB(); } while (0)
        if (wv == 4) { lds[lane] = 0u; asm volatile("s_waitcnt lgkmcnt(0)" ::: "memory"); }
        if (WFIRST) { __builtin_amdgcn_s_sleep(W0DELAY); WLOAD(0); }
        MEMBAR();
        __builtin_amdgcn_s_barrier();
        MEMBAR();
        const bool helper = wv == 7 && is_helper(b);
        u4 GR, GI, FR, FI, HR, HI, T1, T2;
        u32 ev;
        {
            if (!WFIRST) { __builtin_amdgcn_s_sleep(W0DELAY); if (!SCAN) WLOAD(0); }
            const bool split = (b & 6) == 0;
            const int er = split ? 16 * (tile & 15) + ((lane >> 1) & 15) : 8 * tile + ((lane >> 1) & 7);
            const int ec = (split ? (tile & 16) * 16 : 0) - cls + ((lane & 1) ? (split ? 262 : 518) : SCAN ? (split ? 256 : 512) : 0);
            const int sr = split ? 16 * (tile & 15) + (lane >> 2) : 8 * tile + (lane >> 3);
            const int scn = (split ? (tile & 16) * 16 + (lane & 3) * 64 : (lane & 7) * 64) - cls;
            u32 scan = SCAN ? __builtin_amdgcn_raw_buffer_load_b32(rw, ((cls + 8 * sr) * 513 + scn) * 2, 0, W0AUX) : 0u;
            u32 edge = EDGE ? __builtin_amdgcn_raw_buffer_load_b32(rw, ((cls + 8 * er) * 513 + ec) * 2, 0, W0AUX) : 0u;
            ev = __builtin_amdgcn_raw_buffer_load_b32(rws, 0, WS_E, SC1);
            const u4 smp = __builtin_amdgcn_raw_buffer_load_b128(rw, (lane >> 3) * (2 * 131328 + 128) + (lane & 7) * 16, 0, 0);
            MEMBAR();
            if (helper) {
            UNROLL for (int j = 0; j < 4; ++j) {
                GR[j] = rot((float)((4 * q + j) * (cls + 8 * n)) * (1.0f / 128.0f) + (cls == 4 ? 0.25f : 0.0f));
                HR[j] = rot((float)(n * (cls + 8 * (4 * q + j))) * (1.0f / 128.0f));
                FR[j] = rot((float)(n * (4 * q + j)) * (1.0f / 16.0f));
                T1[j] = rot((float)((4 * q + j) * (cls + 8 * n)) * (1.0f / 2048.0f));
                T2[j] = rot((float)(n * (cls + 8 * (4 * q + j))) * (1.0f / 2048.0f));
            }
            GI = quarter(GR); HI = quarter(HR); FI = quarter(FR);
            pin(GR); pin(GI); pin(HR); pin(HI); pin(FR); pin(FI); pin(T1); pin(T2);
            }
            VMWAIT(W0WAIT);
            STAMP(1);
            if (SCAN) { WLOAD(4); VMWAIT(WWIN); }
            const u32 lda = (u32)(size_t)(LAS u32*)&lds[L_LD + wv - 4];
            if (ZHOLD == 1) asm volatile("ds_write_b32 %0, %1" :: "v"(lda), "v"(1u) : "memory");
            WLOAD(1); VMWAIT(WWIN);
            if (ZHOLD == 2) asm volatile("ds_write_b32 %0, %1" :: "v"(lda), "v"(1u) : "memory");
            WLOAD(2); VMWAIT(WWIN);
            if (ZHOLD == 3) asm volatile("ds_write_b32 %0, %1" :: "v"(lda), "v"(1u) : "memory");
            WLOAD(3);
            if (ZHOLD == 4) asm volatile("ds_write_b32 %0, %1" :: "v"(lda), "v"(1u) : "memory");
            pin(edge); pin(scan); pin(code);
            MagMax ms; ms.add(smp.x, smp.y); ms.add(smp.z, smp.w);
            const u32 es = clampe(ms.exponent() + MARGIN);
            VMWAIT(0);
            lset(lds, L_PR + wv - 4, es, lane);
            STAMP(2);
        }
        const u32 ep0 = rfl(ev) + 1u;
        if (wv == 5 || wv == 6) {
            lwait4(lds, L_PR, 1u);
            if (MAXLATE) lwait(lds, L_P2, 1u);
            MagMax m;
            UNROLL for (int c = 0; c < 32; ++c) { const u4 v = *(const u4*)&lds[L_W + ((wv - 5) * 32 + c) * 256 + lane * 4]; m.add(v.x, v.y); m.add(v.z, v.w); }
            const u32 e = clampe(m.exponent());
            MEMBAR(); if (lane == 0) *(u2*)&lds[L_WW + 2 * (wv - 5)] = u2{e, e}; MEMBAR();
        }
        if (VGATE && wv == 6) {
            __builtin_amdgcn_s_setprio(2);
            lwait(lds, L_P2, 1u);
            __builtin_amdgcn_s_sleep(G2DELAY);
            flag_wait(rws, WS_G2, b, lane, ep0);
            lset(lds, L_G2, 1u, lane);
            STAMP(5);
        }
        if (wv == 4) {
            if (ZGATE) { flag_wait(rws, WS_G1, b, lane, ep0); lset(lds, L_G1, 1u, lane); }
            const u32 ex = lwait4(lds, L_EW, 1u), ew = lwait4(lds, L_WW, 1u);
            if (FLATE) lwait(lds, L_P2, 1u);
            flag_store(rws, WS_F, b, lane, ex | (ew << 8), tag_of(ep0, WS_F + b));
            lwait(lds, L_P2, 1u);
            __builtin_amdgcn_s_sleep(FDELAY);
            const u4 f = flag_wait(rws, WS_F, b, lane, ep0);
            const u32 gx = wave_maxu(max4u(f & 0xffu)), gwm = wave_maxu(max4u((f >> 8) & 0xffu));
            const bool redo = gx > lget(lds, L_SX) || gwm > lwait4(lds, L_PR, 1u);
            lset(lds, L_VD, gx | (gwm << 8) | (redo ? 0x20000u : 0x10000u), lane);
            STAMP(5);
        } else if (helper) {
            __builtin_amdgcn_s_setprio(2);
            u32 ew = lwait4(lds, L_PR, 1u);
            NOUNROLL for (u32 pass = 0; pass < 2; ++pass) {
                u4 WH[2];
                const float sw = __builtin_bit_cast(float, (249u - ew) << 23);
                UNROLL for (int r = 0; r < 4; ++r) UNROLL for (int f = 0; f < 2; ++f) {
                    const unsigned short* wp = (const unsigned short*)&lds[L_W + (n * 4 + 2 * f) * 256 + (4 * q + r) * 16] + 16 + cls;
                    float re = bflo(wp[0]) * sw, im = bflo(wp[512]) * sw;
                    pin(re);
                    WH[f][r] = pkh(re, im);
                }
                pin(WH);
                STAMP(3);
                lwait(lds, L_ZL, pass + 1u);
                const u32 tg = tag_of(ep0 + pass, 0u), par = parity_of(ep0 + pass);
                u4 hz;
                NOUNROLL for (int spin = 0; spin < SPIN_CAP; ++spin) {
                    MEMBAR();
                    u32 bad = 0;
                    UNROLL for (int j = 0; j < 4; ++j) { const u2 g = __builtin_amdgcn_raw_buffer_load_b64(rws, (64 * q + n + 16 * j) * HSTR, WS_HB + ((b & 7) >> 1) * (256 * HSTR), SC1); hz[j] = g.x & ~TM; bad |= g.y ^ tg; }
                    if (__all(bad == 0u)) break;
                    __builtin_amdgcn_s_sleep(4);
                }
                STAMP(4);
                const u32 f16 = cls == 0 ? 0x00002c00u : cls == 4 ? 0x2c000000u : cls > 4 ? 0xac002c00u : 0x2c002c00u;
                u4 d, o[2];
                UNROLL for (int j = 0; j < 4; ++j) d[j] = hmul(hz[j], f16);
                f4 yr = mfma16(d, GR), yi = mfma16(d, GI);
                UNROLL for (int r = 0; r < 4; ++r) d[r] = cmul_conj(pkh(yr[r], yi[r]), T1[r]);
                const f4 xr = mfma16(FR, d), xi = mfma16(FI, d);
                UNROLL for (int f = 0; f < 2; ++f) {
                    UNROLL for (int r = 0; r < 4; ++r) d[r] = cmul_swap(pkh(xr[r], xi[r]), WH[f][r]);
                    yr = mfma16(d, FR); yi = mfma16(d, FI);
                    UNROLL for (int r = 0; r < 4; ++r) o[f][r] = cmul_conj(pkh(yr[r], yi[r]), T2[r]);
                }
                const f4 p0 = mfma16(HR, o[0]), p1 = mfma16(HI, o[0]), p2 = mfma16(HR, o[1]), p3 = mfma16(HI, o[1]);
                UNROLL for (int r = 0; r < 4; ++r)
                    store128<SC1>(u4{mark(pkh(p0[r], p1[r]), par, TM), tg, mark(pkh(p2[r], p3[r]), par, TM), tg}, rws, (64 * q + n) * 16 + WS_VL + r * 256 + cls * 4096);
                STAMP(5);
                const u32 vd = lwait(lds, L_VD, 1u);
                if (!(vd & 0x20000u)) break;
                ew = (vd >> 8) & 0xffu;
            }
        }
        if (CODEPF) asm volatile(".Lgf_end:");
        STAMP_OUT();
        return;
    }

    u32 vd = 0;
    NOUNROLL for (u32 pass = 0; pass < 2; ++pass) {
    int t = threadIdx.x, wv = wv0, b = blockIdx.x;
    pin(t);
    asm volatile("" : "+s"(wv));
    asm volatile("" : "+s"(b));
    const int lane = t & 63, q = lane >> 4, n = lane & 15;
    const u32 pv = pass + 1u;
    if (XLAST && pass == 0) { __builtin_amdgcn_s_barrier(); MEMBAR(); __builtin_amdgcn_s_setprio(1); if (XDELAY) __builtin_amdgcn_s_sleep(XDELAY); }
    u2 xv[8];
    const rsrc_t rx = mkrs(x, 2048 * 2048);
    const u4 smp = __builtin_amdgcn_raw_buffer_load_b128(rx, (lane >> 3) * (524288 + 2048 + 256) + (lane & 7) * 16, 0, 0);
    UNROLL for (int s1 = 0; s1 < 8; ++s1) xv[s1] = __builtin_amdgcn_raw_buffer_load_b64(rx, t * 8, (256 * s1 + b) * 2048, XAUX);
    const u32 ev = __builtin_amdgcn_raw_buffer_load_b32(rws, 0, WS_E, SC1);
    const u2 gv = __builtin_amdgcn_raw_buffer_load_b64(mkrs(gw, 2048), t * 8, 0, 0);
    MEMBAR();
    SB();
    if (!XLAST && pass == 0) { __builtin_amdgcn_s_barrier(); MEMBAR(); __builtin_amdgcn_s_setprio(1); }
    u4 MR[4], MI[4], TW[4];
    {
        u4 tw; u2 m;
        core_setup(lane, wv, tw, m);
        *(u4*)&lds[L_M + (2 + wv) * 256 + lane * 4] = tw;
        lds[L_M + (wv >> 1) * 256 + lane * 4 + (wv & 1)] = m.x; lds[L_M + (wv >> 1) * 256 + lane * 4 + 2 + (wv & 1)] = m.y;
    }
    const int zc = perm32(lane & 31) + (lane >> 5) * 256, zcu = zc + ((lane & 31) == 0 ? 543 : 511);
    int zl = (zc >> 4) * 16384 + (zc & 15) * 4, zu = (zcu >> 4) * 16384 + (zcu & 15) * 4;
    int zh = lane == 0 ? 0 : OOB;
    int tro = L_A1 + wv * 1024 + (4 * (lane >> 5) + ((lane & 15) >> 2)) * 16 + ((lane >> 4) & 1) * 8 + (lane & 3) * 2;
    u32 tm = TM;
    pin(zl); pin(zu); pin(zh); pin(tro); pin(tm);
    STAMP(1);
    SB();
    pin(code);
    u32 ex;
    {
        MagMax ms; ms.add(smp.x, smp.y); ms.add(smp.z, smp.w);
        ex = pass ? (vd & 0xffu) : clampe(ms.exponent() + MARGIN);
    }
    STAMP(2);
    u4 tags;
    u32 z[16], z512;
    {
        const float sc = __builtin_bit_cast(float, (250u - ex) << 23);
        const float rs = sc * 0.70710678118654752f;
        u32 o[8][2];
        UNROLL for (int j = 0; j < 2; ++j) {
            f2 v[8];
            UNROLL for (int s1 = 0; s1 < 8; ++s1) v[s1] = f2{bflo(xv[s1][j]), bfhi(xv[s1][j])};
            const f2 a0 = v[0] + v[4], a1 = v[1] + v[5], a2 = v[2] + v[6], a3 = v[3] + v[7];
            const f2 b0 = (v[0] - v[4]) * sc, b1 = v[1] - v[5], b2_ = (v[2] - v[6]) * sc, b3 = v[3] - v[7];
            const f2 e0 = a0 + a2, e1 = (a1 + a3) * sc, dm = b1 - b3, dp = b1 + b3;
            f2 r[8];
            r[0] = e0 * sc + e1; r[1] = e0 * sc - e1;
            r[2] = dm * rs + b0; r[3] = -(dp * rs) - b2_;
            r[4] = (a0 - a2) * sc; r[5] = (a3 - a1) * sc;
            r[6] = b0 - dm * rs; r[7] = b2_ - dp * rs;
            UNROLL for (int p = 0; p < 8; ++p) o[p][j] = pkh(r[p].x, r[p].y);
        }
        UNROLL for (int p = 0; p < 8; ++p) *(u2*)&lds[L_A1 + p * 512 + 2 * t] = u2{o[p][0], o[p][1]};
        lset(lds, L_C1 + wv, pv, lane);
        const u32 epoch = rfl(ev) + pv;
        tags = u4{parity_of(epoch), 0u, epoch, tag_of(epoch, 0u)};
        u4 in[4];
        NOUNROLL for (int spin = 0; spin < LSPIN_CAP; ++spin) {
            MEMBAR();
            const u4 c = *(const u4*)&lds[L_C1];
            MEMBAR();
            MR[0] = *(const u4*)&lds[L_M + lane * 4]; MR[1] = *(const u4*)&lds[L_M + 256 + lane * 4];
            UNROLL for (int s = 0; s < 4; ++s) {
                const u2 re = __builtin_bit_cast(u2, __builtin_amdgcn_ds_read_tr16_b64_v4i16((LAS s4*)&lds[tro + s * 128]));
                const u2 im = __builtin_bit_cast(u2, __builtin_amdgcn_ds_read_tr16_b64_v4i16((LAS s4*)&lds[tro + s * 128 + 512]));
                in[s] = u4{re.x, re.y, im.x, im.y};
            }
            pin(MR[0]); pin(MR[1]);
            if (rfl(min4u(c)) >= pv) break;
        }
        UNROLL for (int i = 0; i < 4; ++i) TW[i] = *(const u4*)&lds[L_M + (2 + i) * 256 + lane * 4];
        STAMP(3);
        if (ZGATE && pass == 0 && wv == 0) flag_store(rws, WS_G1, b, lane, 0u, tag_of(tags.z, WS_G1 + b));
        core_expand(lane, MR, MI);
        f16v zr, zi;
        core_fft<true>(in, MR, MI, TW, zr, zi);
        UNROLL for (int r = 0; r < 16; ++r) z[r] = mark(pkh(zr[r], zi[r]), tags.x, tm);
        z512 = z[8];
        const u32 za = __builtin_amdgcn_readlane(z[8], 32), zb = __builtin_amdgcn_readlane(z[0], 0);
        MEMBAR();
        const u32 zt = lane < 32 ? za : zb;
        UNROLL for (int r = 8; r < 16; ++r) z[r] = (lane & 31) == 0 ? (r < 15 ? z[r + 1] : zt) : z[r];
    }
    pin(z);
    STAMP(4);
    const int zs = WS_Z + wv * 64 * 16384 + (((b >> 2) & 3) * 64 + (b & 3) * 16 + ((b >> 4) & 3) * 4 + (b >> 6)) * 64;
    bool zsent = false;
#define TRY_STORE_Z(force) do { if (!zsent) { \
        bool go = true; \
        if (ZHOLD) { if (force) lwait4(lds, L_LD, 1u); else { MEMBAR(); go = rfl(min4u(*(const u4*)&lds[L_LD])) != 0u; } } \
        if (go) { \
            UNROLL for (int r = 0; r < 16; ++r) __builtin_amdgcn_raw_buffer_store_b32(z[r], rws, r < 8 ? zl : zu, zs + (r & 7) * 32768, SC1); \
            __builtin_amdgcn_raw_buffer_store_b64(u2{z512, tags.w}, rws, zh, WS_HB + (wv * 256 + b) * HSTR, SC1); \
            zsent = true; } } SB(); } while (0)
    TRY_STORE_Z(false);
    const int row = (b & 7) >> 1, cls = cls_of(b), tile = tile_of(b);
    const bool packed = row == 0;
    u4 FR, FI, GR[4], GI[4], HR[4], HI[4], sel1;
    {
        const float ofs = cls == 4 ? 0.25f : 0.0f;
        float gu = (float)(cls + 8 * n) * (1.0f / 2048.0f), g0 = (float)(64 * q + 4 * wv) * gu + ofs;
        u32 s16 = 0x2c002c00u;
        pin(gu); pin(g0); pin(s16);
        UNROLL for (int i = 0; i < 4; ++i) UNROLL for (int j = 0; j < 4; ++j) { const u32 g = rot(fmaf((float)(16 * j + i), gu, g0)); GR[i][j] = hmul(g, s16); GI[i][j] = quarter(g, s16); }
    }
    pin(GR); pin(GI);
    {
        MagMax m;
        UNROLL for (int s1 = 0; s1 < 8; ++s1) m.add(xv[s1].x, xv[s1].y);
        if (wv == 0) lset(lds, L_SX, ex, lane);
        lset(lds, L_EW + wv, clampe(m.exponent()), lane);
    }
    TRY_STORE_Z(false);
    UNROLL for (int j = 0; j < 4; ++j) FR[j] = rot((float)(n * (4 * q + j)) * (1.0f / 16.0f));
    FI = quarter(FR);
    {
        float hs[4], hu[4];
        UNROLL for (int i = 0; i < 4; ++i) { hs[i] = (float)(16 * n + 4 * wv + i) * (1.0f / 2048.0f); hu[i] = (float)(cls + 32 * q + 8 * i); }
        pin(hs); pin(hu);
        UNROLL for (int i = 0; i < 4; ++i) { UNROLL for (int j = 0; j < 4; ++j) { float a = hs[i] * hu[j]; pin(a); HR[i][j] = rot(a); } HI[i] = quarter(HR[i]); }
        const int d = n + cls - 8 * q;
        UNROLL for (int j = 0; j < 4; ++j) sel1[j] = (d >> 1) == j ? ((d & 1) ? 0xffff0000u : 0xffffu) : 0u;
    }
    pin(FR); pin(FI); pin(HR); pin(HI); pin(sel1);
    TRY_STORE_Z(true);
    STAMP(5);
    VMWAIT(WWAIT);
    u4 WA[4], WB[4];
    u32 ew = 0;
    UNROLL for (int i = 0; i < 4; ++i) asm volatile("" : "=v"(WA[i]), "=v"(WB[i]));
#define WIDEN(es) do { \
        u4 wr[4][4]; \
        int wl = L_W / 4 + wv * 1024 + n * 4 + min(q, 2); pin(wl); \
        UNROLL for (int i = 0; i < 4; ++i) UNROLL for (int a = 0; a < 4; ++a) wr[i][a] = ((const u4*)lds)[wl + (i * 4 + a) * 64]; \
        ew = pass ? (vd >> 8) & 0xffu : (es); \
        const u32 sw = ((249u - ew - (packed ? 1u : 0u)) << 7) * 0x10001u;       \
        const u4 sels = sel1 & sw; \
        SB(); \
        f4 wf[4][4]; \
        UNROLL for (int k = 0; k < 10; ++k) { \
            if (k < 8) { wf[k >> 1][2 * (k & 1)] = mfma16b(wr[k >> 1][2 * (k & 1)], sels); wf[k >> 1][2 * (k & 1) + 1] = mfma16b(wr[k >> 1][2 * (k & 1) + 1], sels); } \
            if (k >= 2) { const int i_ = (k - 2) >> 1, f_ = k & 1; UNROLL for (int r = 0; r < 4; ++r) (f_ ? WB : WA)[i_][r] = pkh(wf[i_][2 * f_][r], wf[i_][2 * f_ + 1][r]); } \
            SB(); } \
        pin(WA); pin(WB); } while (0)
    MEMBAR();
    const u4 pr0 = *(const u4*)&lds[L_PR];
    const bool wearly = rfl(min4u(pr0)) != 0u;
    MEMBAR();
    if (wearly) WIDEN(rfl(max4u(pr0)));
    STAMP(6);
    u32 z1[4][4], z2[4][4];
    const int zvm = q * 64 + (63 - tile) * 16384 + (15 - n) * 4;
    const int zv1 = cls <= 4 ? q * 64 + tile * 16384 + n * 4 : zvm;
    int zso = WS_Z + row * 64 * 16384 + wv * 4096;
    asm volatile("" : "+s"(zso));
#define LOAD_Z() do { \
        if (packed) { int a_ = zvm; pin(a_); UNROLL for (int i = 0; i < 4; ++i) UNROLL for (int j = 0; j < 4; ++j) z2[i][j] = __builtin_amdgcn_raw_buffer_load_b32(rws, a_ + (4 * i + j) * 256, zso, SC1); } \
        else { UNROLL for (int i = 0; i < 4; ++i) UNROLL for (int j = 0; j < 4; ++j) z2[i][j] = tags.x; } \
        int c_ = zv1; pin(c_); UNROLL for (int i = 0; i < 4; ++i) UNROLL for (int j = 0; j < 4; ++j) z1[i][j] = __builtin_amdgcn_raw_buffer_load_b32(rws, c_ + (4 * i + j) * 256, zso, SC1); } while (0)
    if (ZGATE) lwait(lds, L_G1, 1u);
    VMWAIT(ZWAIT);
    if (ZDELAY) __builtin_amdgcn_s_sleep(ZDELAY);
    LOAD_Z();
    if (wv == 0) lset(lds, L_ZL, pv, lane);
    SB();
    STAMP(7);
    u32 x1 = tags.x ^ (cls > 4 ? 0x80000000u : 0u), x2 = tags.x ^ (cls == 4 ? 0x00008000u : cls == 0 ? 0x80000000u : 0u);
    const int lw = ((wv * 16 + 4 * q) * 16 + n) * 4;
    int lr = (q * 16 + 4 * wv) * 16 + n;
    const u4* l4 = (const u4*)lds;
    pin(GR); pin(GI); pin(x1); pin(x2); pin(lr);
    STAMP(8);
    SB();
    {
        f4 yr[4], yi[4];
        NOUNROLL for (int spin = 0; spin < SPIN_CAP; ++spin) {
            u32 m = 0;
            UNROLL for (int i = 0; i < 4; ++i) {
                u4 d;
                UNROLL for (int j = 0; j < 4; ++j) { z1[i][j] ^= x1; z2[i][j] ^= x2; }
                UNROLL for (int j = 0; j < 4; ++j) d[j] = U(H(z1[i][j]) + H(z2[i][j]));
                UNROLL for (int j = 0; j < 4; ++j) m |= z1[i][j] | z2[i][j];
                yr[i] = mfma16(GR[i], d); yi[i] = mfma16(GI[i], d);
                SB();
            }
            if (__all((m & TM) == 0u)) break;
            MEMBAR();
            LOAD_Z();
        }
        STAMP(9);
        u4 y[4];
        UNROLL for (int i = 0; i < 4; ++i) UNROLL for (int r = 0; r < 4; ++r) y[r][i] = pkh(yr[i][r], yi[i][r]);
        UNROLL for (int r = 0; r < 4; ++r) *(u4*)&lds[L_A2 + lw + r * 64] = y[r];
        lset(lds, L_C3 + wv, pv, lane);
    }
    if (!wearly) { const u32 es = lwait4(lds, L_PR, 1u); WIDEN(es); }
    {
        u4 d[4];
        NOUNROLL for (int spin = 0; spin < LSPIN_CAP; ++spin) {
            MEMBAR();
            const u4 c = *(const u4*)&lds[L_C3];
            MEMBAR();
            UNROLL for (int i = 0; i < 4; ++i) d[i] = l4[L_A2 / 4 + lr + i * 16];
            pin(d[0]);
            if (rfl(min4u(c)) >= pv) break;
        }
        STAMP(10);
        if (VGATE == 1 && pass == 0 && wv == 0) { flag_store(rws, WS_G2, b, lane, 0u, tag_of(tags.z, WS_G2 + b)); lset(lds, L_P2, 1u, lane); }
        pin(WA); pin(WB);
        f4 xr[4], xi[4];
        u4 xx[4], ya[4], yb[4];
        UNROLL for (int i = 0; i < 4; ++i) {
            xr[i] = mfma16(FR, d[i]); xi[i] = mfma16(FI, d[i]);
            if (i >= 2) { UNROLL for (int r = 0; r < 4; ++r) xx[i - 2][r] = pkh(xr[i - 2][r], xi[i - 2][r]); pin(xx[i - 2]); }
            SB();
        }
        UNROLL for (int r = 0; r < 4; ++r) ya[0][r] = cmul_swap(xx[0][r], WA[0][r]);
        UNROLL for (int i = 2; i < 4; ++i) { UNROLL for (int r = 0; r < 4; ++r) xx[i][r] = pkh(xr[i][r], xi[i][r]); pin(xx[i]); }
        SB();
        f4 p0[4], p1[4], p2[4], p3[4];
        UNROLL for (int i = 0; i < 4; ++i) {
            p0[i] = mfma16(FR, ya[i]); p1[i] = mfma16(FI, ya[i]);
            if (i < 3) { UNROLL for (int r = 0; r < 4; ++r) ya[i + 1][r] = cmul_swap(xx[i + 1][r], WA[i + 1][r]); }
            else { UNROLL for (int r = 0; r < 4; ++r) yb[0][r] = cmul_swap(xx[0][r], WB[0][r]); }
            SB();
        }
        u4 ra[4], rb[4];
        UNROLL for (int i = 0; i < 4; ++i) {
            p2[i] = mfma16(FR, yb[i]); p3[i] = mfma16(FI, yb[i]);
            if (i < 3) { UNROLL for (int r = 0; r < 4; ++r) yb[i + 1][r] = cmul_swap(xx[i + 1][r], WB[i + 1][r]); }
            UNROLL for (int k = 0; k < 4; ++k) ra[i][k] = pkh(p0[k][i], p1[k][i]);
            *(u4*)&lds[L_B + lw + i * 64] = ra[i];
            SB();
        }
        UNROLL for (int i = 0; i < 3; ++i) UNROLL for (int r = 0; r < 4; ++r) rb[r][i] = pkh(p2[i][r], p3[i][r]);
        SB();
        UNROLL for (int r = 0; r < 4; ++r) { rb[r][3] = pkh(p2[3][r], p3[3][r]); *(u4*)&lds[L_B + 4096 + lw + r * 64] = rb[r]; }
        lset(lds, L_C4 + wv, pv, lane);
    }
    STAMP(11);
    if (VGATE == 2 && pass == 0 && wv == 0) { flag_store(rws, WS_G2, b, lane, 0u, tag_of(tags.z, WS_G2 + b)); lset(lds, L_P2, 1u, lane); }
    {
        u4 da[4], db[4];
        NOUNROLL for (int spin = 0; spin < LSPIN_CAP; ++spin) {
            MEMBAR();
            const u4 c = *(const u4*)&lds[L_C4];
            MEMBAR();
            UNROLL for (int i = 0; i < 4; ++i) { da[i] = l4[L_B / 4 + lr + i * 16]; db[i] = l4[L_B / 4 + 1024 + lr + i * 16]; }
            pin(da[0]);
            if (rfl(min4u(c)) >= pv) break;
        }
        const int vo = 64 * q * 32768 + n * 8, so = WS_V + 4 * wv * 32768 + cls * 4096 + tile * 128;
        f4 p0[4], p1[4], p2[4], p3[4];
        UNROLL for (int i = 0; i < 5; ++i) {
            if (i < 4) { p0[i] = mfma16(HR[i], da[i]); p1[i] = mfma16(HI[i], da[i]); p2[i] = mfma16(HR[i], db[i]); p3[i] = mfma16(HI[i], db[i]); }
            if (i > 0) { UNROLL for (int r = 0; r < 4; ++r)
                __builtin_amdgcn_raw_buffer_store_b64(u2{mark(pkh(p0[i - 1][r], p1[i - 1][r]), tags.x, tm), mark(pkh(p2[i - 1][r], p3[i - 1][r]), tags.x, tm)}, rws, vo, so + (16 * r + i - 1) * 32768, SC1); }
            SB();
        }
    }
    if (wv == 0) lset(lds, L_P2, 1u, lane);
    STAMP(12);
    SB();
    {
        pin(xv);
        const f2 g01 = {bflo(gv.x), bfhi(gv.x)}, g23 = {bflo(gv.y), bfhi(gv.y)};
        f4 acc = {};
        UNROLL for (int i = 0; i < 4; ++i) {
            u4 pp;
            UNROLL for (int k = 0; k < 2; ++k) {
                const f2 a = f2{bflo(xv[2 * i + k].x), bfhi(xv[2 * i + k].x)} * g01, c = f2{bflo(xv[2 * i + k].y), bfhi(xv[2 * i + k].y)} * g23;
                pp[2 * k] = pkb(a.x, a.y); pp[2 * k + 1] = pkb(c.x, c.y);
            }
            const u32 s0 = n == 2 * i ? 0x3f803f80u : 0u, s1 = n == 2 * i + 1 ? 0x3f803f80u : 0u;
            acc = __builtin_amdgcn_mfma_f32_16x16x32_bf16(__builtin_bit_cast(b8, pp), __builtin_bit_cast(b8, u4{s0, s0, s1, s1}), acc, 0, 0, 0);
        }
        if (n < 8) lds[L_GS + n * 16 + (t >> 4)] = __builtin_bit_cast(u32, (acc.x + acc.y) + (acc.z + acc.w));
        lset(lds, L_C5 + wv, pv, lane);
    }
    MR[0] = *(const u4*)&lds[L_M + lane * 4]; MR[1] = *(const u4*)&lds[L_M + 256 + lane * 4];
    UNROLL for (int i = 0; i < 4; ++i) TW[i] = *(const u4*)&lds[L_M + (2 + i) * 256 + lane * 4];
    core_expand(lane, MR, MI);
    pin(MR); pin(MI); pin(TW);
    u4 PM;
    {
        lwait4(lds, L_C5, pv);
        float gg[4];
        UNROLL for (int k = 0; k < 2; ++k) {
            const f4* pp = (const f4*)&lds[L_GS + ((n >> 2) * 2 + k) * 16];
            const f4 ps = (pp[0] + pp[1]) + (pp[2] + pp[3]);
            const float lg = rbf((ps.x + ps.y) + (ps.z + ps.w));
            gg[2 * k] = rbf(1.0f / (1.0f + __expf(-lg)));
            gg[2 * k + 1] = rbf(1.0f - gg[2 * k]);
        }
        UNROLL for (int k = 0; k < 4; ++k) { gg[k] *= 0.03125f; pin(gg[k]); }
        const int r = n & 3, sa = (n >> 2) * 2;
        UNROLL for (int cc = 0; cc < 2; ++cc) {
            const int u1 = 2 * q + cc;
            const float ta = (float)(sa * u1) * 0.125f + ((r & 1) ? 0.25f : (r & 2) ? 0.5f : 0.0f);
            const float tb = (float)((sa + 1) * u1) * 0.125f + ((r & 1) ? ((r & 2) ? 0.0f : 0.5f) : 0.25f);
            const float ca = cosr(ta), sa_ = sinr(ta), cb = cosr(tb), sb_ = sinr(tb);
            float e[4] = {gg[2] * cb, gg[2] * sb_, gg[3] * cb, gg[3] * sb_};
            pin(e);
            e[0] = fmaf(gg[0], ca, e[0]); pin(e[0]); e[1] = fmaf(gg[0], sa_, e[1]); pin(e[1]); e[2] = fmaf(gg[1], ca, e[2]); pin(e[2]); e[3] = fmaf(gg[1], sa_, e[3]);
            PM[2 * cc] = pkh(e[0], e[1]); PM[2 * cc + 1] = pkh(e[2], e[3]);
        }
    }
    const float osc = __builtin_bit_cast(float, (u32)min(max((int)ex + (int)ew - 130, 1), 254) << 23) * TRUNC_FIX;
    const int ve = 2 * q * 4096 + (128 * wv + n) * 8;
    int vs0 = WS_V + b * 32768, vs1 = vs0 + 4096, vsl = WS_VL + b * 16;
    asm volatile("" : "+s"(vs0), "+s"(vs1), "+s"(vsl));
    int ov = (4 * perm32(lane & 31) + (lane >> 5)) * 16 + (512 * wv + b) * 2048;
    pin(PM); pin(ov);
    STAMP(13);
    if (VGATE) lwait(lds, L_G2, 1u);
    if (VSTAG) { const int st_ = b & 3; if (st_ == 0) VMWAIT(12); else if (st_ == 1) VMWAIT(8); else if (st_ == 2) VMWAIT(4); else VMWAIT(0); }
    else VMWAIT(VWAIT);
    if (VDELAY) __builtin_amdgcn_s_sleep(VDELAY);
    {
        f4 z[9];
        NOUNROLL for (int spin = 0; spin < SPIN_CAP; ++spin) {
            MEMBAR();
            u2 v0[8], v1[8];
            u4 ve8, vo8;
            int a_ = ve; pin(a_);
            if (wv == 3) {
                ve8 = __builtin_amdgcn_raw_buffer_load_b128(rws, q * 8192, vsl, SC1); vo8 = __builtin_amdgcn_raw_buffer_load_b128(rws, q * 8192 + 4096, vsl, SC1);
            } else {
                const u2 e2 = __builtin_amdgcn_raw_buffer_load_b64(rws, a_ + 1024, vs0, SC1), o2 = __builtin_amdgcn_raw_buffer_load_b64(rws, a_ + 1024, vs1, SC1);
                ve8 = u4{e2.x, e2.y, tags.w, tags.w}; vo8 = u4{o2.x, o2.y, tags.w, tags.w};
            }
            UNROLL for (int i = 0; i < 8; ++i) {
                v0[i] = __builtin_amdgcn_raw_buffer_load_b64(rws, a_ + 128 * i, vs0, SC1);
                v1[i] = __builtin_amdgcn_raw_buffer_load_b64(rws, a_ + 128 * i, vs1, SC1);
            }
            SB();
            u32 m = (((wv == 3 ? ve8.y : ve8.z) ^ tags.w) | (ve8.w ^ tags.w) | ((wv == 3 ? vo8.y : vo8.z) ^ tags.w) | (vo8.w ^ tags.w)) ? TM : 0u;
            UNROLL for (int k = 0; k < 9; ++k) {
                const int i = (k + 8) % 9;
                const u4 d = (i == 8 ? u4{ve8.x, wv == 3 ? ve8.z : ve8.y, vo8.x, wv == 3 ? vo8.z : vo8.y} : u4{v0[i & 7].x, v0[i & 7].y, v1[i & 7].x, v1[i & 7].y}) ^ tags.x;
                m |= d.x | d.y; m |= d.z | d.w;
                z[i] = mfma16(PM, d);
                SB();
            }
            if (__all((m & TM) == 0u)) break;
        }
        STAMP(14);
        if (wv == 0 && n == 0) { z[0].x = 0.5f * (z[0].x + z[0].z); z[0].y = 0.5f * (z[0].y + z[0].w); }
        if (wv == 3) { z[8].z = 0.5f * (z[8].x + z[8].z); z[8].w = 0.5f * (z[8].y + z[8].w); }
        u4* l4w = (u4*)&lds[L_A1];
        UNROLL for (int p = 0; p < 2; ++p)
            l4w[(q * 8 + wv) * 32 + 16 * p + n] = u4{pkh(z[p].x, z[p + 2].x), pkh(z[p + 4].x, z[p + 6].x), pkh(z[p].y, z[p + 2].y), pkh(z[p + 4].y, z[p + 6].y)};
        l4w[(q * 8 + 7 - wv) * 32 + 16 - n] = u4{pkh(z[7].z, z[5].z), pkh(z[3].z, z[1].z), pkh(z[7].w, z[5].w), pkh(z[3].w, z[1].w)};
        f2 m[4];
        UNROLL for (int i = 0; i < 4; ++i) m[i] = n == 0 ? f2{z[8 - 2 * i].z, z[8 - 2 * i].w} : f2{z[6 - 2 * i].z, z[6 - 2 * i].w};
        l4w[(q * 8 + 7 - wv) * 32 + ((32 - n) & 31)] = u4{pkh(m[0].x, m[1].x), pkh(m[2].x, m[3].x), pkh(m[0].y, m[1].y), pkh(m[2].y, m[3].y)};
        lset(lds, L_C6 + wv, 2u * pv - 1u, lane);
    }
    {
        u4 in[4];
        NOUNROLL for (int spin = 0; spin < LSPIN_CAP; ++spin) {
            MEMBAR();
            const u4 c = *(const u4*)&lds[L_C6];
            MEMBAR();
            UNROLL for (int s = 0; s < 4; ++s) in[s] = *(const u4*)&lds[L_A1 + (wv * 256 + s * 64 + lane) * 4];
            pin(in[0]);
            if (rfl(min4u(c)) >= 2u * pv - 1u) break;
        }
        f16v zr, zi;
        core_fft<false>(in, MR, MI, TW, zr, zi);
        zr *= osc; zi *= osc;
        const rsrc_t ro = mkrs(out, 2048 * 2048);
        UNROLL for (int hf = 0; hf < 2; ++hf) {
            u4 c;
            UNROLL for (int j = 0; j < 4; ++j) c[j] = pkb(zr[8 * hf + 2 * j], zr[8 * hf + 2 * j + 1]);
            store128<OAUX>(c, ro, ov + 256 * 2048 + 32 * hf);
        }
        UNROLL for (int hf = 0; hf < 2; ++hf) {
            u4 a;
            UNROLL for (int j = 0; j < 4; ++j) a[j] = pkb(zi[8 * hf + 2 * j], zi[8 * hf + 2 * j + 1]);
            store128<OAUX>(a, ro, ov + 32 * hf);
        }
    }
    STAMP(15);
    vd = lwait(lds, L_VD, 1u);
    if (pass == 1 || !(vd & 0x20000u)) {
        if (b == 0 && t == 0) __builtin_amdgcn_raw_buffer_store_b32(tags.z, rws, 0, WS_E, SC1);
        break;
    }
    lset(lds, L_C6 + wv, 2u, lane);
    lwait4(lds, L_C6, 2u);
    VMWAIT(0);
    }
    STAMP_OUT();
}

extern "C" void kernel_launch(void* const* d_in, const int* in_sizes, int n_in,
                              void* d_out, int out_size, void* d_ws, size_t ws_size,
                              hipStream_t stream) {
    gfnet_fused<<<256, 512, 0, stream>>>(d_in[0], d_in[6], d_in[1], d_in[2], d_in[4], d_in[5], (char*)d_ws, d_out);
}
```

```cpp
#include <hip/hip_runtime.h>
#include <hip/hip_bf16.h>
#include <stdint.h>

typedef _Float16 h2 __attribute__((ext_vector_type(2)));
typedef _Float16 h8 __attribute__((ext_vector_type(8)));
typedef __bf16 b2 __attribute__((ext_vector_type(2)));
typedef __bf16 b8 __attribute__((ext_vector_type(8)));
typedef float f2 __attribute__((ext_vector_type(2)));
typedef float f4 __attribute__((ext_vector_type(4)));
typedef float f16v __attribute__((ext_vector_type(16)));
typedef short s4 __attribute__((ext_vector_type(4)));
typedef unsigned u32;
typedef u32 u2 __attribute__((ext_vector_type(2)));
typedef u32 u4 __attribute__((ext_vector_type(4)));
typedef unsigned short us2 __attribute__((ext_vector_type(2)));
#define DI __device__ __forceinline__
#define UNROLL _Pragma("unroll")
#define NOUNROLL _Pragma("unroll 1")
#define LAS __attribute__((address_space(3)))

#define W0AUX 0
#define W1AUX 0
#define VDELAY 0
#define ZDELAY 0
#define WFIRST 0
#define VSTAG 0
#define GEARLY 0
#define ZGATE 0
#define VGATE 0
#define G2DELAY 50
#define MAXLATE 0
#define FLATE 0
#define FDELAY 24
#define EDGE 1
#define SCAN 0
#define ZHOLD 4
#define ZSLEEP 0
#define XLAST 0
#define XDELAY 0
#define G0 0
#define WWAIT 4
#define DIAGB -1
#define CODEPF 4
#define VUN 4
#define QUICK 170
#define ZUN 0
#define PSLEEP 8
#define WGATE 4
#define W0DELAY 2
#define W0WAIT 0
#define WWIN 12
#define ZWAIT 0
#define VWAIT 0
#define XAUX 2
#define OAUX 2
#define MARGIN 2

#define FSTRIDE 16
constexpr u32 FSTR = FSTRIDE;
constexpr u32 WS_Z = 0;
constexpr u32 WS_V = 4u << 20;
constexpr u32 WS_VL = 12u << 20;
#define HSTR 32
constexpr u32 WS_HB = WS_VL + (32u << 10);
constexpr u32 WS_F = WS_HB + (128u << 10);
constexpr u32 WS_G1 = WS_F + 2048 * FSTR;
constexpr u32 WS_G2 = WS_G1 + 2048 * FSTR;
constexpr u32 WS_E = WS_G2 + 2048 * FSTR;
constexpr u32 WS_BYTES = WS_E + 4096;
constexpr size_t WS_META = 24u << 20;
constexpr int SC1 = 16;
constexpr int SPIN_CAP = 8192;
constexpr int LSPIN_CAP = 1 << 17;
constexpr int OOB = 0x7ffffff0;
constexpr float TRUNC_FIX = 1.00066f;
constexpr u32 TM = 0x00010001u;

template <class T> DI void pin(T& v) { asm volatile("" : "+v"(v)); }
template <class T, int N> DI void pin(T (&v)[N]) { UNROLL for (int i = 0; i < N; ++i) pin(v[i]); }
DI u32 opq(u32 v) { asm("" : "+v"(v)); return v; }
DI u32 pkh(float lo, float hi) { h2 r; r.x = (_Float16)lo; r.y = (_Float16)hi; return __builtin_bit_cast(u32, r); }
DI u32 pkb(float lo, float hi) { b2 r; r.x = (__bf16)lo; r.y = (__bf16)hi; return __builtin_bit_cast(u32, r); }
DI float bflo(u32 v) { return __builtin_bit_cast(float, v << 16); }
DI float bfhi(u32 v) { return __builtin_bit_cast(float, v & 0xffff0000u); }
DI float rbf(float v) { return (float)(__bf16)v; }
DI h2 H(u32 v) { return __builtin_bit_cast(h2, v); }
DI u32 U(h2 v) { return __builtin_bit_cast(u32, v); }
DI f16v mfma32(u4 a, u4 b, f16v c) { return __builtin_amdgcn_mfma_f32_32x32x16_f16(__builtin_bit_cast(h8, a), __builtin_bit_cast(h8, b), c, 0, 0, 0); }
DI f4 mfma16(u4 a, u4 b) { return __builtin_amdgcn_mfma_f32_16x16x32_f16(__builtin_bit_cast(h8, a), __builtin_bit_cast(h8, b), f4{}, 0, 0, 0); }
DI f4 mfma16b(u4 a, u4 b) { return __builtin_amdgcn_mfma_f32_16x16x32_bf16(__builtin_bit_cast(b8, a), __builtin_bit_cast(b8, b), f4{}, 0, 0, 0); }
DI u32 hmul(u32 a, u32 b) { return U(H(a) * H(b)); }
DI u32 hfma(u32 a, u32 b, u32 c) { return U(__builtin_elementwise_fma(H(a), H(b), H(c))); }
DI u32 cmul_swap(u32 x, u32 w) { const h2 a = H(opq(x)), c = H(opq(w)); h2 n = c; n.y = -n.y; return U(__builtin_elementwise_fma(a.yy, n, a.xx * c.yx)); }
DI u32 cmul_conj(u32 x, u32 t) { const h2 a = H(opq(x)), c = H(opq(t)); h2 n = c.yy; n.y = -n.y; return U(__builtin_elementwise_fma(a.yx, n, a * c.xx)); }
DI u32 pkmax(u32 a, u32 b) { return __builtin_bit_cast(u32, __builtin_elementwise_max(__builtin_bit_cast(us2, a), __builtin_bit_cast(us2, b))); }
DI float sinr(float r) { return __builtin_amdgcn_sinf(r); }
DI float cosr(float r) { return __builtin_amdgcn_cosf(r); }
DI int perm32(int n) { return (n & 0x13) | ((n & 4) << 1) | ((n & 8) >> 1); }
DI u32 max3u(u32 a, u32 b, u32 c) { return max(max(a, b), c); }
DI u32 max4u(u4 v) { return max(max(v.x, v.y), max(v.z, v.w)); }
DI u32 min4u(u4 v) { return min(min(v.x, v.y), min(v.z, v.w)); }
DI u32 mark(u32 v, u32 par, u32 tm) { return (par & tm) | (v & ~tm); }
DI u32 rfl(u32 v) { return __builtin_amdgcn_readfirstlane(v); }
DI u32 pow2h(int d) { const float f = __builtin_bit_cast(float, (u32)(127 - min(d, 64)) << 23); return pkh(f, f); }

typedef __amdgpu_buffer_rsrc_t rsrc_t;
DI rsrc_t mkrs(const void* p, u32 bytes) { return __builtin_amdgcn_make_buffer_rsrc((void*)p, (short)0, (int)bytes, 0x00020000); }
template <int AUX> DI void store128(u4 v, rsrc_t r, int off) { __builtin_amdgcn_raw_buffer_store_b128(v, r, off, 0, AUX); }
#define SB() __builtin_amdgcn_sched_barrier(0)
#define MEMBAR() asm volatile("" ::: "memory")
#define VMWAIT(n) asm volatile("s_waitcnt vmcnt(%0)" :: "n"(n) : "memory")
#define STAMP_DECL
#define STAMP(i)
#define STAMP_OUT()
template <int CTRL> DI u32 dpp(u32 v) { return __builtin_amdgcn_update_dpp(0u, v, CTRL, 0xf, 0xf, true); }
DI u32 wave_maxu(u32 v) {
    v = max(v, dpp<0xB1>(v)); v = max(v, dpp<0x4E>(v)); v = max(v, dpp<0x141>(v)); v = max(v, dpp<0x140>(v));
    return max(max((u32)__builtin_amdgcn_readlane(v, 0), (u32)__builtin_amdgcn_readlane(v, 16)), max((u32)__builtin_amdgcn_readlane(v, 32), (u32)__builtin_amdgcn_readlane(v, 48)));
}

DI void core_setup(int lane, int s, u4& tw, u2& m) {
    float fc = (float)perm32(lane & 31) * (1.0f / 1024.0f), p0 = (float)(4 * (lane >> 5) + 8 * s) * fc, p1 = (float)(4 * (lane >> 5) + 8 * (s >> 1) + 2 * (s & 1)) * fc * 32.0f;
    pin(fc); pin(p0); pin(p1);
    float tc[4], ts[4];
    UNROLL for (int i = 0; i < 4; ++i) { const float pt = fmaf((float)i, fc, p0); tc[i] = cosr(pt); ts[i] = sinr(pt); }
    tw = u4{pkh(tc[0], tc[1]), pkh(tc[2], tc[3]), pkh(ts[0], ts[1]), pkh(ts[2], ts[3])};
    const float p2 = fmaf(32.0f, fc, p1);
    m = u2{pkh(cosr(p1), cosr(p2)), pkh(sinr(p1), sinr(p2))};
}
DI void core_expand(int lane, u4 (&MR)[4], u4 (&MI)[4]) {
    const u32 sg = (lane & 1) ? 0x80008000u : 0u;
    MR[2] = MR[0] ^ sg; MR[3] = MR[1] ^ sg;
    UNROLL for (int s = 0; s < 4; ++s) MI[s] = u4{MR[s].z ^ 0x80008000u, MR[s].w ^ 0x80008000u, MR[s].x, MR[s].y};
}
template <bool OUT_B> DI void core_fft(const u4 (&in)[4], const u4 (&MR)[4], const u4 (&MI)[4], const u4 (&TW)[4], f16v& outr, f16v& outi) {
    f16v yr = {}, yi = {};
    UNROLL for (int s = 0; s < 4; ++s) yr = mfma32(in[s], MR[s], yr);
    SB();
    UNROLL for (int s = 0; s < 3; ++s) yi = mfma32(in[s], MI[s], yi);
    SB();
    h2 t0[8], t1[8];
#define TW_RE(p) do { const h2 pr_ = H(pkh(yr[2 * (p)], yr[2 * (p) + 1])); t0[p] = pr_ * H(TW[(p) >> 1][(p) & 1]); t1[p] = pr_ * H(TW[(p) >> 1][2 + ((p) & 1)]); pin(t0[p]); pin(t1[p]); } while (0)
    yi = mfma32(in[3], MI[3], yi);
    SB();
    TW_RE(0); TW_RE(1); TW_RE(2); TW_RE(3); SB();
    outr = f16v{}; outi = f16v{};
    u4 d[4];
#define TW_IM(s) do { UNROLL for (int j = 0; j < 2; ++j) { const int p_ = 2 * (s) + j; const h2 pi_ = H(pkh(yi[2 * p_], yi[2 * p_ + 1])); \
        d[s][j] = U(__builtin_elementwise_fma(pi_, H(TW[s][2 + j]), t0[p_])); d[s][2 + j] = U(__builtin_elementwise_fma(pi_, H(TW[s][j]), -t1[p_])); } } while (0)
    TW_IM(0); SB();
    UNROLL for (int s = 0; s < 4; ++s) {
        outr = OUT_B ? mfma32(MR[s], d[s], outr) : mfma32(d[s], MR[s], outr);
        if (s < 3) TW_IM(s + 1);
        SB();
        if (s < 2) { outi = OUT_B ? mfma32(MI[s], d[s], outi) : mfma32(d[s], MI[s], outi); TW_RE(4 + 2 * s); TW_RE(5 + 2 * s); SB(); }
    }
    UNROLL for (int s = 2; s < 4; ++s) outi = OUT_B ? mfma32(MI[s], d[s], outi) : mfma32(d[s], MI[s], outi);
    SB();
}
DI u32 rot(float ph) { return pkh(cosr(ph), sinr(ph)); }
DI u32 quarter(u32 r, u32 f) { u32 o; asm("v_pk_mul_f16 %0, %1, %2 op_sel:[1,0] op_sel_hi:[0,1] neg_lo:[1,0]" : "=v"(o) : "v"(r), "v"(f)); return o; }
DI u4 quarter(u4 r) { u32 one = 0x3c003c00u; pin(one); return u4{quarter(r.x, one), quarter(r.y, one), quarter(r.z, one), quarter(r.w, one)}; }

DI u32 tag_of(u32 epoch, u32 idx) { u32 h = (epoch ^ (idx * 0x9E3779B1u)) * 0x85EBCA6Bu; return h ^ (h >> 15); }
DI u32 parity_of(u32 epoch) { return (epoch & 1u) ? 0x00000001u : 0x00010000u; }
DI void flag_store(rsrc_t rws, u32 arr, int b, int lane, u32 payload, u32 tag) {
    __builtin_amdgcn_raw_buffer_store_b64(u2{payload, tag}, rws, lane < 8 ? lane * (int)(256 * FSTR) : OOB, arr + b * FSTR, SC1);
}
DI u4 flag_wait(rsrc_t rws, u32 arr, int b, int lane, u32 epoch) {
    u2 f[4]; u32 tg[4];
    UNROLL for (int j = 0; j < 4; ++j) tg[j] = tag_of(epoch, arr + 4 * lane + j);
    NOUNROLL for (int spin = 0; spin < SPIN_CAP; ++spin) {
        MEMBAR();
        UNROLL for (int j = 0; j < 4; ++j) f[j] = __builtin_amdgcn_raw_buffer_load_b64(rws, lane * (4 * FSTR) + j * FSTR, arr + (b & 7) * (256 * FSTR), SC1);
        if (__all(f[0].y == tg[0] && f[1].y == tg[1] && f[2].y == tg[2] && f[3].y == tg[3])) break;
        __builtin_amdgcn_s_sleep(PSLEEP);
    }
    return u4{f[0].x, f[1].x, f[2].x, f[3].x};
}
DI int cls_of(int b) { const int row = (b & 7) >> 1; return row == 0 ? 4 * (b >> 7) : ((b & 1) ? 8 - row : row); }
DI int tile_of(int b) { return (b & 6) == 0 ? 16 * (b & 1) + ((b >> 3) & 15) : b >> 3; }
DI bool is_helper(int b) { return tile_of(b) == 31; }

constexpr int L_C1 = 0, L_C3 = 4, L_C4 = 8, L_C5 = 12, L_C6 = 16;
constexpr int L_EW = 20;
constexpr int L_WW = 24;
constexpr int L_SW = 28;
constexpr int L_PR = 32;
constexpr int L_SX = 36;
constexpr int L_ZL = 37;
constexpr int L_VD = 38;
constexpr int L_G1 = 39, L_G2 = 40, L_P2 = 41;
constexpr int L_LD = 44;
constexpr int L_GS = 64;
constexpr int L_M = 192;
constexpr int L_A1 = L_M + 1536;
constexpr int L_A2 = L_A1 + 4096;
constexpr int L_B = L_A2 + 4096;
constexpr int L_W = L_B + 8192;
constexpr int L_END = L_W + 64 * 256;

DI void lset(u32* lds, int idx, u32 v, int lane) { MEMBAR(); if (lane == 0) lds[idx] = v; MEMBAR(); }
DI u32 lget(u32* lds, int idx) { MEMBAR(); return rfl(lds[idx]); }
DI u32 lwait(u32* lds, int idx, u32 atleast) {
    u32 v = 0;
    NOUNROLL for (int spin = 0; spin < LSPIN_CAP; ++spin) { v = lget(lds, idx); if (v >= atleast) break; __builtin_amdgcn_s_sleep(1); }
    MEMBAR();
    return v;
}
DI u32 lwait4(u32* lds, int idx, u32 atleast) {
    u4 v;
    NOUNROLL for (int spin = 0; spin < LSPIN_CAP; ++spin) { MEMBAR(); v = *(const u4*)&lds[idx]; if (rfl(min4u(v)) >= atleast) break; __builtin_amdgcn_s_sleep(1); }
    MEMBAR();
    return rfl(max4u(v));
}
struct MagMax {
    h2 hi = {}, lo = {};
    DI void add(u32 a, u32 c) {
        hi = __builtin_elementwise_maximum(__builtin_elementwise_maximum(hi, H(a)), H(c));
        lo = __builtin_elementwise_minimum(__builtin_elementwise_minimum(lo, H(a)), H(c));
    }
    DI u32 exponent() const {
        const u32 mx = pkmax(U(hi) & 0x7fff7fffu, U(lo) & 0x7fff7fffu);
        return wave_maxu(max(mx & 0xffffu, mx >> 16)) >> 7;
    }
};
DI u32 clampe(u32 e) { return min(max(e, 8u), 240u); }

__global__ __launch_bounds__(512, 2) void gfnet_fused(const void* x, const void* gw, const void* war, const void* wai, const void* wbr, const void* wbi,
                                                      char* ws, void* out) {
    __shared__ __attribute__((aligned(16))) u32 lds[L_END];
    STAMP_DECL;
    STAMP(0);
    u32 code = 0;
    if (CODEPF) {
        unsigned long long pc0; int len;
        asm volatile("s_getpc_b64 %0\n.Lgf_begin:\n\ts_mov_b32 %1, .Lgf_end-.Lgf_begin" : "=s"(pc0), "=s"(len));
        code = __builtin_amdgcn_raw_buffer_load_b32(mkrs((const void*)pc0, (u32)max(len, 0)), (threadIdx.x - 256u) < 16u * CODEPF ? ((blockIdx.x >> 3) / CODEPF * CODEPF * 16 + threadIdx.x - 256) * 64 : OOB, 0, 0);
        SB();
    }
    asm volatile("" :: "s"(x), "s"(gw), "s"(war), "s"(wai), "s"(wbr), "s"(wbi), "s"(ws), "s"(out));
    const int wv0 = __builtin_amdgcn_readfirstlane(threadIdx.x >> 6);
    const rsrc_t rws = mkrs(ws, WS_BYTES);

    if (wv0 >= 4) {
        const u32 tstart = VUN == 4 ? (u32)__builtin_amdgcn_s_memrealtime() : 0u;
        const int wv = wv0, b = blockIdx.x, cls = cls_of(b), tile = tile_of(b);
        const int lane = threadIdx.x & 63, q = lane >> 4, n = lane & 15;
        const rsrc_t rw = mkrs(wv == 4 ? war : wv == 5 ? wai : wv == 6 ? wbr : wbi, 2048 * 1026);
        const int wvo = (lane >> 2) * (128 * 1026) + min(lane & 3, 2) * 16, wso = (cls * 513 + 16 * tile - cls) * 2;
#define WLOAD(r) do { UNROLL for (int st = 0; st < 4; ++st) { const int c_ = 4 * st + ((tile + (r)) & 3); \
                __builtin_amdgcn_raw_ptr_buffer_load_lds(rw, (LAS void*)&lds[L_W + (c_ * 4 + wv - 4) * 256], 16, wvo, wso + c_ * 8208, 0, (r) == 0 ? W0AUX : W1AUX); } SB(); } while (0)
        if (wv == 4) { lds[lane] = 0u; asm volatile("s_waitcnt lgkmcnt(0)" ::: "memory"); }
        if (WFIRST) { __builtin_amdgcn_s_sleep(W0DELAY); WLOAD(0); }
        MEMBAR();
        __builtin_amdgcn_s_barrier();
        MEMBAR();
        const bool helper = wv == 7 && is_helper(b);
        u4 GR, GI, FR, FI, HR, HI, T1, T2;
        u32 ev;
        {
            if (VUN == 1) __builtin_amdgcn_raw_buffer_store_b32(0u, rws, lane * 128, WS_V + b * 32768 + (wv - 4) * 8192, SC1);
            if (!WFIRST) { __builtin_amdgcn_s_sleep(W0DELAY); if (!SCAN) WLOAD(0); }
            if (VUN == 2) __builtin_amdgcn_raw_buffer_store_b32(0u, rws, lane * 128, WS_V + b * 32768 + (wv - 4) * 8192, SC1);
            if (VUN == 3) { UNROLL for (int i = 0; i < 4; ++i) store128<SC1>(u4{0u, 0u, 0u, 0u}, rws, (lane >> 2) * 128 + (lane & 3) * 16 + WS_V + b * 32768 + (wv - 4) * 8192 + i * 2048); }
            const bool split = (b & 6) == 0;
            const int er = split ? 16 * (tile & 15) + ((lane >> 1) & 15) : 8 * tile + ((lane >> 1) & 7);
            const int ec = (split ? (tile & 16) * 16 : 0) - cls + ((lane & 1) ? (split ? 262 : 518) : SCAN ? (split ? 256 : 512) : 0);
            const int sr = split ? 16 * (tile & 15) + (lane >> 2) : 8 * tile + (lane >> 3);
            const int scn = (split ? (tile & 16) * 16 + (lane & 3) * 64 : (lane & 7) * 64) - cls;
            u32 scan = SCAN ? __builtin_amdgcn_raw_buffer_load_b32(rw, ((cls + 8 * sr) * 513 + scn) * 2, 0, W0AUX) : 0u;
            u32 edge = EDGE ? __builtin_amdgcn_raw_buffer_load_b32(rw, ((cls + 8 * er) * 513 + ec) * 2, 0, W0AUX) : 0u;
            ev = __builtin_amdgcn_raw_buffer_load_b32(rws, 0, WS_E, SC1);
            const u4 smp = __builtin_amdgcn_raw_buffer_load_b128(rw, (lane >> 3) * (2 * 131328 + 128) + (lane & 7) * 16, 0, 0);
            MEMBAR();
            if (helper) {
            UNROLL for (int j = 0; j < 4; ++j) {
                GR[j] = rot((float)((4 * q + j) * (cls + 8 * n)) * (1.0f / 128.0f) + (cls == 4 ? 0.25f : 0.0f));
                HR[j] = rot((float)(n * (cls + 8 * (4 * q + j))) * (1.0f / 128.0f));
                FR[j] = rot((float)(n * (4 * q + j)) * (1.0f / 16.0f));
                T1[j] = rot((float)((4 * q + j) * (cls + 8 * n)) * (1.0f / 2048.0f));
                T2[j] = rot((float)(n * (cls + 8 * (4 * q + j))) * (1.0f / 2048.0f));
            }
            GI = quarter(GR); HI = quarter(HR); FI = quarter(FR);
            pin(GR); pin(GI); pin(HR); pin(HI); pin(FR); pin(FI); pin(T1); pin(T2);
            }
            VMWAIT(W0WAIT);
            STAMP(1);
            if (SCAN) { WLOAD(4); VMWAIT(WWIN); }
            const bool quick = VUN == 4 && (int)((u32)__builtin_amdgcn_s_memrealtime() - tstart) < QUICK;
            if (quick) {
                __builtin_amdgcn_raw_buffer_store_b32(0u, rws, lane * 128, WS_V + b * 32768 + (wv - 4) * 8192, SC1);
                if (ZUN && wv < 6) { const int rg = ((b & 7) >> 1) * 64 + (cls >= 4 ? 63 - tile : tile);
                    __hip_atomic_fetch_or((__attribute__((address_space(1))) u32*)(ws + WS_Z + rg * 16384 + (wv - 4) * 8192 + lane * 128), opq(0u), __ATOMIC_RELAXED, __HIP_MEMORY_SCOPE_AGENT); }
            }
            const u32 lda = (u32)(size_t)(LAS u32*)&lds[L_LD + wv - 4];
            if (ZHOLD == 1) asm volatile("ds_write_b32 %0, %1" :: "v"(lda), "v"(1u) : "memory");
            WLOAD(1); VMWAIT(WWIN);
            if (ZHOLD == 2) asm volatile("ds_write_b32 %0, %1" :: "v"(lda), "v"(1u) : "memory");
            WLOAD(2); VMWAIT(WWIN);
            if (ZHOLD == 3) asm volatile("ds_write_b32 %0, %1" :: "v"(lda), "v"(1u) : "memory");
            WLOAD(3);
            if (VUN == 4) VMWAIT(12);
            if (ZHOLD == 4) asm volatile("ds_write_b32 %0, %1" :: "v"(lda), "v"(1u) : "memory");
            pin(edge); pin(scan); pin(code);
            MagMax ms; ms.add(smp.x, smp.y); ms.add(smp.z, smp.w);
            const u32 es = clampe(ms.exponent() + MARGIN);
            VMWAIT(0);
            lset(lds, L_PR + wv - 4, es, lane);
            STAMP(2);
        }
        const u32 ep0 = rfl(ev) + 1u;
        if (wv == 5 || wv == 6) {
            lwait4(lds, L_PR, 1u);
            if (MAXLATE) lwait(lds, L_P2, 1u);
            MagMax m;
            UNROLL for (int c = 0; c < 32; ++c) { const u4 v = *(const u4*)&lds[L_W + ((wv - 5) * 32 + c) * 256 + lane * 4]; m.add(v.x, v.y); m.add(v.z, v.w); }
            const u32 e = clampe(m.exponent());
            MEMBAR(); if (lane == 0) *(u2*)&lds[L_WW + 2 * (wv - 5)] = u2{e, e}; MEMBAR();
        }
        if (VGATE && wv == 6) {
            __builtin_amdgcn_s_setprio(2);
            lwait(lds, L_P2, 1u);
            __builtin_amdgcn_s_sleep(G2DELAY);
            flag_wait(rws, WS_G2, b, lane, ep0);
            lset(lds, L_G2, 1u, lane);
            STAMP(5);
        }
        if (wv == 4) {
            if (ZGATE) { flag_wait(rws, WS_G1, b, lane, ep0); lset(lds, L_G1, 1u, lane); }
            const u32 ex = lwait4(lds, L_EW, 1u), ew = lwait4(lds, L_WW, 1u);
            if (FLATE) lwait(lds, L_P2, 1u);
            flag_store(rws, WS_F, b, lane, ex | (ew << 8), tag_of(ep0, WS_F + b));
            lwait(lds, L_P2, 1u);
            __builtin_amdgcn_s_sleep(FDELAY);
            const u4 f = flag_wait(rws, WS_F, b, lane, ep0);
            const u32 gx = wave_maxu(max4u(f & 0xffu)), gwm = wave_maxu(max4u((f >> 8) & 0xffu));
            const bool redo = gx > lget(lds, L_SX) || gwm > lwait4(lds, L_PR, 1u);
            lset(lds, L_VD, gx | (gwm << 8) | (redo ? 0x20000u : 0x10000u), lane);
            STAMP(5);
        } else if (helper) {
            __builtin_amdgcn_s_setprio(2);
            u32 ew = lwait4(lds, L_PR, 1u);
            NOUNROLL for (u32 pass = 0; pass < 2; ++pass) {
                u4 WH[2];
                const float sw = __builtin_bit_cast(float, (249u - ew) << 23);
                UNROLL for (int r = 0; r < 4; ++r) UNROLL for (int f = 0; f < 2; ++f) {
                    const unsigned short* wp = (const unsigned short*)&lds[L_W + (n * 4 + 2 * f) * 256 + (4 * q + r) * 16] + 16 + cls;
                    float re = bflo(wp[0]) * sw, im = bflo(wp[512]) * sw;
                    pin(re);
                    WH[f][r] = pkh(re, im);
                }
                pin(WH);
                STAMP(3);
                lwait(lds, L_ZL, pass + 1u);
                const u32 tg = tag_of(ep0 + pass, 0u), par = parity_of(ep0 + pass);
                u4 hz;
                NOUNROLL for (int spin = 0; spin < SPIN_CAP; ++spin) {
                    MEMBAR();
                    u32 bad = 0;
                    UNROLL for (int j = 0; j < 4; ++j) { const u2 g = __builtin_amdgcn_raw_buffer_load_b64(rws, (64 * q + n + 16 * j) * HSTR, WS_HB + ((b & 7) >> 1) * (256 * HSTR), SC1); hz[j] = g.x & ~TM; bad |= g.y ^ tg; }
                    if (__all(bad == 0u)) break;
                    __builtin_amdgcn_s_sleep(4);
                }
                STAMP(4);
                const u32 f16 = cls == 0 ? 0x00002c00u : cls == 4 ? 0x2c000000u : cls > 4 ? 0xac002c00u : 0x2c002c00u;
                u4 d, o[2];
                UNROLL for (int j = 0; j < 4; ++j) d[j] = hmul(hz[j], f16);
                f4 yr = mfma16(d, GR), yi = mfma16(d, GI);
                UNROLL for (int r = 0; r < 4; ++r) d[r] = cmul_conj(pkh(yr[r], yi[r]), T1[r]);
                const f4 xr = mfma16(FR, d), xi = mfma16(FI, d);
                UNROLL for (int f = 0; f < 2; ++f) {
                    UNROLL for (int r = 0; r < 4; ++r) d[r] = cmul_swap(pkh(xr[r], xi[r]), WH[f][r]);
                    yr = mfma16(d, FR); yi = mfma16(d, FI);
                    UNROLL for (int r = 0; r < 4; ++r) o[f][r] = cmul_conj(pkh(yr[r], yi[r]), T2[r]);
                }
                const f4 p0 = mfma16(HR, o[0]), p1 = mfma16(HI, o[0]), p2 = mfma16(HR, o[1]), p3 = mfma16(HI, o[1]);
                UNROLL for (int r = 0; r < 4; ++r)
                    store128<SC1>(u4{mark(pkh(p0[r], p1[r]), par, TM), tg, mark(pkh(p2[r], p3[r]), par, TM), tg}, rws, (64 * q + n) * 16 + WS_VL + r * 256 + cls * 4096);
                STAMP(5);
                const u32 vd = lwait(lds, L_VD, 1u);
                if (!(vd & 0x20000u)) break;
                ew = (vd >> 8) & 0xffu;
            }
        }
        if (CODEPF) asm volatile(".Lgf_end:");
        STAMP_OUT();
        return;
    }

    u32 vd = 0;
    NOUNROLL for (u32 pass = 0; pass < 2; ++pass) {
    int t = threadIdx.x, wv = wv0, b = blockIdx.x;
    pin(t);
    asm volatile("" : "+s"(wv));
    asm volatile("" : "+s"(b));
    const int lane = t & 63, q = lane >> 4, n = lane & 15;
    const u32 pv = pass + 1u;
    if (XLAST && pass == 0) { __builtin_amdgcn_s_barrier(); MEMBAR(); __builtin_amdgcn_s_setprio(1); if (XDELAY) __builtin_amdgcn_s_sleep(XDELAY); }
    u2 xv[8];
    const rsrc_t rx = mkrs(x, 2048 * 2048);
    const u4 smp = __builtin_amdgcn_raw_buffer_load_b128(rx, (lane >> 3) * (524288 + 2048 + 256) + (lane & 7) * 16, 0, 0);
    UNROLL for (int s1 = 0; s1 < 8; ++s1) xv[s1] = __builtin_amdgcn_raw_buffer_load_b64(rx, t * 8, (256 * s1 + b) * 2048, XAUX);
    const u32 ev = __builtin_amdgcn_raw_buffer_load_b32(rws, 0, WS_E, SC1);
    const u2 gv = __builtin_amdgcn_raw_buffer_load_b64(mkrs(gw, 2048), t * 8, 0, 0);
    MEMBAR();
    SB();
    if (!XLAST && pass == 0) { __builtin_amdgcn_s_barrier(); MEMBAR(); __builtin_amdgcn_s_setprio(1); }
    u4 MR[4], MI[4], TW[4];
    {
        u4 tw; u2 m;
        core_setup(lane, wv, tw, m);
        *(u4*)&lds[L_M + (2 + wv) * 256 + lane * 4] = tw;
        lds[L_M + (wv >> 1) * 256 + lane * 4 + (wv & 1)] = m.x; lds[L_M + (wv >> 1) * 256 + lane * 4 + 2 + (wv & 1)] = m.y;
    }
    const int zc = perm32(lane & 31) + (lane >> 5) * 256, zcu = zc + ((lane & 31) == 0 ? 543 : 511);
    int zl = (zc >> 4) * 16384 + (zc & 15) * 4, zu = (zcu >> 4) * 16384 + (zcu & 15) * 4;
    int zh = lane == 0 ? 0 : OOB;
    int tro = L_A1 + wv * 1024 + (4 * (lane >> 5) + ((lane & 15) >> 2)) * 16 + ((lane >> 4) & 1) * 8 + (lane & 3) * 2;
    u32 tm = TM;
    pin(zl); pin(zu); pin(zh); pin(tro); pin(tm);
    STAMP(1);
    SB();
    pin(code);
    u32 ex;
    {
        MagMax ms; ms.add(smp.x, smp.y); ms.add(smp.z, smp.w);
        ex = pass ? (vd & 0xffu) : clampe(ms.exponent() + MARGIN);
    }
    STAMP(2);
    u4 tags;
    u32 z[16], z512;
    {
        const float sc = __builtin_bit_cast(float, (250u - ex) << 23);
        const float rs = sc * 0.70710678118654752f;
        u32 o[8][2];
        UNROLL for (int j = 0; j < 2; ++j) {
            f2 v[8];
            UNROLL for (int s1 = 0; s1 < 8; ++s1) v[s1] = f2{bflo(xv[s1][j]), bfhi(xv[s1][j])};
            const f2 a0 = v[0] + v[4], a1 = v[1] + v[5], a2 = v[2] + v[6], a3 = v[3] + v[7];
            const f2 b0 = (v[0] - v[4]) * sc, b1 = v[1] - v[5], b2_ = (v[2] - v[6]) * sc, b3 = v[3] - v[7];
            const f2 e0 = a0 + a2, e1 = (a1 + a3) * sc, dm = b1 - b3, dp = b1 + b3;
            f2 r[8];
            r[0] = e0 * sc + e1; r[1] = e0 * sc - e1;
            r[2] = dm * rs + b0; r[3] = -(dp * rs) - b2_;
            r[4] = (a0 - a2) * sc; r[5] = (a3 - a1) * sc;
            r[6] = b0 - dm * rs; r[7] = b2_ - dp * rs;
            UNROLL for (int p = 0; p < 8; ++p) o[p][j] = pkh(r[p].x, r[p].y);
        }
        UNROLL for (int p = 0; p < 8; ++p) *(u2*)&lds[L_A1 + p * 512 + 2 * t] = u2{o[p][0], o[p][1]};
        lset(lds, L_C1 + wv, pv, lane);
        const u32 epoch = rfl(ev) + pv;
        tags = u4{parity_of(epoch), 0u, epoch, tag_of(epoch, 0u)};
        u4 in[4];
        NOUNROLL for (int spin = 0; spin < LSPIN_CAP; ++spin) {
            MEMBAR();
            const u4 c = *(const u4*)&lds[L_C1];
            MEMBAR();
            MR[0] = *(const u4*)&lds[L_M + lane * 4]; MR[1] = *(const u4*)&lds[L_M + 256 + lane * 4];
            UNROLL for (int s = 0; s < 4; ++s) {
                const u2 re = __builtin_bit_cast(u2, __builtin_amdgcn_ds_read_tr16_b64_v4i16((LAS s4*)&lds[tro + s * 128]));
                const u2 im = __builtin_bit_cast(u2, __builtin_amdgcn_ds_read_tr16_b64_v4i16((LAS s4*)&lds[tro + s * 128 + 512]));
                in[s] = u4{re.x, re.y, im.x, im.y};
            }
            pin(MR[0]); pin(MR[1]);
            if (rfl(min4u(c)) >= pv) break;
        }
        UNROLL for (int i = 0; i < 4; ++i) TW[i] = *(const u4*)&lds[L_M + (2 + i) * 256 + lane * 4];
        STAMP(3);
        if (ZGATE && pass == 0 && wv == 0) flag_store(rws, WS_G1, b, lane, 0u, tag_of(tags.z, WS_G1 + b));
        core_expand(lane, MR, MI);
        f16v zr, zi;
        core_fft<true>(in, MR, MI, TW, zr, zi);
        UNROLL for (int r = 0; r < 16; ++r) z[r] = mark(pkh(zr[r], zi[r]), tags.x, tm);
        z512 = z[8];
        const u32 za = __builtin_amdgcn_readlane(z[8], 32), zb = __builtin_amdgcn_readlane(z[0], 0);
        MEMBAR();
        const u32 zt = lane < 32 ? za : zb;
        UNROLL for (int r = 8; r < 16; ++r) z[r] = (lane & 31) == 0 ? (r < 15 ? z[r + 1] : zt) : z[r];
    }
    pin(z);
    STAMP(4);
    const int zs = WS_Z + wv * 64 * 16384 + (((b >> 2) & 3) * 64 + (b & 3) * 16 + ((b >> 4) & 3) * 4 + (b >> 6)) * 64;
    bool zsent = false;
#define TRY_STORE_Z(force) do { if (!zsent) { \
        bool go = true; \
        if (ZHOLD) { if (force) lwait4(lds, L_LD, 1u); else { MEMBAR(); go = rfl(min4u(*(const u4*)&lds[L_LD])) != 0u; } } \
        if (go) { \
            UNROLL for (int r = 0; r < 16; ++r) __builtin_amdgcn_raw_buffer_store_b32(z[r], rws, r < 8 ? zl : zu, zs + (r & 7) * 32768, SC1); \
            __builtin_amdgcn_raw_buffer_store_b64(u2{z512, tags.w}, rws, zh, WS_HB + (wv * 256 + b) * HSTR, SC1); \
            zsent = true; } } SB(); } while (0)
    TRY_STORE_Z(false);
    const int row = (b & 7) >> 1, cls = cls_of(b), tile = tile_of(b);
    const bool packed = row == 0;
    u4 FR, FI, GR[4], GI[4], HR[4], HI[4], sel1;
    {
        const float ofs = cls == 4 ? 0.25f : 0.0f;
        float gu = (float)(cls + 8 * n) * (1.0f / 2048.0f), g0 = (float)(64 * q + 4 * wv) * gu + ofs;
        u32 s16 = 0x2c002c00u;
        pin(gu); pin(g0); pin(s16);
        UNROLL for (int i = 0; i < 4; ++i) UNROLL for (int j = 0; j < 4; ++j) { const u32 g = rot(fmaf((float)(16 * j + i), gu, g0)); GR[i][j] = hmul(g, s16); GI[i][j] = quarter(g, s16); }
    }
    pin(GR); pin(GI);
    {
        MagMax m;
        UNROLL for (int s1 = 0; s1 < 8; ++s1) m.add(xv[s1].x, xv[s1].y);
        if (wv == 0) lset(lds, L_SX, ex, lane);
        lset(lds, L_EW + wv, clampe(m.exponent()), lane);
    }
    TRY_STORE_Z(false);
    UNROLL for (int j = 0; j < 4; ++j) FR[j] = rot((float)(n * (4 * q + j)) * (1.0f / 16.0f));
    FI = quarter(FR);
    {
        float hs[4], hu[4];
        UNROLL for (int i = 0; i < 4; ++i) { hs[i] = (float)(16 * n + 4 * wv + i) * (1.0f / 2048.0f); hu[i] = (float)(cls + 32 * q + 8 * i); }
        pin(hs); pin(hu);
        UNROLL for (int i = 0; i < 4; ++i) { UNROLL for (int j = 0; j < 4; ++j) { float a = hs[i] * hu[j]; pin(a); HR[i][j] = rot(a); } HI[i] = quarter(HR[i]); }
        const int d = n + cls - 8 * q;
        UNROLL for (int j = 0; j < 4; ++j) sel1[j] = (d >> 1) == j ? ((d & 1) ? 0xffff0000u : 0xffffu) : 0u;
    }
    pin(FR); pin(FI); pin(HR); pin(HI); pin(sel1);
    TRY_STORE_Z(true);
    STAMP(5);
    VMWAIT(WWAIT);
    u4 WA[4], WB[4];
    u32 ew = 0;
    UNROLL for (int i = 0; i < 4; ++i) asm volatile("" : "=v"(WA[i]), "=v"(WB[i]));
#define WIDEN(es) do { \
        u4 wr[4][4]; \
        int wl = L_W / 4 + wv * 1024 + n * 4 + min(q, 2); pin(wl); \
        UNROLL for (int i = 0; i < 4; ++i) UNROLL for (int a = 0; a < 4; ++a) wr[i][a] = ((const u4*)lds)[wl + (i * 4 + a) * 64]; \
        ew = pass ? (vd >> 8) & 0xffu : (es); \
        const u32 sw = ((249u - ew - (packed ? 1u : 0u)) << 7) * 0x10001u;       \
        const u4 sels = sel1 & sw; \
        SB(); \
        f4 wf[4][4]; \
        UNROLL for (int k = 0; k < 10; ++k) { \
            if (k < 8) { wf[k >> 1][2 * (k & 1)] = mfma16b(wr[k >> 1][2 * (k & 1)], sels); wf[k >> 1][2 * (k & 1) + 1] = mfma16b(wr[k >> 1][2 * (k & 1) + 1], sels); } \
            if (k >= 2) { const int i_ = (k - 2) >> 1, f_ = k & 1; UNROLL for (int r = 0; r < 4; ++r) (f_ ? WB : WA)[i_][r] = pkh(wf[i_][2 * f_][r], wf[i_][2 * f_ + 1][r]); } \
            SB(); } \
        pin(WA); pin(WB); } while (0)
    MEMBAR();
    const u4 pr0 = *(const u4*)&lds[L_PR];
    const bool wearly = rfl(min4u(pr0)) != 0u;
    MEMBAR();
    if (wearly) WIDEN(rfl(max4u(pr0)));
    STAMP(6);
    u32 z1[4][4], z2[4][4];
    const int zvm = q * 64 + (63 - tile) * 16384 + (15 - n) * 4;
    const int zv1 = cls <= 4 ? q * 64 + tile * 16384 + n * 4 : zvm;
    int zso = WS_Z + row * 64 * 16384 + wv * 4096;
    asm volatile("" : "+s"(zso));
#define LOAD_Z() do { \
        if (packed) { int a_ = zvm; pin(a_); UNROLL for (int i = 0; i < 4; ++i) UNROLL for (int j = 0; j < 4; ++j) z2[i][j] = __builtin_amdgcn_raw_buffer_load_b32(rws, a_ + (4 * i + j) * 256, zso, SC1); } \
        else { UNROLL for (int i = 0; i < 4; ++i) UNROLL for (int j = 0; j < 4; ++j) z2[i][j] = tags.x; } \
        int c_ = zv1; pin(c_); UNROLL for (int i = 0; i < 4; ++i) UNROLL for (int j = 0; j < 4; ++j) z1[i][j] = __builtin_amdgcn_raw_buffer_load_b32(rws, c_ + (4 * i + j) * 256, zso, SC1); } while (0)
    if (ZGATE) lwait(lds, L_G1, 1u);
    VMWAIT(ZWAIT);
    if (ZDELAY) __builtin_amdgcn_s_sleep(ZDELAY);
    LOAD_Z();
    if (wv == 0) lset(lds, L_ZL, pv, lane);
    SB();
    STAMP(7);
    u32 x1 = tags.x ^ (cls > 4 ? 0x80000000u : 0u), x2 = tags.x ^ (cls == 4 ? 0x00008000u : cls == 0 ? 0x80000000u : 0u);
    const int lw = ((wv * 16 + 4 * q) * 16 + n) * 4;
    int lr = (q * 16 + 4 * wv) * 16 + n;
    const u4* l4 = (const u4*)lds;
    pin(GR); pin(GI); pin(x1); pin(x2); pin(lr);
    STAMP(8);
    SB();
    {
        f4 yr[4], yi[4];
        NOUNROLL for (int spin = 0; spin < SPIN_CAP; ++spin) {
            u32 m = 0;
            UNROLL for (int i = 0; i < 4; ++i) {
                u4 d;
                UNROLL for (int j = 0; j < 4; ++j) { z1[i][j] ^= x1; z2[i][j] ^= x2; }
                UNROLL for (int j = 0; j < 4; ++j) d[j] = U(H(z1[i][j]) + H(z2[i][j]));
                UNROLL for (int j = 0; j < 4; ++j) m |= z1[i][j] | z2[i][j];
                yr[i] = mfma16(GR[i], d); yi[i] = mfma16(GI[i], d);
                SB();
            }
            if (__all((m & TM) == 0u)) break;
            MEMBAR();
            LOAD_Z();
        }
        STAMP(9);
        u4 y[4];
        UNROLL for (int i = 0; i < 4; ++i) UNROLL for (int r = 0; r < 4; ++r) y[r][i] = pkh(yr[i][r], yi[i][r]);
        UNROLL for (int r = 0; r < 4; ++r) *(u4*)&lds[L_A2 + lw + r * 64] = y[r];
        lset(lds, L_C3 + wv, pv, lane);
    }
    if (!wearly) { const u32 es = lwait4(lds, L_PR, 1u); WIDEN(es); }
    {
        u4 d[4];
        NOUNROLL for (int spin = 0; spin < LSPIN_CAP; ++spin) {
            MEMBAR();
            const u4 c = *(const u4*)&lds[L_C3];
            MEMBAR();
            UNROLL for (int i = 0; i < 4; ++i) d[i] = l4[L_A2 / 4 + lr + i * 16];
            pin(d[0]);
            if (rfl(min4u(c)) >= pv) break;
        }
        STAMP(10);
        if (VGATE == 1 && pass == 0 && wv == 0) { flag_store(rws, WS_G2, b, lane, 0u, tag_of(tags.z, WS_G2 + b)); lset(lds, L_P2, 1u, lane); }
        pin(WA); pin(WB);
        f4 xr[4], xi[4];
        u4 xx[4], ya[4], yb[4];
        UNROLL for (int i = 0; i < 4; ++i) {
            xr[i] = mfma16(FR, d[i]); xi[i] = mfma16(FI, d[i]);
            if (i >= 2) { UNROLL for (int r = 0; r < 4; ++r) xx[i - 2][r] = pkh(xr[i - 2][r], xi[i - 2][r]); pin(xx[i - 2]); }
            SB();
        }
        UNROLL for (int r = 0; r < 4; ++r) ya[0][r] = cmul_swap(xx[0][r], WA[0][r]);
        UNROLL for (int i = 2; i < 4; ++i) { UNROLL for (int r = 0; r < 4; ++r) xx[i][r] = pkh(xr[i][r], xi[i][r]); pin(xx[i]); }
        SB();
        f4 p0[4], p1[4], p2[4], p3[4];
        UNROLL for (int i = 0; i < 4; ++i) {
            p0[i] = mfma16(FR, ya[i]); p1[i] = mfma16(FI, ya[i]);
            if (i < 3) { UNROLL for (int r = 0; r < 4; ++r) ya[i + 1][r] = cmul_swap(xx[i + 1][r], WA[i + 1][r]); }
            else { UNROLL for (int r = 0; r < 4; ++r) yb[0][r] = cmul_swap(xx[0][r], WB[0][r]); }
            SB();
        }
        u4 ra[4], rb[4];
        UNROLL for (int i = 0; i < 4; ++i) {
            p2[i] = mfma16(FR, yb[i]); p3[i] = mfma16(FI, yb[i]);
            if (i < 3) { UNROLL for (int r = 0; r < 4; ++r) yb[i + 1][r] = cmul_swap(xx[i + 1][r], WB[i + 1][r]); }
            UNROLL for (int k = 0; k < 4; ++k) ra[i][k] = pkh(p0[k][i], p1[k][i]);
            *(u4*)&lds[L_B + lw + i * 64] = ra[i];
            SB();
        }
        UNROLL for (int i = 0; i < 3; ++i) UNROLL for (int r = 0; r < 4; ++r) rb[r][i] = pkh(p2[i][r], p3[i][r]);
        SB();
        UNROLL for (int r = 0; r < 4; ++r) { rb[r][3] = pkh(p2[3][r], p3[3][r]); *(u4*)&lds[L_B + 4096 + lw + r * 64] = rb[r]; }
        lset(lds, L_C4 + wv, pv, lane);
    }
    STAMP(11);
    if (VGATE == 2 && pass == 0 && wv == 0) { flag_store(rws, WS_G2, b, lane, 0u, tag_of(tags.z, WS_G2 + b)); lset(lds, L_P2, 1u, lane); }
    {
        u4 da[4], db[4];
        NOUNROLL for (int spin = 0; spin < LSPIN_CAP; ++spin) {
            MEMBAR();
            const u4 c = *(const u4*)&lds[L_C4];
            MEMBAR();
            UNROLL for (int i = 0; i < 4; ++i) { da[i] = l4[L_B / 4 + lr + i * 16]; db[i] = l4[L_B / 4 + 1024 + lr + i * 16]; }
            pin(da[0]);
            if (rfl(min4u(c)) >= pv) break;
        }
        const int vo = 64 * q * 32768 + n * 8, so = WS_V + 4 * wv * 32768 + cls * 4096 + tile * 128;
        f4 p0[4], p1[4], p2[4], p3[4];
        UNROLL for (int i = 0; i < 5; ++i) {
            if (i < 4) { p0[i] = mfma16(HR[i], da[i]); p1[i] = mfma16(HI[i], da[i]); p2[i] = mfma16(HR[i], db[i]); p3[i] = mfma16(HI[i], db[i]); }
            if (i > 0) { UNROLL for (int r = 0; r < 4; ++r)
                __builtin_amdgcn_raw_buffer_store_b64(u2{mark(pkh(p0[i - 1][r], p1[i - 1][r]), tags.x, tm), mark(pkh(p2[i - 1][r], p3[i - 1][r]), tags.x, tm)}, rws, vo, so + (16 * r + i - 1) * 32768, SC1); }
            SB();
        }
    }
    if (wv == 0) lset(lds, L_P2, 1u, lane);
    STAMP(12);
    SB();
    {
        pin(xv);
        const f2 g01 = {bflo(gv.x), bfhi(gv.x)}, g23 = {bflo(gv.y), bfhi(gv.y)};
        f4 acc = {};
        UNROLL for (int i = 0; i < 4; ++i) {
            u4 pp;
            UNROLL for (int k = 0; k < 2; ++k) {
                const f2 a = f2{bflo(xv[2 * i + k].x), bfhi(xv[2 * i + k].x)} * g01, c = f2{bflo(xv[2 * i + k].y), bfhi(xv[2 * i + k].y)} * g23;
                pp[2 * k] = pkb(a.x, a.y); pp[2 * k + 1] = pkb(c.x, c.y);
            }
            const u32 s0 = n == 2 * i ? 0x3f803f80u : 0u, s1 = n == 2 * i + 1 ? 0x3f803f80u : 0u;
            acc = __builtin_amdgcn_mfma_f32_16x16x32_bf16(__builtin_bit_cast(b8, pp), __builtin_bit_cast(b8, u4{s0, s0, s1, s1}), acc, 0, 0, 0);
        }
        if (n < 8) lds[L_GS + n * 16 + (t >> 4)] = __builtin_bit_cast(u32, (acc.x + acc.y) + (acc.z + acc.w));
        lset(lds, L_C5 + wv, pv, lane);
    }
    MR[0] = *(const u4*)&lds[L_M + lane * 4]; MR[1] = *(const u4*)&lds[L_M + 256 + lane * 4];
    UNROLL for (int i = 0; i < 4; ++i) TW[i] = *(const u4*)&lds[L_M + (2 + i) * 256 + lane * 4];
    core_expand(lane, MR, MI);
    pin(MR); pin(MI); pin(TW);
    u4 PM;
    {
        lwait4(lds, L_C5, pv);
        float gg[4];
        UNROLL for (int k = 0; k < 2; ++k) {
            const f4* pp = (const f4*)&lds[L_GS + ((n >> 2) * 2 + k) * 16];
            const f4 ps = (pp[0] + pp[1]) + (pp[2] + pp[3]);
            const float lg = rbf((ps.x + ps.y) + (ps.z + ps.w));
            gg[2 * k] = rbf(1.0f / (1.0f + __expf(-lg)));
            gg[2 * k + 1] = rbf(1.0f - gg[2 * k]);
        }
        UNROLL for (int k = 0; k < 4; ++k) { gg[k] *= 0.03125f; pin(gg[k]); }
        const int r = n & 3, sa = (n >> 2) * 2;
        UNROLL for (int cc = 0; cc < 2; ++cc) {
            const int u1 = 2 * q + cc;
            const float ta = (float)(sa * u1) * 0.125f + ((r & 1) ? 0.25f : (r & 2) ? 0.5f : 0.0f);
            const float tb = (float)((sa + 1) * u1) * 0.125f + ((r & 1) ? ((r & 2) ? 0.0f : 0.5f) : 0.25f);
            const float ca = cosr(ta), sa_ = sinr(ta), cb = cosr(tb), sb_ = sinr(tb);
            float e[4] = {gg[2] * cb, gg[2] * sb_, gg[3] * cb, gg[3] * sb_};
            pin(e);
            e[0] = fmaf(gg[0], ca, e[0]); pin(e[0]); e[1] = fmaf(gg[0], sa_, e[1]); pin(e[1]); e[2] = fmaf(gg[1], ca, e[2]); pin(e[2]); e[3] = fmaf(gg[1], sa_, e[3]);
            PM[2 * cc] = pkh(e[0], e[1]); PM[2 * cc + 1] = pkh(e[2], e[3]);
        }
    }
    int a3d = (q * 8 + wv) * 32 + n, a3m = (q * 8 + 7 - wv) * 32 + 16 - n, a3z = (q * 8 + 7 - wv) * 32 + ((32 - n) & 31), a3r = wv * 256 + lane;
    pin(a3d); pin(a3m); pin(a3z); pin(a3r);
    float osc = __builtin_bit_cast(float, (u32)min(max((int)ex + (int)ew - 130, 1), 254) << 23) * TRUNC_FIX;
    const int ve = 2 * q * 4096 + (128 * wv + n) * 8;
    int vs0 = WS_V + b * 32768, vs1 = vs0 + 4096, vsl = WS_VL + b * 16;
    asm volatile("" : "+s"(vs0), "+s"(vs1), "+s"(vsl));
    int ov = (4 * perm32(lane & 31) + (lane >> 5)) * 16 + (512 * wv + b) * 2048;
    pin(PM); pin(ov); pin(osc);
    STAMP(13);
    if (VGATE) lwait(lds, L_G2, 1u);
    if (VSTAG) { const int st_ = b & 3; if (st_ == 0) VMWAIT(12); else if (st_ == 1) VMWAIT(8); else if (st_ == 2) VMWAIT(4); else VMWAIT(0); }
    else VMWAIT(VWAIT);
    if (VDELAY) __builtin_amdgcn_s_sleep(VDELAY);
    {
        f4 z[9];
        NOUNROLL for (int spin = 0; spin < SPIN_CAP; ++spin) {
            MEMBAR();
            u2 v0[8], v1[8];
            u4 ve8, vo8;
            int a_ = ve; pin(a_);
            if (wv == 3) {
                ve8 = __builtin_amdgcn_raw_buffer_load_b128(rws, q * 8192, vsl, SC1); vo8 = __builtin_amdgcn_raw_buffer_load_b128(rws, q * 8192 + 4096, vsl, SC1);
            } else {
                const u2 e2 = __builtin_amdgcn_raw_buffer_load_b64(rws, a_ + 1024, vs0, SC1), o2 = __builtin_amdgcn_raw_buffer_load_b64(rws, a_ + 1024, vs1, SC1);
                ve8 = u4{e2.x, e2.y, tags.w, tags.w}; vo8 = u4{o2.x, o2.y, tags.w, tags.w};
            }
            UNROLL for (int i = 0; i < 8; ++i) {
                v0[i] = __builtin_amdgcn_raw_buffer_load_b64(rws, a_ + 128 * i, vs0, SC1);
                v1[i] = __builtin_amdgcn_raw_buffer_load_b64(rws, a_ + 128 * i, vs1, SC1);
            }
            SB();
            u32 m = (((wv == 3 ? ve8.y : ve8.z) ^ tags.w) | (ve8.w ^ tags.w) | ((wv == 3 ? vo8.y : vo8.z) ^ tags.w) | (vo8.w ^ tags.w)) ? TM : 0u;
            UNROLL for (int k = 0; k < 9; ++k) {
                const int i = (k + 8) % 9;
                const u4 d = (i == 8 ? u4{ve8.x, wv == 3 ? ve8.z : ve8.y, vo8.x, wv == 3 ? vo8.z : vo8.y} : u4{v0[i & 7].x, v0[i & 7].y, v1[i & 7].x, v1[i & 7].y}) ^ tags.x;
                m |= d.x | d.y; m |= d.z | d.w;
                z[i] = mfma16(PM, d);
                SB();
            }
            if (__all((m & TM) == 0u)) break;
        }
        STAMP(14);
        if (wv == 0 && n == 0) { z[0].x = 0.5f * (z[0].x + z[0].z); z[0].y = 0.5f * (z[0].y + z[0].w); }
        if (wv == 3) { z[8].z = 0.5f * (z[8].x + z[8].z); z[8].w = 0.5f * (z[8].y + z[8].w); }
        u4* l4w = (u4*)&lds[L_A1];
        UNROLL for (int p = 0; p < 2; ++p)
            l4w[a3d + 16 * p] = u4{pkh(z[p].x, z[p + 2].x), pkh(z[p + 4].x, z[p + 6].x), pkh(z[p].y, z[p + 2].y), pkh(z[p + 4].y, z[p + 6].y)};
        l4w[a3m] = u4{pkh(z[7].z, z[5].z), pkh(z[3].z, z[1].z), pkh(z[7].w, z[5].w), pkh(z[3].w, z[1].w)};
        f2 m[4];
        UNROLL for (int i = 0; i < 4; ++i) m[i] = n == 0 ? f2{z[8 - 2 * i].z, z[8 - 2 * i].w} : f2{z[6 - 2 * i].z, z[6 - 2 * i].w};
        l4w[a3z] = u4{pkh(m[0].x, m[1].x), pkh(m[2].x, m[3].x), pkh(m[0].y, m[1].y), pkh(m[2].y, m[3].y)};
        lset(lds, L_C6 + wv, 2u * pv - 1u, lane);
    }
    {
        u4 in[4];
        NOUNROLL for (int spin = 0; spin < LSPIN_CAP; ++spin) {
            MEMBAR();
            const u4 c = *(const u4*)&lds[L_C6];
            MEMBAR();
            UNROLL for (int s = 0; s < 4; ++s) in[s] = ((const u4*)&lds[L_A1])[a3r + s * 64];
            pin(in[0]);
            if (rfl(min4u(c)) >= 2u * pv - 1u) break;
        }
        f16v zr, zi;
        core_fft<false>(in, MR, MI, TW, zr, zi);
        zr *= osc; zi *= osc;
        const rsrc_t ro = mkrs(out, 2048 * 2048);
        UNROLL for (int hf = 0; hf < 2; ++hf) {
            u4 c;
            UNROLL for (int j = 0; j < 4; ++j) c[j] = pkb(zr[8 * hf + 2 * j], zr[8 * hf + 2 * j + 1]);
            store128<OAUX>(c, ro, ov + 256 * 2048 + 32 * hf);
        }
        UNROLL for (int hf = 0; hf < 2; ++hf) {
            u4 a;
            UNROLL for (int j = 0; j < 4; ++j) a[j] = pkb(zi[8 * hf + 2 * j], zi[8 * hf + 2 * j + 1]);
            store128<OAUX>(a, ro, ov + 32 * hf);
        }
    }
    STAMP(15);
    vd = lwait(lds, L_VD, 1u);
    if (pass == 1 || !(vd & 0x20000u)) {
        if (b == 0 && t == 0) __builtin_amdgcn_raw_buffer_store_b32(tags.z, rws, 0, WS_E, SC1);
        break;
    }
    lset(lds, L_C6 + wv, 2u, lane);
    lwait4(lds, L_C6, 2u);
    VMWAIT(0);
    }
    STAMP_OUT();
}

extern "C" void kernel_launch(void* const* d_in, const int* in_sizes, int n_in,
                              void* d_out, int out_size, void* d_ws, size_t ws_size,
                              hipStream_t stream) {
    gfnet_fused<<<256, 512, 0, stream>>>(d_in[0], d_in[6], d_in[1], d_in[2], d_in[4], d_in[5], (char*)d_ws, d_out);
}
```

```cpp
#include <hip/hip_runtime.h>
#include <hip/hip_bf16.h>
#include <stdint.h>

typedef _Float16 h2 __attribute__((ext_vector_type(2)));
typedef _Float16 h8 __attribute__((ext_vector_type(8)));
typedef __bf16 b2 __attribute__((ext_vector_type(2)));
typedef __bf16 b8 __attribute__((ext_vector_type(8)));
typedef float f2 __attribute__((ext_vector_type(2)));
typedef float f4 __attribute__((ext_vector_type(4)));
typedef float f16v __attribute__((ext_vector_type(16)));
typedef short s4 __attribute__((ext_vector_type(4)));
typedef unsigned u32;
typedef u32 u2 __attribute__((ext_vector_type(2)));
typedef u32 u4 __attribute__((ext_vector_type(4)));
typedef unsigned short us2 __attribute__((ext_vector_type(2)));
#define DI __device__ __forceinline__
#define UNROLL _Pragma("unroll")
#define NOUNROLL _Pragma("unroll 1")
#define LAS __attribute__((address_space(3)))

#define W0AUX 0
#define W1AUX 0
#define VDELAY 0
#define ZDELAY 0
#define WFIRST 0
#define VSTAG 0
#define GEARLY 0
#define ZGATE 0
#define VGATE 0
#define G2DELAY 50
#define MAXLATE 0
#define FLATE 0
#define FDELAY 24
#define EDGE 1
#define SCAN 0
#define ZHOLD 4
#define ZSLEEP 0
#define XLAST 0
#define XDELAY 0
#define G0 0
#define WWAIT 4
#define DIAGB -1
#define CODEPF 4
#define VUN 4
#define QUICK 170
#define ZUN 0
#define ROT 1
#define PSLEEP 8
#define WGATE 4
#define W0DELAY 2
#define W0WAIT 0
#define WWIN 12
#define ZWAIT 0
#define VWAIT 0
#define XAUX 2
#define OAUX 2
#define MARGIN 2

#define FSTRIDE 16
constexpr u32 FSTR = FSTRIDE;
constexpr u32 WS_Z = 0;
constexpr u32 WS_V = 4u << 20;
constexpr u32 WS_VL = 12u << 20;
#define HSTR 32
constexpr u32 WS_HB = WS_VL + (32u << 10);
constexpr u32 WS_F = WS_HB + (128u << 10);
constexpr u32 WS_G1 = WS_F + 2048 * FSTR;
constexpr u32 WS_G2 = WS_G1 + 2048 * FSTR;
constexpr u32 WS_E = WS_G2 + 2048 * FSTR;
constexpr u32 WS_BYTES = WS_E + 4096;
constexpr size_t WS_META = 24u << 20;
constexpr int SC1 = 16;
constexpr int SPIN_CAP = 8192;
constexpr int LSPIN_CAP = 1 << 17;
constexpr int OOB = 0x7ffffff0;
constexpr float TRUNC_FIX = 1.00066f;
constexpr u32 TM = 0x00010001u;

template <class T> DI void pin(T& v) { asm volatile("" : "+v"(v)); }
template <class T, int N> DI void pin(T (&v)[N]) { UNROLL for (int i = 0; i < N; ++i) pin(v[i]); }
DI u32 opq(u32 v) { asm("" : "+v"(v)); return v; }
DI u32 pkh(float lo, float hi) { h2 r; r.x = (_Float16)lo; r.y = (_Float16)hi; return __builtin_bit_cast(u32, r); }
DI u32 pkb(float lo, float hi) { b2 r; r.x = (__bf16)lo; r.y = (__bf16)hi; return __builtin_bit_cast(u32, r); }
DI float bflo(u32 v) { return __builtin_bit_cast(float, v << 16); }
DI float bfhi(u32 v) { return __builtin_bit_cast(float, v & 0xffff0000u); }
DI float rbf(float v) { return (float)(__bf16)v; }
DI h2 H(u32 v) { return __builtin_bit_cast(h2, v); }
DI u32 U(h2 v) { return __builtin_bit_cast(u32, v); }
DI f16v mfma32(u4 a, u4 b, f16v c) { return __builtin_amdgcn_mfma_f32_32x32x16_f16(__builtin_bit_cast(h8, a), __builtin_bit_cast(h8, b), c, 0, 0, 0); }
DI f4 mfma16(u4 a, u4 b) { return __builtin_amdgcn_mfma_f32_16x16x32_f16(__builtin_bit_cast(h8, a), __builtin_bit_cast(h8, b), f4{}, 0, 0, 0); }
DI f4 mfma16b(u4 a, u4 b) { return __builtin_amdgcn_mfma_f32_16x16x32_bf16(__builtin_bit_cast(b8, a), __builtin_bit_cast(b8, b), f4{}, 0, 0, 0); }
DI u32 hmul(u32 a, u32 b) { return U(H(a) * H(b)); }
DI u32 hfma(u32 a, u32 b, u32 c) { return U(__builtin_elementwise_fma(H(a), H(b), H(c))); }
DI u32 cmul_swap(u32 x, u32 w) { const h2 a = H(opq(x)), c = H(opq(w)); h2 n = c; n.y = -n.y; return U(__builtin_elementwise_fma(a.yy, n, a.xx * c.yx)); }
DI u32 cmul_conj(u32 x, u32 t) { const h2 a = H(opq(x)), c = H(opq(t)); h2 n = c.yy; n.y = -n.y; return U(__builtin_elementwise_fma(a.yx, n, a * c.xx)); }
DI u32 pkmax(u32 a, u32 b) { return __builtin_bit_cast(u32, __builtin_elementwise_max(__builtin_bit_cast(us2, a), __builtin_bit_cast(us2, b))); }
DI float sinr(float r) { return __builtin_amdgcn_sinf(r); }
DI float cosr(float r) { return __builtin_amdgcn_cosf(r); }
DI int perm32(int n) { return (n & 0x13) | ((n & 4) << 1) | ((n & 8) >> 1); }
DI u32 max3u(u32 a, u32 b, u32 c) { return max(max(a, b), c); }
DI u32 max4u(u4 v) { return max(max(v.x, v.y), max(v.z, v.w)); }
DI u32 min4u(u4 v) { return min(min(v.x, v.y), min(v.z, v.w)); }
DI u32 mark(u32 v, u32 par, u32 tm) { return (par & tm) | (v & ~tm); }
DI u32 rfl(u32 v) { return __builtin_amdgcn_readfirstlane(v); }
DI u32 pow2h(int d) { const float f = __builtin_bit_cast(float, (u32)(127 - min(d, 64)) << 23); return pkh(f, f); }

typedef __amdgpu_buffer_rsrc_t rsrc_t;
DI rsrc_t mkrs(const void* p, u32 bytes) { return __builtin_amdgcn_make_buffer_rsrc((void*)p, (short)0, (int)bytes, 0x00020000); }
template <int AUX> DI void store128(u4 v, rsrc_t r, int off) { __builtin_amdgcn_raw_buffer_store_b128(v, r, off, 0, AUX); }
#define SB() __builtin_amdgcn_sched_barrier(0)
#define MEMBAR() asm volatile("" ::: "memory")
#define VMWAIT(n) asm volatile("s_waitcnt vmcnt(%0)" :: "n"(n) : "memory")
#define STAMP_DECL
#define STAMP(i)
#define STAMP_OUT()
template <int CTRL> DI u32 dpp(u32 v) { return __builtin_amdgcn_update_dpp(0u, v, CTRL, 0xf, 0xf, true); }
DI u32 wave_maxu(u32 v) {
    v = max(v, dpp<0xB1>(v)); v = max(v, dpp<0x4E>(v)); v = max(v, dpp<0x141>(v)); v = max(v, dpp<0x140>(v));
    return max(max((u32)__builtin_amdgcn_readlane(v, 0), (u32)__builtin_amdgcn_readlane(v, 16)), max((u32)__builtin_amdgcn_readlane(v, 32), (u32)__builtin_amdgcn_readlane(v, 48)));
}

DI void core_setup(int lane, int s, u4& tw, u2& m) {
    float fc = (float)perm32(lane & 31) * (1.0f / 1024.0f), p0 = (float)(4 * (lane >> 5) + 8 * s) * fc, p1 = (float)(4 * (lane >> 5) + 8 * (s >> 1) + 2 * (s & 1)) * fc * 32.0f;
    pin(fc); pin(p0); pin(p1);
    float tc[4], ts[4];
    UNROLL for (int i = 0; i < 4; ++i) { const float pt = fmaf((float)i, fc, p0); tc[i] = cosr(pt); ts[i] = sinr(pt); }
    tw = u4{pkh(tc[0], tc[1]), pkh(tc[2], tc[3]), pkh(ts[0], ts[1]), pkh(ts[2], ts[3])};
    const float p2 = fmaf(32.0f, fc, p1);
    m = u2{pkh(cosr(p1), cosr(p2)), pkh(sinr(p1), sinr(p2))};
}
DI void core_expand(int lane, u4 (&MR)[4], u4 (&MI)[4]) {
    const u32 sg = (lane & 1) ? 0x80008000u : 0u;
    MR[2] = MR[0] ^ sg; MR[3] = MR[1] ^ sg;
    UNROLL for (int s = 0; s < 4; ++s) MI[s] = u4{MR[s].z ^ 0x80008000u, MR[s].w ^ 0x80008000u, MR[s].x, MR[s].y};
}
template <bool OUT_B> DI void core_fft(const u4 (&in)[4], const u4 (&MR)[4], const u4 (&MI)[4], const u4 (&TW)[4], f16v& outr, f16v& outi) {
    f16v yr = {}, yi = {};
    UNROLL for (int s = 0; s < 4; ++s) yr = mfma32(in[s], MR[s], yr);
    SB();
    yi = mfma32(in[0], MI[0], yi); yi = mfma32(in[1], MI[1], yi);
    SB();
    h2 t0[8], t1[8];
#define TW_RE(p) do { const h2 pr_ = H(pkh(yr[2 * (p)], yr[2 * (p) + 1])); t0[p] = pr_ * H(TW[(p) >> 1][(p) & 1]); t1[p] = pr_ * H(TW[(p) >> 1][2 + ((p) & 1)]); } while (0)
    TW_RE(0); TW_RE(1); SB();
    yi = mfma32(in[2], MI[2], yi);
    TW_RE(2); TW_RE(3); SB();
    yi = mfma32(in[3], MI[3], yi);
    TW_RE(4); TW_RE(5); TW_RE(6); TW_RE(7); SB();
    outr = f16v{}; outi = f16v{};
    u4 d[4];
#define TW_IM(s) do { UNROLL for (int j = 0; j < 2; ++j) { const int p_ = 2 * (s) + j; const h2 pi_ = H(pkh(yi[2 * p_], yi[2 * p_ + 1])); \
        d[s][j] = U(__builtin_elementwise_fma(pi_, H(TW[s][2 + j]), t0[p_])); d[s][2 + j] = U(__builtin_elementwise_fma(pi_, H(TW[s][j]), -t1[p_])); } } while (0)
    TW_IM(0); SB();
    UNROLL for (int s = 0; s < 4; ++s) {
        outr = OUT_B ? mfma32(MR[s], d[s], outr) : mfma32(d[s], MR[s], outr);
        if (s < 3) TW_IM(s + 1);
        SB();
    }
    UNROLL for (int s = 0; s < 4; ++s) outi = OUT_B ? mfma32(MI[s], d[s], outi) : mfma32(d[s], MI[s], outi);
}
DI u32 rot(float ph) { return pkh(cosr(ph), sinr(ph)); }
DI u32 quarter(u32 r, u32 f) { u32 o; asm("v_pk_mul_f16 %0, %1, %2 op_sel:[1,0] op_sel_hi:[0,1] neg_lo:[1,0]" : "=v"(o) : "v"(r), "v"(f)); return o; }
DI u4 quarter(u4 r) { u32 one = 0x3c003c00u; pin(one); return u4{quarter(r.x, one), quarter(r.y, one), quarter(r.z, one), quarter(r.w, one)}; }

DI u32 tag_of(u32 epoch, u32 idx) { u32 h = (epoch ^ (idx * 0x9E3779B1u)) * 0x85EBCA6Bu; return h ^ (h >> 15); }
DI u32 parity_of(u32 epoch) { return (epoch & 1u) ? 0x00000001u : 0x00010000u; }
DI void flag_store(rsrc_t rws, u32 arr, int b, int lane, u32 payload, u32 tag) {
    __builtin_amdgcn_raw_buffer_store_b64(u2{payload, tag}, rws, lane < 8 ? lane * (int)(256 * FSTR) : OOB, arr + b * FSTR, SC1);
}
DI u4 flag_wait(rsrc_t rws, u32 arr, int b, int lane, u32 epoch) {
    u2 f[4]; u32 tg[4];
    UNROLL for (int j = 0; j < 4; ++j) tg[j] = tag_of(epoch, arr + 4 * lane + j);
    NOUNROLL for (int spin = 0; spin < SPIN_CAP; ++spin) {
        MEMBAR();
        UNROLL for (int j = 0; j < 4; ++j) f[j] = __builtin_amdgcn_raw_buffer_load_b64(rws, lane * (4 * FSTR) + j * FSTR, arr + (b & 7) * (256 * FSTR), SC1);
        if (__all(f[0].y == tg[0] && f[1].y == tg[1] && f[2].y == tg[2] && f[3].y == tg[3])) break;
        __builtin_amdgcn_s_sleep(PSLEEP);
    }
    return u4{f[0].x, f[1].x, f[2].x, f[3].x};
}
DI int cls_of(int b) { const int row = (b & 7) >> 1; return row == 0 ? 4 * (b >> 7) : ((b & 1) ? 8 - row : row); }
DI int tile_of(int b) { return (b & 6) == 0 ? 16 * (b & 1) + ((b >> 3) & 15) : b >> 3; }
DI bool is_helper(int b) { return tile_of(b) == 31; }

constexpr int L_C1 = 0, L_C3 = 4, L_C4 = 8, L_C5 = 12, L_C6 = 16;
constexpr int L_EW = 20;
constexpr int L_WW = 24;
constexpr int L_SW = 28;
constexpr int L_PR = 32;
constexpr int L_SX = 36;
constexpr int L_ZL = 37;
constexpr int L_VD = 38;
constexpr int L_G1 = 39, L_G2 = 40, L_P2 = 41;
constexpr int L_LD = 44;
constexpr int L_GS = 64;
constexpr int L_M = 192;
constexpr int L_A1 = L_M + 1536;
constexpr int L_A2 = L_A1 + 4096;
constexpr int L_B = L_A2 + 4096;
constexpr int L_W = L_B + 8192;
constexpr int L_END = L_W + 64 * 256;

DI void lset(u32* lds, int idx, u32 v, int lane) { MEMBAR(); if (lane == 0) lds[idx] = v; MEMBAR(); }
DI u32 lget(u32* lds, int idx) { MEMBAR(); return rfl(lds[idx]); }
DI u32 lwait(u32* lds, int idx, u32 atleast) {
    u32 v = 0;
    NOUNROLL for (int spin = 0; spin < LSPIN_CAP; ++spin) { v = lget(lds, idx); if (v >= atleast) break; __builtin_amdgcn_s_sleep(1); }
    MEMBAR();
    return v;
}
DI u32 lwait4(u32* lds, int idx, u32 atleast) {
    u4 v;
    NOUNROLL for (int spin = 0; spin < LSPIN_CAP; ++spin) { MEMBAR(); v = *(const u4*)&lds[idx]; if (rfl(min4u(v)) >= atleast) break; __builtin_amdgcn_s_sleep(1); }
    MEMBAR();
    return rfl(max4u(v));
}
struct MagMax {
    h2 hi = {}, lo = {};
    DI void add(u32 a, u32 c) {
        hi = __builtin_elementwise_maximum(__builtin_elementwise_maximum(hi, H(a)), H(c));
        lo = __builtin_elementwise_minimum(__builtin_elementwise_minimum(lo, H(a)), H(c));
    }
    DI u32 exponent() const {
        const u32 mx = pkmax(U(hi) & 0x7fff7fffu, U(lo) & 0x7fff7fffu);
        return wave_maxu(max(mx & 0xffffu, mx >> 16)) >> 7;
    }
};
DI u32 clampe(u32 e) { return min(max(e, 8u), 240u); }

__global__ __launch_bounds__(512, 2) void gfnet_fused(const void* x, const void* gw, const void* war, const void* wai, const void* wbr, const void* wbi,
                                                      char* ws, void* out) {
    __shared__ __attribute__((aligned(16))) u32 lds[L_END];
    STAMP_DECL;
    STAMP(0);
    u32 code = 0;
    if (CODEPF) {
        unsigned long long pc0; int len;
        asm volatile("s_getpc_b64 %0\n.Lgf_begin:\n\ts_mov_b32 %1, .Lgf_end-.Lgf_begin" : "=s"(pc0), "=s"(len));
        code = __builtin_amdgcn_raw_buffer_load_b32(mkrs((const void*)pc0, (u32)max(len, 0)), (threadIdx.x - 256u) < 16u * CODEPF ? ((blockIdx.x >> 3) / CODEPF * CODEPF * 16 + threadIdx.x - 256) * 64 : OOB, 0, 0);
        SB();
    }
    asm volatile("" :: "s"(x), "s"(gw), "s"(war), "s"(wai), "s"(wbr), "s"(wbi), "s"(ws), "s"(out));
    const int wv0 = __builtin_amdgcn_readfirstlane(threadIdx.x >> 6);
    const rsrc_t rws = mkrs(ws, WS_BYTES);

    if (wv0 >= 4) {
        const u32 tstart = VUN == 4 ? (u32)__builtin_amdgcn_s_memrealtime() : 0u;
        const int wv = wv0, b = blockIdx.x, cls = cls_of(b), tile = tile_of(b);
        const int lane = threadIdx.x & 63, q = lane >> 4, n = lane & 15;
        const rsrc_t rw = mkrs(wv == 4 ? war : wv == 5 ? wai : wv == 6 ? wbr : wbi, 2048 * 1026);
        const int wvo = (lane >> 2) * (128 * 1026) + min(lane & 3, 2) * 16, wso = (cls * 513 + 16 * tile - cls) * 2;
#define WLOAD(r) do { UNROLL for (int st = 0; st < 4; ++st) { const int c_ = 4 * st + ((ROT * tile + (r)) & 3); \
                __builtin_amdgcn_raw_ptr_buffer_load_lds(rw, (LAS void*)&lds[L_W + (c_ * 4 + wv - 4) * 256], 16, wvo, wso + c_ * 8208, 0, (r) == 0 ? W0AUX : W1AUX); } SB(); } while (0)
        if (wv == 4) { lds[lane] = 0u; asm volatile("s_waitcnt lgkmcnt(0)" ::: "memory"); }
        if (WFIRST) { __builtin_amdgcn_s_sleep(W0DELAY); WLOAD(0); }
        MEMBAR();
        __builtin_amdgcn_s_barrier();
        MEMBAR();
        const bool helper = wv == 7 && is_helper(b);
        u4 GR, GI, FR, FI, HR, HI, T1, T2;
        u32 ev;
        {
            if (VUN == 1) __builtin_amdgcn_raw_buffer_store_b32(0u, rws, lane * 128, WS_V + b * 32768 + (wv - 4) * 8192, SC1);
            if (!WFIRST) { __builtin_amdgcn_s_sleep(W0DELAY); if (!SCAN) WLOAD(0); }
            if (VUN == 2) __builtin_amdgcn_raw_buffer_store_b32(0u, rws, lane * 128, WS_V + b * 32768 + (wv - 4) * 8192, SC1);
            if (VUN == 3) { UNROLL for (int i = 0; i < 4; ++i) store128<SC1>(u4{0u, 0u, 0u, 0u}, rws, (lane >> 2) * 128 + (lane & 3) * 16 + WS_V + b * 32768 + (wv - 4) * 8192 + i * 2048); }
            const bool split = (b & 6) == 0;
            const int er = split ? 16 * (tile & 15) + ((lane >> 1) & 15) : 8 * tile + ((lane >> 1) & 7);
            const int ec = (split ? (tile & 16) * 16 : 0) - cls + ((lane & 1) ? (split ? 262 : 518) : SCAN ? (split ? 256 : 512) : 0);
            const int sr = split ? 16 * (tile & 15) + (lane >> 2) : 8 * tile + (lane >> 3);
            const int scn = (split ? (tile & 16) * 16 + (lane & 3) * 64 : (lane & 7) * 64) - cls;
            u32 scan = SCAN ? __builtin_amdgcn_raw_buffer_load_b32(rw, ((cls + 8 * sr) * 513 + scn) * 2, 0, W0AUX) : 0u;
            u32 edge = EDGE ? __builtin_amdgcn_raw_buffer_load_b32(rw, ((cls + 8 * er) * 513 + ec) * 2, 0, W0AUX) : 0u;
            ev = __builtin_amdgcn_raw_buffer_load_b32(rws, 0, WS_E, SC1);
            const u4 smp = __builtin_amdgcn_raw_buffer_load_b128(rw, (lane >> 3) * (2 * 131328 + 128) + (lane & 7) * 16, 0, 0);
            MEMBAR();
            if (helper) {
            UNROLL for (int j = 0; j < 4; ++j) {
                GR[j] = rot((float)((4 * q + j) * (cls + 8 * n)) * (1.0f / 128.0f) + (cls == 4 ? 0.25f : 0.0f));
                HR[j] = rot((float)(n * (cls + 8 * (4 * q + j))) * (1.0f / 128.0f));
                FR[j] = rot((float)(n * (4 * q + j)) * (1.0f / 16.0f));
                T1[j] = rot((float)((4 * q + j) * (cls + 8 * n)) * (1.0f / 2048.0f));
                T2[j] = rot((float)(n * (cls + 8 * (4 * q + j))) * (1.0f / 2048.0f));
            }
            GI = quarter(GR); HI = quarter(HR); FI = quarter(FR);
            pin(GR); pin(GI); pin(HR); pin(HI); pin(FR); pin(FI); pin(T1); pin(T2);
            }
            VMWAIT(W0WAIT);
            STAMP(1);
            if (SCAN) { WLOAD(4); VMWAIT(WWIN); }
            const bool quick = VUN == 4 && (int)((u32)__builtin_amdgcn_s_memrealtime() - tstart) < QUICK;
            if (quick) {
                __builtin_amdgcn_raw_buffer_store_b32(0u, rws, lane * 128, WS_V + b * 32768 + (wv - 4) * 8192, SC1);
                if (ZUN && wv < 6) { const int rg = ((b & 7) >> 1) * 64 + (cls >= 4 ? 63 - tile : tile);
                    __hip_atomic_fetch_or((__attribute__((address_space(1))) u32*)(ws + WS_Z + rg * 16384 + (wv - 4) * 8192 + lane * 128), opq(0u), __ATOMIC_RELAXED, __HIP_MEMORY_SCOPE_AGENT); }
            }
            const u32 lda = (u32)(size_t)(LAS u32*)&lds[L_LD + wv - 4];
            if (ZHOLD == 1) asm volatile("ds_write_b32 %0, %1" :: "v"(lda), "v"(1u) : "memory");
            WLOAD(1); VMWAIT(WWIN);
            if (ZHOLD == 2) asm volatile("ds_write_b32 %0, %1" :: "v"(lda), "v"(1u) : "memory");
            WLOAD(2); VMWAIT(WWIN);
            if (ZHOLD == 3) asm volatile("ds_write_b32 %0, %1" :: "v"(lda), "v"(1u) : "memory");
            WLOAD(3);
            if (VUN == 4) VMWAIT(12);
            if (ZHOLD == 4) asm volatile("ds_write_b32 %0, %1" :: "v"(lda), "v"(1u) : "memory");
            pin(edge); pin(scan); pin(code);
            MagMax ms; ms.add(smp.x, smp.y); ms.add(smp.z, smp.w);
            const u32 es = clampe(ms.exponent() + MARGIN);
            VMWAIT(0);
            lset(lds, L_PR + wv - 4, es, lane);
            STAMP(2);
        }
        const u32 ep0 = rfl(ev) + 1u;
        if (wv == 5 || wv == 6) {
            lwait4(lds, L_PR, 1u);
            if (MAXLATE) lwait(lds, L_P2, 1u);
            MagMax m;
            UNROLL for (int c = 0; c < 32; ++c) { const u4 v = *(const u4*)&lds[L_W + ((wv - 5) * 32 + c) * 256 + lane * 4]; m.add(v.x, v.y); m.add(v.z, v.w); }
            const u32 e = clampe(m.exponent());
            MEMBAR(); if (lane == 0) *(u2*)&lds[L_WW + 2 * (wv - 5)] = u2{e, e}; MEMBAR();
        }
        if (VGATE && wv == 6) {
            __builtin_amdgcn_s_setprio(2);
            lwait(lds, L_P2, 1u);
            __builtin_amdgcn_s_sleep(G2DELAY);
            flag_wait(rws, WS_G2, b, lane, ep0);
            lset(lds, L_G2, 1u, lane);
            STAMP(5);
        }
        if (wv == 4) {
            if (ZGATE) { flag_wait(rws, WS_G1, b, lane, ep0); lset(lds, L_G1, 1u, lane); }
            const u32 ex = lwait4(lds, L_EW, 1u), ew = lwait4(lds, L_WW, 1u);
            if (FLATE) lwait(lds, L_P2, 1u);
            flag_store(rws, WS_F, b, lane, ex | (ew << 8), tag_of(ep0, WS_F + b));
            lwait(lds, L_P2, 1u);
            __builtin_amdgcn_s_sleep(FDELAY);
            const u4 f = flag_wait(rws, WS_F, b, lane, ep0);
            const u32 gx = wave_maxu(max4u(f & 0xffu)), gwm = wave_maxu(max4u((f >> 8) & 0xffu));
            const bool redo = gx > lget(lds, L_SX) || gwm > lwait4(lds, L_PR, 1u);
            lset(lds, L_VD, gx | (gwm << 8) | (redo ? 0x20000u : 0x10000u), lane);
            STAMP(5);
        } else if (helper) {
            __builtin_amdgcn_s_setprio(2);
            u32 ew = lwait4(lds, L_PR, 1u);
            NOUNROLL for (u32 pass = 0; pass < 2; ++pass) {
                u4 WH[2];
                const float sw = __builtin_bit_cast(float, (249u - ew) << 23);
                UNROLL for (int r = 0; r < 4; ++r) UNROLL for (int f = 0; f < 2; ++f) {
                    const unsigned short* wp = (const unsigned short*)&lds[L_W + (n * 4 + 2 * f) * 256 + (4 * q + r) * 16] + 16 + cls;
                    float re = bflo(wp[0]) * sw, im = bflo(wp[512]) * sw;
                    pin(re);
                    WH[f][r] = pkh(re, im);
                }
                pin(WH);
                STAMP(3);
                lwait(lds, L_ZL, pass + 1u);
                const u32 tg = tag_of(ep0 + pass, 0u), par = parity_of(ep0 + pass);
                u4 hz;
                NOUNROLL for (int spin = 0; spin < SPIN_CAP; ++spin) {
                    MEMBAR();
                    u32 bad = 0;
                    UNROLL for (int j = 0; j < 4; ++j) { const u2 g = __builtin_amdgcn_raw_buffer_load_b64(rws, (64 * q + n + 16 * j) * HSTR, WS_HB + ((b & 7) >> 1) * (256 * HSTR), SC1); hz[j] = g.x & ~TM; bad |= g.y ^ tg; }
                    if (__all(bad == 0u)) break;
                    __builtin_amdgcn_s_sleep(4);
                }
                STAMP(4);
                const u32 f16 = cls == 0 ? 0x00002c00u : cls == 4 ? 0x2c000000u : cls > 4 ? 0xac002c00u : 0x2c002c00u;
                u4 d, o[2];
                UNROLL for (int j = 0; j < 4; ++j) d[j] = hmul(hz[j], f16);
                f4 yr = mfma16(d, GR), yi = mfma16(d, GI);
                UNROLL for (int r = 0; r < 4; ++r) d[r] = cmul_conj(pkh(yr[r], yi[r]), T1[r]);
                const f4 xr = mfma16(FR, d), xi = mfma16(FI, d);
                UNROLL for (int f = 0; f < 2; ++f) {
                    UNROLL for (int r = 0; r < 4; ++r) d[r] = cmul_swap(pkh(xr[r], xi[r]), WH[f][r]);
                    yr = mfma16(d, FR); yi = mfma16(d, FI);
                    UNROLL for (int r = 0; r < 4; ++r) o[f][r] = cmul_conj(pkh(yr[r], yi[r]), T2[r]);
                }
                const f4 p0 = mfma16(HR, o[0]), p1 = mfma16(HI, o[0]), p2 = mfma16(HR, o[1]), p3 = mfma16(HI, o[1]);
                UNROLL for (int r = 0; r < 4; ++r)
                    store128<SC1>(u4{mark(pkh(p0[r], p1[r]), par, TM), tg, mark(pkh(p2[r], p3[r]), par, TM), tg}, rws, (64 * q + n) * 16 + WS_VL + r * 256 + cls * 4096);
                STAMP(5);
                const u32 vd = lwait(lds, L_VD, 1u);
                if (!(vd & 0x20000u)) break;
                ew = (vd >> 8) & 0xffu;
            }
        }
        if (CODEPF) asm volatile(".Lgf_end:");
        STAMP_OUT();
        return;
    }

    u32 vd = 0;
    NOUNROLL for (u32 pass = 0; pass < 2; ++pass) {
    int t = threadIdx.x, wv = wv0, b = blockIdx.x;
    pin(t);
    asm volatile("" : "+s"(wv));
    asm volatile("" : "+s"(b));
    const int lane = t & 63, q = lane >> 4, n = lane & 15;
    const u32 pv = pass + 1u;
    if (XLAST && pass == 0) { __builtin_amdgcn_s_barrier(); MEMBAR(); __builtin_amdgcn_s_setprio(1); if (XDELAY) __builtin_amdgcn_s_sleep(XDELAY); }
    u2 xv[8];
    const rsrc_t rx = mkrs(x, 2048 * 2048);
    const u4 smp = __builtin_amdgcn_raw_buffer_load_b128(rx, (lane >> 3) * (524288 + 2048 + 256) + (lane & 7) * 16, 0, 0);
    UNROLL for (int s1 = 0; s1 < 8; ++s1) xv[s1] = __builtin_amdgcn_raw_buffer_load_b64(rx, t * 8, (256 * s1 + b) * 2048, XAUX);
    const u32 ev = __builtin_amdgcn_raw_buffer_load_b32(rws, 0, WS_E, SC1);
    const u2 gv = __builtin_amdgcn_raw_buffer_load_b64(mkrs(gw, 2048), t * 8, 0, 0);
    MEMBAR();
    SB();
    if (!XLAST && pass == 0) { __builtin_amdgcn_s_barrier(); MEMBAR(); __builtin_amdgcn_s_setprio(1); }
    u4 MR[4], MI[4], TW[4];
    {
        u4 tw; u2 m;
        core_setup(lane, wv, tw, m);
        *(u4*)&lds[L_M + (2 + wv) * 256 + lane * 4] = tw;
        lds[L_M + (wv >> 1) * 256 + lane * 4 + (wv & 1)] = m.x; lds[L_M + (wv >> 1) * 256 + lane * 4 + 2 + (wv & 1)] = m.y;
    }
    const int zc = perm32(lane & 31) + (lane >> 5) * 256, zcu = zc + ((lane & 31) == 0 ? 543 : 511);
    int zl = (zc >> 4) * 16384 + (zc & 15) * 4, zu = (zcu >> 4) * 16384 + (zcu & 15) * 4;
    int zh = lane == 0 ? 0 : OOB;
    int tro = L_A1 + wv * 1024 + (4 * (lane >> 5) + ((lane & 15) >> 2)) * 16 + ((lane >> 4) & 1) * 8 + (lane & 3) * 2;
    u32 tm = TM;
    pin(zl); pin(zu); pin(zh); pin(tro); pin(tm);
    STAMP(1);
    SB();
    pin(code);
    u32 ex;
    {
        MagMax ms; ms.add(smp.x, smp.y); ms.add(smp.z, smp.w);
        ex = pass ? (vd & 0xffu) : clampe(ms.exponent() + MARGIN);
    }
    STAMP(2);
    u4 tags;
    u32 z[16], z512;
    {
        const float sc = __builtin_bit_cast(float, (250u - ex) << 23);
        const float rs = sc * 0.70710678118654752f;
        u32 o[8][2];
        UNROLL for (int j = 0; j < 2; ++j) {
            f2 v[8];
            UNROLL for (int s1 = 0; s1 < 8; ++s1) v[s1] = f2{bflo(xv[s1][j]), bfhi(xv[s1][j])};
            const f2 a0 = v[0] + v[4], a1 = v[1] + v[5], a2 = v[2] + v[6], a3 = v[3] + v[7];
            const f2 b0 = (v[0] - v[4]) * sc, b1 = v[1] - v[5], b2_ = (v[2] - v[6]) * sc, b3 = v[3] - v[7];
            const f2 e0 = a0 + a2, e1 = (a1 + a3) * sc, dm = b1 - b3, dp = b1 + b3;
            f2 r[8];
            r[0] = e0 * sc + e1; r[1] = e0 * sc - e1;
            r[2] = dm * rs + b0; r[3] = -(dp * rs) - b2_;
            r[4] = (a0 - a2) * sc; r[5] = (a3 - a1) * sc;
            r[6] = b0 - dm * rs; r[7] = b2_ - dp * rs;
            UNROLL for (int p = 0; p < 8; ++p) o[p][j] = pkh(r[p].x, r[p].y);
        }
        UNROLL for (int p = 0; p < 8; ++p) *(u2*)&lds[L_A1 + p * 512 + 2 * t] = u2{o[p][0], o[p][1]};
        lset(lds, L_C1 + wv, pv, lane);
        const u32 epoch = rfl(ev) + pv;
        tags = u4{parity_of(epoch), 0u, epoch, tag_of(epoch, 0u)};
        u4 in[4];
        NOUNROLL for (int spin = 0; spin < LSPIN_CAP; ++spin) {
            MEMBAR();
            const u4 c = *(const u4*)&lds[L_C1];
            MEMBAR();
            MR[0] = *(const u4*)&lds[L_M + lane * 4]; MR[1] = *(const u4*)&lds[L_M + 256 + lane * 4];
            UNROLL for (int s = 0; s < 4; ++s) {
                const u2 re = __builtin_bit_cast(u2, __builtin_amdgcn_ds_read_tr16_b64_v4i16((LAS s4*)&lds[tro + s * 128]));
                const u2 im = __builtin_bit_cast(u2, __builtin_amdgcn_ds_read_tr16_b64_v4i16((LAS s4*)&lds[tro + s * 128 + 512]));
                in[s] = u4{re.x, re.y, im.x, im.y};
            }
            pin(MR[0]); pin(MR[1]);
            if (rfl(min4u(c)) >= pv) break;
        }
        UNROLL for (int i = 0; i < 4; ++i) TW[i] = *(const u4*)&lds[L_M + (2 + i) * 256 + lane * 4];
        STAMP(3);
        if (ZGATE && pass == 0 && wv == 0) flag_store(rws, WS_G1, b, lane, 0u, tag_of(tags.z, WS_G1 + b));
        core_expand(lane, MR, MI);
        f16v zr, zi;
        core_fft<true>(in, MR, MI, TW, zr, zi);
        UNROLL for (int r = 0; r < 16; ++r) z[r] = mark(pkh(zr[r], zi[r]), tags.x, tm);
        z512 = z[8];
        const u32 za = __builtin_amdgcn_readlane(z[8], 32), zb = __builtin_amdgcn_readlane(z[0], 0);
        MEMBAR();
        const u32 zt = lane < 32 ? za : zb;
        UNROLL for (int r = 8; r < 16; ++r) z[r] = (lane & 31) == 0 ? (r < 15 ? z[r + 1] : zt) : z[r];
    }
    pin(z);
    STAMP(4);
    const int zs = WS_Z + wv * 64 * 16384 + (((b >> 2) & 3) * 64 + (b & 3) * 16 + ((b >> 4) & 3) * 4 + (b >> 6)) * 64;
    bool zsent = false;
#define TRY_STORE_Z(force) do { if (!zsent) { \
        bool go = true; \
        if (ZHOLD) { if (force) lwait4(lds, L_LD, 1u); else { MEMBAR(); go = rfl(min4u(*(const u4*)&lds[L_LD])) != 0u; } } \
        if (go) { \
            UNROLL for (int r = 0; r < 16; ++r) __builtin_amdgcn_raw_buffer_store_b32(z[r], rws, r < 8 ? zl : zu, zs + (r & 7) * 32768, SC1); \
            __builtin_amdgcn_raw_buffer_store_b64(u2{z512, tags.w}, rws, zh, WS_HB + (wv * 256 + b) * HSTR, SC1); \
            zsent = true; } } SB(); } while (0)
    TRY_STORE_Z(false);
    const int row = (b & 7) >> 1, cls = cls_of(b), tile = tile_of(b);
    const bool packed = row == 0;
    u4 FR, FI, GR[4], GI[4], HR[4], HI[4], sel1;
    {
        const float ofs = cls == 4 ? 0.25f : 0.0f;
        float gu = (float)(cls + 8 * n) * (1.0f / 2048.0f), g0 = (float)(64 * q + 4 * wv) * gu + ofs;
        u32 s16 = 0x2c002c00u;
        pin(gu); pin(g0); pin(s16);
        UNROLL for (int i = 0; i < 4; ++i) UNROLL for (int j = 0; j < 4; ++j) { const u32 g = rot(fmaf((float)(16 * j + i), gu, g0)); GR[i][j] = hmul(g, s16); GI[i][j] = quarter(g, s16); }
    }
    pin(GR); pin(GI);
    {
        MagMax m;
        UNROLL for (int s1 = 0; s1 < 8; ++s1) m.add(xv[s1].x, xv[s1].y);
        if (wv == 0) lset(lds, L_SX, ex, lane);
        lset(lds, L_EW + wv, clampe(m.exponent()), lane);
    }
    TRY_STORE_Z(false);
    UNROLL for (int j = 0; j < 4; ++j) FR[j] = rot((float)(n * (4 * q + j)) * (1.0f / 16.0f));
    FI = quarter(FR);
    {
        float hs[4], hu[4];
        UNROLL for (int i = 0; i < 4; ++i) { hs[i] = (float)(16 * n + 4 * wv + i) * (1.0f / 2048.0f); hu[i] = (float)(cls + 32 * q + 8 * i); }
        pin(hs); pin(hu);
        UNROLL for (int i = 0; i < 4; ++i) { UNROLL for (int j = 0; j < 4; ++j) { float a = hs[i] * hu[j]; pin(a); HR[i][j] = rot(a); } HI[i] = quarter(HR[i]); }
        const int d = n + cls - 8 * q;
        UNROLL for (int j = 0; j < 4; ++j) sel1[j] = (d >> 1) == j ? ((d & 1) ? 0xffff0000u : 0xffffu) : 0u;
    }
    pin(FR); pin(FI); pin(HR); pin(HI); pin(sel1);
    TRY_STORE_Z(true);
    STAMP(5);
    VMWAIT(WWAIT);
    u4 WA[4], WB[4];
    u32 ew = 0;
    UNROLL for (int i = 0; i < 4; ++i) asm volatile("" : "=v"(WA[i]), "=v"(WB[i]));
#define WIDEN(es) do { \
        u4 wr[4][4]; \
        int wl = L_W / 4 + wv * 1024 + n * 4 + min(q, 2); pin(wl); \
        UNROLL for (int i = 0; i < 4; ++i) UNROLL for (int a = 0; a < 4; ++a) wr[i][a] = ((const u4*)lds)[wl + (i * 4 + a) * 64]; \
        ew = pass ? (vd >> 8) & 0xffu : (es); \
        const u32 sw = ((249u - ew - (packed ? 1u : 0u)) << 7) * 0x10001u;       \
        const u4 sels = sel1 & sw; \
        SB(); \
        f4 wf[4][4]; \
        UNROLL for (int k = 0; k < 10; ++k) { \
            if (k < 8) { wf[k >> 1][2 * (k & 1)] = mfma16b(wr[k >> 1][2 * (k & 1)], sels); wf[k >> 1][2 * (k & 1) + 1] = mfma16b(wr[k >> 1][2 * (k & 1) + 1], sels); } \
            if (k >= 2) { const int i_ = (k - 2) >> 1, f_ = k & 1; UNROLL for (int r = 0; r < 4; ++r) (f_ ? WB : WA)[i_][r] = pkh(wf[i_][2 * f_][r], wf[i_][2 * f_ + 1][r]); } \
            SB(); } \
        pin(WA); pin(WB); } while (0)
    MEMBAR();
    const u4 pr0 = *(const u4*)&lds[L_PR];
    const bool wearly = rfl(min4u(pr0)) != 0u;
    MEMBAR();
    if (wearly) WIDEN(rfl(max4u(pr0)));
    STAMP(6);
    u32 z1[4][4], z2[4][4];
    const int zvm = q * 64 + (63 - tile) * 16384 + (15 - n) * 4;
    const int zv1 = cls <= 4 ? q * 64 + tile * 16384 + n * 4 : zvm;
    int zso = WS_Z + row * 64 * 16384 + wv * 4096;
    asm volatile("" : "+s"(zso));
#define LOAD_Z() do { \
        if (packed) { int a_ = zvm; pin(a_); UNROLL for (int i = 0; i < 4; ++i) UNROLL for (int j = 0; j < 4; ++j) z2[i][j] = __builtin_amdgcn_raw_buffer_load_b32(rws, a_ + (4 * i + j) * 256, zso, SC1); } \
        else { UNROLL for (int i = 0; i < 4; ++i) UNROLL for (int j = 0; j < 4; ++j) z2[i][j] = tags.x; } \
        int c_ = zv1; pin(c_); UNROLL for (int i = 0; i < 4; ++i) UNROLL for (int j = 0; j < 4; ++j) z1[i][j] = __builtin_amdgcn_raw_buffer_load_b32(rws, c_ + (4 * i + j) * 256, zso, SC1); } while (0)
    if (ZGATE) lwait(lds, L_G1, 1u);
    VMWAIT(ZWAIT);
    if (ZDELAY) __builtin_amdgcn_s_sleep(ZDELAY);
    LOAD_Z();
    if (wv == 0) lset(lds, L_ZL, pv, lane);
    SB();
    STAMP(7);
    u32 x1 = tags.x ^ (cls > 4 ? 0x80000000u : 0u), x2 = tags.x ^ (cls == 4 ? 0x00008000u : cls == 0 ? 0x80000000u : 0u);
    const int lw = ((wv * 16 + 4 * q) * 16 + n) * 4;
    int lr = (q * 16 + 4 * wv) * 16 + n;
    const u4* l4 = (const u4*)lds;
    pin(GR); pin(GI); pin(x1); pin(x2); pin(lr);
    STAMP(8);
    SB();
    {
        f4 yr[4], yi[4];
        NOUNROLL for (int spin = 0; spin < SPIN_CAP; ++spin) {
            u32 m = 0;
            UNROLL for (int i = 0; i < 4; ++i) {
                u4 d;
                UNROLL for (int j = 0; j < 4; ++j) { z1[i][j] ^= x1; z2[i][j] ^= x2; }
                UNROLL for (int j = 0; j < 4; ++j) d[j] = U(H(z1[i][j]) + H(z2[i][j]));
                UNROLL for (int j = 0; j < 4; ++j) m |= z1[i][j] | z2[i][j];
                yr[i] = mfma16(GR[i], d); yi[i] = mfma16(GI[i], d);
                SB();
            }
            if (__all((m & TM) == 0u)) break;
            MEMBAR();
            LOAD_Z();
        }
        STAMP(9);
        u4 y[4];
        UNROLL for (int i = 0; i < 4; ++i) UNROLL for (int r = 0; r < 4; ++r) y[r][i] = pkh(yr[i][r], yi[i][r]);
        UNROLL for (int r = 0; r < 4; ++r) *(u4*)&lds[L_A2 + lw + r * 64] = y[r];
        lset(lds, L_C3 + wv, pv, lane);
    }
    if (!wearly) { const u32 es = lwait4(lds, L_PR, 1u); WIDEN(es); }
    {
        u4 d[4];
        NOUNROLL for (int spin = 0; spin < LSPIN_CAP; ++spin) {
            MEMBAR();
            const u4 c = *(const u4*)&lds[L_C3];
            MEMBAR();
            UNROLL for (int i = 0; i < 4; ++i) d[i] = l4[L_A2 / 4 + lr + i * 16];
            pin(d[0]);
            if (rfl(min4u(c)) >= pv) break;
        }
        STAMP(10);
        if (VGATE == 1 && pass == 0 && wv == 0) { flag_store(rws, WS_G2, b, lane, 0u, tag_of(tags.z, WS_G2 + b)); lset(lds, L_P2, 1u, lane); }
        pin(WA); pin(WB);
        f4 xr[4], xi[4];
        u4 xx[4], ya[4], yb[4];
        UNROLL for (int i = 0; i < 4; ++i) {
            xr[i] = mfma16(FR, d[i]); xi[i] = mfma16(FI, d[i]);
            if (i >= 2) { UNROLL for (int r = 0; r < 4; ++r) xx[i - 2][r] = pkh(xr[i - 2][r], xi[i - 2][r]); pin(xx[i - 2]); }
            SB();
        }
        UNROLL for (int r = 0; r < 4; ++r) ya[0][r] = cmul_swap(xx[0][r], WA[0][r]);
        UNROLL for (int i = 2; i < 4; ++i) { UNROLL for (int r = 0; r < 4; ++r) xx[i][r] = pkh(xr[i][r], xi[i][r]); pin(xx[i]); }
        SB();
        f4 p0[4], p1[4], p2[4], p3[4];
        UNROLL for (int i = 0; i < 4; ++i) {
            p0[i] = mfma16(FR, ya[i]); p1[i] = mfma16(FI, ya[i]);
            if (i < 3) { UNROLL for (int r = 0; r < 4; ++r) ya[i + 1][r] = cmul_swap(xx[i + 1][r], WA[i + 1][r]); }
            else { UNROLL for (int r = 0; r < 4; ++r) yb[0][r] = cmul_swap(xx[0][r], WB[0][r]); }
            SB();
        }
        u4 ra[4], rb[4];
        UNROLL for (int i = 0; i < 4; ++i) {
            p2[i] = mfma16(FR, yb[i]); p3[i] = mfma16(FI, yb[i]);
            if (i < 3) { UNROLL for (int r = 0; r < 4; ++r) yb[i + 1][r] = cmul_swap(xx[i + 1][r], WB[i + 1][r]); }
            UNROLL for (int k = 0; k < 4; ++k) ra[i][k] = pkh(p0[k][i], p1[k][i]);
            *(u4*)&lds[L_B + lw + i * 64] = ra[i];
            SB();
        }
        UNROLL for (int i = 0; i < 3; ++i) UNROLL for (int r = 0; r < 4; ++r) rb[r][i] = pkh(p2[i][r], p3[i][r]);
        SB();
        UNROLL for (int r = 0; r < 4; ++r) { rb[r][3] = pkh(p2[3][r], p3[3][r]); *(u4*)&lds[L_B + 4096 + lw + r * 64] = rb[r]; }
        lset(lds, L_C4 + wv, pv, lane);
    }
    STAMP(11);
    if (VGATE == 2 && pass == 0 && wv == 0) { flag_store(rws, WS_G2, b, lane, 0u, tag_of(tags.z, WS_G2 + b)); lset(lds, L_P2, 1u, lane); }
    {
        u4 da[4], db[4];
        NOUNROLL for (int spin = 0; spin < LSPIN_CAP; ++spin) {
            MEMBAR();
            const u4 c = *(const u4*)&lds[L_C4];
            MEMBAR();
            UNROLL for (int i = 0; i < 4; ++i) { da[i] = l4[L_B / 4 + lr + i * 16]; db[i] = l4[L_B / 4 + 1024 + lr + i * 16]; }
            pin(da[0]);
            if (rfl(min4u(c)) >= pv) break;
        }
        const int vo = 64 * q * 32768 + n * 8, so = WS_V + 4 * wv * 32768 + cls * 4096 + tile * 128;
        f4 p0[4], p1[4], p2[4], p3[4];
        UNROLL for (int i = 0; i < 5; ++i) {
            if (i < 4) { p0[i] = mfma16(HR[i], da[i]); p1[i] = mfma16(HI[i], da[i]); p2[i] = mfma16(HR[i], db[i]); p3[i] = mfma16(HI[i], db[i]); }
            if (i > 0) { UNROLL for (int r = 0; r < 4; ++r)
                __builtin_amdgcn_raw_buffer_store_b64(u2{mark(pkh(p0[i - 1][r], p1[i - 1][r]), tags.x, tm), mark(pkh(p2[i - 1][r], p3[i - 1][r]), tags.x, tm)}, rws, vo, so + (16 * r + i - 1) * 32768, SC1); }
            SB();
        }
    }
    if (wv == 0) lset(lds, L_P2, 1u, lane);
    STAMP(12);
    SB();
    {
        pin(xv);
        const f2 g01 = {bflo(gv.x), bfhi(gv.x)}, g23 = {bflo(gv.y), bfhi(gv.y)};
        f4 acc = {};
        UNROLL for (int i = 0; i < 4; ++i) {
            u4 pp;
            UNROLL for (int k = 0; k < 2; ++k) {
                const f2 a = f2{bflo(xv[2 * i + k].x), bfhi(xv[2 * i + k].x)} * g01, c = f2{bflo(xv[2 * i + k].y), bfhi(xv[2 * i + k].y)} * g23;
                pp[2 * k] = pkb(a.x, a.y); pp[2 * k + 1] = pkb(c.x, c.y);
            }
            const u32 s0 = n == 2 * i ? 0x3f803f80u : 0u, s1 = n == 2 * i + 1 ? 0x3f803f80u : 0u;
            acc = __builtin_amdgcn_mfma_f32_16x16x32_bf16(__builtin_bit_cast(b8, pp), __builtin_bit_cast(b8, u4{s0, s0, s1, s1}), acc, 0, 0, 0);
        }
        if (n < 8) lds[L_GS + n * 16 + (t >> 4)] = __builtin_bit_cast(u32, (acc.x + acc.y) + (acc.z + acc.w));
        lset(lds, L_C5 + wv, pv, lane);
    }
    MR[0] = *(const u4*)&lds[L_M + lane * 4]; MR[1] = *(const u4*)&lds[L_M + 256 + lane * 4];
    UNROLL for (int i = 0; i < 4; ++i) TW[i] = *(const u4*)&lds[L_M + (2 + i) * 256 + lane * 4];
    core_expand(lane, MR, MI);
    pin(MR); pin(MI); pin(TW);
    u4 PM;
    {
        lwait4(lds, L_C5, pv);
        float gg[4];
        UNROLL for (int k = 0; k < 2; ++k) {
            const f4* pp = (const f4*)&lds[L_GS + ((n >> 2) * 2 + k) * 16];
            const f4 ps = (pp[0] + pp[1]) + (pp[2] + pp[3]);
            const float lg = rbf((ps.x + ps.y) + (ps.z + ps.w));
            gg[2 * k] = rbf(1.0f / (1.0f + __expf(-lg)));
            gg[2 * k + 1] = rbf(1.0f - gg[2 * k]);
        }
        UNROLL for (int k = 0; k < 4; ++k) { gg[k] *= 0.03125f; pin(gg[k]); }
        const int r = n & 3, sa = (n >> 2) * 2;
        UNROLL for (int cc = 0; cc < 2; ++cc) {
            const int u1 = 2 * q + cc;
            const float ta = (float)(sa * u1) * 0.125f + ((r & 1) ? 0.25f : (r & 2) ? 0.5f : 0.0f);
            const float tb = (float)((sa + 1) * u1) * 0.125f + ((r & 1) ? ((r & 2) ? 0.0f : 0.5f) : 0.25f);
            const float ca = cosr(ta), sa_ = sinr(ta), cb = cosr(tb), sb_ = sinr(tb);
            float e[4] = {gg[2] * cb, gg[2] * sb_, gg[3] * cb, gg[3] * sb_};
            pin(e);
            e[0] = fmaf(gg[0], ca, e[0]); pin(e[0]); e[1] = fmaf(gg[0], sa_, e[1]); pin(e[1]); e[2] = fmaf(gg[1], ca, e[2]); pin(e[2]); e[3] = fmaf(gg[1], sa_, e[3]);
            PM[2 * cc] = pkh(e[0], e[1]); PM[2 * cc + 1] = pkh(e[2], e[3]);
        }
    }
    const float osc = __builtin_bit_cast(float, (u32)min(max((int)ex + (int)ew - 130, 1), 254) << 23) * TRUNC_FIX;
    const int ve = 2 * q * 4096 + (128 * wv + n) * 8;
    int vs0 = WS_V + b * 32768, vs1 = vs0 + 4096, vsl = WS_VL + b * 16;
    asm volatile("" : "+s"(vs0), "+s"(vs1), "+s"(vsl));
    int ov = (4 * perm32(lane & 31) + (lane >> 5)) * 16 + (512 * wv + b) * 2048;
    pin(PM); pin(ov);
    STAMP(13);
    if (VGATE) lwait(lds, L_G2, 1u);
    if (VSTAG) { const int st_ = b & 3; if (st_ == 0) VMWAIT(12); else if (st_ == 1) VMWAIT(8); else if (st_ == 2) VMWAIT(4); else VMWAIT(0); }
    else VMWAIT(VWAIT);
    if (VDELAY) __builtin_amdgcn_s_sleep(VDELAY);
    {
        f4 z[9];
        NOUNROLL for (int spin = 0; spin < SPIN_CAP; ++spin) {
            MEMBAR();
            u2 v0[8], v1[8];
            u4 ve8, vo8;
            int a_ = ve; pin(a_);
            if (wv == 3) {
                ve8 = __builtin_amdgcn_raw_buffer_load_b128(rws, q * 8192, vsl, SC1); vo8 = __builtin_amdgcn_raw_buffer_load_b128(rws, q * 8192 + 4096, vsl, SC1);
            } else {
                const u2 e2 = __builtin_amdgcn_raw_buffer_load_b64(rws, a_ + 1024, vs0, SC1), o2 = __builtin_amdgcn_raw_buffer_load_b64(rws, a_ + 1024, vs1, SC1);
                ve8 = u4{e2.x, e2.y, tags.w, tags.w}; vo8 = u4{o2.x, o2.y, tags.w, tags.w};
            }
            UNROLL for (int i = 0; i < 8; ++i) {
                v0[i] = __builtin_amdgcn_raw_buffer_load_b64(rws, a_ + 128 * i, vs0, SC1);
                v1[i] = __builtin_amdgcn_raw_buffer_load_b64(rws, a_ + 128 * i, vs1, SC1);
            }
            SB();
            u32 m = (((wv == 3 ? ve8.y : ve8.z) ^ tags.w) | (ve8.w ^ tags.w) | ((wv == 3 ? vo8.y : vo8.z) ^ tags.w) | (vo8.w ^ tags.w)) ? TM : 0u;
            UNROLL for (int k = 0; k < 9; ++k) {
                const int i = (k + 8) % 9;
                const u4 d = (i == 8 ? u4{ve8.x, wv == 3 ? ve8.z : ve8.y, vo8.x, wv == 3 ? vo8.z : vo8.y} : u4{v0[i & 7].x, v0[i & 7].y, v1[i & 7].x, v1[i & 7].y}) ^ tags.x;
                m |= d.x | d.y; m |= d.z | d.w;
                z[i] = mfma16(PM, d);
                SB();
            }
            if (__all((m & TM) == 0u)) break;
        }
        STAMP(14);
        if (wv == 0 && n == 0) { z[0].x = 0.5f * (z[0].x + z[0].z); z[0].y = 0.5f * (z[0].y + z[0].w); }
        if (wv == 3) { z[8].z = 0.5f * (z[8].x + z[8].z); z[8].w = 0.5f * (z[8].y + z[8].w); }
        u4* l4w = (u4*)&lds[L_A1];
        UNROLL for (int p = 0; p < 2; ++p)
            l4w[(q * 8 + wv) * 32 + 16 * p + n] = u4{pkh(z[p].x, z[p + 2].x), pkh(z[p + 4].x, z[p + 6].x), pkh(z[p].y, z[p + 2].y), pkh(z[p + 4].y, z[p + 6].y)};
        l4w[(q * 8 + 7 - wv) * 32 + 16 - n] = u4{pkh(z[7].z, z[5].z), pkh(z[3].z, z[1].z), pkh(z[7].w, z[5].w), pkh(z[3].w, z[1].w)};
        f2 m[4];
        UNROLL for (int i = 0; i < 4; ++i) m[i] = n == 0 ? f2{z[8 - 2 * i].z, z[8 - 2 * i].w} : f2{z[6 - 2 * i].z, z[6 - 2 * i].w};
        l4w[(q * 8 + 7 - wv) * 32 + ((32 - n) & 31)] = u4{pkh(m[0].x, m[1].x), pkh(m[2].x, m[3].x), pkh(m[0].y, m[1].y), pkh(m[2].y, m[3].y)};
        lset(lds, L_C6 + wv, 2u * pv - 1u, lane);
    }
    {
        u4 in[4];
        NOUNROLL for (int spin = 0; spin < LSPIN_CAP; ++spin) {
            MEMBAR();
            const u4 c = *(const u4*)&lds[L_C6];
            MEMBAR();
            UNROLL for (int s = 0; s < 4; ++s) in[s] = *(const u4*)&lds[L_A1 + (wv * 256 + s * 64 + lane) * 4];
            pin(in[0]);
            if (rfl(min4u(c)) >= 2u * pv - 1u) break;
        }
        f16v zr, zi;
        core_fft<false>(in, MR, MI, TW, zr, zi);
        zr *= osc; zi *= osc;
        const rsrc_t ro = mkrs(out, 2048 * 2048);
        UNROLL for (int hf = 0; hf < 2; ++hf) {
            u4 c;
            UNROLL for (int j = 0; j < 4; ++j) c[j] = pkb(zr[8 * hf + 2 * j], zr[8 * hf + 2 * j + 1]);
            store128<OAUX>(c, ro, ov + 256 * 2048 + 32 * hf);
        }
        UNROLL for (int hf = 0; hf < 2; ++hf) {
            u4 a;
            UNROLL for (int j = 0; j < 4; ++j) a[j] = pkb(zi[8 * hf + 2 * j], zi[8 * hf + 2 * j + 1]);
            store128<OAUX>(a, ro, ov + 32 * hf);
        }
    }
    STAMP(15);
    vd = lwait(lds, L_VD, 1u);
    if (pass == 1 || !(vd & 0x20000u)) {
        if (b == 0 && t == 0) __builtin_amdgcn_raw_buffer_store_b32(tags.z, rws, 0, WS_E, SC1);
        break;
    }
    lset(lds, L_C6 + wv, 2u, lane);
    lwait4(lds, L_C6, 2u);
    VMWAIT(0);
    }
    STAMP_OUT();
}

extern "C" void kernel_launch(void* const* d_in, const int* in_sizes, int n_in,
                              void* d_out, int out_size, void* d_ws, size_t ws_size,
                              hipStream_t stream) {
    gfnet_fused<<<256, 512, 0, stream>>>(d_in[0], d_in[6], d_in[1], d_in[2], d_in[4], d_in[5], (char*)d_ws, d_out);
}
```

```cpp
#define DIAG 5
#define DIAGB 100
#define VUN 0
#include <hip/hip_runtime.h>
#include <hip/hip_bf16.h>
#include <stdint.h>

typedef _Float16 h2 __attribute__((ext_vector_type(2)));
typedef _Float16 h8 __attribute__((ext_vector_type(8)));
typedef __bf16 b2 __attribute__((ext_vector_type(2)));
typedef __bf16 b8 __attribute__((ext_vector_type(8)));
typedef float f2 __attribute__((ext_vector_type(2)));
typedef float f4 __attribute__((ext_vector_type(4)));
typedef float f16v __attribute__((ext_vector_type(16)));
typedef short s4 __attribute__((ext_vector_type(4)));
typedef unsigned u32;
typedef u32 u2 __attribute__((ext_vector_type(2)));
typedef u32 u4 __attribute__((ext_vector_type(4)));
typedef unsigned short us2 __attribute__((ext_vector_type(2)));
#define DI __device__ __forceinline__
#define UNROLL _Pragma("unroll")
#define NOUNROLL _Pragma("unroll 1")
#define LAS __attribute__((address_space(3)))

#define W0AUX 0
#define W1AUX 0
#define VDELAY 0
#define ZDELAY 0
#define WFIRST 0
#define VSTAG 0
#define GEARLY 0
#define ZGATE 0
#define VGATE 0
#define G2DELAY 50
#define MAXLATE 0
#define FLATE 0
#define FDELAY 24
#define EDGE 1
#define SCAN 0
#define ZHOLD 4
#define ZSLEEP 0
#define XLAST 0
#define XDELAY 0
#define G0 0
#define WWAIT 4
#define CODEPF 4
#define QUICK 170
#define ZUN 0
#define ROT 1
#define PSLEEP 8
#define WGATE 4
#define W0DELAY 2
#define W0WAIT 0
#define WWIN 12
#define ZWAIT 0
#define VWAIT 0
#define XAUX 2
#define OAUX 2
#define MARGIN 2

#define FSTRIDE 16
constexpr u32 FSTR = FSTRIDE;
constexpr u32 WS_Z = 0;
constexpr u32 WS_V = 4u << 20;
constexpr u32 WS_VL = 12u << 20;
#define HSTR 32
constexpr u32 WS_HB = WS_VL + (32u << 10);
constexpr u32 WS_F = WS_HB + (128u << 10);
constexpr u32 WS_G1 = WS_F + 2048 * FSTR;
constexpr u32 WS_G2 = WS_G1 + 2048 * FSTR;
constexpr u32 WS_E = WS_G2 + 2048 * FSTR;
constexpr u32 WS_BYTES = WS_E + 4096;
constexpr size_t WS_META = 24u << 20;
constexpr int SC1 = 16;
constexpr int SPIN_CAP = 8192;
constexpr int LSPIN_CAP = 1 << 17;
constexpr int OOB = 0x7ffffff0;
constexpr float TRUNC_FIX = 1.00066f;
constexpr u32 TM = 0x00010001u;

template <class T> DI void pin(T& v) { asm volatile("" : "+v"(v)); }
template <class T, int N> DI void pin(T (&v)[N]) { UNROLL for (int i = 0; i < N; ++i) pin(v[i]); }
DI u32 opq(u32 v) { asm("" : "+v"(v)); return v; }
DI u32 pkh(float lo, float hi) { h2 r; r.x = (_Float16)lo; r.y = (_Float16)hi; return __builtin_bit_cast(u32, r); }
DI u32 pkb(float lo, float hi) { b2 r; r.x = (__bf16)lo; r.y = (__bf16)hi; return __builtin_bit_cast(u32, r); }
DI float bflo(u32 v) { return __builtin_bit_cast(float, v << 16); }
DI float bfhi(u32 v) { return __builtin_bit_cast(float, v & 0xffff0000u); }
DI float rbf(float v) { return (float)(__bf16)v; }
DI h2 H(u32 v) { return __builtin_bit_cast(h2, v); }
DI u32 U(h2 v) { return __builtin_bit_cast(u32, v); }
DI f16v mfma32(u4 a, u4 b, f16v c) { return __builtin_amdgcn_mfma_f32_32x32x16_f16(__builtin_bit_cast(h8, a), __builtin_bit_cast(h8, b), c, 0, 0, 0); }
DI f4 mfma16(u4 a, u4 b) { return __builtin_amdgcn_mfma_f32_16x16x32_f16(__builtin_bit_cast(h8, a), __builtin_bit_cast(h8, b), f4{}, 0, 0, 0); }
DI f4 mfma16b(u4 a, u4 b) { return __builtin_amdgcn_mfma_f32_16x16x32_bf16(__builtin_bit_cast(b8, a), __builtin_bit_cast(b8, b), f4{}, 0, 0, 0); }
DI u32 hmul(u32 a, u32 b) { return U(H(a) * H(b)); }
DI u32 hfma(u32 a, u32 b, u32 c) { return U(__builtin_elementwise_fma(H(a), H(b), H(c))); }
DI u32 cmul_swap(u32 x, u32 w) { const h2 a = H(opq(x)), c = H(opq(w)); h2 n = c; n.y = -n.y; return U(__builtin_elementwise_fma(a.yy, n, a.xx * c.yx)); }
DI u32 cmul_conj(u32 x, u32 t) { const h2 a = H(opq(x)), c = H(opq(t)); h2 n = c.yy; n.y = -n.y; return U(__builtin_elementwise_fma(a.yx, n, a * c.xx)); }
DI u32 pkmax(u32 a, u32 b) { return __builtin_bit_cast(u32, __builtin_elementwise_max(__builtin_bit_cast(us2, a), __builtin_bit_cast(us2, b))); }
DI float sinr(float r) { return __builtin_amdgcn_sinf(r); }
DI float cosr(float r) { return __builtin_amdgcn_cosf(r); }
DI int perm32(int n) { return (n & 0x13) | ((n & 4) << 1) | ((n & 8) >> 1); }
DI u32 max3u(u32 a, u32 b, u32 c) { return max(max(a, b), c); }
DI u32 max4u(u4 v) { return max(max(v.x, v.y), max(v.z, v.w)); }
DI u32 min4u(u4 v) { return min(min(v.x, v.y), min(v.z, v.w)); }
DI u32 mark(u32 v, u32 par, u32 tm) { return (par & tm) | (v & ~tm); }
DI u32 rfl(u32 v) { return __builtin_amdgcn_readfirstlane(v); }
DI u32 pow2h(int d) { const float f = __builtin_bit_cast(float, (u32)(127 - min(d, 64)) << 23); return pkh(f, f); }

typedef __amdgpu_buffer_rsrc_t rsrc_t;
DI rsrc_t mkrs(const void* p, u32 bytes) { return __builtin_amdgcn_make_buffer_rsrc((void*)p, (short)0, (int)bytes, 0x00020000); }
template <int AUX> DI void store128(u4 v, rsrc_t r, int off) { __builtin_amdgcn_raw_buffer_store_b128(v, r, off, 0, AUX); }
#define SB() __builtin_amdgcn_sched_barrier(0)
#define MEMBAR() asm volatile("" ::: "memory")
#define VMWAIT(n) asm volatile("s_waitcnt vmcnt(%0)" :: "n"(n) : "memory")
#define STAMP_DECL unsigned long long st_[16] = {}
#define STAMP(i) do { if (i == 0 || i == DIAG % 20) { SB(); asm volatile("s_memrealtime %0\n\ts_waitcnt lgkmcnt(0)" : "=s"(st_[i]) :: "memory"); SB(); } } while (0)
#define STAMP_OUT() do { if ((DIAG < 20 ? wv0 < 4 : (wv0 >= 4 && st_[DIAG % 20])) && (DIAGB < 0 || (int)blockIdx.x == DIAGB)) { while (__builtin_amdgcn_s_memrealtime() - st_[0] < 3000 + 4 * (st_[DIAG % 20] - st_[0])) __builtin_amdgcn_s_sleep(1); } } while (0)
template <int CTRL> DI u32 dpp(u32 v) { return __builtin_amdgcn_update_dpp(0u, v, CTRL, 0xf, 0xf, true); }
DI u32 wave_maxu(u32 v) {
    v = max(v, dpp<0xB1>(v)); v = max(v, dpp<0x4E>(v)); v = max(v, dpp<0x141>(v)); v = max(v, dpp<0x140>(v));
    return max(max((u32)__builtin_amdgcn_readlane(v, 0), (u32)__builtin_amdgcn_readlane(v, 16)), max((u32)__builtin_amdgcn_readlane(v, 32), (u32)__builtin_amdgcn_readlane(v, 48)));
}

DI void core_setup(int lane, int s, u4& tw, u2& m) {
    float fc = (float)perm32(lane & 31) * (1.0f / 1024.0f), p0 = (float)(4 * (lane >> 5) + 8 * s) * fc, p1 = (float)(4 * (lane >> 5) + 8 * (s >> 1) + 2 * (s & 1)) * fc * 32.0f;
    pin(fc); pin(p0); pin(p1);
    float tc[4], ts[4];
    UNROLL for (int i = 0; i < 4; ++i) { const float pt = fmaf((float)i, fc, p0); tc[i] = cosr(pt); ts[i] = sinr(pt); }
    tw = u4{pkh(tc[0], tc[1]), pkh(tc[2], tc[3]), pkh(ts[0], ts[1]), pkh(ts[2], ts[3])};
    const float p2 = fmaf(32.0f, fc, p1);
    m = u2{pkh(cosr(p1), cosr(p2)), pkh(sinr(p1), sinr(p2))};
}
DI void core_expand(int lane, u4 (&MR)[4], u4 (&MI)[4]) {
    const u32 sg = (lane & 1) ? 0x80008000u : 0u;
    MR[2] = MR[0] ^ sg; MR[3] = MR[1] ^ sg;
    UNROLL for (int s = 0; s < 4; ++s) MI[s] = u4{MR[s].z ^ 0x80008000u, MR[s].w ^ 0x80008000u, MR[s].x, MR[s].y};
}
template <bool OUT_B> DI void core_fft(const u4 (&in)[4], const u4 (&MR)[4], const u4 (&MI)[4], const u4 (&TW)[4], f16v& outr, f16v& outi) {
    f16v yr = {}, yi = {};
    UNROLL for (int s = 0; s < 4; ++s) yr = mfma32(in[s], MR[s], yr);
    SB();
    yi = mfma32(in[0], MI[0], yi); yi = mfma32(in[1], MI[1], yi);
    SB();
    h2 t0[8], t1[8];
#define TW_RE(p) do { const h2 pr_ = H(pkh(yr[2 * (p)], yr[2 * (p) + 1])); t0[p] = pr_ * H(TW[(p) >> 1][(p) & 1]); t1[p] = pr_ * H(TW[(p) >> 1][2 + ((p) & 1)]); } while (0)
    TW_RE(0); TW_RE(1); SB();
    yi = mfma32(in[2], MI[2], yi);
    TW_RE(2); TW_RE(3); SB();
    yi = mfma32(in[3], MI[3], yi);
    TW_RE(4); TW_RE(5); TW_RE(6); TW_RE(7); SB();
    outr = f16v{}; outi = f16v{};
    u4 d[4];
#define TW_IM(s) do { UNROLL for (int j = 0; j < 2; ++j) { const int p_ = 2 * (s) + j; const h2 pi_ = H(pkh(yi[2 * p_], yi[2 * p_ + 1])); \
        d[s][j] = U(__builtin_elementwise_fma(pi_, H(TW[s][2 + j]), t0[p_])); d[s][2 + j] = U(__builtin_elementwise_fma(pi_, H(TW[s][j]), -t1[p_])); } } while (0)
    TW_IM(0); SB();
    UNROLL for (int s = 0; s < 4; ++s) {
        outr = OUT_B ? mfma32(MR[s], d[s], outr) : mfma32(d[s], MR[s], outr);
        if (s < 3) TW_IM(s + 1);
        SB();
    }
    UNROLL for (int s = 0; s < 4; ++s) outi = OUT_B ? mfma32(MI[s], d[s], outi) : mfma32(d[s], MI[s], outi);
}
DI u32 rot(float ph) { return pkh(cosr(ph), sinr(ph)); }
DI u32 quarter(u32 r, u32 f) { u32 o; asm("v_pk_mul_f16 %0, %1, %2 op_sel:[1,0] op_sel_hi:[0,1] neg_lo:[1,0]" : "=v"(o) : "v"(r), "v"(f)); return o; }
DI u4 quarter(u4 r) { u32 one = 0x3c003c00u; pin(one); return u4{quarter(r.x, one), quarter(r.y, one), quarter(r.z, one), quarter(r.w, one)}; }

DI u32 tag_of(u32 epoch, u32 idx) { u32 h = (epoch ^ (idx * 0x9E3779B1u)) * 0x85EBCA6Bu; return h ^ (h >> 15); }
DI u32 parity_of(u32 epoch) { return (epoch & 1u) ? 0x00000001u : 0x00010000u; }
DI void flag_store(rsrc_t rws, u32 arr, int b, int lane, u32 payload, u32 tag) {
    __builtin_amdgcn_raw_buffer_store_b64(u2{payload, tag}, rws, lane < 8 ? lane * (int)(256 * FSTR) : OOB, arr + b * FSTR, SC1);
}
DI u4 flag_wait(rsrc_t rws, u32 arr, int b, int lane, u32 epoch) {
    u2 f[4]; u32 tg[4];
    UNROLL for (int j = 0; j < 4; ++j) tg[j] = tag_of(epoch, arr + 4 * lane + j);
    NOUNROLL for (int spin = 0; spin < SPIN_CAP; ++spin) {
        MEMBAR();
        UNROLL for (int j = 0; j < 4; ++j) f[j] = __builtin_amdgcn_raw_buffer_load_b64(rws, lane * (4 * FSTR) + j * FSTR, arr + (b & 7) * (256 * FSTR), SC1);
        if (__all(f[0].y == tg[0] && f[1].y == tg[1] && f[2].y == tg[2] && f[3].y == tg[3])) break;
        __builtin_amdgcn_s_sleep(PSLEEP);
    }
    return u4{f[0].x, f[1].x, f[2].x, f[3].x};
}
DI int cls_of(int b) { const int row = (b & 7) >> 1; return row == 0 ? 4 * (b >> 7) : ((b & 1) ? 8 - row : row); }
DI int tile_of(int b) { return (b & 6) == 0 ? 16 * (b & 1) + ((b >> 3) & 15) : b >> 3; }
DI bool is_helper(int b) { return tile_of(b) == 31; }

constexpr int L_C1 = 0, L_C3 = 4, L_C4 = 8, L_C5 = 12, L_C6 = 16;
constexpr int L_EW = 20;
constexpr int L_WW = 24;
constexpr int L_SW = 28;
constexpr int L_PR = 32;
constexpr int L_SX = 36;
constexpr int L_ZL = 37;
constexpr int L_VD = 38;
constexpr int L_G1 = 39, L_G2 = 40, L_P2 = 41;
constexpr int L_LD = 44;
constexpr int L_GS = 64;
constexpr int L_M = 192;
constexpr int L_A1 = L_M + 1536;
constexpr int L_A2 = L_A1 + 4096;
constexpr int L_B = L_A2 + 4096;
constexpr int L_W = L_B + 8192;
constexpr int L_END = L_W + 64 * 256;

DI void lset(u32* lds, int idx, u32 v, int lane) { MEMBAR(); if (lane == 0) lds[idx] = v; MEMBAR(); }
DI u32 lget(u32* lds, int idx) { MEMBAR(); return rfl(lds[idx]); }
DI u32 lwait(u32* lds, int idx, u32 atleast) {
    u32 v = 0;
    NOUNROLL for (int spin = 0; spin < LSPIN_CAP; ++spin) { v = lget(lds, idx); if (v >= atleast) break; __builtin_amdgcn_s_sleep(1); }
    MEMBAR();
    return v;
}
DI u32 lwait4(u32* lds, int idx, u32 atleast) {
    u4 v;
    NOUNROLL for (int spin = 0; spin < LSPIN_CAP; ++spin) { MEMBAR(); v = *(const u4*)&lds[idx]; if (rfl(min4u(v)) >= atleast) break; __builtin_amdgcn_s_sleep(1); }
    MEMBAR();
    return rfl(max4u(v));
}
struct MagMax {
    h2 hi = {}, lo = {};
    DI void add(u32 a, u32 c) {
        hi = __builtin_elementwise_maximum(__builtin_elementwise_maximum(hi, H(a)), H(c));
        lo = __builtin_elementwise_minimum(__builtin_elementwise_minimum(lo, H(a)), H(c));
    }
    DI u32 exponent() const {
        const u32 mx = pkmax(U(hi) & 0x7fff7fffu, U(lo) & 0x7fff7fffu);
        return wave_maxu(max(mx & 0xffffu, mx >> 16)) >> 7;
    }
};
DI u32 clampe(u32 e) { return min(max(e, 8u), 240u); }

__global__ __launch_bounds__(512, 2) void gfnet_fused(const void* x, const void* gw, const void* war, const void* wai, const void* wbr, const void* wbi,
                                                      char* ws, void* out) {
    __shared__ __attribute__((aligned(16))) u32 lds[L_END];
    STAMP_DECL;
    STAMP(0);
    u32 code = 0;
    if (CODEPF) {
        unsigned long long pc0; int len;
        asm volatile("s_getpc_b64 %0\n.Lgf_begin:\n\ts_mov_b32 %1, .Lgf_end-.Lgf_begin" : "=s"(pc0), "=s"(len));
        code = __builtin_amdgcn_raw_buffer_load_b32(mkrs((const void*)pc0, (u32)max(len, 0)), (threadIdx.x - 256u) < 16u * CODEPF ? ((blockIdx.x >> 3) / CODEPF * CODEPF * 16 + threadIdx.x - 256) * 64 : OOB, 0, 0);
        SB();
    }
    asm volatile("" :: "s"(x), "s"(gw), "s"(war), "s"(wai), "s"(wbr), "s"(wbi), "s"(ws), "s"(out));
    const int wv0 = __builtin_amdgcn_readfirstlane(threadIdx.x >> 6);
    const rsrc_t rws = mkrs(ws, WS_BYTES);

    if (wv0 >= 4) {
        const u32 tstart = VUN == 4 ? (u32)__builtin_amdgcn_s_memrealtime() : 0u;
        const int wv = wv0, b = blockIdx.x, cls = cls_of(b), tile = tile_of(b);
        const int lane = threadIdx.x & 63, q = lane >> 4, n = lane & 15;
        const rsrc_t rw = mkrs(wv == 4 ? war : wv == 5 ? wai : wv == 6 ? wbr : wbi, 2048 * 1026);
        const int wvo = (lane >> 2) * (128 * 1026) + min(lane & 3, 2) * 16, wso = (cls * 513 + 16 * tile - cls) * 2;
#define WLOAD(r) do { UNROLL for (int st = 0; st < 4; ++st) { const int c_ = 4 * st + ((ROT * tile + (r)) & 3); \
                __builtin_amdgcn_raw_ptr_buffer_load_lds(rw, (LAS void*)&lds[L_W + (c_ * 4 + wv - 4) * 256], 16, wvo, wso + c_ * 8208, 0, (r) == 0 ? W0AUX : W1AUX); } SB(); } while (0)
        if (wv == 4) { lds[lane] = 0u; asm volatile("s_waitcnt lgkmcnt(0)" ::: "memory"); }
        if (WFIRST) { __builtin_amdgcn_s_sleep(W0DELAY); WLOAD(0); }
        MEMBAR();
        __builtin_amdgcn_s_barrier();
        MEMBAR();
        const bool helper = wv == 7 && is_helper(b);
        u4 GR, GI, FR, FI, HR, HI, T1, T2;
        u32 ev;
        {
            if (VUN == 1) __builtin_amdgcn_raw_buffer_store_b32(0u, rws, lane * 128, WS_V + b * 32768 + (wv - 4) * 8192, SC1);
            if (!WFIRST) { __builtin_amdgcn_s_sleep(W0DELAY); if (!SCAN) WLOAD(0); }
            if (VUN == 2) __builtin_amdgcn_raw_buffer_store_b32(0u, rws, lane * 128, WS_V + b * 32768 + (wv - 4) * 8192, SC1);
            if (VUN == 3) { UNROLL for (int i = 0; i < 4; ++i) store128<SC1>(u4{0u, 0u, 0u, 0u}, rws, (lane >> 2) * 128 + (lane & 3) * 16 + WS_V + b * 32768 + (wv - 4) * 8192 + i * 2048); }
            const bool split = (b & 6) == 0;
            const int er = split ? 16 * (tile & 15) + ((lane >> 1) & 15) : 8 * tile + ((lane >> 1) & 7);
            const int ec = (split ? (tile & 16) * 16 : 0) - cls + ((lane & 1) ? (split ? 262 : 518) : SCAN ? (split ? 256 : 512) : 0);
            const int sr = split ? 16 * (tile & 15) + (lane >> 2) : 8 * tile + (lane >> 3);
            const int scn = (split ? (tile & 16) * 16 + (lane & 3) * 64 : (lane & 7) * 64) - cls;
            u32 scan = SCAN ? __builtin_amdgcn_raw_buffer_load_b32(rw, ((cls + 8 * sr) * 513 + scn) * 2, 0, W0AUX) : 0u;
            u32 edge = EDGE ? __builtin_amdgcn_raw_buffer_load_b32(rw, ((cls + 8 * er) * 513 + ec) * 2, 0, W0AUX) : 0u;
            ev = __builtin_amdgcn_raw_buffer_load_b32(rws, 0, WS_E, SC1);
            const u4 smp = __builtin_amdgcn_raw_buffer_load_b128(rw, (lane >> 3) * (2 * 131328 + 128) + (lane & 7) * 16, 0, 0);
            MEMBAR();
            if (helper) {
            UNROLL for (int j = 0; j < 4; ++j) {
                GR[j] = rot((float)((4 * q + j) * (cls + 8 * n)) * (1.0f / 128.0f) + (cls == 4 ? 0.25f : 0.0f));
                HR[j] = rot((float)(n * (cls + 8 * (4 * q + j))) * (1.0f / 128.0f));
                FR[j] = rot((float)(n * (4 * q + j)) * (1.0f / 16.0f));
                T1[j] = rot((float)((4 * q + j) * (cls + 8 * n)) * (1.0f / 2048.0f));
                T2[j] = rot((float)(n * (cls + 8 * (4 * q + j))) * (1.0f / 2048.0f));
            }
            GI = quarter(GR); HI = quarter(HR); FI = quarter(FR);
            pin(GR); pin(GI); pin(HR); pin(HI); pin(FR); pin(FI); pin(T1); pin(T2);
            }
            VMWAIT(W0WAIT);
            STAMP(1);
            if (SCAN) { WLOAD(4); VMWAIT(WWIN); }
            const bool quick = VUN == 4 && (int)((u32)__builtin_amdgcn_s_memrealtime() - tstart) < QUICK;
            if (quick) {
                __builtin_amdgcn_raw_buffer_store_b32(0u, rws, lane * 128, WS_V + b * 32768 + (wv - 4) * 8192, SC1);
                if (ZUN && wv < 6) { const int rg = ((b & 7) >> 1) * 64 + (cls >= 4 ? 63 - tile : tile);
                    __hip_atomic_fetch_or((__attribute__((address_space(1))) u32*)(ws + WS_Z + rg * 16384 + (wv - 4) * 8192 + lane * 128), opq(0u), __ATOMIC_RELAXED, __HIP_MEMORY_SCOPE_AGENT); }
            }
            const u32 lda = (u32)(size_t)(LAS u32*)&lds[L_LD + wv - 4];
            if (ZHOLD == 1) asm volatile("ds_write_b32 %0, %1" :: "v"(lda), "v"(1u) : "memory");
            WLOAD(1); VMWAIT(WWIN);
            if (ZHOLD == 2) asm volatile("ds_write_b32 %0, %1" :: "v"(lda), "v"(1u) : "memory");
            WLOAD(2); VMWAIT(WWIN);
            if (ZHOLD == 3) asm volatile("ds_write_b32 %0, %1" :: "v"(lda), "v"(1u) : "memory");
            WLOAD(3);
            if (VUN == 4) VMWAIT(12);
            if (ZHOLD == 4) asm volatile("ds_write_b32 %0, %1" :: "v"(lda), "v"(1u) : "memory");
            pin(edge); pin(scan); pin(code);
            MagMax ms; ms.add(smp.x, smp.y); ms.add(smp.z, smp.w);
            const u32 es = clampe(ms.exponent() + MARGIN);
            VMWAIT(0);
            lset(lds, L_PR + wv - 4, es, lane);
            STAMP(2);
        }
        const u32 ep0 = rfl(ev) + 1u;
        if (wv == 5 || wv == 6) {
            lwait4(lds, L_PR, 1u);
            if (MAXLATE) lwait(lds, L_P2, 1u);
            MagMax m;
            UNROLL for (int c = 0; c < 32; ++c) { const u4 v = *(const u4*)&lds[L_W + ((wv - 5) * 32 + c) * 256 + lane * 4]; m.add(v.x, v.y); m.add(v.z, v.w); }
            const u32 e = clampe(m.exponent());
            MEMBAR(); if (lane == 0) *(u2*)&lds[L_WW + 2 * (wv - 5)] = u2{e, e}; MEMBAR();
        }
        if (VGATE && wv == 6) {
            __builtin_amdgcn_s_setprio(2);
            lwait(lds, L_P2, 1u);
            __builtin_amdgcn_s_sleep(G2DELAY);
            flag_wait(rws, WS_G2, b, lane, ep0);
            lset(lds, L_G2, 1u, lane);
            STAMP(5);
        }
        if (wv == 4) {
            if (ZGATE) { flag_wait(rws, WS_G1, b, lane, ep0); lset(lds, L_G1, 1u, lane); }
            const u32 ex = lwait4(lds, L_EW, 1u), ew = lwait4(lds, L_WW, 1u);
            if (FLATE) lwait(lds, L_P2, 1u);
            flag_store(rws, WS_F, b, lane, ex | (ew << 8), tag_of(ep0, WS_F + b));
            lwait(lds, L_P2, 1u);
            __builtin_amdgcn_s_sleep(FDELAY);
            const u4 f = flag_wait(rws, WS_F, b, lane, ep0);
            const u32 gx = wave_maxu(max4u(f & 0xffu)), gwm = wave_maxu(max4u((f >> 8) & 0xffu));
            const bool redo = gx > lget(lds, L_SX) || gwm > lwait4(lds, L_PR, 1u);
            lset(lds, L_VD, gx | (gwm << 8) | (redo ? 0x20000u : 0x10000u), lane);
            STAMP(5);
        } else if (helper) {
            __builtin_amdgcn_s_setprio(2);
            u32 ew = lwait4(lds, L_PR, 1u);
            NOUNROLL for (u32 pass = 0; pass < 2; ++pass) {
                u4 WH[2];
                const float sw = __builtin_bit_cast(float, (249u - ew) << 23);
                UNROLL for (int r = 0; r < 4; ++r) UNROLL for (int f = 0; f < 2; ++f) {
                    const unsigned short* wp = (const unsigned short*)&lds[L_W + (n * 4 + 2 * f) * 256 + (4 * q + r) * 16] + 16 + cls;
                    float re = bflo(wp[0]) * sw, im = bflo(wp[512]) * sw;
                    pin(re);
                    WH[f][r] = pkh(re, im);
                }
                pin(WH);
                STAMP(3);
                lwait(lds, L_ZL, pass + 1u);
                const u32 tg = tag_of(ep0 + pass, 0u), par = parity_of(ep0 + pass);
                u4 hz;
                NOUNROLL for (int spin = 0; spin < SPIN_CAP; ++spin) {
                    MEMBAR();
                    u32 bad = 0;
                    UNROLL for (int j = 0; j < 4; ++j) { const u2 g = __builtin_amdgcn_raw_buffer_load_b64(rws, (64 * q + n + 16 * j) * HSTR, WS_HB + ((b & 7) >> 1) * (256 * HSTR), SC1); hz[j] = g.x & ~TM; bad |= g.y ^ tg; }
                    if (__all(bad == 0u)) break;
                    __builtin_amdgcn_s_sleep(4);
                }
                STAMP(4);
                const u32 f16 = cls == 0 ? 0x00002c00u : cls == 4 ? 0x2c000000u : cls > 4 ? 0xac002c00u : 0x2c002c00u;
                u4 d, o[2];
                UNROLL for (int j = 0; j < 4; ++j) d[j] = hmul(hz[j], f16);
                f4 yr = mfma16(d, GR), yi = mfma16(d, GI);
                UNROLL for (int r = 0; r < 4; ++r) d[r] = cmul_conj(pkh(yr[r], yi[r]), T1[r]);
                const f4 xr = mfma16(FR, d), xi = mfma16(FI, d);
                UNROLL for (int f = 0; f < 2; ++f) {
                    UNROLL for (int r = 0; r < 4; ++r) d[r] = cmul_swap(pkh(xr[r], xi[r]), WH[f][r]);
                    yr = mfma16(d, FR); yi = mfma16(d, FI);
                    UNROLL for (int r = 0; r < 4; ++r) o[f][r] = cmul_conj(pkh(yr[r], yi[r]), T2[r]);
                }
                const f4 p0 = mfma16(HR, o[0]), p1 = mfma16(HI, o[0]), p2 = mfma16(HR, o[1]), p3 = mfma16(HI, o[1]);
                UNROLL for (int r = 0; r < 4; ++r)
                    store128<SC1>(u4{mark(pkh(p0[r], p1[r]), par, TM), tg, mark(pkh(p2[r], p3[r]), par, TM), tg}, rws, (64 * q + n) * 16 + WS_VL + r * 256 + cls * 4096);
                STAMP(5);
                const u32 vd = lwait(lds, L_VD, 1u);
                if (!(vd & 0x20000u)) break;
                ew = (vd >> 8) & 0xffu;
            }
        }
        if (CODEPF) asm volatile(".Lgf_end:");
        STAMP_OUT();
        return;
    }

    u32 vd = 0;
    NOUNROLL for (u32 pass = 0; pass < 2; ++pass) {
    int t = threadIdx.x, wv = wv0, b = blockIdx.x;
    pin(t);
    asm volatile("" : "+s"(wv));
    asm volatile("" : "+s"(b));
    const int lane = t & 63, q = lane >> 4, n = lane & 15;
    const u32 pv = pass + 1u;
    if (XLAST && pass == 0) { __builtin_amdgcn_s_barrier(); MEMBAR(); __builtin_amdgcn_s_setprio(1); if (XDELAY) __builtin_amdgcn_s_sleep(XDELAY); }
    u2 xv[8];
    const rsrc_t rx = mkrs(x, 2048 * 2048);
    const u4 smp = __builtin_amdgcn_raw_buffer_load_b128(rx, (lane >> 3) * (524288 + 2048 + 256) + (lane & 7) * 16, 0, 0);
    UNROLL for (int s1 = 0; s1 < 8; ++s1) xv[s1] = __builtin_amdgcn_raw_buffer_load_b64(rx, t * 8, (256 * s1 + b) * 2048, XAUX);
    const u32 ev = __builtin_amdgcn_raw_buffer_load_b32(rws, 0, WS_E, SC1);
    const u2 gv = __builtin_amdgcn_raw_buffer_load_b64(mkrs(gw, 2048), t * 8, 0, 0);
    MEMBAR();
    SB();
    if (!XLAST && pass == 0) { __builtin_amdgcn_s_barrier(); MEMBAR(); __builtin_amdgcn_s_setprio(1); }
    u4 MR[4], MI[4], TW[4];
    {
        u4 tw; u2 m;
        core_setup(lane, wv, tw, m);
        *(u4*)&lds[L_M + (2 + wv) * 256 + lane * 4] = tw;
        lds[L_M + (wv >> 1) * 256 + lane * 4 + (wv & 1)] = m.x; lds[L_M + (wv >> 1) * 256 + lane * 4 + 2 + (wv & 1)] = m.y;
    }
    const int zc = perm32(lane & 31) + (lane >> 5) * 256, zcu = zc + ((lane & 31) == 0 ? 543 : 511);
    int zl = (zc >> 4) * 16384 + (zc & 15) * 4, zu = (zcu >> 4) * 16384 + (zcu & 15) * 4;
    int zh = lane == 0 ? 0 : OOB;
    int tro = L_A1 + wv * 1024 + (4 * (lane >> 5) + ((lane & 15) >> 2)) * 16 + ((lane >> 4) & 1) * 8 + (lane & 3) * 2;
    u32 tm = TM;
    pin(zl); pin(zu); pin(zh); pin(tro); pin(tm);
    STAMP(1);
    SB();
    pin(code);
    u32 ex;
    {
        MagMax ms; ms.add(smp.x, smp.y); ms.add(smp.z, smp.w);
        ex = pass ? (vd & 0xffu) : clampe(ms.exponent() + MARGIN);
    }
    STAMP(2);
    u4 tags;
    u32 z[16], z512;
    {
        const float sc = __builtin_bit_cast(float, (250u - ex) << 23);
        const float rs = sc * 0.70710678118654752f;
        u32 o[8][2];
        UNROLL for (int j = 0; j < 2; ++j) {
            f2 v[8];
            UNROLL for (int s1 = 0; s1 < 8; ++s1) v[s1] = f2{bflo(xv[s1][j]), bfhi(xv[s1][j])};
            const f2 a0 = v[0] + v[4], a1 = v[1] + v[5], a2 = v[2] + v[6], a3 = v[3] + v[7];
            const f2 b0 = (v[0] - v[4]) * sc, b1 = v[1] - v[5], b2_ = (v[2] - v[6]) * sc, b3 = v[3] - v[7];
            const f2 e0 = a0 + a2, e1 = (a1 + a3) * sc, dm = b1 - b3, dp = b1 + b3;
            f2 r[8];
            r[0] = e0 * sc + e1; r[1] = e0 * sc - e1;
            r[2] = dm * rs + b0; r[3] = -(dp * rs) - b2_;
            r[4] = (a0 - a2) * sc; r[5] = (a3 - a1) * sc;
            r[6] = b0 - dm * rs; r[7] = b2_ - dp * rs;
            UNROLL for (int p = 0; p < 8; ++p) o[p][j] = pkh(r[p].x, r[p].y);
        }
        UNROLL for (int p = 0; p < 8; ++p) *(u2*)&lds[L_A1 + p * 512 + 2 * t] = u2{o[p][0], o[p][1]};
        lset(lds, L_C1 + wv, pv, lane);
        const u32 epoch = rfl(ev) + pv;
        tags = u4{parity_of(epoch), 0u, epoch, tag_of(epoch, 0u)};
        u4 in[4];
        NOUNROLL for (int spin = 0; spin < LSPIN_CAP; ++spin) {
            MEMBAR();
            const u4 c = *(const u4*)&lds[L_C1];
            MEMBAR();
            MR[0] = *(const u4*)&lds[L_M + lane * 4]; MR[1] = *(const u4*)&lds[L_M + 256 + lane * 4];
            UNROLL for (int s = 0; s < 4; ++s) {
                const u2 re = __builtin_bit_cast(u2, __builtin_amdgcn_ds_read_tr16_b64_v4i16((LAS s4*)&lds[tro + s * 128]));
                const u2 im = __builtin_bit_cast(u2, __builtin_amdgcn_ds_read_tr16_b64_v4i16((LAS s4*)&lds[tro + s * 128 + 512]));
                in[s] = u4{re.x, re.y, im.x, im.y};
            }
            pin(MR[0]); pin(MR[1]);
            if (rfl(min4u(c)) >= pv) break;
        }
        UNROLL for (int i = 0; i < 4; ++i) TW[i] = *(const u4*)&lds[L_M + (2 + i) * 256 + lane * 4];
        STAMP(3);
        if (ZGATE && pass == 0 && wv == 0) flag_store(rws, WS_G1, b, lane, 0u, tag_of(tags.z, WS_G1 + b));
        core_expand(lane, MR, MI);
        f16v zr, zi;
        core_fft<true>(in, MR, MI, TW, zr, zi);
        UNROLL for (int r = 0; r < 16; ++r) z[r] = mark(pkh(zr[r], zi[r]), tags.x, tm);
        z512 = z[8];
        const u32 za = __builtin_amdgcn_readlane(z[8], 32), zb = __builtin_amdgcn_readlane(z[0], 0);
        MEMBAR();
        const u32 zt = lane < 32 ? za : zb;
        UNROLL for (int r = 8; r < 16; ++r) z[r] = (lane & 31) == 0 ? (r < 15 ? z[r + 1] : zt) : z[r];
    }
    pin(z);
    STAMP(4);
    const int zs = WS_Z + wv * 64 * 16384 + (((b >> 2) & 3) * 64 + (b & 3) * 16 + ((b >> 4) & 3) * 4 + (b >> 6)) * 64;
    bool zsent = false;
#define TRY_STORE_Z(force) do { if (!zsent) { \
        bool go = true; \
        if (ZHOLD) { if (force) lwait4(lds, L_LD, 1u); else { MEMBAR(); go = rfl(min4u(*(const u4*)&lds[L_LD])) != 0u; } } \
        if (go) { \
            UNROLL for (int r = 0; r < 16; ++r) __builtin_amdgcn_raw_buffer_store_b32(z[r], rws, r < 8 ? zl : zu, zs + (r & 7) * 32768, SC1); \
            __builtin_amdgcn_raw_buffer_store_b64(u2{z512, tags.w}, rws, zh, WS_HB + (wv * 256 + b) * HSTR, SC1); \
            zsent = true; } } SB(); } while (0)
    TRY_STORE_Z(false);
    const int row = (b & 7) >> 1, cls = cls_of(b), tile = tile_of(b);
    const bool packed = row == 0;
    u4 FR, FI, GR[4], GI[4], HR[4], HI[4], sel1;
    {
        const float ofs = cls == 4 ? 0.25f : 0.0f;
        float gu = (float)(cls + 8 * n) * (1.0f / 2048.0f), g0 = (float)(64 * q + 4 * wv) * gu + ofs;
        u32 s16 = 0x2c002c00u;
        pin(gu); pin(g0); pin(s16);
        UNROLL for (int i = 0; i < 4; ++i) UNROLL for (int j = 0; j < 4; ++j) { const u32 g = rot(fmaf((float)(16 * j + i), gu, g0)); GR[i][j] = hmul(g, s16); GI[i][j] = quarter(g, s16); }
    }
    pin(GR); pin(GI);
    {
        MagMax m;
        UNROLL for (int s1 = 0; s1 < 8; ++s1) m.add(xv[s1].x, xv[s1].y);
        if (wv == 0) lset(lds, L_SX, ex, lane);
        lset(lds, L_EW + wv, clampe(m.exponent()), lane);
    }
    TRY_STORE_Z(false);
    UNROLL for (int j = 0; j < 4; ++j) FR[j] = rot((float)(n * (4 * q + j)) * (1.0f / 16.0f));
    FI = quarter(FR);
    {
        float hs[4], hu[4];
        UNROLL for (int i = 0; i < 4; ++i) { hs[i] = (float)(16 * n + 4 * wv + i) * (1.0f / 2048.0f); hu[i] = (float)(cls + 32 * q + 8 * i); }
        pin(hs); pin(hu);
        UNROLL for (int i = 0; i < 4; ++i) { UNROLL for (int j = 0; j < 4; ++j) { float a = hs[i] * hu[j]; pin(a); HR[i][j] = rot(a); } HI[i] = quarter(HR[i]); }
        const int d = n + cls - 8 * q;
        UNROLL for (int j = 0; j < 4; ++j) sel1[j] = (d >> 1) == j ? ((d & 1) ? 0xffff0000u : 0xffffu) : 0u;
    }
    pin(FR); pin(FI); pin(HR); pin(HI); pin(sel1);
    TRY_STORE_Z(true);
    STAMP(5);
    VMWAIT(WWAIT);
    u4 WA[4], WB[4];
    u32 ew = 0;
    UNROLL for (int i = 0; i < 4; ++i) asm volatile("" : "=v"(WA[i]), "=v"(WB[i]));
#define WIDEN(es) do { \
        u4 wr[4][4]; \
        int wl = L_W / 4 + wv * 1024 + n * 4 + min(q, 2); pin(wl); \
        UNROLL for (int i = 0; i < 4; ++i) UNROLL for (int a = 0; a < 4; ++a) wr[i][a] = ((const u4*)lds)[wl + (i * 4 + a) * 64]; \
        ew = pass ? (vd >> 8) & 0xffu : (es); \
        const u32 sw = ((249u - ew - (packed ? 1u : 0u)) << 7) * 0x10001u;       \
        const u4 sels = sel1 & sw; \
        SB(); \
        f4 wf[4][4]; \
        UNROLL for (int k = 0; k < 10; ++k) { \
            if (k < 8) { wf[k >> 1][2 * (k & 1)] = mfma16b(wr[k >> 1][2 * (k & 1)], sels); wf[k >> 1][2 * (k & 1) + 1] = mfma16b(wr[k >> 1][2 * (k & 1) + 1], sels); } \
            if (k >= 2) { const int i_ = (k - 2) >> 1, f_ = k & 1; UNROLL for (int r = 0; r < 4; ++r) (f_ ? WB : WA)[i_][r] = pkh(wf[i_][2 * f_][r], wf[i_][2 * f_ + 1][r]); } \
            SB(); } \
        pin(WA); pin(WB); } while (0)
    MEMBAR();
    const u4 pr0 = *(const u4*)&lds[L_PR];
    const bool wearly = rfl(min4u(pr0)) != 0u;
    MEMBAR();
    if (wearly) WIDEN(rfl(max4u(pr0)));
    STAMP(6);
    u32 z1[4][4], z2[4][4];
    const int zvm = q * 64 + (63 - tile) * 16384 + (15 - n) * 4;
    const int zv1 = cls <= 4 ? q * 64 + tile * 16384 + n * 4 : zvm;
    int zso = WS_Z + row * 64 * 16384 + wv * 4096;
    asm volatile("" : "+s"(zso));
#define LOAD_Z() do { \
        if (packed) { int a_ = zvm; pin(a_); UNROLL for (int i = 0; i < 4; ++i) UNROLL for (int j = 0; j < 4; ++j) z2[i][j] = __builtin_amdgcn_raw_buffer_load_b32(rws, a_ + (4 * i + j) * 256, zso, SC1); } \
        else { UNROLL for (int i = 0; i < 4; ++i) UNROLL for (int j = 0; j < 4; ++j) z2[i][j] = tags.x; } \
        int c_ = zv1; pin(c_); UNROLL for (int i = 0; i < 4; ++i) UNROLL for (int j = 0; j < 4; ++j) z1[i][j] = __builtin_amdgcn_raw_buffer_load_b32(rws, c_ + (4 * i + j) * 256, zso, SC1); } while (0)
    if (ZGATE) lwait(lds, L_G1, 1u);
    VMWAIT(ZWAIT);
    if (ZDELAY) __builtin_amdgcn_s_sleep(ZDELAY);
    LOAD_Z();
    if (wv == 0) lset(lds, L_ZL, pv, lane);
    SB();
    STAMP(7);
    u32 x1 = tags.x ^ (cls > 4 ? 0x80000000u : 0u), x2 = tags.x ^ (cls == 4 ? 0x00008000u : cls == 0 ? 0x80000000u : 0u);
    const int lw = ((wv * 16 + 4 * q) * 16 + n) * 4;
    int lr = (q * 16 + 4 * wv) * 16 + n;
    const u4* l4 = (const u4*)lds;
    pin(GR); pin(GI); pin(x1); pin(x2); pin(lr);
    STAMP(8);
    SB();
    {
        f4 yr[4], yi[4];
        NOUNROLL for (int spin = 0; spin < SPIN_CAP; ++spin) {
            u32 m = 0;
            UNROLL for (int i = 0; i < 4; ++i) {
                u4 d;
                UNROLL for (int j = 0; j < 4; ++j) { z1[i][j] ^= x1; z2[i][j] ^= x2; }
                UNROLL for (int j = 0; j < 4; ++j) d[j] = U(H(z1[i][j]) + H(z2[i][j]));
                UNROLL for (int j = 0; j < 4; ++j) m |= z1[i][j] | z2[i][j];
                yr[i] = mfma16(GR[i], d); yi[i] = mfma16(GI[i], d);
                SB();
            }
            if (__all((m & TM) == 0u)) break;
            MEMBAR();
            LOAD_Z();
        }
        STAMP(9);
        u4 y[4];
        UNROLL for (int i = 0; i < 4; ++i) UNROLL for (int r = 0; r < 4; ++r) y[r][i] = pkh(yr[i][r], yi[i][r]);
        UNROLL for (int r = 0; r < 4; ++r) *(u4*)&lds[L_A2 + lw + r * 64] = y[r];
        lset(lds, L_C3 + wv, pv, lane);
    }
    if (!wearly) { const u32 es = lwait4(lds, L_PR, 1u); WIDEN(es); }
    {
        u4 d[4];
        NOUNROLL for (int spin = 0; spin < LSPIN_CAP; ++spin) {
            MEMBAR();
            const u4 c = *(const u4*)&lds[L_C3];
            MEMBAR();
            UNROLL for (int i = 0; i < 4; ++i) d[i] = l4[L_A2 / 4 + lr + i * 16];
            pin(d[0]);
            if (rfl(min4u(c)) >= pv) break;
        }
        STAMP(10);
        if (VGATE == 1 && pass == 0 && wv == 0) { flag_store(rws, WS_G2, b, lane, 0u, tag_of(tags.z, WS_G2 + b)); lset(lds, L_P2, 1u, lane); }
        pin(WA); pin(WB);
        f4 xr[4], xi[4];
        u4 xx[4], ya[4], yb[4];
        UNROLL for (int i = 0; i < 4; ++i) {
            xr[i] = mfma16(FR, d[i]); xi[i] = mfma16(FI, d[i]);
            if (i >= 2) { UNROLL for (int r = 0; r < 4; ++r) xx[i - 2][r] = pkh(xr[i - 2][r], xi[i - 2][r]); pin(xx[i - 2]); }
            SB();
        }
        UNROLL for (int r = 0; r < 4; ++r) ya[0][r] = cmul_swap(xx[0][r], WA[0][r]);
        UNROLL for (int i = 2; i < 4; ++i) { UNROLL for (int r = 0; r < 4; ++r) xx[i][r] = pkh(xr[i][r], xi[i][r]); pin(xx[i]); }
        SB();
        f4 p0[4], p1[4], p2[4], p3[4];
        UNROLL for (int i = 0; i < 4; ++i) {
            p0[i] = mfma16(FR, ya[i]); p1[i] = mfma16(FI, ya[i]);
            if (i < 3) { UNROLL for (int r = 0; r < 4; ++r) ya[i + 1][r] = cmul_swap(xx[i + 1][r], WA[i + 1][r]); }
            else { UNROLL for (int r = 0; r < 4; ++r) yb[0][r] = cmul_swap(xx[0][r], WB[0][r]); }
            SB();
        }
        u4 ra[4], rb[4];
        UNROLL for (int i = 0; i < 4; ++i) {
            p2[i] = mfma16(FR, yb[i]); p3[i] = mfma16(FI, yb[i]);
            if (i < 3) { UNROLL for (int r = 0; r < 4; ++r) yb[i + 1][r] = cmul_swap(xx[i + 1][r], WB[i + 1][r]); }
            UNROLL for (int k = 0; k < 4; ++k) ra[i][k] = pkh(p0[k][i], p1[k][i]);
            *(u4*)&lds[L_B + lw + i * 64] = ra[i];
            SB();
        }
        UNROLL for (int i = 0; i < 3; ++i) UNROLL for (int r = 0; r < 4; ++r) rb[r][i] = pkh(p2[i][r], p3[i][r]);
        SB();
        UNROLL for (int r = 0; r < 4; ++r) { rb[r][3] = pkh(p2[3][r], p3[3][r]); *(u4*)&lds[L_B + 4096 + lw + r * 64] = rb[r]; }
        lset(lds, L_C4 + wv, pv, lane);
    }
    STAMP(11);
    if (VGATE == 2 && pass == 0 && wv == 0) { flag_store(rws, WS_G2, b, lane, 0u, tag_of(tags.z, WS_G2 + b)); lset(lds, L_P2, 1u, lane); }
    {
        u4 da[4], db[4];
        NOUNROLL for (int spin = 0; spin < LSPIN_CAP; ++spin) {
            MEMBAR();
            const u4 c = *(const u4*)&lds[L_C4];
            MEMBAR();
            UNROLL for (int i = 0; i < 4; ++i) { da[i] = l4[L_B / 4 + lr + i * 16]; db[i] = l4[L_B / 4 + 1024 + lr + i * 16]; }
            pin(da[0]);
            if (rfl(min4u(c)) >= pv) break;
        }
        const int vo = 64 * q * 32768 + n * 8, so = WS_V + 4 * wv * 32768 + cls * 4096 + tile * 128;
        f4 p0[4], p1[4], p2[4], p3[4];
        UNROLL for (int i = 0; i < 5; ++i) {
            if (i < 4) { p0[i] = mfma16(HR[i], da[i]); p1[i] = mfma16(HI[i], da[i]); p2[i] = mfma16(HR[i], db[i]); p3[i] = mfma16(HI[i], db[i]); }
            if (i > 0) { UNROLL for (int r = 0; r < 4; ++r)
                __builtin_amdgcn_raw_buffer_store_b64(u2{mark(pkh(p0[i - 1][r], p1[i - 1][r]), tags.x, tm), mark(pkh(p2[i - 1][r], p3[i - 1][r]), tags.x, tm)}, rws, vo, so + (16 * r + i - 1) * 32768, SC1); }
            SB();
        }
    }
    if (wv == 0) lset(lds, L_P2, 1u, lane);
    STAMP(12);
    SB();
    {
        pin(xv);
        const f2 g01 = {bflo(gv.x), bfhi(gv.x)}, g23 = {bflo(gv.y), bfhi(gv.y)};
        f4 acc = {};
        UNROLL for (int i = 0; i < 4; ++i) {
            u4 pp;
            UNROLL for (int k = 0; k < 2; ++k) {
                const f2 a = f2{bflo(xv[2 * i + k].x), bfhi(xv[2 * i + k].x)} * g01, c = f2{bflo(xv[2 * i + k].y), bfhi(xv[2 * i + k].y)} * g23;
                pp[2 * k] = pkb(a.x, a.y); pp[2 * k + 1] = pkb(c.x, c.y);
            }
            const u32 s0 = n == 2 * i ? 0x3f803f80u : 0u, s1 = n == 2 * i + 1 ? 0x3f803f80u : 0u;
            acc = __builtin_amdgcn_mfma_f32_16x16x32_bf16(__builtin_bit_cast(b8, pp), __builtin_bit_cast(b8, u4{s0, s0, s1, s1}), acc, 0, 0, 0);
        }
        if (n < 8) lds[L_GS + n * 16 + (t >> 4)] = __builtin_bit_cast(u32, (acc.x + acc.y) + (acc.z + acc.w));
        lset(lds, L_C5 + wv, pv, lane);
    }
    MR[0] = *(const u4*)&lds[L_M + lane * 4]; MR[1] = *(const u4*)&lds[L_M + 256 + lane * 4];
    UNROLL for (int i = 0; i < 4; ++i) TW[i] = *(const u4*)&lds[L_M + (2 + i) * 256 + lane * 4];
    core_expand(lane, MR, MI);
    pin(MR); pin(MI); pin(TW);
    u4 PM;
    {
        lwait4(lds, L_C5, pv);
        float gg[4];
        UNROLL for (int k = 0; k < 2; ++k) {
            const f4* pp = (const f4*)&lds[L_GS + ((n >> 2) * 2 + k) * 16];
            const f4 ps = (pp[0] + pp[1]) + (pp[2] + pp[3]);
            const float lg = rbf((ps.x + ps.y) + (ps.z + ps.w));
            gg[2 * k] = rbf(1.0f / (1.0f + __expf(-lg)));
            gg[2 * k + 1] = rbf(1.0f - gg[2 * k]);
        }
        UNROLL for (int k = 0; k < 4; ++k) { gg[k] *= 0.03125f; pin(gg[k]); }
        const int r = n & 3, sa = (n >> 2) * 2;
        UNROLL for (int cc = 0; cc < 2; ++cc) {
            const int u1 = 2 * q + cc;
            const float ta = (float)(sa * u1) * 0.125f + ((r & 1) ? 0.25f : (r & 2) ? 0.5f : 0.0f);
            const float tb = (float)((sa + 1) * u1) * 0.125f + ((r & 1) ? ((r & 2) ? 0.0f : 0.5f) : 0.25f);
            const float ca = cosr(ta), sa_ = sinr(ta), cb = cosr(tb), sb_ = sinr(tb);
            float e[4] = {gg[2] * cb, gg[2] * sb_, gg[3] * cb, gg[3] * sb_};
            pin(e);
            e[0] = fmaf(gg[0], ca, e[0]); pin(e[0]); e[1] = fmaf(gg[0], sa_, e[1]); pin(e[1]); e[2] = fmaf(gg[1], ca, e[2]); pin(e[2]); e[3] = fmaf(gg[1], sa_, e[3]);
            PM[2 * cc] = pkh(e[0], e[1]); PM[2 * cc + 1] = pkh(e[2], e[3]);
        }
    }
    const float osc = __builtin_bit_cast(float, (u32)min(max((int)ex + (int)ew - 130, 1), 254) << 23) * TRUNC_FIX;
    const int ve = 2 * q * 4096 + (128 * wv + n) * 8;
    int vs0 = WS_V + b * 32768, vs1 = vs0 + 4096, vsl = WS_VL + b * 16;
    asm volatile("" : "+s"(vs0), "+s"(vs1), "+s"(vsl));
    int ov = (4 * perm32(lane & 31) + (lane >> 5)) * 16 + (512 * wv + b) * 2048;
    pin(PM); pin(ov);
    STAMP(13);
    if (VGATE) lwait(lds, L_G2, 1u);
    if (VSTAG) { const int st_ = b & 3; if (st_ == 0) VMWAIT(12); else if (st_ == 1) VMWAIT(8); else if (st_ == 2) VMWAIT(4); else VMWAIT(0); }
    else VMWAIT(VWAIT);
    if (VDELAY) __builtin_amdgcn_s_sleep(VDELAY);
    {
        f4 z[9];
        NOUNROLL for (int spin = 0; spin < SPIN_CAP; ++spin) {
            MEMBAR();
            u2 v0[8], v1[8];
            u4 ve8, vo8;
            int a_ = ve; pin(a_);
            if (wv == 3) {
                ve8 = __builtin_amdgcn_raw_buffer_load_b128(rws, q * 8192, vsl, SC1); vo8 = __builtin_amdgcn_raw_buffer_load_b128(rws, q * 8192 + 4096, vsl, SC1);
            } else {
                const u2 e2 = __builtin_amdgcn_raw_buffer_load_b64(rws, a_ + 1024, vs0, SC1), o2 = __builtin_amdgcn_raw_buffer_load_b64(rws, a_ + 1024, vs1, SC1);
                ve8 = u4{e2.x, e2.y, tags.w, tags.w}; vo8 = u4{o2.x, o2.y, tags.w, tags.w};
            }
            UNROLL for (int i = 0; i < 8; ++i) {
                v0[i] = __builtin_amdgcn_raw_buffer_load_b64(rws, a_ + 128 * i, vs0, SC1);
                v1[i] = __builtin_amdgcn_raw_buffer_load_b64(rws, a_ + 128 * i, vs1, SC1);
            }
            SB();
            u32 m = (((wv == 3 ? ve8.y : ve8.z) ^ tags.w) | (ve8.w ^ tags.w) | ((wv == 3 ? vo8.y : vo8.z) ^ tags.w) | (vo8.w ^ tags.w)) ? TM : 0u;
            UNROLL for (int k = 0; k < 9; ++k) {
                const int i = (k + 8) % 9;
                const u4 d = (i == 8 ? u4{ve8.x, wv == 3 ? ve8.z : ve8.y, vo8.x, wv == 3 ? vo8.z : vo8.y} : u4{v0[i & 7].x, v0[i & 7].y, v1[i & 7].x, v1[i & 7].y}) ^ tags.x;
                m |= d.x | d.y; m |= d.z | d.w;
                z[i] = mfma16(PM, d);
                SB();
            }
            if (__all((m & TM) == 0u)) break;
        }
        STAMP(14);
        if (wv == 0 && n == 0) { z[0].x = 0.5f * (z[0].x + z[0].z); z[0].y = 0.5f * (z[0].y + z[0].w); }
        if (wv == 3) { z[8].z = 0.5f * (z[8].x + z[8].z); z[8].w = 0.5f * (z[8].y + z[8].w); }
        u4* l4w = (u4*)&lds[L_A1];
        UNROLL for (int p = 0; p < 2; ++p)
            l4w[(q * 8 + wv) * 32 + 16 * p + n] = u4{pkh(z[p].x, z[p + 2].x), pkh(z[p + 4].x, z[p + 6].x), pkh(z[p].y, z[p + 2].y), pkh(z[p + 4].y, z[p + 6].y)};
        l4w[(q * 8 + 7 - wv) * 32 + 16 - n] = u4{pkh(z[7].z, z[5].z), pkh(z[3].z, z[1].z), pkh(z[7].w, z[5].w), pkh(z[3].w, z[1].w)};
        f2 m[4];
        UNROLL for (int i = 0; i < 4; ++i) m[i] = n == 0 ? f2{z[8 - 2 * i].z, z[8 - 2 * i].w} : f2{z[6 - 2 * i].z, z[6 - 2 * i].w};
        l4w[(q * 8 + 7 - wv) * 32 + ((32 - n) & 31)] = u4{pkh(m[0].x, m[1].x), pkh(m[2].x, m[3].x), pkh(m[0].y, m[1].y), pkh(m[2].y, m[3].y)};
        lset(lds, L_C6 + wv, 2u * pv - 1u, lane);
    }
    {
        u4 in[4];
        NOUNROLL for (int spin = 0; spin < LSPIN_CAP; ++spin) {
            MEMBAR();
            const u4 c = *(const u4*)&lds[L_C6];
            MEMBAR();
            UNROLL for (int s = 0; s < 4; ++s) in[s] = *(const u4*)&lds[L_A1 + (wv * 256 + s * 64 + lane) * 4];
            pin(in[0]);
            if (rfl(min4u(c)) >= 2u * pv - 1u) break;
        }
        f16v zr, zi;
        core_fft<false>(in, MR, MI, TW, zr, zi);
        zr *= osc; zi *= osc;
        const rsrc_t ro = mkrs(out, 2048 * 2048);
        UNROLL for (int hf = 0; hf < 2; ++hf) {
            u4 c;
            UNROLL for (int j = 0; j < 4; ++j) c[j] = pkb(zr[8 * hf + 2 * j], zr[8 * hf + 2 * j + 1]);
            store128<OAUX>(c, ro, ov + 256 * 2048 + 32 * hf);
        }
        UNROLL for (int hf = 0; hf < 2; ++hf) {
            u4 a;
            UNROLL for (int j = 0; j < 4; ++j) a[j] = pkb(zi[8 * hf + 2 * j], zi[8 * hf + 2 * j + 1]);
            store128<OAUX>(a, ro, ov + 32 * hf);
        }
    }
    STAMP(15);
    vd = lwait(lds, L_VD, 1u);
    if (pass == 1 || !(vd & 0x20000u)) {
        if (b == 0 && t == 0) __builtin_amdgcn_raw_buffer_store_b32(tags.z, rws, 0, WS_E, SC1);
        break;
    }
    lset(lds, L_C6 + wv, 2u, lane);
    lwait4(lds, L_C6, 2u);
    VMWAIT(0);
    }
    STAMP_OUT();
}

extern "C" void kernel_launch(void* const* d_in, const int* in_sizes, int n_in,
                              void* d_out, int out_size, void* d_ws, size_t ws_size,
                              hipStream_t stream) {
    gfnet_fused<<<256, 512, 0, stream>>>(d_in[0], d_in[6], d_in[1], d_in[2], d_in[4], d_in[5], (char*)d_ws, d_out);
}
```

```cpp
#include <hip/hip_runtime.h>
#include <hip/hip_bf16.h>
#include <stdint.h>

typedef _Float16 h2 __attribute__((ext_vector_type(2)));
typedef _Float16 h8 __attribute__((ext_vector_type(8)));
typedef __bf16 b2 __attribute__((ext_vector_type(2)));
typedef __bf16 b8 __attribute__((ext_vector_type(8)));
typedef float f2 __attribute__((ext_vector_type(2)));
typedef float f4 __attribute__((ext_vector_type(4)));
typedef float f16v __attribute__((ext_vector_type(16)));
typedef short s4 __attribute__((ext_vector_type(4)));
typedef unsigned u32;
typedef u32 u2 __attribute__((ext_vector_type(2)));
typedef u32 u4 __attribute__((ext_vector_type(4)));
typedef unsigned short us2 __attribute__((ext_vector_type(2)));
typedef __amdgpu_buffer_rsrc_t rsrc_t;
#define DI __device__ __forceinline__
#define UNROLL _Pragma("unroll")
#define NOUNROLL _Pragma("unroll 1")
#define LAS __attribute__((address_space(3)))
#define SB() __builtin_amdgcn_sched_barrier(0)
#define MEMBAR() asm volatile("" ::: "memory")
#define VMWAIT(n) asm volatile("s_waitcnt vmcnt(%0)" :: "n"(n) : "memory")

#define MARGIN 2
#define QUICK 145

constexpr u32 FSTR = 16, HSTR = 32;
constexpr u32 WS_Z = 0;
constexpr u32 WS_V = 4u << 20;
constexpr u32 WS_VL = 12u << 20;
constexpr u32 WS_HB = WS_VL + (32u << 10);
constexpr u32 WS_F = WS_HB + (32u << 10);
constexpr u32 WS_E = WS_F + 2048 * FSTR;
constexpr u32 WS_BYTES = WS_E + 4096;
constexpr int SC1 = 16, NT = 2;
constexpr int SPIN_CAP = 8192, LSPIN_CAP = 1 << 17;
constexpr int OOB = 0x7ffffff0;
constexpr u32 TM = 0x00010001u;
constexpr float TRUNC_FIX = 1.00066f;

template <class T> DI void pin(T& v) { asm volatile("" : "+v"(v)); }
template <class T, int N> DI void pin(T (&v)[N]) { UNROLL for (int i = 0; i < N; ++i) pin(v[i]); }
DI u32 opq(u32 v) { asm("" : "+v"(v)); return v; }
DI u32 pkh(float lo, float hi) { h2 r; r.x = (_Float16)lo; r.y = (_Float16)hi; return __builtin_bit_cast(u32, r); }
DI u32 pkb(float lo, float hi) { b2 r; r.x = (__bf16)lo; r.y = (__bf16)hi; return __builtin_bit_cast(u32, r); }
DI float bflo(u32 v) { return __builtin_bit_cast(float, v << 16); }
DI float bfhi(u32 v) { return __builtin_bit_cast(float, v & 0xffff0000u); }
DI float rbf(float v) { return (float)(__bf16)v; }
DI h2 H(u32 v) { return __builtin_bit_cast(h2, v); }
DI u32 U(h2 v) { return __builtin_bit_cast(u32, v); }
DI f16v mfma32(u4 a, u4 b, f16v c) { return __builtin_amdgcn_mfma_f32_32x32x16_f16(__builtin_bit_cast(h8, a), __builtin_bit_cast(h8, b), c, 0, 0, 0); }
DI f4 mfma16(u4 a, u4 b) { return __builtin_amdgcn_mfma_f32_16x16x32_f16(__builtin_bit_cast(h8, a), __builtin_bit_cast(h8, b), f4{}, 0, 0, 0); }
DI f4 mfma16b(u4 a, u4 b, f4 c) { return __builtin_amdgcn_mfma_f32_16x16x32_bf16(__builtin_bit_cast(b8, a), __builtin_bit_cast(b8, b), c, 0, 0, 0); }
DI u32 hmul(u32 a, u32 b) { return U(H(a) * H(b)); }
DI u32 cmul_swap(u32 x, u32 w) { const h2 a = H(opq(x)), c = H(opq(w)); h2 n = c; n.y = -n.y; return U(__builtin_elementwise_fma(a.yy, n, a.xx * c.yx)); }
DI u32 cmul_conj(u32 x, u32 t) { const h2 a = H(opq(x)), c = H(opq(t)); h2 n = c.yy; n.y = -n.y; return U(__builtin_elementwise_fma(a.yx, n, a * c.xx)); }
DI u32 pkmax(u32 a, u32 b) { return __builtin_bit_cast(u32, __builtin_elementwise_max(__builtin_bit_cast(us2, a), __builtin_bit_cast(us2, b))); }
DI float sinr(float r) { return __builtin_amdgcn_sinf(r); }
DI float cosr(float r) { return __builtin_amdgcn_cosf(r); }
DI u32 rot(float ph) { return pkh(cosr(ph), sinr(ph)); }
DI u32 quarter(u32 r, u32 f) { u32 o; asm("v_pk_mul_f16 %0, %1, %2 op_sel:[1,0] op_sel_hi:[0,1] neg_lo:[1,0]" : "=v"(o) : "v"(r), "v"(f)); return o; }
DI u4 quarter(u4 r) { u32 one = 0x3c003c00u; pin(one); return u4{quarter(r.x, one), quarter(r.y, one), quarter(r.z, one), quarter(r.w, one)}; }
DI int perm32(int n) { return (n & 0x13) | ((n & 4) << 1) | ((n & 8) >> 1); }
DI u32 max4u(u4 v) { return max(max(v.x, v.y), max(v.z, v.w)); }
DI u32 min4u(u4 v) { return min(min(v.x, v.y), min(v.z, v.w)); }
DI u32 mark(u32 v, u32 par, u32 tm) { return (par & tm) | (v & ~tm); }
DI u32 rfl(u32 v) { return __builtin_amdgcn_readfirstlane(v); }
DI rsrc_t mkrs(const void* p, u32 bytes) { return __builtin_amdgcn_make_buffer_rsrc((void*)p, (short)0, (int)bytes, 0x00020000); }
template <int AUX> DI void store128(u4 v, rsrc_t r, int off) { __builtin_amdgcn_raw_buffer_store_b128(v, r, off, 0, AUX); }
template <int CTRL> DI u32 dpp(u32 v) { return __builtin_amdgcn_update_dpp(0u, v, CTRL, 0xf, 0xf, true); }
DI u32 wave_maxu(u32 v) {
    v = max(v, dpp<0xB1>(v)); v = max(v, dpp<0x4E>(v)); v = max(v, dpp<0x141>(v)); v = max(v, dpp<0x140>(v));
    return max(max((u32)__builtin_amdgcn_readlane(v, 0), (u32)__builtin_amdgcn_readlane(v, 16)), max((u32)__builtin_amdgcn_readlane(v, 32), (u32)__builtin_amdgcn_readlane(v, 48)));
}
#define STAMP_DECL
#define STAMP(i)
#define STAMP_OUT()

DI void core_setup(int lane, int s, u4& tw, u2& m) {
    float fc = (float)perm32(lane & 31) * (1.0f / 1024.0f), p0 = (float)(4 * (lane >> 5) + 8 * s) * fc, p1 = (float)(4 * (lane >> 5) + 8 * (s >> 1) + 2 * (s & 1)) * fc * 32.0f;
    pin(fc); pin(p0); pin(p1);
    float tc[4], ts[4];
    UNROLL for (int i = 0; i < 4; ++i) { const float pt = fmaf((float)i, fc, p0); tc[i] = cosr(pt); ts[i] = sinr(pt); }
    tw = u4{pkh(tc[0], tc[1]), pkh(tc[2], tc[3]), pkh(ts[0], ts[1]), pkh(ts[2], ts[3])};
    const float p2 = fmaf(32.0f, fc, p1);
    m = u2{pkh(cosr(p1), cosr(p2)), pkh(sinr(p1), sinr(p2))};
}
DI void core_expand(int lane, u4 (&MR)[4], u4 (&MI)[4]) {
    const u32 sg = (lane & 1) ? 0x80008000u : 0u;
    MR[2] = MR[0] ^ sg; MR[3] = MR[1] ^ sg;
    UNROLL for (int s = 0; s < 4; ++s) MI[s] = u4{MR[s].z ^ 0x80008000u, MR[s].w ^ 0x80008000u, MR[s].x, MR[s].y};
}
template <bool OUT_B> DI void core_fft(const u4 (&in)[4], const u4 (&MR)[4], const u4 (&MI)[4], const u4 (&TW)[4], f16v& outr, f16v& outi) {
    f16v yr = {}, yi = {};
    UNROLL for (int s = 0; s < 4; ++s) yr = mfma32(in[s], MR[s], yr);
    SB();
    UNROLL for (int s = 0; s < 4; ++s) yi = mfma32(in[s], MI[s], yi);
    SB();
    h2 t0[8], t1[8];
    u4 d[4];
#define TW_RE(p) do { const h2 pr_ = H(pkh(yr[2 * (p)], yr[2 * (p) + 1])); t0[p] = pr_ * H(TW[(p) >> 1][(p) & 1]); t1[p] = pr_ * H(TW[(p) >> 1][2 + ((p) & 1)]); pin(t0[p]); pin(t1[p]); } while (0)
#define TW_IM(s) do { UNROLL for (int j = 0; j < 2; ++j) { const int p_ = 2 * (s) + j; const h2 pi_ = H(pkh(yi[2 * p_], yi[2 * p_ + 1])); \
        d[s][j] = U(__builtin_elementwise_fma(pi_, H(TW[s][2 + j]), t0[p_])); d[s][2 + j] = U(__builtin_elementwise_fma(pi_, H(TW[s][j]), -t1[p_])); } } while (0)
    TW_RE(0); TW_RE(1); TW_RE(2); TW_RE(3); SB();
    outr = f16v{}; outi = f16v{};
    TW_IM(0); SB();
    UNROLL for (int s = 0; s < 4; ++s) {
        outr = OUT_B ? mfma32(MR[s], d[s], outr) : mfma32(d[s], MR[s], outr);
        if (s < 3) TW_IM(s + 1);
        SB();
        if (s < 2) { outi = OUT_B ? mfma32(MI[s], d[s], outi) : mfma32(d[s], MI[s], outi); TW_RE(4 + 2 * s); TW_RE(5 + 2 * s); SB(); }
    }
    UNROLL for (int s = 2; s < 4; ++s) outi = OUT_B ? mfma32(MI[s], d[s], outi) : mfma32(d[s], MI[s], outi);
    SB();
}

DI u32 tag_of(u32 epoch, u32 idx) { u32 h = (epoch ^ (idx * 0x9E3779B1u)) * 0x85EBCA6Bu; return h ^ (h >> 15); }
DI u32 valid_bits(u32 epoch, int slot) { return (((0x6A3Cu >> slot) ^ epoch) & 1u) ? 0x00000001u : 0x00010000u; }
DI int cls_of(int b) { const int row = (b & 7) >> 1; return row == 0 ? 4 * (b >> 7) : ((b & 1) ? 8 - row : row); }
DI int tile_of(int b) { return (b & 6) == 0 ? 16 * (b & 1) + ((b >> 3) & 15) : b >> 3; }

constexpr int L_C1 = 0, L_C3 = 4, L_C4 = 8, L_C5 = 12, L_C6 = 16;
constexpr int L_EW = 20;
constexpr int L_WW = 24;
constexpr int L_PR = 28;
constexpr int L_LD = 32;
constexpr int L_SX = 36;
constexpr int L_ZL = 37;
constexpr int L_VD = 38;
constexpr int L_P2 = 39;
constexpr int L_GS = 64;
constexpr int L_M = 192;
constexpr int L_A1 = L_M + 1536;
constexpr int L_A2 = L_A1 + 4096;
constexpr int L_B = L_A2 + 4096;
constexpr int L_W = L_B + 8192;
constexpr int L_END = L_W + 64 * 256;

DI void lset(u32* lds, int idx, u32 v, int lane) { MEMBAR(); if (lane == 0) lds[idx] = v; MEMBAR(); }
DI u32 lget(u32* lds, int idx) { MEMBAR(); return rfl(lds[idx]); }
DI u32 lwait(u32* lds, int idx, u32 atleast) {
    u32 v = 0;
    NOUNROLL for (int spin = 0; spin < LSPIN_CAP; ++spin) { v = lget(lds, idx); if (v >= atleast) break; __builtin_amdgcn_s_sleep(1); }
    MEMBAR();
    return v;
}
DI u32 lwait4(u32* lds, int idx, u32 atleast) {
    u4 v;
    NOUNROLL for (int spin = 0; spin < LSPIN_CAP; ++spin) { MEMBAR(); v = *(const u4*)&lds[idx]; if (rfl(min4u(v)) >= atleast) break; __builtin_amdgcn_s_sleep(1); }
    MEMBAR();
    return rfl(max4u(v));
}
struct MagMax {
    h2 hi = {}, lo = {};
    DI void add(u32 a, u32 c) {
        hi = __builtin_elementwise_maximum(__builtin_elementwise_maximum(hi, H(a)), H(c));
        lo = __builtin_elementwise_minimum(__builtin_elementwise_minimum(lo, H(a)), H(c));
    }
    DI u32 exponent() const {
        const u32 mx = pkmax(U(hi) & 0x7fff7fffu, U(lo) & 0x7fff7fffu);
        return wave_maxu(max(mx & 0xffffu, mx >> 16)) >> 7;
    }
};
DI u32 clampe(u32 e) { return min(max(e, 8u), 240u); }

__global__ __launch_bounds__(512, 2) void gfnet_fused(const void* x, const void* gw, const void* war, const void* wai, const void* wbr, const void* wbi,
                                                      char* ws, void* out) {
    __shared__ __attribute__((aligned(16))) u32 lds[L_END];
    STAMP_DECL;
    STAMP(0);
    u32 code;
    {
        unsigned long long pc0; int len;
        asm volatile("s_getpc_b64 %0\n.Lgf_begin:\n\ts_mov_b32 %1, .Lgf_end-.Lgf_begin" : "=s"(pc0), "=s"(len));
        code = __builtin_amdgcn_raw_buffer_load_b32(mkrs((const void*)pc0, (u32)max(len, 0)), (threadIdx.x >> 6) == 4 ? ((blockIdx.x >> 5) * 64 + (threadIdx.x & 63)) * 64 : OOB, 0, 0);
        SB();
    }
    asm volatile("" :: "s"(x), "s"(gw), "s"(war), "s"(wai), "s"(wbr), "s"(wbi), "s"(ws), "s"(out));
    const int wv0 = __builtin_amdgcn_readfirstlane(threadIdx.x >> 6);
    const rsrc_t rws = mkrs(ws, WS_BYTES);

    if (wv0 >= 4) {
        const u32 tstart = (u32)__builtin_amdgcn_s_memrealtime();
        const int wv = wv0, b = blockIdx.x, cls = cls_of(b), tile = tile_of(b);
        const int lane = threadIdx.x & 63, q = lane >> 4, n = lane & 15;
        const rsrc_t rw = mkrs(wv == 4 ? war : wv == 5 ? wai : wv == 6 ? wbr : wbi, 2048 * 1026);
        if (wv == 4) { lds[lane] = 0u; asm volatile("s_waitcnt lgkmcnt(0)" ::: "memory"); }
        MEMBAR();
        __builtin_amdgcn_s_barrier();
        MEMBAR();
        const bool helper = wv == 7 && tile == 31;
        u4 GR, GI, FR, FI, HR, HI, T1, T2;
        u32 ev;
        {
            const int wvo = (lane >> 2) * (128 * 1026) + min(lane & 3, 2) * 16, wso = (cls * 513 + 16 * tile - cls) * 2;
#define WLOAD(r) do { UNROLL for (int st = 0; st < 4; ++st) { const int c_ = 4 * st + ((tile + (r)) & 3); \
                __builtin_amdgcn_raw_ptr_buffer_load_lds(rw, (LAS void*)&lds[L_W + (c_ * 4 + wv - 4) * 256], 16, wvo, wso + c_ * 8208, 0, 0); } SB(); } while (0)
            __builtin_amdgcn_s_sleep(2);
            WLOAD(0);
            const bool split = (b & 6) == 0;
            const int er = split ? 16 * (tile & 15) + ((lane >> 1) & 15) : 8 * tile + ((lane >> 1) & 7);
            const int ec = (split ? (tile & 16) * 16 : 0) - cls + ((lane & 1) ? (split ? 262 : 518) : 0);
            u32 edge = __builtin_amdgcn_raw_buffer_load_b32(rw, ((cls + 8 * er) * 513 + ec) * 2, 0, 0);
            ev = __builtin_amdgcn_raw_buffer_load_b32(rws, 0, WS_E, SC1);
            const u4 smp = __builtin_amdgcn_raw_buffer_load_b128(rw, (lane >> 3) * (2 * 131328 + 128) + (lane & 7) * 16, 0, 0);
            MEMBAR();
            if (helper) {
                UNROLL for (int j = 0; j < 4; ++j) {
                    GR[j] = rot((float)((4 * q + j) * (cls + 8 * n)) * (1.0f / 128.0f) + (cls == 4 ? 0.25f : 0.0f));
                    HR[j] = rot((float)(n * (cls + 8 * (4 * q + j))) * (1.0f / 128.0f));
                    FR[j] = rot((float)(n * (4 * q + j)) * (1.0f / 16.0f));
                    T1[j] = rot((float)((4 * q + j) * (cls + 8 * n)) * (1.0f / 2048.0f));
                    T2[j] = rot((float)(n * (cls + 8 * (4 * q + j))) * (1.0f / 2048.0f));
                }
                GI = quarter(GR); HI = quarter(HR); FI = quarter(FR);
                pin(GR); pin(GI); pin(HR); pin(HI); pin(FR); pin(FI); pin(T1); pin(T2);
            }
            VMWAIT(0);
            STAMP(1);
            if ((int)((u32)__builtin_amdgcn_s_memrealtime() - tstart) < QUICK)
                __builtin_amdgcn_raw_buffer_store_b32(0u, rws, lane * 128, WS_V + b * 32768 + (wv - 4) * 8192, SC1);
            MEMBAR();
            WLOAD(1); WLOAD(2); WLOAD(3);
            VMWAIT(12);
            asm volatile("ds_write_b32 %0, %1" :: "v"((u32)(size_t)(LAS u32*)&lds[L_LD + wv - 4]), "v"(1u) : "memory");
            pin(edge); pin(code);
            MagMax ms; ms.add(smp.x, smp.y); ms.add(smp.z, smp.w);
            const u32 es = clampe(ms.exponent() + MARGIN);
            VMWAIT(0);
            lset(lds, L_PR + wv - 4, es, lane);
            STAMP(2);
        }
        const u32 ep0 = rfl(ev) + 1u;
        if (wv == 5 || wv == 6) {
            lwait4(lds, L_PR, 1u);
            MagMax m;
            UNROLL for (int c = 0; c < 32; ++c) { const u4 v = *(const u4*)&lds[L_W + ((wv - 5) * 32 + c) * 256 + lane * 4]; m.add(v.x, v.y); m.add(v.z, v.w); }
            const u32 e = clampe(m.exponent());
            MEMBAR(); if (lane == 0) *(u2*)&lds[L_WW + 2 * (wv - 5)] = u2{e, e}; MEMBAR();
        } else if (wv == 4) {
            const u32 ex = lwait4(lds, L_EW, 1u), ew = lwait4(lds, L_WW, 1u);
            __builtin_amdgcn_raw_buffer_store_b64(u2{ex | (ew << 8), tag_of(ep0, b + 1)}, rws, lane < 8 ? lane * (int)(256 * FSTR) : OOB, WS_F + b * FSTR, SC1);
            lwait(lds, L_P2, 1u);
            __builtin_amdgcn_s_sleep(24);
            u2 f[4]; u32 tg[4];
            UNROLL for (int j = 0; j < 4; ++j) tg[j] = tag_of(ep0, 4 * lane + j + 1);
            NOUNROLL for (int spin = 0; spin < SPIN_CAP; ++spin) {
                MEMBAR();
                UNROLL for (int j = 0; j < 4; ++j) f[j] = __builtin_amdgcn_raw_buffer_load_b64(rws, lane * (4 * FSTR) + j * FSTR, WS_F + (b & 7) * (256 * FSTR), SC1);
                if (__all(f[0].y == tg[0] && f[1].y == tg[1] && f[2].y == tg[2] && f[3].y == tg[3])) break;
                __builtin_amdgcn_s_sleep(8);
            }
            const u4 e4 = {f[0].x, f[1].x, f[2].x, f[3].x};
            const u32 gx = wave_maxu(max4u(e4 & 0xffu)), gwm = wave_maxu(max4u((e4 >> 8) & 0xffu));
            const bool redo = gx > lget(lds, L_SX) || gwm > lwait4(lds, L_PR, 1u);
            lset(lds, L_VD, gx | (gwm << 8) | (redo ? 0x20000u : 0x10000u), lane);
            STAMP(5);
        } else if (helper) {
            __builtin_amdgcn_s_setprio(2);
            u32 ew = lwait4(lds, L_PR, 1u);
            NOUNROLL for (u32 pass = 0; pass < 2; ++pass) {
                u4 WH[2];
                const float sw = __builtin_bit_cast(float, (249u - ew) << 23);
                UNROLL for (int r = 0; r < 4; ++r) UNROLL for (int f = 0; f < 2; ++f) {
                    const unsigned short* wp = (const unsigned short*)&lds[L_W + (n * 4 + 2 * f) * 256 + (4 * q + r) * 16] + 16 + cls;
                    float re = bflo(wp[0]) * sw, im = bflo(wp[512]) * sw;
                    pin(re);
                    WH[f][r] = pkh(re, im);
                }
                pin(WH);
                STAMP(3);
                lwait(lds, L_ZL, pass + 1u);
                const u32 tg = tag_of(ep0 + pass, 0u);
                u4 hz;
                NOUNROLL for (int spin = 0; spin < SPIN_CAP; ++spin) {
                    MEMBAR();
                    u32 bad = 0;
                    UNROLL for (int j = 0; j < 4; ++j) { const u2 g = __builtin_amdgcn_raw_buffer_load_b64(rws, (64 * q + n + 16 * j) * HSTR, WS_HB + ((b & 7) >> 1) * (256 * HSTR), SC1); hz[j] = g.x & ~TM; bad |= g.y ^ tg; }
                    if (__all(bad == 0u)) break;
                    __builtin_amdgcn_s_sleep(4);
                }
                STAMP(4);
                const u32 f16 = cls == 0 ? 0x00002c00u : cls == 4 ? 0x2c000000u : cls > 4 ? 0xac002c00u : 0x2c002c00u;
                u4 d, o[2];
                UNROLL for (int j = 0; j < 4; ++j) d[j] = hmul(hz[j], f16);
                f4 yr = mfma16(d, GR), yi = mfma16(d, GI);
                UNROLL for (int r = 0; r < 4; ++r) d[r] = cmul_conj(pkh(yr[r], yi[r]), T1[r]);
                const f4 xr = mfma16(FR, d), xi = mfma16(FI, d);
                UNROLL for (int f = 0; f < 2; ++f) {
                    UNROLL for (int r = 0; r < 4; ++r) d[r] = cmul_swap(pkh(xr[r], xi[r]), WH[f][r]);
                    yr = mfma16(d, FR); yi = mfma16(d, FI);
                    UNROLL for (int r = 0; r < 4; ++r) o[f][r] = cmul_conj(pkh(yr[r], yi[r]), T2[r]);
                }
                const f4 p0 = mfma16(HR, o[0]), p1 = mfma16(HI, o[0]), p2 = mfma16(HR, o[1]), p3 = mfma16(HI, o[1]);
                UNROLL for (int r = 0; r < 4; ++r)
                    store128<SC1>(u4{pkh(p0[r], p1[r]) & ~TM, tg, pkh(p2[r], p3[r]) & ~TM, tg}, rws, (64 * q + n) * 16 + WS_VL + r * 256 + cls * 4096);
                STAMP(5);
                const u32 vd = lwait(lds, L_VD, 1u);
                if (!(vd & 0x20000u)) break;
                ew = (vd >> 8) & 0xffu;
            }
        }
        asm volatile(".Lgf_end:");
        STAMP_OUT();
        return;
    }

    u32 vd = 0;
    NOUNROLL for (u32 pass = 0; pass < 2; ++pass) {
    int t = threadIdx.x, wv = wv0, b = blockIdx.x;
    pin(t);
    asm volatile("" : "+s"(wv));
    asm volatile("" : "+s"(b));
    const int lane = t & 63, q = lane >> 4, n = lane & 15;
    const u32 pv = pass + 1u;
    u2 xv[8];
    const rsrc_t rx = mkrs(x, 2048 * 2048);
    const u4 smp = __builtin_amdgcn_raw_buffer_load_b128(rx, (lane >> 3) * (524288 + 2048 + 256) + (lane & 7) * 16, 0, 0);
    UNROLL for (int s1 = 0; s1 < 8; ++s1) xv[s1] = __builtin_amdgcn_raw_buffer_load_b64(rx, t * 8, (256 * s1 + b) * 2048, NT);
    const u32 ev = __builtin_amdgcn_raw_buffer_load_b32(rws, 0, WS_E, SC1);
    const u2 gv = __builtin_amdgcn_raw_buffer_load_b64(mkrs(gw, 2048), t * 8, 0, 0);
    MEMBAR();
    SB();
    if (pass == 0) { __builtin_amdgcn_s_barrier(); MEMBAR(); __builtin_amdgcn_s_setprio(1); }
    u4 MR[4], MI[4], TW[4];
    {
        u4 tw; u2 m;
        core_setup(lane, wv, tw, m);
        *(u4*)&lds[L_M + (2 + wv) * 256 + lane * 4] = tw;
        lds[L_M + (wv >> 1) * 256 + lane * 4 + (wv & 1)] = m.x; lds[L_M + (wv >> 1) * 256 + lane * 4 + 2 + (wv & 1)] = m.y;
    }
    const int zc = perm32(lane & 31) + (lane >> 5) * 256, zcu = zc + ((lane & 31) == 0 ? 543 : 511);
    int zl = (zc >> 4) * 16384 + (zc & 15) * 4, zu = (zcu >> 4) * 16384 + (zcu & 15) * 4;
    int zh = lane == 0 ? 0 : OOB;
    int tro = L_A1 + wv * 1024 + (4 * (lane >> 5) + ((lane & 15) >> 2)) * 16 + ((lane >> 4) & 1) * 8 + (lane & 3) * 2;
    u32 tm = TM, vl0 = valid_bits(0u, zc & 15), vu0 = valid_bits(0u, zcu & 15);
    pin(zl); pin(zu); pin(zh); pin(tro); pin(tm); pin(vl0); pin(vu0);
    STAMP(1);
    SB();
    pin(code);
    u32 ex;
    {
        MagMax ms; ms.add(smp.x, smp.y); ms.add(smp.z, smp.w);
        ex = pass ? (vd & 0xffu) : clampe(ms.exponent() + MARGIN);
    }
    STAMP(2);
    u32 epoch, tag0, flip;
    u32 z[16], z512;
    {
        const float sc = __builtin_bit_cast(float, (250u - ex) << 23);
        const float rs = sc * 0.70710678118654752f;
        u32 o[8][2];
        UNROLL for (int j = 0; j < 2; ++j) {
            f2 v[8];
            UNROLL for (int s1 = 0; s1 < 8; ++s1) v[s1] = f2{bflo(xv[s1][j]), bfhi(xv[s1][j])};
            const f2 a0 = v[0] + v[4], a1 = v[1] + v[5], a2 = v[2] + v[6], a3 = v[3] + v[7];
            const f2 b0 = (v[0] - v[4]) * sc, b1 = v[1] - v[5], b2_ = (v[2] - v[6]) * sc, b3 = v[3] - v[7];
            const f2 e0 = a0 + a2, e1 = (a1 + a3) * sc, dm = b1 - b3, dp = b1 + b3;
            f2 r[8];
            r[0] = e0 * sc + e1; r[1] = e0 * sc - e1;
            r[2] = dm * rs + b0; r[3] = -(dp * rs) - b2_;
            r[4] = (a0 - a2) * sc; r[5] = (a3 - a1) * sc;
            r[6] = b0 - dm * rs; r[7] = b2_ - dp * rs;
            UNROLL for (int p = 0; p < 8; ++p) o[p][j] = pkh(r[p].x, r[p].y);
        }
        UNROLL for (int p = 0; p < 8; ++p) *(u2*)&lds[L_A1 + p * 512 + 2 * t] = u2{o[p][0], o[p][1]};
        lset(lds, L_C1 + wv, pv, lane);
        epoch = rfl(ev) + pv; tag0 = tag_of(epoch, 0u); flip = (epoch & 1u) ? TM : 0u;
        const u32 vl = vl0 ^ flip, vu = vu0 ^ flip;
        u4 in[4];
        NOUNROLL for (int spin = 0; spin < LSPIN_CAP; ++spin) {
            MEMBAR();
            const u4 c = *(const u4*)&lds[L_C1];
            MEMBAR();
            MR[0] = *(const u4*)&lds[L_M + lane * 4]; MR[1] = *(const u4*)&lds[L_M + 256 + lane * 4];
            UNROLL for (int s = 0; s < 4; ++s) {
                const u2 re = __builtin_bit_cast(u2, __builtin_amdgcn_ds_read_tr16_b64_v4i16((LAS s4*)&lds[tro + s * 128]));
                const u2 im = __builtin_bit_cast(u2, __builtin_amdgcn_ds_read_tr16_b64_v4i16((LAS s4*)&lds[tro + s * 128 + 512]));
                in[s] = u4{re.x, re.y, im.x, im.y};
            }
            pin(MR[0]); pin(MR[1]);
            if (rfl(min4u(c)) >= pv) break;
        }
        UNROLL for (int i = 0; i < 4; ++i) TW[i] = *(const u4*)&lds[L_M + (2 + i) * 256 + lane * 4];
        STAMP(3);
        core_expand(lane, MR, MI);
        f16v zr, zi;
        core_fft<true>(in, MR, MI, TW, zr, zi);
        UNROLL for (int r = 0; r < 16; ++r) z[r] = pkh(zr[r], zi[r]);
        z512 = z[8];
        const u32 za = __builtin_amdgcn_readlane(z[8], 32), zb = __builtin_amdgcn_readlane(z[0], 0);
        MEMBAR();
        const u32 zt = lane < 32 ? za : zb;
        UNROLL for (int r = 0; r < 8; ++r) z[r] = mark(z[r], vl, tm);
        UNROLL for (int r = 8; r < 16; ++r) z[r] = mark((lane & 31) == 0 ? (r < 15 ? z[r + 1] : zt) : z[r], vu, tm);
    }
    pin(z);
    STAMP(4);
    const int zs = WS_Z + wv * 64 * 16384 + (((b >> 2) & 3) * 64 + (b & 3) * 16 + ((b >> 4) & 3) * 4 + (b >> 6)) * 64;
    bool zsent = false;
#define TRY_STORE_Z(force) do { if (!zsent) { \
        bool go = true; \
        if (force) lwait4(lds, L_LD, 1u); else { MEMBAR(); go = rfl(min4u(*(const u4*)&lds[L_LD])) != 0u; MEMBAR(); } \
        if (go) { \
            UNROLL for (int r = 0; r < 16; ++r) __builtin_amdgcn_raw_buffer_store_b32(z[r], rws, r < 8 ? zl : zu, zs + (r & 7) * 32768, SC1); \
            __builtin_amdgcn_raw_buffer_store_b64(u2{z512, tag0}, rws, zh, WS_HB + (wv * 256 + b) * HSTR, SC1); \
            zsent = true; } } SB(); } while (0)
    TRY_STORE_Z(false);
    const int row = (b & 7) >> 1, cls = cls_of(b), tile = tile_of(b);
    const bool packed = row == 0;
    u4 FR, FI, GR[4], GI[4], HR[4], HI[4], sel1;
    {
        const float ofs = cls == 4 ? 0.25f : 0.0f;
        float gu = (float)(cls + 8 * n) * (1.0f / 2048.0f), g0 = (float)(64 * q + 4 * wv) * gu + ofs;
        u32 s16 = 0x2c002c00u;
        pin(gu); pin(g0); pin(s16);
        UNROLL for (int i = 0; i < 4; ++i) UNROLL for (int j = 0; j < 4; ++j) { const u32 g = rot(fmaf((float)(16 * j + i), gu, g0)); GR[i][j] = hmul(g, s16); GI[i][j] = quarter(g, s16); }
    }
    pin(GR); pin(GI);
    {
        MagMax m;
        UNROLL for (int s1 = 0; s1 < 8; ++s1) m.add(xv[s1].x, xv[s1].y);
        if (wv == 0) lset(lds, L_SX, ex, lane);
        lset(lds, L_EW + wv, clampe(m.exponent()), lane);
    }
    TRY_STORE_Z(false);
    UNROLL for (int j = 0; j < 4; ++j) FR[j] = rot((float)(n * (4 * q + j)) * (1.0f / 16.0f));
    FI = quarter(FR);
    {
        float hs[4], hu[4];
        UNROLL for (int i = 0; i < 4; ++i) { hs[i] = (float)(16 * n + 4 * wv + i) * (1.0f / 2048.0f); hu[i] = (float)(cls + 32 * q + 8 * i); }
        pin(hs); pin(hu);
        UNROLL for (int i = 0; i < 4; ++i) { UNROLL for (int j = 0; j < 4; ++j) { float a = hs[i] * hu[j]; pin(a); HR[i][j] = rot(a); } HI[i] = quarter(HR[i]); }
        const int d = n + cls - 8 * q;
        UNROLL for (int j = 0; j < 4; ++j) sel1[j] = (d >> 1) == j ? ((d & 1) ? 0xffff0000u : 0xffffu) : 0u;
    }
    pin(FR); pin(FI); pin(HR); pin(HI); pin(sel1);
    TRY_STORE_Z(true);
    STAMP(5);
    VMWAIT(4);
    u4 WA[4], WB[4];
    u32 ew = 0;
    UNROLL for (int i = 0; i < 4; ++i) asm volatile("" : "=v"(WA[i]), "=v"(WB[i]));
#define WIDEN(es) do { \
        u4 wr[4][4]; \
        int wl = L_W / 4 + wv * 1024 + n * 4 + min(q, 2); pin(wl); \
        UNROLL for (int i = 0; i < 4; ++i) UNROLL for (int a = 0; a < 4; ++a) wr[i][a] = ((const u4*)lds)[wl + (i * 4 + a) * 64]; \
        ew = pass ? (vd >> 8) & 0xffu : (es); \
        const u32 sw = ((249u - ew - (packed ? 1u : 0u)) << 7) * 0x10001u;       \
        const u4 sels = sel1 & sw; \
        SB(); \
        f4 wf[4][4]; \
        UNROLL for (int k = 0; k < 10; ++k) { \
            if (k < 8) { wf[k >> 1][2 * (k & 1)] = mfma16b(wr[k >> 1][2 * (k & 1)], sels, f4{}); wf[k >> 1][2 * (k & 1) + 1] = mfma16b(wr[k >> 1][2 * (k & 1) + 1], sels, f4{}); } \
            if (k >= 2) { const int i_ = (k - 2) >> 1, f_ = k & 1; UNROLL for (int r = 0; r < 4; ++r) (f_ ? WB : WA)[i_][r] = pkh(wf[i_][2 * f_][r], wf[i_][2 * f_ + 1][r]); } \
            SB(); } \
        pin(WA); pin(WB); } while (0)
    MEMBAR();
    const u4 pr0 = *(const u4*)&lds[L_PR];
    const bool wearly = rfl(min4u(pr0)) != 0u;
    MEMBAR();
    if (wearly) WIDEN(rfl(max4u(pr0)));
    STAMP(6);
    const u32 vn = valid_bits(epoch, n), vm = valid_bits(epoch, 15 - n);
    u32 x1 = cls > 4 ? vm ^ 0x80000000u : vn, x2 = vm ^ (cls == 4 ? 0x00008000u : cls == 0 ? 0x80000000u : 0u);
    u32 z1[4][4], z2[4][4];
    const int zvm = q * 64 + (63 - tile) * 16384 + (15 - n) * 4;
    const int zv1 = cls <= 4 ? q * 64 + tile * 16384 + n * 4 : zvm;
    int zso = WS_Z + row * 64 * 16384 + wv * 4096;
    asm volatile("" : "+s"(zso));
#define LOAD_Z() do { \
        if (packed) { int a_ = zvm; pin(a_); UNROLL for (int i = 0; i < 4; ++i) UNROLL for (int j = 0; j < 4; ++j) z2[i][j] = __builtin_amdgcn_raw_buffer_load_b32(rws, a_ + (4 * i + j) * 256, zso, SC1); } \
        else { UNROLL for (int i = 0; i < 4; ++i) UNROLL for (int j = 0; j < 4; ++j) z2[i][j] = x2; } \
        int c_ = zv1; pin(c_); UNROLL for (int i = 0; i < 4; ++i) UNROLL for (int j = 0; j < 4; ++j) z1[i][j] = __builtin_amdgcn_raw_buffer_load_b32(rws, c_ + (4 * i + j) * 256, zso, SC1); } while (0)
    pin(x1); pin(x2);
    VMWAIT(0);
    LOAD_Z();
    if (wv == 0) lset(lds, L_ZL, pv, lane);
    SB();
    STAMP(7);
    const int lw = ((wv * 16 + 4 * q) * 16 + n) * 4;
    int lr = (q * 16 + 4 * wv) * 16 + n;
    const u4* l4 = (const u4*)lds;
    pin(lr);
    STAMP(8);
    SB();
    {
        f4 yr[4], yi[4];
        NOUNROLL for (int spin = 0; spin < SPIN_CAP; ++spin) {
            u32 m = 0;
            UNROLL for (int i = 0; i < 4; ++i) {
                u4 d;
                UNROLL for (int j = 0; j < 4; ++j) { z1[i][j] ^= x1; z2[i][j] ^= x2; }
                UNROLL for (int j = 0; j < 4; ++j) d[j] = U(H(z1[i][j]) + H(z2[i][j]));
                UNROLL for (int j = 0; j < 4; ++j) m |= z1[i][j] | z2[i][j];
                yr[i] = mfma16(GR[i], d); yi[i] = mfma16(GI[i], d);
                SB();
            }
            if (__all((m & TM) == 0u)) break;
            MEMBAR();
            LOAD_Z();
        }
        STAMP(9);
        u4 y[4];
        UNROLL for (int i = 0; i < 4; ++i) UNROLL for (int r = 0; r < 4; ++r) y[r][i] = pkh(yr[i][r], yi[i][r]);
        UNROLL for (int r = 0; r < 4; ++r) *(u4*)&lds[L_A2 + lw + r * 64] = y[r];
        lset(lds, L_C3 + wv, pv, lane);
    }
    if (!wearly) { const u32 es = lwait4(lds, L_PR, 1u); WIDEN(es); }
    {
        u4 d[4];
        NOUNROLL for (int spin = 0; spin < LSPIN_CAP; ++spin) {
            MEMBAR();
            const u4 c = *(const u4*)&lds[L_C3];
            MEMBAR();
            UNROLL for (int i = 0; i < 4; ++i) d[i] = l4[L_A2 / 4 + lr + i * 16];
            pin(d[0]);
            if (rfl(min4u(c)) >= pv) break;
        }
        STAMP(10);
        pin(WA); pin(WB);
        f4 xr[4], xi[4];
        u4 xx[4], ya[4], yb[4];
        UNROLL for (int i = 0; i < 4; ++i) {
            xr[i] = mfma16(FR, d[i]); xi[i] = mfma16(FI, d[i]);
            if (i >= 2) { UNROLL for (int r = 0; r < 4; ++r) xx[i - 2][r] = pkh(xr[i - 2][r], xi[i - 2][r]); pin(xx[i - 2]); }
            SB();
        }
        UNROLL for (int r = 0; r < 4; ++r) ya[0][r] = cmul_swap(xx[0][r], WA[0][r]);
        UNROLL for (int i = 2; i < 4; ++i) { UNROLL for (int r = 0; r < 4; ++r) xx[i][r] = pkh(xr[i][r], xi[i][r]); pin(xx[i]); }
        SB();
        f4 p0[4], p1[4], p2[4], p3[4];
        UNROLL for (int i = 0; i < 4; ++i) {
            p0[i] = mfma16(FR, ya[i]); p1[i] = mfma16(FI, ya[i]);
            if (i < 3) { UNROLL for (int r = 0; r < 4; ++r) ya[i + 1][r] = cmul_swap(xx[i + 1][r], WA[i + 1][r]); }
            else { UNROLL for (int r = 0; r < 4; ++r) yb[0][r] = cmul_swap(xx[0][r], WB[0][r]); }
            SB();
        }
        u4 ra[4], rb[4];
        UNROLL for (int i = 0; i < 4; ++i) {
            p2[i] = mfma16(FR, yb[i]); p3[i] = mfma16(FI, yb[i]);
            if (i < 3) { UNROLL for (int r = 0; r < 4; ++r) yb[i + 1][r] = cmul_swap(xx[i + 1][r], WB[i + 1][r]); }
            UNROLL for (int k = 0; k < 4; ++k) ra[i][k] = pkh(p0[k][i], p1[k][i]);
            *(u4*)&lds[L_B + lw + i * 64] = ra[i];
            SB();
        }
        UNROLL for (int i = 0; i < 3; ++i) UNROLL for (int r = 0; r < 4; ++r) rb[r][i] = pkh(p2[i][r], p3[i][r]);
        SB();
        UNROLL for (int r = 0; r < 4; ++r) { rb[r][3] = pkh(p2[3][r], p3[3][r]); *(u4*)&lds[L_B + 4096 + lw + r * 64] = rb[r]; }
        lset(lds, L_C4 + wv, pv, lane);
    }
    STAMP(11);
    {
        u4 da[4], db[4];
        NOUNROLL for (int spin = 0; spin < LSPIN_CAP; ++spin) {
            MEMBAR();
            const u4 c = *(const u4*)&lds[L_C4];
            MEMBAR();
            UNROLL for (int i = 0; i < 4; ++i) { da[i] = l4[L_B / 4 + lr + i * 16]; db[i] = l4[L_B / 4 + 1024 + lr + i * 16]; }
            pin(da[0]);
            if (rfl(min4u(c)) >= pv) break;
        }
        const int vo = 64 * q * 32768 + n * 8, so = WS_V + 4 * wv * 32768 + cls * 4096 + tile * 128;
        f4 p0[4], p1[4], p2[4], p3[4];
        UNROLL for (int i = 0; i < 5; ++i) {
            if (i < 4) { p0[i] = mfma16(HR[i], da[i]); p1[i] = mfma16(HI[i], da[i]); p2[i] = mfma16(HR[i], db[i]); p3[i] = mfma16(HI[i], db[i]); }
            if (i > 0) { UNROLL for (int r = 0; r < 4; ++r)
                __builtin_amdgcn_raw_buffer_store_b64(u2{mark(pkh(p0[i - 1][r], p1[i - 1][r]), vn, tm), mark(pkh(p2[i - 1][r], p3[i - 1][r]), vn, tm)}, rws, vo, so + (16 * r + i - 1) * 32768, SC1); }
            SB();
        }
    }
    if (wv == 0) lset(lds, L_P2, 1u, lane);
    STAMP(12);
    SB();
    {
        pin(xv);
        const f2 g01 = {bflo(gv.x), bfhi(gv.x)}, g23 = {bflo(gv.y), bfhi(gv.y)};
        f4 acc = {};
        UNROLL for (int i = 0; i < 4; ++i) {
            u4 pp;
            UNROLL for (int k = 0; k < 2; ++k) {
                const f2 a = f2{bflo(xv[2 * i + k].x), bfhi(xv[2 * i + k].x)} * g01, c = f2{bflo(xv[2 * i + k].y), bfhi(xv[2 * i + k].y)} * g23;
                pp[2 * k] = pkb(a.x, a.y); pp[2 * k + 1] = pkb(c.x, c.y);
            }
            const u32 s0 = n == 2 * i ? 0x3f803f80u : 0u, s1 = n == 2 * i + 1 ? 0x3f803f80u : 0u;
            acc = mfma16b(pp, u4{s0, s0, s1, s1}, acc);
        }
        if (n < 8) lds[L_GS + n * 16 + (t >> 4)] = __builtin_bit_cast(u32, (acc.x + acc.y) + (acc.z + acc.w));
        lset(lds, L_C5 + wv, pv, lane);
    }
    MR[0] = *(const u4*)&lds[L_M + lane * 4]; MR[1] = *(const u4*)&lds[L_M + 256 + lane * 4];
    UNROLL for (int i = 0; i < 4; ++i) TW[i] = *(const u4*)&lds[L_M + (2 + i) * 256 + lane * 4];
    core_expand(lane, MR, MI);
    pin(MR); pin(MI); pin(TW);
    u4 PM;
    {
        lwait4(lds, L_C5, pv);
        float gg[4];
        UNROLL for (int k = 0; k < 2; ++k) {
            const f4* pp = (const f4*)&lds[L_GS + ((n >> 2) * 2 + k) * 16];
            const f4 ps = (pp[0] + pp[1]) + (pp[2] + pp[3]);
            const float lg = rbf((ps.x + ps.y) + (ps.z + ps.w));
            gg[2 * k] = rbf(1.0f / (1.0f + __expf(-lg)));
            gg[2 * k + 1] = rbf(1.0f - gg[2 * k]);
        }
        UNROLL for (int k = 0; k < 4; ++k) { gg[k] *= 0.03125f; pin(gg[k]); }
        const int r = n & 3, sa = (n >> 2) * 2;
        UNROLL for (int cc = 0; cc < 2; ++cc) {
            const int u1 = 2 * q + cc;
            const float ta = (float)(sa * u1) * 0.125f + ((r & 1) ? 0.25f : (r & 2) ? 0.5f : 0.0f);
            const float tb = (float)((sa + 1) * u1) * 0.125f + ((r & 1) ? ((r & 2) ? 0.0f : 0.5f) : 0.25f);
            const float ca = cosr(ta), sa_ = sinr(ta), cb = cosr(tb), sb_ = sinr(tb);
            float e[4] = {gg[2] * cb, gg[2] * sb_, gg[3] * cb, gg[3] * sb_};
            pin(e);
            e[0] = fmaf(gg[0], ca, e[0]); pin(e[0]); e[1] = fmaf(gg[0], sa_, e[1]); pin(e[1]); e[2] = fmaf(gg[1], ca, e[2]); pin(e[2]); e[3] = fmaf(gg[1], sa_, e[3]);
            PM[2 * cc] = pkh(e[0], e[1]); PM[2 * cc + 1] = pkh(e[2], e[3]);
        }
    }
    int a3d = (q * 8 + wv) * 32 + n, a3m = (q * 8 + 7 - wv) * 32 + 16 - n, a3z = (q * 8 + 7 - wv) * 32 + ((32 - n) & 31), a3r = wv * 256 + lane;
    float osc = __builtin_bit_cast(float, (u32)min(max((int)ex + (int)ew - 130, 1), 254) << 23) * TRUNC_FIX;
    const int ve = 2 * q * 4096 + (128 * wv + n) * 8;
    int vs0 = WS_V + b * 32768, vs1 = vs0 + 4096, vsl = WS_VL + b * 16;
    asm volatile("" : "+s"(vs0), "+s"(vs1), "+s"(vsl));
    int ov = (4 * perm32(lane & 31) + (lane >> 5)) * 16 + (512 * wv + b) * 2048;
    u32 x8 = wv == 3 ? 0u : vn;
    pin(a3d); pin(a3m); pin(a3z); pin(a3r); pin(PM); pin(ov); pin(osc); pin(x8);
    STAMP(13);
    VMWAIT(0);
    {
        f4 z3[9];
        NOUNROLL for (int spin = 0; spin < SPIN_CAP; ++spin) {
            MEMBAR();
            u2 v0[8], v1[8];
            u4 ve8, vo8;
            int a_ = ve; pin(a_);
            if (wv == 3) {
                ve8 = __builtin_amdgcn_raw_buffer_load_b128(rws, q * 8192, vsl, SC1); vo8 = __builtin_amdgcn_raw_buffer_load_b128(rws, q * 8192 + 4096, vsl, SC1);
            } else {
                const u2 e2 = __builtin_amdgcn_raw_buffer_load_b64(rws, a_ + 1024, vs0, SC1), o2 = __builtin_amdgcn_raw_buffer_load_b64(rws, a_ + 1024, vs1, SC1);
                ve8 = u4{e2.x, e2.y, tag0, tag0}; vo8 = u4{o2.x, o2.y, tag0, tag0};
            }
            UNROLL for (int i = 0; i < 8; ++i) {
                v0[i] = __builtin_amdgcn_raw_buffer_load_b64(rws, a_ + 128 * i, vs0, SC1);
                v1[i] = __builtin_amdgcn_raw_buffer_load_b64(rws, a_ + 128 * i, vs1, SC1);
            }
            SB();
            u32 m = (((wv == 3 ? ve8.y : ve8.z) ^ tag0) | (ve8.w ^ tag0) | ((wv == 3 ? vo8.y : vo8.z) ^ tag0) | (vo8.w ^ tag0)) ? TM : 0u;
            UNROLL for (int k = 0; k < 9; ++k) {
                const int i = (k + 8) % 9;
                const u4 d = i == 8 ? u4{ve8.x, wv == 3 ? ve8.z : ve8.y, vo8.x, wv == 3 ? vo8.z : vo8.y} ^ x8 : u4{v0[i & 7].x, v0[i & 7].y, v1[i & 7].x, v1[i & 7].y} ^ vn;
                m |= d.x | d.y; m |= d.z | d.w;
                z3[i] = mfma16(PM, d);
                SB();
            }
            if (__all((m & TM) == 0u)) break;
        }
        STAMP(14);
        if (wv == 0 && n == 0) { z3[0].x = 0.5f * (z3[0].x + z3[0].z); z3[0].y = 0.5f * (z3[0].y + z3[0].w); }
        if (wv == 3) { z3[8].z = 0.5f * (z3[8].x + z3[8].z); z3[8].w = 0.5f * (z3[8].y + z3[8].w); }
        u4* l4w = (u4*)&lds[L_A1];
        UNROLL for (int p = 0; p < 2; ++p)
            l4w[a3d + 16 * p] = u4{pkh(z3[p].x, z3[p + 2].x), pkh(z3[p + 4].x, z3[p + 6].x), pkh(z3[p].y, z3[p + 2].y), pkh(z3[p + 4].y, z3[p + 6].y)};
        l4w[a3m] = u4{pkh(z3[7].z, z3[5].z), pkh(z3[3].z, z3[1].z), pkh(z3[7].w, z3[5].w), pkh(z3[3].w, z3[1].w)};
        f2 m[4];
        UNROLL for (int i = 0; i < 4; ++i) m[i] = n == 0 ? f2{z3[8 - 2 * i].z, z3[8 - 2 * i].w} : f2{z3[6 - 2 * i].z, z3[6 - 2 * i].w};
        l4w[a3z] = u4{pkh(m[0].x, m[1].x), pkh(m[2].x, m[3].x), pkh(m[0].y, m[1].y), pkh(m[2].y, m[3].y)};
        lset(lds, L_C6 + wv, 2u * pv - 1u, lane);
    }
    {
        u4 in[4];
        NOUNROLL for (int spin = 0; spin < LSPIN_CAP; ++spin) {
            MEMBAR();
            const u4 c = *(const u4*)&lds[L_C6];
            MEMBAR();
            UNROLL for (int s = 0; s < 4; ++s) in[s] = ((const u4*)&lds[L_A1])[a3r + s * 64];
            pin(in[0]);
            if (rfl(min4u(c)) >= 2u * pv - 1u) break;
        }
        f16v zr, zi;
        core_fft<false>(in, MR, MI, TW, zr, zi);
        zr *= osc; zi *= osc;
        const rsrc_t ro = mkrs(out, 2048 * 2048);
        UNROLL for (int hf = 0; hf < 2; ++hf) {
            u4 c;
            UNROLL for (int j = 0; j < 4; ++j) c[j] = pkb(zr[8 * hf + 2 * j], zr[8 * hf + 2 * j + 1]);
            store128<NT>(c, ro, ov + 256 * 2048 + 32 * hf);
        }
        UNROLL for (int hf = 0; hf < 2; ++hf) {
            u4 a;
            UNROLL for (int j = 0; j < 4; ++j) a[j] = pkb(zi[8 * hf + 2 * j], zi[8 * hf + 2 * j + 1]);
            store128<NT>(a, ro, ov + 32 * hf);
        }
    }
    STAMP(15);
    vd = lwait(lds, L_VD, 1u);
    if (pass == 1 || !(vd & 0x20000u)) {
        if (b == 0 && t == 0) __builtin_amdgcn_raw_buffer_store_b32(epoch, rws, 0, WS_E, SC1);
        break;
    }
    lset(lds, L_C6 + wv, 2u, lane);
    lwait4(lds, L_C6, 2u);
    VMWAIT(0);
    }
    STAMP_OUT();
}

extern "C" void kernel_launch(void* const* d_in, const int* in_sizes, int n_in,
                              void* d_out, int out_size, void* d_ws, size_t ws_size,
                              hipStream_t stream) {
    gfnet_fused<<<256, 512, 0, stream>>>(d_in[0], d_in[6], d_in[1], d_in[2], d_in[4], d_in[5], (char*)d_ws, d_out);
}
```

```cpp
#include <hip/hip_runtime.h>
#include <hip/hip_bf16.h>
#include <stdint.h>

typedef _Float16 h2 __attribute__((ext_vector_type(2)));
typedef _Float16 h8 __attribute__((ext_vector_type(8)));
typedef __bf16 b2 __attribute__((ext_vector_type(2)));
typedef __bf16 b8 __attribute__((ext_vector_type(8)));
typedef float f2 __attribute__((ext_vector_type(2)));
typedef float f4 __attribute__((ext_vector_type(4)));
typedef float f16v __attribute__((ext_vector_type(16)));
typedef short s4 __attribute__((ext_vector_type(4)));
typedef unsigned u32;
typedef u32 u2 __attribute__((ext_vector_type(2)));
typedef u32 u4 __attribute__((ext_vector_type(4)));
typedef unsigned short us2 __attribute__((ext_vector_type(2)));
typedef __amdgpu_buffer_rsrc_t rsrc_t;
#define DI __device__ __forceinline__
#define UNROLL _Pragma("unroll")
#define NOUNROLL _Pragma("unroll 1")
#define LAS __attribute__((address_space(3)))
#define SB() __builtin_amdgcn_sched_barrier(0)
#define MEMBAR() asm volatile("" ::: "memory")
#define VMWAIT(n) asm volatile("s_waitcnt vmcnt(%0)" :: "n"(n) : "memory")

#define MARGIN 2
#define QUICK 145

constexpr u32 FSTR = 16, HSTR = 32;
constexpr u32 WS_Z = 0;
constexpr u32 WS_V = 4u << 20;
constexpr u32 WS_VL = 12u << 20;
constexpr u32 WS_HB = WS_VL + (32u << 10);
constexpr u32 WS_F = WS_HB + (32u << 10);
constexpr u32 WS_E = WS_F + 2048 * FSTR;
constexpr u32 WS_BYTES = WS_E + 4096;
constexpr int SC1 = 16, NT = 2;
constexpr int SPIN_CAP = 8192, LSPIN_CAP = 1 << 17;
constexpr int OOB = 0x7ffffff0;
constexpr u32 TM = 0x00010001u;
constexpr float TRUNC_FIX = 1.00066f;

template <class T> DI void pin(T& v) { asm volatile("" : "+v"(v)); }
template <class T, int N> DI void pin(T (&v)[N]) { UNROLL for (int i = 0; i < N; ++i) pin(v[i]); }
DI u32 opq(u32 v) { asm("" : "+v"(v)); return v; }
DI u32 pkh(float lo, float hi) { h2 r; r.x = (_Float16)lo; r.y = (_Float16)hi; return __builtin_bit_cast(u32, r); }
DI u32 pkb(float lo, float hi) { b2 r; r.x = (__bf16)lo; r.y = (__bf16)hi; return __builtin_bit_cast(u32, r); }
DI float bflo(u32 v) { return __builtin_bit_cast(float, v << 16); }
DI float bfhi(u32 v) { return __builtin_bit_cast(float, v & 0xffff0000u); }
DI float rbf(float v) { return (float)(__bf16)v; }
DI h2 H(u32 v) { return __builtin_bit_cast(h2, v); }
DI u32 U(h2 v) { return __builtin_bit_cast(u32, v); }
DI f16v mfma32(u4 a, u4 b, f16v c) { return __builtin_amdgcn_mfma_f32_32x32x16_f16(__builtin_bit_cast(h8, a), __builtin_bit_cast(h8, b), c, 0, 0, 0); }
DI f4 mfma16(u4 a, u4 b) { return __builtin_amdgcn_mfma_f32_16x16x32_f16(__builtin_bit_cast(h8, a), __builtin_bit_cast(h8, b), f4{}, 0, 0, 0); }
DI f4 mfma16b(u4 a, u4 b, f4 c) { return __builtin_amdgcn_mfma_f32_16x16x32_bf16(__builtin_bit_cast(b8, a), __builtin_bit_cast(b8, b), c, 0, 0, 0); }
DI u32 hmul(u32 a, u32 b) { return U(H(a) * H(b)); }
DI u32 cmul_swap(u32 x, u32 w) { const h2 a = H(opq(x)), c = H(opq(w)); h2 n = c; n.y = -n.y; return U(__builtin_elementwise_fma(a.yy, n, a.xx * c.yx)); }
DI u32 cmul_conj(u32 x, u32 t) { const h2 a = H(opq(x)), c = H(opq(t)); h2 n = c.yy; n.y = -n.y; return U(__builtin_elementwise_fma(a.yx, n, a * c.xx)); }
DI u32 pkmax(u32 a, u32 b) { return __builtin_bit_cast(u32, __builtin_elementwise_max(__builtin_bit_cast(us2, a), __builtin_bit_cast(us2, b))); }
DI float sinr(float r) { return __builtin_amdgcn_sinf(r); }
DI float cosr(float r) { return __builtin_amdgcn_cosf(r); }
DI u32 rot(float ph) { return pkh(cosr(ph), sinr(ph)); }
DI u32 quarter(u32 r, u32 f) { u32 o; asm("v_pk_mul_f16 %0, %1, %2 op_sel:[1,0] op_sel_hi:[0,1] neg_lo:[1,0]" : "=v"(o) : "v"(r), "v"(f)); return o; }
DI u4 quarter(u4 r) { u32 one = 0x3c003c00u; pin(one); return u4{quarter(r.x, one), quarter(r.y, one), quarter(r.z, one), quarter(r.w, one)}; }
DI int perm32(int n) { return (n & 0x13) | ((n & 4) << 1) | ((n & 8) >> 1); }
DI u32 max4u(u4 v) { return max(max(v.x, v.y), max(v.z, v.w)); }
DI u32 min4u(u4 v) { return min(min(v.x, v.y), min(v.z, v.w)); }
DI u32 mark(u32 v, u32 par, u32 tm) { return (par & tm) | (v & ~tm); }
DI u32 rfl(u32 v) { return __builtin_amdgcn_readfirstlane(v); }
DI rsrc_t mkrs(const void* p, u32 bytes) { return __builtin_amdgcn_make_buffer_rsrc((void*)p, (short)0, (int)bytes, 0x00020000); }
template <int AUX> DI void store128(u4 v, rsrc_t r, int off) { __builtin_amdgcn_raw_buffer_store_b128(v, r, off, 0, AUX); }
template <int CTRL> DI u32 dpp(u32 v) { return __builtin_amdgcn_update_dpp(0u, v, CTRL, 0xf, 0xf, true); }
DI u32 wave_maxu(u32 v) {
    v = max(v, dpp<0xB1>(v)); v = max(v, dpp<0x4E>(v)); v = max(v, dpp<0x141>(v)); v = max(v, dpp<0x140>(v));
    return max(max((u32)__builtin_amdgcn_readlane(v, 0), (u32)__builtin_amdgcn_readlane(v, 16)), max((u32)__builtin_amdgcn_readlane(v, 32), (u32)__builtin_amdgcn_readlane(v, 48)));
}
#define STAMP_DECL
#define STAMP(i)
#define STAMP_OUT()

DI void core_setup(int lane, int s, u4& tw, u2& m) {
    float fc = (float)perm32(lane & 31) * (1.0f / 1024.0f), p0 = (float)(4 * (lane >> 5) + 8 * s) * fc, p1 = (float)(4 * (lane >> 5) + 8 * (s >> 1) + 2 * (s & 1)) * fc * 32.0f;
    pin(fc); pin(p0); pin(p1);
    float tc[4], ts[4];
    UNROLL for (int i = 0; i < 4; ++i) { const float pt = fmaf((float)i, fc, p0); tc[i] = cosr(pt); ts[i] = sinr(pt); }
    tw = u4{pkh(tc[0], tc[1]), pkh(tc[2], tc[3]), pkh(ts[0], ts[1]), pkh(ts[2], ts[3])};
    const float p2 = fmaf(32.0f, fc, p1);
    m = u2{pkh(cosr(p1), cosr(p2)), pkh(sinr(p1), sinr(p2))};
}
DI void core_expand(int lane, u4 (&MR)[4], u4 (&MI)[4]) {
    const u32 sg = (lane & 1) ? 0x80008000u : 0u;
    MR[2] = MR[0] ^ sg; MR[3] = MR[1] ^ sg;
    UNROLL for (int s = 0; s < 4; ++s) MI[s] = u4{MR[s].z ^ 0x80008000u, MR[s].w ^ 0x80008000u, MR[s].x, MR[s].y};
}
template <bool OUT_B> DI void core_fft(const u4 (&in)[4], const u4 (&MR)[4], const u4 (&MI)[4], const u4 (&TW)[4], f16v& outr, f16v& outi) {
    f16v yr = {}, yi = {};
    UNROLL for (int s = 0; s < 4; ++s) yr = mfma32(in[s], MR[s], yr);
    SB();
    yi = mfma32(in[0], MI[0], yi); yi = mfma32(in[1], MI[1], yi);
    SB();
    h2 t0[8], t1[8];
#define TW_RE(p) do { const h2 pr_ = H(pkh(yr[2 * (p)], yr[2 * (p) + 1])); t0[p] = pr_ * H(TW[(p) >> 1][(p) & 1]); t1[p] = pr_ * H(TW[(p) >> 1][2 + ((p) & 1)]); } while (0)
    TW_RE(0); TW_RE(1); SB();
    yi = mfma32(in[2], MI[2], yi);
    TW_RE(2); TW_RE(3); SB();
    yi = mfma32(in[3], MI[3], yi);
    TW_RE(4); TW_RE(5); TW_RE(6); TW_RE(7); SB();
    outr = f16v{}; outi = f16v{};
    u4 d[4];
#define TW_IM(s) do { UNROLL for (int j = 0; j < 2; ++j) { const int p_ = 2 * (s) + j; const h2 pi_ = H(pkh(yi[2 * p_], yi[2 * p_ + 1])); \
        d[s][j] = U(__builtin_elementwise_fma(pi_, H(TW[s][2 + j]), t0[p_])); d[s][2 + j] = U(__builtin_elementwise_fma(pi_, H(TW[s][j]), -t1[p_])); } } while (0)
    TW_IM(0); SB();
    UNROLL for (int s = 0; s < 4; ++s) {
        outr = OUT_B ? mfma32(MR[s], d[s], outr) : mfma32(d[s], MR[s], outr);
        if (s < 3) TW_IM(s + 1);
        SB();
    }
    UNROLL for (int s = 0; s < 4; ++s) outi = OUT_B ? mfma32(MI[s], d[s], outi) : mfma32(d[s], MI[s], outi);
}

DI u32 tag_of(u32 epoch, u32 idx) { u32 h = (epoch ^ (idx * 0x9E3779B1u)) * 0x85EBCA6Bu; return h ^ (h >> 15); }
DI u32 valid_bits(u32 epoch, int slot) { return (((0x6A3Cu >> slot) ^ epoch) & 1u) ? 0x00000001u : 0x00010000u; }
DI int cls_of(int b) { const int row = (b & 7) >> 1; return row == 0 ? 4 * (b >> 7) : ((b & 1) ? 8 - row : row); }
DI int tile_of(int b) { return (b & 6) == 0 ? 16 * (b & 1) + ((b >> 3) & 15) : b >> 3; }

constexpr int L_C1 = 0, L_C3 = 4, L_C4 = 8, L_C5 = 12, L_C6 = 16;
constexpr int L_EW = 20;
constexpr int L_WW = 24;
constexpr int L_PR = 28;
constexpr int L_LD = 32;
constexpr int L_SX = 36;
constexpr int L_ZL = 37;
constexpr int L_VD = 38;
constexpr int L_P2 = 39;
constexpr int L_GS = 64;
constexpr int L_M = 192;
constexpr int L_A1 = L_M + 1536;
constexpr int L_A2 = L_A1 + 4096;
constexpr int L_B = L_A2 + 4096;
constexpr int L_W = L_B + 8192;
constexpr int L_END = L_W + 64 * 256;

DI void lset(u32* lds, int idx, u32 v, int lane) { MEMBAR(); if (lane == 0) lds[idx] = v; MEMBAR(); }
DI u32 lget(u32* lds, int idx) { MEMBAR(); return rfl(lds[idx]); }
DI u32 lwait(u32* lds, int idx, u32 atleast) {
    u32 v = 0;
    NOUNROLL for (int spin = 0; spin < LSPIN_CAP; ++spin) { v = lget(lds, idx); if (v >= atleast) break; __builtin_amdgcn_s_sleep(1); }
    MEMBAR();
    return v;
}
DI u32 lwait4(u32* lds, int idx, u32 atleast) {
    u4 v;
    NOUNROLL for (int spin = 0; spin < LSPIN_CAP; ++spin) { MEMBAR(); v = *(const u4*)&lds[idx]; if (rfl(min4u(v)) >= atleast) break; __builtin_amdgcn_s_sleep(1); }
    MEMBAR();
    return rfl(max4u(v));
}
struct MagMax {
    h2 hi = {}, lo = {};
    DI void add(u32 a, u32 c) {
        hi = __builtin_elementwise_maximum(__builtin_elementwise_maximum(hi, H(a)), H(c));
        lo = __builtin_elementwise_minimum(__builtin_elementwise_minimum(lo, H(a)), H(c));
    }
    DI u32 exponent() const {
        const u32 mx = pkmax(U(hi) & 0x7fff7fffu, U(lo) & 0x7fff7fffu);
        return wave_maxu(max(mx & 0xffffu, mx >> 16)) >> 7;
    }
};
DI u32 clampe(u32 e) { return min(max(e, 8u), 240u); }

__global__ __launch_bounds__(512, 2) void gfnet_fused(const void* x, const void* gw, const void* war, const void* wai, const void* wbr, const void* wbi,
                                                      char* ws, void* out) {
    __shared__ __attribute__((aligned(16))) u32 lds[L_END];
    STAMP_DECL;
    STAMP(0);
    u32 code;
    {
        unsigned long long pc0; int len;
        asm volatile("s_getpc_b64 %0\n.Lgf_begin:\n\ts_mov_b32 %1, .Lgf_end-.Lgf_begin" : "=s"(pc0), "=s"(len));
        code = __builtin_amdgcn_raw_buffer_load_b32(mkrs((const void*)pc0, (u32)max(len, 0)), (threadIdx.x >> 6) == 4 ? ((blockIdx.x >> 5) * 64 + (threadIdx.x & 63)) * 64 : OOB, 0, 0);
        SB();
    }
    asm volatile("" :: "s"(x), "s"(gw), "s"(war), "s"(wai), "s"(wbr), "s"(wbi), "s"(ws), "s"(out));
    const int wv0 = __builtin_amdgcn_readfirstlane(threadIdx.x >> 6);
    const rsrc_t rws = mkrs(ws, WS_BYTES);

    if (wv0 >= 4) {
        const u32 tstart = (u32)__builtin_amdgcn_s_memrealtime();
        const int wv = wv0, b = blockIdx.x, cls = cls_of(b), tile = tile_of(b);
        const int lane = threadIdx.x & 63, q = lane >> 4, n = lane & 15;
        const rsrc_t rw = mkrs(wv == 4 ? war : wv == 5 ? wai : wv == 6 ? wbr : wbi, 2048 * 1026);
        if (wv == 4) { lds[lane] = 0u; asm volatile("s_waitcnt lgkmcnt(0)" ::: "memory"); }
        MEMBAR();
        __builtin_amdgcn_s_barrier();
        MEMBAR();
        const bool helper = wv == 7 && tile == 31;
        u4 GR, GI, FR, FI, HR, HI, T1, T2;
        u32 ev;
        {
            const int wvo = (lane >> 2) * (128 * 1026) + min(lane & 3, 2) * 16, wso = (cls * 513 + 16 * tile - cls) * 2;
#define WLOAD(r) do { UNROLL for (int st = 0; st < 4; ++st) { const int c_ = 4 * st + ((tile + (r)) & 3); \
                __builtin_amdgcn_raw_ptr_buffer_load_lds(rw, (LAS void*)&lds[L_W + (c_ * 4 + wv - 4) * 256], 16, wvo, wso + c_ * 8208, 0, 0); } SB(); } while (0)
            __builtin_amdgcn_s_sleep(2);
            WLOAD(0);
            const bool split = (b & 6) == 0;
            const int er = split ? 16 * (tile & 15) + ((lane >> 1) & 15) : 8 * tile + ((lane >> 1) & 7);
            const int ec = (split ? (tile & 16) * 16 : 0) - cls + ((lane & 1) ? (split ? 262 : 518) : 0);
            u32 edge = __builtin_amdgcn_raw_buffer_load_b32(rw, ((cls + 8 * er) * 513 + ec) * 2, 0, 0);
            ev = __builtin_amdgcn_raw_buffer_load_b32(rws, 0, WS_E, SC1);
            const u4 smp = __builtin_amdgcn_raw_buffer_load_b128(rw, (lane >> 3) * (2 * 131328 + 128) + (lane & 7) * 16, 0, 0);
            MEMBAR();
            if (helper) {
                UNROLL for (int j = 0; j < 4; ++j) {
                    GR[j] = rot((float)((4 * q + j) * (cls + 8 * n)) * (1.0f / 128.0f) + (cls == 4 ? 0.25f : 0.0f));
                    HR[j] = rot((float)(n * (cls + 8 * (4 * q + j))) * (1.0f / 128.0f));
                    FR[j] = rot((float)(n * (4 * q + j)) * (1.0f / 16.0f));
                    T1[j] = rot((float)((4 * q + j) * (cls + 8 * n)) * (1.0f / 2048.0f));
                    T2[j] = rot((float)(n * (cls + 8 * (4 * q + j))) * (1.0f / 2048.0f));
                }
                GI = quarter(GR); HI = quarter(HR); FI = quarter(FR);
                pin(GR); pin(GI); pin(HR); pin(HI); pin(FR); pin(FI); pin(T1); pin(T2);
            }
            VMWAIT(0);
            STAMP(1);
            if ((int)((u32)__builtin_amdgcn_s_memrealtime() - tstart) < QUICK)
                __builtin_amdgcn_raw_buffer_store_b32(0u, rws, lane * 128, WS_V + b * 32768 + (wv - 4) * 8192, SC1);
            MEMBAR();
            WLOAD(1); WLOAD(2); WLOAD(3);
            VMWAIT(12);
            asm volatile("ds_write_b32 %0, %1" :: "v"((u32)(size_t)(LAS u32*)&lds[L_LD + wv - 4]), "v"(1u) : "memory");
            pin(edge); pin(code);
            MagMax ms; ms.add(smp.x, smp.y); ms.add(smp.z, smp.w);
            const u32 es = clampe(ms.exponent() + MARGIN);
            VMWAIT(0);
            lset(lds, L_PR + wv - 4, es, lane);
            STAMP(2);
        }
        const u32 ep0 = rfl(ev) + 1u;
        if (wv == 5 || wv == 6) {
            lwait4(lds, L_PR, 1u);
            MagMax m;
            UNROLL for (int c = 0; c < 32; ++c) { const u4 v = *(const u4*)&lds[L_W + ((wv - 5) * 32 + c) * 256 + lane * 4]; m.add(v.x, v.y); m.add(v.z, v.w); }
            const u32 e = clampe(m.exponent());
            MEMBAR(); if (lane == 0) *(u2*)&lds[L_WW + 2 * (wv - 5)] = u2{e, e}; MEMBAR();
        } else if (wv == 4) {
            const u32 ex = lwait4(lds, L_EW, 1u), ew = lwait4(lds, L_WW, 1u);
            __builtin_amdgcn_raw_buffer_store_b64(u2{ex | (ew << 8), tag_of(ep0, b + 1)}, rws, lane < 8 ? lane * (int)(256 * FSTR) : OOB, WS_F + b * FSTR, SC1);
            lwait(lds, L_P2, 1u);
            __builtin_amdgcn_s_sleep(24);
            u2 f[4]; u32 tg[4];
            UNROLL for (int j = 0; j < 4; ++j) tg[j] = tag_of(ep0, 4 * lane + j + 1);
            NOUNROLL for (int spin = 0; spin < SPIN_CAP; ++spin) {
                MEMBAR();
                UNROLL for (int j = 0; j < 4; ++j) f[j] = __builtin_amdgcn_raw_buffer_load_b64(rws, lane * (4 * FSTR) + j * FSTR, WS_F + (b & 7) * (256 * FSTR), SC1);
                if (__all(f[0].y == tg[0] && f[1].y == tg[1] && f[2].y == tg[2] && f[3].y == tg[3])) break;
                __builtin_amdgcn_s_sleep(8);
            }
            const u4 e4 = {f[0].x, f[1].x, f[2].x, f[3].x};
            const u32 gx = wave_maxu(max4u(e4 & 0xffu)), gwm = wave_maxu(max4u((e4 >> 8) & 0xffu));
            const bool redo = gx > lget(lds, L_SX) || gwm > lwait4(lds, L_PR, 1u);
            lset(lds, L_VD, gx | (gwm << 8) | (redo ? 0x20000u : 0x10000u), lane);
            STAMP(5);
        } else if (helper) {
            __builtin_amdgcn_s_setprio(2);
            u32 ew = lwait4(lds, L_PR, 1u);
            NOUNROLL for (u32 pass = 0; pass < 2; ++pass) {
                u4 WH[2];
                const float sw = __builtin_bit_cast(float, (249u - ew) << 23);
                UNROLL for (int r = 0; r < 4; ++r) UNROLL for (int f = 0; f < 2; ++f) {
                    const unsigned short* wp = (const unsigned short*)&lds[L_W + (n * 4 + 2 * f) * 256 + (4 * q + r) * 16] + 16 + cls;
                    float re = bflo(wp[0]) * sw, im = bflo(wp[512]) * sw;
                    pin(re);
                    WH[f][r] = pkh(re, im);
                }
                pin(WH);
                STAMP(3);
                lwait(lds, L_ZL, pass + 1u);
                const u32 tg = tag_of(ep0 + pass, 0u);
                u4 hz;
                NOUNROLL for (int spin = 0; spin < SPIN_CAP; ++spin) {
                    MEMBAR();
                    u32 bad = 0;
                    UNROLL for (int j = 0; j < 4; ++j) { const u2 g = __builtin_amdgcn_raw_buffer_load_b64(rws, (64 * q + n + 16 * j) * HSTR, WS_HB + ((b & 7) >> 1) * (256 * HSTR), SC1); hz[j] = g.x & ~TM; bad |= g.y ^ tg; }
                    if (__all(bad == 0u)) break;
                    __builtin_amdgcn_s_sleep(4);
                }
                STAMP(4);
                const u32 f16 = cls == 0 ? 0x00002c00u : cls == 4 ? 0x2c000000u : cls > 4 ? 0xac002c00u : 0x2c002c00u;
                u4 d, o[2];
                UNROLL for (int j = 0; j < 4; ++j) d[j] = hmul(hz[j], f16);
                f4 yr = mfma16(d, GR), yi = mfma16(d, GI);
                UNROLL for (int r = 0; r < 4; ++r) d[r] = cmul_conj(pkh(yr[r], yi[r]), T1[r]);
                const f4 xr = mfma16(FR, d), xi = mfma16(FI, d);
                UNROLL for (int f = 0; f < 2; ++f) {
                    UNROLL for (int r = 0; r < 4; ++r) d[r] = cmul_swap(pkh(xr[r], xi[r]), WH[f][r]);
                    yr = mfma16(d, FR); yi = mfma16(d, FI);
                    UNROLL for (int r = 0; r < 4; ++r) o[f][r] = cmul_conj(pkh(yr[r], yi[r]), T2[r]);
                }
                const f4 p0 = mfma16(HR, o[0]), p1 = mfma16(HI, o[0]), p2 = mfma16(HR, o[1]), p3 = mfma16(HI, o[1]);
                UNROLL for (int r = 0; r < 4; ++r)
                    store128<SC1>(u4{pkh(p0[r], p1[r]) & ~TM, tg, pkh(p2[r], p3[r]) & ~TM, tg}, rws, (64 * q + n) * 16 + WS_VL + r * 256 + cls * 4096);
                STAMP(5);
                const u32 vd = lwait(lds, L_VD, 1u);
                if (!(vd & 0x20000u)) break;
                ew = (vd >> 8) & 0xffu;
            }
        }
        asm volatile(".Lgf_end:");
        STAMP_OUT();
        return;
    }

    u32 vd = 0;
    NOUNROLL for (u32 pass = 0; pass < 2; ++pass) {
    int t = threadIdx.x, wv = wv0, b = blockIdx.x;
    pin(t);
    asm volatile("" : "+s"(wv));
    asm volatile("" : "+s"(b));
    const int lane = t & 63, q = lane >> 4, n = lane & 15;
    const u32 pv = pass + 1u;
    u2 xv[8];
    const rsrc_t rx = mkrs(x, 2048 * 2048);
    const u4 smp = __builtin_amdgcn_raw_buffer_load_b128(rx, (lane >> 3) * (524288 + 2048 + 256) + (lane & 7) * 16, 0, 0);
    UNROLL for (int s1 = 0; s1 < 8; ++s1) xv[s1] = __builtin_amdgcn_raw_buffer_load_b64(rx, t * 8, (256 * s1 + b) * 2048, NT);
    const u32 ev = __builtin_amdgcn_raw_buffer_load_b32(rws, 0, WS_E, SC1);
    const u2 gv = __builtin_amdgcn_raw_buffer_load_b64(mkrs(gw, 2048), t * 8, 0, 0);
    MEMBAR();
    SB();
    if (pass == 0) { __builtin_amdgcn_s_barrier(); MEMBAR(); __builtin_amdgcn_s_setprio(1); }
    u4 MR[4], MI[4], TW[4];
    {
        u4 tw; u2 m;
        core_setup(lane, wv, tw, m);
        *(u4*)&lds[L_M + (2 + wv) * 256 + lane * 4] = tw;
        lds[L_M + (wv >> 1) * 256 + lane * 4 + (wv & 1)] = m.x; lds[L_M + (wv >> 1) * 256 + lane * 4 + 2 + (wv & 1)] = m.y;
    }
    const int zc = perm32(lane & 31) + (lane >> 5) * 256, zcu = zc + ((lane & 31) == 0 ? 543 : 511);
    int zl = (zc >> 4) * 16384 + (zc & 15) * 4, zu = (zcu >> 4) * 16384 + (zcu & 15) * 4;
    int zh = lane == 0 ? 0 : OOB;
    int tro = L_A1 + wv * 1024 + (4 * (lane >> 5) + ((lane & 15) >> 2)) * 16 + ((lane >> 4) & 1) * 8 + (lane & 3) * 2;
    u32 tm = TM, vl0 = valid_bits(0u, zc & 15), vu0 = valid_bits(0u, zcu & 15);
    pin(zl); pin(zu); pin(zh); pin(tro); pin(tm); pin(vl0); pin(vu0);
    STAMP(1);
    SB();
    pin(code);
    u32 ex;
    {
        MagMax ms; ms.add(smp.x, smp.y); ms.add(smp.z, smp.w);
        ex = pass ? (vd & 0xffu) : clampe(ms.exponent() + MARGIN);
    }
    STAMP(2);
    u32 epoch, tag0, flip;
    u32 z[16], z512;
    {
        const float sc = __builtin_bit_cast(float, (250u - ex) << 23);
        const float rs = sc * 0.70710678118654752f;
        u32 o[8][2];
        UNROLL for (int j = 0; j < 2; ++j) {
            f2 v[8];
            UNROLL for (int s1 = 0; s1 < 8; ++s1) v[s1] = f2{bflo(xv[s1][j]), bfhi(xv[s1][j])};
            const f2 a0 = v[0] + v[4], a1 = v[1] + v[5], a2 = v[2] + v[6], a3 = v[3] + v[7];
            const f2 b0 = (v[0] - v[4]) * sc, b1 = v[1] - v[5], b2_ = (v[2] - v[6]) * sc, b3 = v[3] - v[7];
            const f2 e0 = a0 + a2, e1 = (a1 + a3) * sc, dm = b1 - b3, dp = b1 + b3;
            f2 r[8];
            r[0] = e0 * sc + e1; r[1] = e0 * sc - e1;
            r[2] = dm * rs + b0; r[3] = -(dp * rs) - b2_;
            r[4] = (a0 - a2) * sc; r[5] = (a3 - a1) * sc;
            r[6] = b0 - dm * rs; r[7] = b2_ - dp * rs;
            UNROLL for (int p = 0; p < 8; ++p) o[p][j] = pkh(r[p].x, r[p].y);
        }
        UNROLL for (int p = 0; p < 8; ++p) *(u2*)&lds[L_A1 + p * 512 + 2 * t] = u2{o[p][0], o[p][1]};
        lset(lds, L_C1 + wv, pv, lane);
        epoch = rfl(ev) + pv; tag0 = tag_of(epoch, 0u); flip = (epoch & 1u) ? TM : 0u;
        const u32 vl = vl0 ^ flip, vu = vu0 ^ flip;
        u4 in[4];
        NOUNROLL for (int spin = 0; spin < LSPIN_CAP; ++spin) {
            MEMBAR();
            const u4 c = *(const u4*)&lds[L_C1];
            MEMBAR();
            MR[0] = *(const u4*)&lds[L_M + lane * 4]; MR[1] = *(const u4*)&lds[L_M + 256 + lane * 4];
            UNROLL for (int s = 0; s < 4; ++s) {
                const u2 re = __builtin_bit_cast(u2, __builtin_amdgcn_ds_read_tr16_b64_v4i16((LAS s4*)&lds[tro + s * 128]));
                const u2 im = __builtin_bit_cast(u2, __builtin_amdgcn_ds_read_tr16_b64_v4i16((LAS s4*)&lds[tro + s * 128 + 512]));
                in[s] = u4{re.x, re.y, im.x, im.y};
            }
            pin(MR[0]); pin(MR[1]);
            if (rfl(min4u(c)) >= pv) break;
        }
        UNROLL for (int i = 0; i < 4; ++i) TW[i] = *(const u4*)&lds[L_M + (2 + i) * 256 + lane * 4];
        STAMP(3);
        core_expand(lane, MR, MI);
        f16v zr, zi;
        core_fft<true>(in, MR, MI, TW, zr, zi);
        UNROLL for (int r = 0; r < 16; ++r) z[r] = pkh(zr[r], zi[r]);
        z512 = z[8];
        const u32 za = __builtin_amdgcn_readlane(z[8], 32), zb = __builtin_amdgcn_readlane(z[0], 0);
        MEMBAR();
        const u32 zt = lane < 32 ? za : zb;
        UNROLL for (int r = 0; r < 8; ++r) z[r] = mark(z[r], vl, tm);
        UNROLL for (int r = 8; r < 16; ++r) z[r] = mark((lane & 31) == 0 ? (r < 15 ? z[r + 1] : zt) : z[r], vu, tm);
    }
    pin(z);
    STAMP(4);
    const int zs = WS_Z + wv * 64 * 16384 + (((b >> 2) & 3) * 64 + (b & 3) * 16 + ((b >> 4) & 3) * 4 + (b >> 6)) * 64;
    bool zsent = false;
#define TRY_STORE_Z(force) do { if (!zsent) { \
        bool go = true; \
        if (force) lwait4(lds, L_LD, 1u); else { MEMBAR(); go = rfl(min4u(*(const u4*)&lds[L_LD])) != 0u; MEMBAR(); } \
        if (go) { \
            UNROLL for (int r = 0; r < 16; ++r) __builtin_amdgcn_raw_buffer_store_b32(z[r], rws, r < 8 ? zl : zu, zs + (r & 7) * 32768, SC1); \
            __builtin_amdgcn_raw_buffer_store_b64(u2{z512, tag0}, rws, zh, WS_HB + (wv * 256 + b) * HSTR, SC1); \
            zsent = true; } } SB(); } while (0)
    TRY_STORE_Z(false);
    const int row = (b & 7) >> 1, cls = cls_of(b), tile = tile_of(b);
    const bool packed = row == 0;
    u4 FR, FI, GR[4], GI[4], HR[4], HI[4], sel1;
    {
        const float ofs = cls == 4 ? 0.25f : 0.0f;
        float gu = (float)(cls + 8 * n) * (1.0f / 2048.0f), g0 = (float)(64 * q + 4 * wv) * gu + ofs;
        u32 s16 = 0x2c002c00u;
        pin(gu); pin(g0); pin(s16);
        UNROLL for (int i = 0; i < 4; ++i) UNROLL for (int j = 0; j < 4; ++j) { const u32 g = rot(fmaf((float)(16 * j + i), gu, g0)); GR[i][j] = hmul(g, s16); GI[i][j] = quarter(g, s16); }
    }
    pin(GR); pin(GI);
    {
        MagMax m;
        UNROLL for (int s1 = 0; s1 < 8; ++s1) m.add(xv[s1].x, xv[s1].y);
        if (wv == 0) lset(lds, L_SX, ex, lane);
        lset(lds, L_EW + wv, clampe(m.exponent()), lane);
    }
    TRY_STORE_Z(false);
    UNROLL for (int j = 0; j < 4; ++j) FR[j] = rot((float)(n * (4 * q + j)) * (1.0f / 16.0f));
    FI = quarter(FR);
    {
        float hs[4], hu[4];
        UNROLL for (int i = 0; i < 4; ++i) { hs[i] = (float)(16 * n + 4 * wv + i) * (1.0f / 2048.0f); hu[i] = (float)(cls + 32 * q + 8 * i); }
        pin(hs); pin(hu);
        UNROLL for (int i = 0; i < 4; ++i) { UNROLL for (int j = 0; j < 4; ++j) { float a = hs[i] * hu[j]; pin(a); HR[i][j] = rot(a); } HI[i] = quarter(HR[i]); }
        const int d = n + cls - 8 * q;
        UNROLL for (int j = 0; j < 4; ++j) sel1[j] = (d >> 1) == j ? ((d & 1) ? 0xffff0000u : 0xffffu) : 0u;
    }
    pin(FR); pin(FI); pin(HR); pin(HI); pin(sel1);
    TRY_STORE_Z(true);
    STAMP(5);
    VMWAIT(4);
    u4 WA[4], WB[4];
    u32 ew = 0;
    UNROLL for (int i = 0; i < 4; ++i) asm volatile("" : "=v"(WA[i]), "=v"(WB[i]));
#define WIDEN(es) do { \
        u4 wr[4][4]; \
        int wl = L_W / 4 + wv * 1024 + n * 4 + min(q, 2); pin(wl); \
        UNROLL for (int i = 0; i < 4; ++i) UNROLL for (int a = 0; a < 4; ++a) wr[i][a] = ((const u4*)lds)[wl + (i * 4 + a) * 64]; \
        ew = pass ? (vd >> 8) & 0xffu : (es); \
        const u32 sw = ((249u - ew - (packed ? 1u : 0u)) << 7) * 0x10001u;       \
        const u4 sels = sel1 & sw; \
        SB(); \
        f4 wf[4][4]; \
        UNROLL for (int k = 0; k < 10; ++k) { \
            if (k < 8) { wf[k >> 1][2 * (k & 1)] = mfma16b(wr[k >> 1][2 * (k & 1)], sels, f4{}); wf[k >> 1][2 * (k & 1) + 1] = mfma16b(wr[k >> 1][2 * (k & 1) + 1], sels, f4{}); } \
            if (k >= 2) { const int i_ = (k - 2) >> 1, f_ = k & 1; UNROLL for (int r = 0; r < 4; ++r) (f_ ? WB : WA)[i_][r] = pkh(wf[i_][2 * f_][r], wf[i_][2 * f_ + 1][r]); } \
            SB(); } \
        pin(WA); pin(WB); } while (0)
    MEMBAR();
    const u4 pr0 = *(const u4*)&lds[L_PR];
    const bool wearly = rfl(min4u(pr0)) != 0u;
    MEMBAR();
    if (wearly) WIDEN(rfl(max4u(pr0)));
    STAMP(6);
    const u32 vn = valid_bits(epoch, n), vm = valid_bits(epoch, 15 - n);
    u32 x1 = cls > 4 ? vm ^ 0x80000000u : vn, x2 = vm ^ (cls == 4 ? 0x00008000u : cls == 0 ? 0x80000000u : 0u);
    u32 z1[4][4], z2[4][4];
    const int zvm = q * 64 + (63 - tile) * 16384 + (15 - n) * 4;
    const int zv1 = cls <= 4 ? q * 64 + tile * 16384 + n * 4 : zvm;
    int zso = WS_Z + row * 64 * 16384 + wv * 4096;
    asm volatile("" : "+s"(zso));
#define LOAD_Z() do { \
        if (packed) { int a_ = zvm; pin(a_); UNROLL for (int i = 0; i < 4; ++i) UNROLL for (int j = 0; j < 4; ++j) z2[i][j] = __builtin_amdgcn_raw_buffer_load_b32(rws, a_ + (4 * i + j) * 256, zso, SC1); } \
        else { UNROLL for (int i = 0; i < 4; ++i) UNROLL for (int j = 0; j < 4; ++j) z2[i][j] = x2; } \
        int c_ = zv1; pin(c_); UNROLL for (int i = 0; i < 4; ++i) UNROLL for (int j = 0; j < 4; ++j) z1[i][j] = __builtin_amdgcn_raw_buffer_load_b32(rws, c_ + (4 * i + j) * 256, zso, SC1); } while (0)
    pin(x1); pin(x2);
    VMWAIT(0);
    LOAD_Z();
    if (wv == 0) lset(lds, L_ZL, pv, lane);
    SB();
    STAMP(7);
    const int lw = ((wv * 16 + 4 * q) * 16 + n) * 4;
    int lr = (q * 16 + 4 * wv) * 16 + n;
    const u4* l4 = (const u4*)lds;
    pin(lr);
    STAMP(8);
    SB();
    {
        f4 yr[4], yi[4];
        NOUNROLL for (int spin = 0; spin < SPIN_CAP; ++spin) {
            u32 m = 0;
            UNROLL for (int i = 0; i < 4; ++i) {
                u4 d;
                UNROLL for (int j = 0; j < 4; ++j) { z1[i][j] ^= x1; z2[i][j] ^= x2; }
                UNROLL for (int j = 0; j < 4; ++j) d[j] = U(H(z1[i][j]) + H(z2[i][j]));
                UNROLL for (int j = 0; j < 4; ++j) m |= z1[i][j] | z2[i][j];
                yr[i] = mfma16(GR[i], d); yi[i] = mfma16(GI[i], d);
                SB();
            }
            if (__all((m & TM) == 0u)) break;
            MEMBAR();
            LOAD_Z();
        }
        STAMP(9);
        u4 y[4];
        UNROLL for (int i = 0; i < 4; ++i) UNROLL for (int r = 0; r < 4; ++r) y[r][i] = pkh(yr[i][r], yi[i][r]);
        UNROLL for (int r = 0; r < 4; ++r) *(u4*)&lds[L_A2 + lw + r * 64] = y[r];
        lset(lds, L_C3 + wv, pv, lane);
    }
    if (!wearly) { const u32 es = lwait4(lds, L_PR, 1u); WIDEN(es); }
    {
        u4 d[4];
        NOUNROLL for (int spin = 0; spin < LSPIN_CAP; ++spin) {
            MEMBAR();
            const u4 c = *(const u4*)&lds[L_C3];
            MEMBAR();
            UNROLL for (int i = 0; i < 4; ++i) d[i] = l4[L_A2 / 4 + lr + i * 16];
            pin(d[0]);
            if (rfl(min4u(c)) >= pv) break;
        }
        STAMP(10);
        pin(WA); pin(WB);
        f4 xr[4], xi[4];
        u4 xx[4], ya[4], yb[4];
        UNROLL for (int i = 0; i < 4; ++i) {
            xr[i] = mfma16(FR, d[i]); xi[i] = mfma16(FI, d[i]);
            if (i >= 2) { UNROLL for (int r = 0; r < 4; ++r) xx[i - 2][r] = pkh(xr[i - 2][r], xi[i - 2][r]); pin(xx[i - 2]); }
            SB();
        }
        UNROLL for (int r = 0; r < 4; ++r) ya[0][r] = cmul_swap(xx[0][r], WA[0][r]);
        UNROLL for (int i = 2; i < 4; ++i) { UNROLL for (int r = 0; r < 4; ++r) xx[i][r] = pkh(xr[i][r], xi[i][r]); pin(xx[i]); }
        SB();
        f4 p0[4], p1[4], p2[4], p3[4];
        UNROLL for (int i = 0; i < 4; ++i) {
            p0[i] = mfma16(FR, ya[i]); p1[i] = mfma16(FI, ya[i]);
            if (i < 3) { UNROLL for (int r = 0; r < 4; ++r) ya[i + 1][r] = cmul_swap(xx[i + 1][r], WA[i + 1][r]); }
            else { UNROLL for (int r = 0; r < 4; ++r) yb[0][r] = cmul_swap(xx[0][r], WB[0][r]); }
            SB();
        }
        u4 ra[4], rb[4];
        UNROLL for (int i = 0; i < 4; ++i) {
            p2[i] = mfma16(FR, yb[i]); p3[i] = mfma16(FI, yb[i]);
            if (i < 3) { UNROLL for (int r = 0; r < 4; ++r) yb[i + 1][r] = cmul_swap(xx[i + 1][r], WB[i + 1][r]); }
            UNROLL for (int k = 0; k < 4; ++k) ra[i][k] = pkh(p0[k][i], p1[k][i]);
            *(u4*)&lds[L_B + lw + i * 64] = ra[i];
            SB();
        }
        UNROLL for (int i = 0; i < 3; ++i) UNROLL for (int r = 0; r < 4; ++r) rb[r][i] = pkh(p2[i][r], p3[i][r]);
        SB();
        UNROLL for (int r = 0; r < 4; ++r) { rb[r][3] = pkh(p2[3][r], p3[3][r]); *(u4*)&lds[L_B + 4096 + lw + r * 64] = rb[r]; }
        lset(lds, L_C4 + wv, pv, lane);
    }
    STAMP(11);
    {
        u4 da[4], db[4];
        NOUNROLL for (int spin = 0; spin < LSPIN_CAP; ++spin) {
            MEMBAR();
            const u4 c = *(const u4*)&lds[L_C4];
            MEMBAR();
            UNROLL for (int i = 0; i < 4; ++i) { da[i] = l4[L_B / 4 + lr + i * 16]; db[i] = l4[L_B / 4 + 1024 + lr + i * 16]; }
            pin(da[0]);
            if (rfl(min4u(c)) >= pv) break;
        }
        const int vo = 64 * q * 32768 + n * 8, so = WS_V + 4 * wv * 32768 + cls * 4096 + tile * 128;
        f4 p0[4], p1[4], p2[4], p3[4];
        UNROLL for (int i = 0; i < 5; ++i) {
            if (i < 4) { p0[i] = mfma16(HR[i], da[i]); p1[i] = mfma16(HI[i], da[i]); p2[i] = mfma16(HR[i], db[i]); p3[i] = mfma16(HI[i], db[i]); }
            if (i > 0) { UNROLL for (int r = 0; r < 4; ++r)
                __builtin_amdgcn_raw_buffer_store_b64(u2{mark(pkh(p0[i - 1][r], p1[i - 1][r]), vn, tm), mark(pkh(p2[i - 1][r], p3[i - 1][r]), vn, tm)}, rws, vo, so + (16 * r + i - 1) * 32768, SC1); }
            SB();
        }
    }
    if (wv == 0) lset(lds, L_P2, 1u, lane);
    STAMP(12);
    SB();
    {
        pin(xv);
        const f2 g01 = {bflo(gv.x), bfhi(gv.x)}, g23 = {bflo(gv.y), bfhi(gv.y)};
        f4 acc = {};
        UNROLL for (int i = 0; i < 4; ++i) {
            u4 pp;
            UNROLL for (int k = 0; k < 2; ++k) {
                const f2 a = f2{bflo(xv[2 * i + k].x), bfhi(xv[2 * i + k].x)} * g01, c = f2{bflo(xv[2 * i + k].y), bfhi(xv[2 * i + k].y)} * g23;
                pp[2 * k] = pkb(a.x, a.y); pp[2 * k + 1] = pkb(c.x, c.y);
            }
            const u32 s0 = n == 2 * i ? 0x3f803f80u : 0u, s1 = n == 2 * i + 1 ? 0x3f803f80u : 0u;
            acc = mfma16b(pp, u4{s0, s0, s1, s1}, acc);
        }
        if (n < 8) lds[L_GS + n * 16 + (t >> 4)] = __builtin_bit_cast(u32, (acc.x + acc.y) + (acc.z + acc.w));
        lset(lds, L_C5 + wv, pv, lane);
    }
    MR[0] = *(const u4*)&lds[L_M + lane * 4]; MR[1] = *(const u4*)&lds[L_M + 256 + lane * 4];
    UNROLL for (int i = 0; i < 4; ++i) TW[i] = *(const u4*)&lds[L_M + (2 + i) * 256 + lane * 4];
    core_expand(lane, MR, MI);
    pin(MR); pin(MI); pin(TW);
    u4 PM;
    {
        lwait4(lds, L_C5, pv);
        float gg[4];
        UNROLL for (int k = 0; k < 2; ++k) {
            const f4* pp = (const f4*)&lds[L_GS + ((n >> 2) * 2 + k) * 16];
            const f4 ps = (pp[0] + pp[1]) + (pp[2] + pp[3]);
            const float lg = rbf((ps.x + ps.y) + (ps.z + ps.w));
            gg[2 * k] = rbf(1.0f / (1.0f + __expf(-lg)));
            gg[2 * k + 1] = rbf(1.0f - gg[2 * k]);
        }
        UNROLL for (int k = 0; k < 4; ++k) { gg[k] *= 0.03125f; pin(gg[k]); }
        const int r = n & 3, sa = (n >> 2) * 2;
        UNROLL for (int cc = 0; cc < 2; ++cc) {
            const int u1 = 2 * q + cc;
            const float ta = (float)(sa * u1) * 0.125f + ((r & 1) ? 0.25f : (r & 2) ? 0.5f : 0.0f);
            const float tb = (float)((sa + 1) * u1) * 0.125f + ((r & 1) ? ((r & 2) ? 0.0f : 0.5f) : 0.25f);
            const float ca = cosr(ta), sa_ = sinr(ta), cb = cosr(tb), sb_ = sinr(tb);
            float e[4] = {gg[2] * cb, gg[2] * sb_, gg[3] * cb, gg[3] * sb_};
            pin(e);
            e[0] = fmaf(gg[0], ca, e[0]); pin(e[0]); e[1] = fmaf(gg[0], sa_, e[1]); pin(e[1]); e[2] = fmaf(gg[1], ca, e[2]); pin(e[2]); e[3] = fmaf(gg[1], sa_, e[3]);
            PM[2 * cc] = pkh(e[0], e[1]); PM[2 * cc + 1] = pkh(e[2], e[3]);
        }
    }
    int a3d = (q * 8 + wv) * 32 + n, a3m = (q * 8 + 7 - wv) * 32 + 16 - n, a3z = (q * 8 + 7 - wv) * 32 + ((32 - n) & 31), a3r = wv * 256 + lane;
    float osc = __builtin_bit_cast(float, (u32)min(max((int)ex + (int)ew - 130, 1), 254) << 23) * TRUNC_FIX;
    const int ve = 2 * q * 4096 + (128 * wv + n) * 8;
    int vs0 = WS_V + b * 32768, vs1 = vs0 + 4096, vsl = WS_VL + b * 16;
    asm volatile("" : "+s"(vs0), "+s"(vs1), "+s"(vsl));
    int ov = (4 * perm32(lane & 31) + (lane >> 5)) * 16 + (512 * wv + b) * 2048;
    u32 x8 = wv == 3 ? 0u : vn;
    pin(a3d); pin(a3m); pin(a3z); pin(a3r); pin(PM); pin(ov); pin(osc); pin(x8);
    STAMP(13);
    VMWAIT(0);
    {
        f4 z3[9];
        NOUNROLL for (int spin = 0; spin < SPIN_CAP; ++spin) {
            MEMBAR();
            u2 v0[8], v1[8];
            u4 ve8, vo8;
            int a_ = ve; pin(a_);
            if (wv == 3) {
                ve8 = __builtin_amdgcn_raw_buffer_load_b128(rws, q * 8192, vsl, SC1); vo8 = __builtin_amdgcn_raw_buffer_load_b128(rws, q * 8192 + 4096, vsl, SC1);
            } else {
                const u2 e2 = __builtin_amdgcn_raw_buffer_load_b64(rws, a_ + 1024, vs0, SC1), o2 = __builtin_amdgcn_raw_buffer_load_b64(rws, a_ + 1024, vs1, SC1);
                ve8 = u4{e2.x, e2.y, tag0, tag0}; vo8 = u4{o2.x, o2.y, tag0, tag0};
            }
            UNROLL for (int i = 0; i < 8; ++i) {
                v0[i] = __builtin_amdgcn_raw_buffer_load_b64(rws, a_ + 128 * i, vs0, SC1);
                v1[i] = __builtin_amdgcn_raw_buffer_load_b64(rws, a_ + 128 * i, vs1, SC1);
            }
            SB();
            u32 m = (((wv == 3 ? ve8.y : ve8.z) ^ tag0) | (ve8.w ^ tag0) | ((wv == 3 ? vo8.y : vo8.z) ^ tag0) | (vo8.w ^ tag0)) ? TM : 0u;
            UNROLL for (int k = 0; k < 9; ++k) {
                const int i = (k + 8) % 9;
                const u4 d = i == 8 ? u4{ve8.x, wv == 3 ? ve8.z : ve8.y, vo8.x, wv == 3 ? vo8.z : vo8.y} ^ x8 : u4{v0[i & 7].x, v0[i & 7].y, v1[i & 7].x, v1[i & 7].y} ^ vn;
                m |= d.x | d.y; m |= d.z | d.w;
                z3[i] = mfma16(PM, d);
                SB();
            }
            if (__all((m & TM) == 0u)) break;
        }
        STAMP(14);
        if (wv == 0 && n == 0) { z3[0].x = 0.5f * (z3[0].x + z3[0].z); z3[0].y = 0.5f * (z3[0].y + z3[0].w); }
        if (wv == 3) { z3[8].z = 0.5f * (z3[8].x + z3[8].z); z3[8].w = 0.5f * (z3[8].y + z3[8].w); }
        u4* l4w = (u4*)&lds[L_A1];
        UNROLL for (int p = 0; p < 2; ++p)
            l4w[a3d + 16 * p] = u4{pkh(z3[p].x, z3[p + 2].x), pkh(z3[p + 4].x, z3[p + 6].x), pkh(z3[p].y, z3[p + 2].y), pkh(z3[p + 4].y, z3[p + 6].y)};
        l4w[a3m] = u4{pkh(z3[7].z, z3[5].z), pkh(z3[3].z, z3[1].z), pkh(z3[7].w, z3[5].w), pkh(z3[3].w, z3[1].w)};
        f2 m[4];
        UNROLL for (int i = 0; i < 4; ++i) m[i] = n == 0 ? f2{z3[8 - 2 * i].z, z3[8 - 2 * i].w} : f2{z3[6 - 2 * i].z, z3[6 - 2 * i].w};
        l4w[a3z] = u4{pkh(m[0].x, m[1].x), pkh(m[2].x, m[3].x), pkh(m[0].y, m[1].y), pkh(m[2].y, m[3].y)};
        lset(lds, L_C6 + wv, 2u * pv - 1u, lane);
    }
    {
        u4 in[4];
        NOUNROLL for (int spin = 0; spin < LSPIN_CAP; ++spin) {
            MEMBAR();
            const u4 c = *(const u4*)&lds[L_C6];
            MEMBAR();
            UNROLL for (int s = 0; s < 4; ++s) in[s] = ((const u4*)&lds[L_A1])[a3r + s * 64];
            pin(in[0]);
            if (rfl(min4u(c)) >= 2u * pv - 1u) break;
        }
        f16v zr, zi;
        core_fft<false>(in, MR, MI, TW, zr, zi);
        zr *= osc; zi *= osc;
        const rsrc_t ro = mkrs(out, 2048 * 2048);
        UNROLL for (int hf = 0; hf < 2; ++hf) {
            u4 c;
            UNROLL for (int j = 0; j < 4; ++j) c[j] = pkb(zr[8 * hf + 2 * j], zr[8 * hf + 2 * j + 1]);
            store128<NT>(c, ro, ov + 256 * 2048 + 32 * hf);
        }
        UNROLL for (int hf = 0; hf < 2; ++hf) {
            u4 a;
            UNROLL for (int j = 0; j < 4; ++j) a[j] = pkb(zi[8 * hf + 2 * j], zi[8 * hf + 2 * j + 1]);
            store128<NT>(a, ro, ov + 32 * hf);
        }
    }
    STAMP(15);
    vd = lwait(lds, L_VD, 1u);
    if (pass == 1 || !(vd & 0x20000u)) {
        if (b == 0 && t == 0) __builtin_amdgcn_raw_buffer_store_b32(epoch, rws, 0, WS_E, SC1);
        break;
    }
    lset(lds, L_C6 + wv, 2u, lane);
    lwait4(lds, L_C6, 2u);
    VMWAIT(0);
    }
    STAMP_OUT();
}

extern "C" void kernel_launch(void* const* d_in, const int* in_sizes, int n_in,
                              void* d_out, int out_size, void* d_ws, size_t ws_size,
                              hipStream_t stream) {
    gfnet_fused<<<256, 512, 0, stream>>>(d_in[0], d_in[6], d_in[1], d_in[2], d_in[4], d_in[5], (char*)d_ws, d_out);
}
```

```cpp
#define OAUX 16
#include <hip/hip_runtime.h>
#include <hip/hip_bf16.h>
#include <stdint.h>

typedef _Float16 h2 __attribute__((ext_vector_type(2)));
typedef _Float16 h8 __attribute__((ext_vector_type(8)));
typedef __bf16 b2 __attribute__((ext_vector_type(2)));
typedef __bf16 b8 __attribute__((ext_vector_type(8)));
typedef float f2 __attribute__((ext_vector_type(2)));
typedef float f4 __attribute__((ext_vector_type(4)));
typedef float f16v __attribute__((ext_vector_type(16)));
typedef short s4 __attribute__((ext_vector_type(4)));
typedef unsigned u32;
typedef u32 u2 __attribute__((ext_vector_type(2)));
typedef u32 u4 __attribute__((ext_vector_type(4)));
typedef unsigned short us2 __attribute__((ext_vector_type(2)));
typedef __amdgpu_buffer_rsrc_t rsrc_t;
#define DI __device__ __forceinline__
#define UNROLL _Pragma("unroll")
#define NOUNROLL _Pragma("unroll 1")
#define LAS __attribute__((address_space(3)))
#define SB() __builtin_amdgcn_sched_barrier(0)
#define MEMBAR() asm volatile("" ::: "memory")
#define VMWAIT(n) asm volatile("s_waitcnt vmcnt(%0)" :: "n"(n) : "memory")

#define MARGIN 2
#define FULLLINE 1
#define QUICK 145

constexpr u32 FSTR = 16, HSTR = 32;
constexpr u32 WS_Z = 0;
constexpr u32 WS_V = 4u << 20;
constexpr u32 WS_VL = 12u << 20;
constexpr u32 WS_HB = WS_VL + (32u << 10);
constexpr u32 WS_F = WS_HB + (32u << 10);
constexpr u32 WS_E = WS_F + 2048 * FSTR;
constexpr u32 WS_BYTES = WS_E + 4096;
constexpr int SC1 = 16, NT = 2;
constexpr int SPIN_CAP = 8192, LSPIN_CAP = 1 << 17;
constexpr int OOB = 0x7ffffff0;
constexpr u32 TM = 0x00010001u;
constexpr float TRUNC_FIX = 1.00066f;

template <class T> DI void pin(T& v) { asm volatile("" : "+v"(v)); }
template <class T, int N> DI void pin(T (&v)[N]) { UNROLL for (int i = 0; i < N; ++i) pin(v[i]); }
DI u32 opq(u32 v) { asm("" : "+v"(v)); return v; }
DI u32 pkh(float lo, float hi) { h2 r; r.x = (_Float16)lo; r.y = (_Float16)hi; return __builtin_bit_cast(u32, r); }
DI u32 pkb(float lo, float hi) { b2 r; r.x = (__bf16)lo; r.y = (__bf16)hi; return __builtin_bit_cast(u32, r); }
DI float bflo(u32 v) { return __builtin_bit_cast(float, v << 16); }
DI float bfhi(u32 v) { return __builtin_bit_cast(float, v & 0xffff0000u); }
DI float rbf(float v) { return (float)(__bf16)v; }
DI h2 H(u32 v) { return __builtin_bit_cast(h2, v); }
DI u32 U(h2 v) { return __builtin_bit_cast(u32, v); }
DI f16v mfma32(u4 a, u4 b, f16v c) { return __builtin_amdgcn_mfma_f32_32x32x16_f16(__builtin_bit_cast(h8, a), __builtin_bit_cast(h8, b), c, 0, 0, 0); }
DI f4 mfma16(u4 a, u4 b) { return __builtin_amdgcn_mfma_f32_16x16x32_f16(__builtin_bit_cast(h8, a), __builtin_bit_cast(h8, b), f4{}, 0, 0, 0); }
DI f4 mfma16b(u4 a, u4 b, f4 c) { return __builtin_amdgcn_mfma_f32_16x16x32_bf16(__builtin_bit_cast(b8, a), __builtin_bit_cast(b8, b), c, 0, 0, 0); }
DI u32 hmul(u32 a, u32 b) { return U(H(a) * H(b)); }
DI u32 cmul_swap(u32 x, u32 w) { const h2 a = H(opq(x)), c = H(opq(w)); h2 n = c; n.y = -n.y; return U(__builtin_elementwise_fma(a.yy, n, a.xx * c.yx)); }
DI u32 cmul_conj(u32 x, u32 t) { const h2 a = H(opq(x)), c = H(opq(t)); h2 n = c.yy; n.y = -n.y; return U(__builtin_elementwise_fma(a.yx, n, a * c.xx)); }
DI u32 pkmax(u32 a, u32 b) { return __builtin_bit_cast(u32, __builtin_elementwise_max(__builtin_bit_cast(us2, a), __builtin_bit_cast(us2, b))); }
DI float sinr(float r) { return __builtin_amdgcn_sinf(r); }
DI float cosr(float r) { return __builtin_amdgcn_cosf(r); }
DI u32 rot(float ph) { return pkh(cosr(ph), sinr(ph)); }
DI u32 quarter(u32 r, u32 f) { u32 o; asm("v_pk_mul_f16 %0, %1, %2 op_sel:[1,0] op_sel_hi:[0,1] neg_lo:[1,0]" : "=v"(o) : "v"(r), "v"(f)); return o; }
DI u4 quarter(u4 r) { u32 one = 0x3c003c00u; pin(one); return u4{quarter(r.x, one), quarter(r.y, one), quarter(r.z, one), quarter(r.w, one)}; }
DI int perm32(int n) { return (n & 0x13) | ((n & 4) << 1) | ((n & 8) >> 1); }
DI u32 max4u(u4 v) { return max(max(v.x, v.y), max(v.z, v.w)); }
DI u32 min4u(u4 v) { return min(min(v.x, v.y), min(v.z, v.w)); }
DI u32 mark(u32 v, u32 par, u32 tm) { return (par & tm) | (v & ~tm); }
DI u32 rfl(u32 v) { return __builtin_amdgcn_readfirstlane(v); }
DI rsrc_t mkrs(const void* p, u32 bytes) { return __builtin_amdgcn_make_buffer_rsrc((void*)p, (short)0, (int)bytes, 0x00020000); }
template <int AUX> DI void store128(u4 v, rsrc_t r, int off) { __builtin_amdgcn_raw_buffer_store_b128(v, r, off, 0, AUX); }
template <int CTRL> DI u32 dpp(u32 v) { return __builtin_amdgcn_update_dpp(0u, v, CTRL, 0xf, 0xf, true); }
DI u32 wave_maxu(u32 v) {
    v = max(v, dpp<0xB1>(v)); v = max(v, dpp<0x4E>(v)); v = max(v, dpp<0x141>(v)); v = max(v, dpp<0x140>(v));
    return max(max((u32)__builtin_amdgcn_readlane(v, 0), (u32)__builtin_amdgcn_readlane(v, 16)), max((u32)__builtin_amdgcn_readlane(v, 32), (u32)__builtin_amdgcn_readlane(v, 48)));
}
#define STAMP_DECL
#define STAMP(i)
#define STAMP_OUT()

DI void core_setup(int lane, int s, u4& tw, u2& m) {
    float fc = (float)perm32(lane & 31) * (1.0f / 1024.0f), p0 = (float)(4 * (lane >> 5) + 8 * s) * fc, p1 = (float)(4 * (lane >> 5) + 8 * (s >> 1) + 2 * (s & 1)) * fc * 32.0f;
    pin(fc); pin(p0); pin(p1);
    float tc[4], ts[4];
    UNROLL for (int i = 0; i < 4; ++i) { const float pt = fmaf((float)i, fc, p0); tc[i] = cosr(pt); ts[i] = sinr(pt); }
    tw = u4{pkh(tc[0], tc[1]), pkh(tc[2], tc[3]), pkh(ts[0], ts[1]), pkh(ts[2], ts[3])};
    const float p2 = fmaf(32.0f, fc, p1);
    m = u2{pkh(cosr(p1), cosr(p2)), pkh(sinr(p1), sinr(p2))};
}
DI void core_expand(int lane, u4 (&MR)[4], u4 (&MI)[4]) {
    const u32 sg = (lane & 1) ? 0x80008000u : 0u;
    MR[2] = MR[0] ^ sg; MR[3] = MR[1] ^ sg;
    UNROLL for (int s = 0; s < 4; ++s) MI[s] = u4{MR[s].z ^ 0x80008000u, MR[s].w ^ 0x80008000u, MR[s].x, MR[s].y};
}
template <bool OUT_B> DI void core_fft(const u4 (&in)[4], const u4 (&MR)[4], const u4 (&MI)[4], const u4 (&TW)[4], f16v& outr, f16v& outi) {
    f16v yr = {}, yi = {};
    UNROLL for (int s = 0; s < 4; ++s) yr = mfma32(in[s], MR[s], yr);
    SB();
    yi = mfma32(in[0], MI[0], yi); yi = mfma32(in[1], MI[1], yi);
    SB();
    h2 t0[8], t1[8];
#define TW_RE(p) do { const h2 pr_ = H(pkh(yr[2 * (p)], yr[2 * (p) + 1])); t0[p] = pr_ * H(TW[(p) >> 1][(p) & 1]); t1[p] = pr_ * H(TW[(p) >> 1][2 + ((p) & 1)]); } while (0)
    TW_RE(0); TW_RE(1); SB();
    yi = mfma32(in[2], MI[2], yi);
    TW_RE(2); TW_RE(3); SB();
    yi = mfma32(in[3], MI[3], yi);
    TW_RE(4); TW_RE(5); TW_RE(6); TW_RE(7); SB();
    outr = f16v{}; outi = f16v{};
    u4 d[4];
#define TW_IM(s) do { UNROLL for (int j = 0; j < 2; ++j) { const int p_ = 2 * (s) + j; const h2 pi_ = H(pkh(yi[2 * p_], yi[2 * p_ + 1])); \
        d[s][j] = U(__builtin_elementwise_fma(pi_, H(TW[s][2 + j]), t0[p_])); d[s][2 + j] = U(__builtin_elementwise_fma(pi_, H(TW[s][j]), -t1[p_])); } } while (0)
    TW_IM(0); SB();
    UNROLL for (int s = 0; s < 4; ++s) {
        outr = OUT_B ? mfma32(MR[s], d[s], outr) : mfma32(d[s], MR[s], outr);
        if (s < 3) TW_IM(s + 1);
        SB();
    }
    UNROLL for (int s = 0; s < 4; ++s) outi = OUT_B ? mfma32(MI[s], d[s], outi) : mfma32(d[s], MI[s], outi);
}

DI u32 tag_of(u32 epoch, u32 idx) { u32 h = (epoch ^ (idx * 0x9E3779B1u)) * 0x85EBCA6Bu; return h ^ (h >> 15); }
DI u32 valid_bits(u32 epoch, int slot) { return (((0x6A3Cu >> slot) ^ epoch) & 1u) ? 0x00000001u : 0x00010000u; }
DI int cls_of(int b) { const int row = (b & 7) >> 1; return row == 0 ? 4 * (b >> 7) : ((b & 1) ? 8 - row : row); }
DI int tile_of(int b) { return (b & 6) == 0 ? 16 * (b & 1) + ((b >> 3) & 15) : b >> 3; }

constexpr int L_C1 = 0, L_C3 = 4, L_C4 = 8, L_C5 = 12, L_C6 = 16;
constexpr int L_EW = 20;
constexpr int L_WW = 24;
constexpr int L_PR = 28;
constexpr int L_LD = 32;
constexpr int L_SX = 36;
constexpr int L_ZL = 37;
constexpr int L_VD = 38;
constexpr int L_P2 = 39;
constexpr int L_GS = 64;
constexpr int L_M = 192;
constexpr int L_A1 = L_M + 1536;
constexpr int L_A2 = L_A1 + 4096;
constexpr int L_B = L_A2 + 4096;
constexpr int L_W = L_B + 8192;
constexpr int L_END = L_W + 64 * 256;

DI void lset(u32* lds, int idx, u32 v, int lane) { MEMBAR(); if (lane == 0) lds[idx] = v; MEMBAR(); }
DI u32 lget(u32* lds, int idx) { MEMBAR(); return rfl(lds[idx]); }
DI u32 lwait(u32* lds, int idx, u32 atleast) {
    u32 v = 0;
    NOUNROLL for (int spin = 0; spin < LSPIN_CAP; ++spin) { v = lget(lds, idx); if (v >= atleast) break; __builtin_amdgcn_s_sleep(1); }
    MEMBAR();
    return v;
}
DI u32 lwait4(u32* lds, int idx, u32 atleast) {
    u4 v;
    NOUNROLL for (int spin = 0; spin < LSPIN_CAP; ++spin) { MEMBAR(); v = *(const u4*)&lds[idx]; if (rfl(min4u(v)) >= atleast) break; __builtin_amdgcn_s_sleep(1); }
    MEMBAR();
    return rfl(max4u(v));
}
struct MagMax {
    h2 hi = {}, lo = {};
    DI void add(u32 a, u32 c) {
        hi = __builtin_elementwise_maximum(__builtin_elementwise_maximum(hi, H(a)), H(c));
        lo = __builtin_elementwise_minimum(__builtin_elementwise_minimum(lo, H(a)), H(c));
    }
    DI u32 exponent() const {
        const u32 mx = pkmax(U(hi) & 0x7fff7fffu, U(lo) & 0x7fff7fffu);
        return wave_maxu(max(mx & 0xffffu, mx >> 16)) >> 7;
    }
};
DI u32 clampe(u32 e) { return min(max(e, 8u), 240u); }

__global__ __launch_bounds__(512, 2) void gfnet_fused(const void* x, const void* gw, const void* war, const void* wai, const void* wbr, const void* wbi,
                                                      char* ws, void* out) {
    __shared__ __attribute__((aligned(16))) u32 lds[L_END];
    STAMP_DECL;
    STAMP(0);
    u32 code;
    {
        unsigned long long pc0; int len;
        asm volatile("s_getpc_b64 %0\n.Lgf_begin:\n\ts_mov_b32 %1, .Lgf_end-.Lgf_begin" : "=s"(pc0), "=s"(len));
        code = __builtin_amdgcn_raw_buffer_load_b32(mkrs((const void*)pc0, (u32)max(len, 0)), (threadIdx.x >> 6) == 4 ? ((blockIdx.x >> 5) * 64 + (threadIdx.x & 63)) * 64 : OOB, 0, 0);
        SB();
    }
    asm volatile("" :: "s"(x), "s"(gw), "s"(war), "s"(wai), "s"(wbr), "s"(wbi), "s"(ws), "s"(out));
    const int wv0 = __builtin_amdgcn_readfirstlane(threadIdx.x >> 6);
    const rsrc_t rws = mkrs(ws, WS_BYTES);

    if (wv0 >= 4) {
        const u32 tstart = (u32)__builtin_amdgcn_s_memrealtime();
        const int wv = wv0, b = blockIdx.x, cls = cls_of(b), tile = tile_of(b);
        const int lane = threadIdx.x & 63, q = lane >> 4, n = lane & 15;
        const rsrc_t rw = mkrs(wv == 4 ? war : wv == 5 ? wai : wv == 6 ? wbr : wbi, 2048 * 1026);
        if (wv == 4) { lds[lane] = 0u; asm volatile("s_waitcnt lgkmcnt(0)" ::: "memory"); }
        MEMBAR();
        __builtin_amdgcn_s_barrier();
        MEMBAR();
        const bool helper = wv == 7 && tile == 31;
        u4 GR, GI, FR, FI, HR, HI, T1, T2;
        u32 ev;
        {
            const int wvo = (lane >> 2) * (128 * 1026) + min(lane & 3, 2) * 16, wso = (cls * 513 + 16 * tile - cls) * 2;
#define WLOAD(r) do { UNROLL for (int st = 0; st < 4; ++st) { const int c_ = 4 * st + ((tile + (r)) & 3); \
                __builtin_amdgcn_raw_ptr_buffer_load_lds(rw, (LAS void*)&lds[L_W + (c_ * 4 + wv - 4) * 256], 16, wvo, wso + c_ * 8208, 0, 0); } SB(); } while (0)
            __builtin_amdgcn_s_sleep(2);
            WLOAD(0);
            const bool split = (b & 6) == 0;
            const int er = split ? 16 * (tile & 15) + ((lane >> 1) & 15) : 8 * tile + ((lane >> 1) & 7);
            const int ec = (split ? (tile & 16) * 16 : 0) - cls + ((lane & 1) ? (split ? 262 : 518) : 0);
            u32 edge = __builtin_amdgcn_raw_buffer_load_b32(rw, ((cls + 8 * er) * 513 + ec) * 2, 0, 0);
            ev = __builtin_amdgcn_raw_buffer_load_b32(rws, 0, WS_E, SC1);
            const u4 smp = __builtin_amdgcn_raw_buffer_load_b128(rw, (lane >> 3) * (2 * 131328 + 128) + (lane & 7) * 16, 0, 0);
            MEMBAR();
            if (helper) {
                UNROLL for (int j = 0; j < 4; ++j) {
                    GR[j] = rot((float)((4 * q + j) * (cls + 8 * n)) * (1.0f / 128.0f) + (cls == 4 ? 0.25f : 0.0f));
                    HR[j] = rot((float)(n * (cls + 8 * (4 * q + j))) * (1.0f / 128.0f));
                    FR[j] = rot((float)(n * (4 * q + j)) * (1.0f / 16.0f));
                    T1[j] = rot((float)((4 * q + j) * (cls + 8 * n)) * (1.0f / 2048.0f));
                    T2[j] = rot((float)(n * (cls + 8 * (4 * q + j))) * (1.0f / 2048.0f));
                }
                GI = quarter(GR); HI = quarter(HR); FI = quarter(FR);
                pin(GR); pin(GI); pin(HR); pin(HI); pin(FR); pin(FI); pin(T1); pin(T2);
            }
            VMWAIT(0);
            STAMP(1);
            if ((int)((u32)__builtin_amdgcn_s_memrealtime() - tstart) < QUICK)
                __builtin_amdgcn_raw_buffer_store_b32(0u, rws, lane * 128, WS_V + b * 32768 + (wv - 4) * 8192, SC1);
            MEMBAR();
            WLOAD(1); WLOAD(2); WLOAD(3);
            VMWAIT(12);
            asm volatile("ds_write_b32 %0, %1" :: "v"((u32)(size_t)(LAS u32*)&lds[L_LD + wv - 4]), "v"(1u) : "memory");
            pin(edge); pin(code);
            MagMax ms; ms.add(smp.x, smp.y); ms.add(smp.z, smp.w);
            const u32 es = clampe(ms.exponent() + MARGIN);
            VMWAIT(0);
            lset(lds, L_PR + wv - 4, es, lane);
            STAMP(2);
        }
        const u32 ep0 = rfl(ev) + 1u;
        if (wv == 5 || wv == 6) {
            lwait4(lds, L_PR, 1u);
            MagMax m;
            UNROLL for (int c = 0; c < 32; ++c) { const u4 v = *(const u4*)&lds[L_W + ((wv - 5) * 32 + c) * 256 + lane * 4]; m.add(v.x, v.y); m.add(v.z, v.w); }
            const u32 e = clampe(m.exponent());
            MEMBAR(); if (lane == 0) *(u2*)&lds[L_WW + 2 * (wv - 5)] = u2{e, e}; MEMBAR();
        } else if (wv == 4) {
            const u32 ex = lwait4(lds, L_EW, 1u), ew = lwait4(lds, L_WW, 1u);
            __builtin_amdgcn_raw_buffer_store_b64(u2{ex | (ew << 8), tag_of(ep0, b + 1)}, rws, lane < 8 ? lane * (int)(256 * FSTR) : OOB, WS_F + b * FSTR, SC1);
            lwait(lds, L_P2, 1u);
            __builtin_amdgcn_s_sleep(24);
            u2 f[4]; u32 tg[4];
            UNROLL for (int j = 0; j < 4; ++j) tg[j] = tag_of(ep0, 4 * lane + j + 1);
            NOUNROLL for (int spin = 0; spin < SPIN_CAP; ++spin) {
                MEMBAR();
                UNROLL for (int j = 0; j < 4; ++j) f[j] = __builtin_amdgcn_raw_buffer_load_b64(rws, lane * (4 * FSTR) + j * FSTR, WS_F + (b & 7) * (256 * FSTR), SC1);
                if (__all(f[0].y == tg[0] && f[1].y == tg[1] && f[2].y == tg[2] && f[3].y == tg[3])) break;
                __builtin_amdgcn_s_sleep(8);
            }
            const u4 e4 = {f[0].x, f[1].x, f[2].x, f[3].x};
            const u32 gx = wave_maxu(max4u(e4 & 0xffu)), gwm = wave_maxu(max4u((e4 >> 8) & 0xffu));
            const bool redo = gx > lget(lds, L_SX) || gwm > lwait4(lds, L_PR, 1u);
            lset(lds, L_VD, gx | (gwm << 8) | (redo ? 0x20000u : 0x10000u), lane);
            STAMP(5);
        } else if (helper) {
            __builtin_amdgcn_s_setprio(2);
            u32 ew = lwait4(lds, L_PR, 1u);
            NOUNROLL for (u32 pass = 0; pass < 2; ++pass) {
                u4 WH[2];
                const float sw = __builtin_bit_cast(float, (249u - ew) << 23);
                UNROLL for (int r = 0; r < 4; ++r) UNROLL for (int f = 0; f < 2; ++f) {
                    const unsigned short* wp = (const unsigned short*)&lds[L_W + (n * 4 + 2 * f) * 256 + (4 * q + r) * 16] + 16 + cls;
                    float re = bflo(wp[0]) * sw, im = bflo(wp[512]) * sw;
                    pin(re);
                    WH[f][r] = pkh(re, im);
                }
                pin(WH);
                STAMP(3);
                lwait(lds, L_ZL, pass + 1u);
                const u32 tg = tag_of(ep0 + pass, 0u);
                u4 hz;
                NOUNROLL for (int spin = 0; spin < SPIN_CAP; ++spin) {
                    MEMBAR();
                    u32 bad = 0;
                    UNROLL for (int j = 0; j < 4; ++j) { const u2 g = __builtin_amdgcn_raw_buffer_load_b64(rws, (64 * q + n + 16 * j) * HSTR, WS_HB + ((b & 7) >> 1) * (256 * HSTR), SC1); hz[j] = g.x & ~TM; bad |= g.y ^ tg; }
                    if (__all(bad == 0u)) break;
                    __builtin_amdgcn_s_sleep(4);
                }
                STAMP(4);
                const u32 f16 = cls == 0 ? 0x00002c00u : cls == 4 ? 0x2c000000u : cls > 4 ? 0xac002c00u : 0x2c002c00u;
                u4 d, o[2];
                UNROLL for (int j = 0; j < 4; ++j) d[j] = hmul(hz[j], f16);
                f4 yr = mfma16(d, GR), yi = mfma16(d, GI);
                UNROLL for (int r = 0; r < 4; ++r) d[r] = cmul_conj(pkh(yr[r], yi[r]), T1[r]);
                const f4 xr = mfma16(FR, d), xi = mfma16(FI, d);
                UNROLL for (int f = 0; f < 2; ++f) {
                    UNROLL for (int r = 0; r < 4; ++r) d[r] = cmul_swap(pkh(xr[r], xi[r]), WH[f][r]);
                    yr = mfma16(d, FR); yi = mfma16(d, FI);
                    UNROLL for (int r = 0; r < 4; ++r) o[f][r] = cmul_conj(pkh(yr[r], yi[r]), T2[r]);
                }
                const f4 p0 = mfma16(HR, o[0]), p1 = mfma16(HI, o[0]), p2 = mfma16(HR, o[1]), p3 = mfma16(HI, o[1]);
                UNROLL for (int r = 0; r < 4; ++r)
                    store128<SC1>(u4{pkh(p0[r], p1[r]) & ~TM, tg, pkh(p2[r], p3[r]) & ~TM, tg}, rws, (64 * q + n) * 16 + WS_VL + r * 256 + cls * 4096);
                STAMP(5);
                const u32 vd = lwait(lds, L_VD, 1u);
                if (!(vd & 0x20000u)) break;
                ew = (vd >> 8) & 0xffu;
            }
        }
        asm volatile(".Lgf_end:");
        STAMP_OUT();
        return;
    }

    u32 vd = 0;
    NOUNROLL for (u32 pass = 0; pass < 2; ++pass) {
    int t = threadIdx.x, wv = wv0, b = blockIdx.x;
    pin(t);
    asm volatile("" : "+s"(wv));
    asm volatile("" : "+s"(b));
    const int lane = t & 63, q = lane >> 4, n = lane & 15;
    const u32 pv = pass + 1u;
    u2 xv[8];
    const rsrc_t rx = mkrs(x, 2048 * 2048);
    const u4 smp = __builtin_amdgcn_raw_buffer_load_b128(rx, (lane >> 3) * (524288 + 2048 + 256) + (lane & 7) * 16, 0, 0);
    UNROLL for (int s1 = 0; s1 < 8; ++s1) xv[s1] = __builtin_amdgcn_raw_buffer_load_b64(rx, t * 8, (256 * s1 + b) * 2048, NT);
    const u32 ev = __builtin_amdgcn_raw_buffer_load_b32(rws, 0, WS_E, SC1);
    const u2 gv = __builtin_amdgcn_raw_buffer_load_b64(mkrs(gw, 2048), t * 8, 0, 0);
    MEMBAR();
    SB();
    if (pass == 0) { __builtin_amdgcn_s_barrier(); MEMBAR(); __builtin_amdgcn_s_setprio(1); }
    u4 MR[4], MI[4], TW[4];
    {
        u4 tw; u2 m;
        core_setup(lane, wv, tw, m);
        *(u4*)&lds[L_M + (2 + wv) * 256 + lane * 4] = tw;
        lds[L_M + (wv >> 1) * 256 + lane * 4 + (wv & 1)] = m.x; lds[L_M + (wv >> 1) * 256 + lane * 4 + 2 + (wv & 1)] = m.y;
    }
    const int zc = perm32(lane & 31) + (lane >> 5) * 256, zcu = zc + ((lane & 31) == 0 ? 543 : 511);
    int zl = (zc >> 4) * 16384 + (zc & 15) * 4, zu = (zcu >> 4) * 16384 + (zcu & 15) * 4;
    int zh = lane == 0 ? 0 : OOB;
    int tro = L_A1 + wv * 1024 + (4 * (lane >> 5) + ((lane & 15) >> 2)) * 16 + ((lane >> 4) & 1) * 8 + (lane & 3) * 2;
    u32 tm = TM, vl0 = valid_bits(0u, zc & 15), vu0 = valid_bits(0u, zcu & 15);
    pin(zl); pin(zu); pin(zh); pin(tro); pin(tm); pin(vl0); pin(vu0);
    STAMP(1);
    SB();
    pin(code);
    u32 ex;
    {
        MagMax ms; ms.add(smp.x, smp.y); ms.add(smp.z, smp.w);
        ex = pass ? (vd & 0xffu) : clampe(ms.exponent() + MARGIN);
    }
    STAMP(2);
    u32 epoch, tag0, flip;
    u32 z[16], z512;
    {
        const float sc = __builtin_bit_cast(float, (250u - ex) << 23);
        const float rs = sc * 0.70710678118654752f;
        u32 o[8][2];
        UNROLL for (int j = 0; j < 2; ++j) {
            f2 v[8];
            UNROLL for (int s1 = 0; s1 < 8; ++s1) v[s1] = f2{bflo(xv[s1][j]), bfhi(xv[s1][j])};
            const f2 a0 = v[0] + v[4], a1 = v[1] + v[5], a2 = v[2] + v[6], a3 = v[3] + v[7];
            const f2 b0 = (v[0] - v[4]) * sc, b1 = v[1] - v[5], b2_ = (v[2] - v[6]) * sc, b3 = v[3] - v[7];
            const f2 e0 = a0 + a2, e1 = (a1 + a3) * sc, dm = b1 - b3, dp = b1 + b3;
            f2 r[8];
            r[0] = e0 * sc + e1; r[1] = e0 * sc - e1;
            r[2] = dm * rs + b0; r[3] = -(dp * rs) - b2_;
            r[4] = (a0 - a2) * sc; r[5] = (a3 - a1) * sc;
            r[6] = b0 - dm * rs; r[7] = b2_ - dp * rs;
            UNROLL for (int p = 0; p < 8; ++p) o[p][j] = pkh(r[p].x, r[p].y);
        }
        UNROLL for (int p = 0; p < 8; ++p) *(u2*)&lds[L_A1 + p * 512 + 2 * t] = u2{o[p][0], o[p][1]};
        lset(lds, L_C1 + wv, pv, lane);
        epoch = rfl(ev) + pv; tag0 = tag_of(epoch, 0u); flip = (epoch & 1u) ? TM : 0u;
        const u32 vl = vl0 ^ flip, vu = vu0 ^ flip;
        u4 in[4];
        NOUNROLL for (int spin = 0; spin < LSPIN_CAP; ++spin) {
            MEMBAR();
            const u4 c = *(const u4*)&lds[L_C1];
            MEMBAR();
            MR[0] = *(const u4*)&lds[L_M + lane * 4]; MR[1] = *(const u4*)&lds[L_M + 256 + lane * 4];
            UNROLL for (int s = 0; s < 4; ++s) {
                const u2 re = __builtin_bit_cast(u2, __builtin_amdgcn_ds_read_tr16_b64_v4i16((LAS s4*)&lds[tro + s * 128]));
                const u2 im = __builtin_bit_cast(u2, __builtin_amdgcn_ds_read_tr16_b64_v4i16((LAS s4*)&lds[tro + s * 128 + 512]));
                in[s] = u4{re.x, re.y, im.x, im.y};
            }
            pin(MR[0]); pin(MR[1]);
            if (rfl(min4u(c)) >= pv) break;
        }
        UNROLL for (int i = 0; i < 4; ++i) TW[i] = *(const u4*)&lds[L_M + (2 + i) * 256 + lane * 4];
        STAMP(3);
        core_expand(lane, MR, MI);
        f16v zr, zi;
        core_fft<true>(in, MR, MI, TW, zr, zi);
        UNROLL for (int r = 0; r < 16; ++r) z[r] = pkh(zr[r], zi[r]);
        z512 = z[8];
        const u32 za = __builtin_amdgcn_readlane(z[8], 32), zb = __builtin_amdgcn_readlane(z[0], 0);
        MEMBAR();
        const u32 zt = lane < 32 ? za : zb;
        UNROLL for (int r = 0; r < 8; ++r) z[r] = mark(z[r], vl, tm);
        UNROLL for (int r = 8; r < 16; ++r) z[r] = mark((lane & 31) == 0 ? (r < 15 ? z[r + 1] : zt) : z[r], vu, tm);
    }
    pin(z);
    STAMP(4);
    const int zs = WS_Z + wv * 64 * 16384 + (((b >> 2) & 3) * 64 + (b & 3) * 16 + ((b >> 4) & 3) * 4 + (b >> 6)) * 64;
    bool zsent = false;
#define TRY_STORE_Z(force) do { if (!zsent) { \
        bool go = true; \
        if (force) lwait4(lds, L_LD, 1u); else { MEMBAR(); go = rfl(min4u(*(const u4*)&lds[L_LD])) != 0u; MEMBAR(); } \
        if (go) { \
            UNROLL for (int r = 0; r < 16; ++r) __builtin_amdgcn_raw_buffer_store_b32(z[r], rws, r < 8 ? zl : zu, zs + (r & 7) * 32768, SC1); \
            __builtin_amdgcn_raw_buffer_store_b64(u2{z512, tag0}, rws, zh, WS_HB + (wv * 256 + b) * HSTR, SC1); \
            zsent = true; } } SB(); } while (0)
    TRY_STORE_Z(false);
    const int row = (b & 7) >> 1, cls = cls_of(b), tile = tile_of(b);
    const bool packed = row == 0;
    u4 FR, FI, GR[4], GI[4], HR[4], HI[4], sel1;
    {
        const float ofs = cls == 4 ? 0.25f : 0.0f;
        float gu = (float)(cls + 8 * n) * (1.0f / 2048.0f), g0 = (float)(64 * q + 4 * wv) * gu + ofs;
        u32 s16 = 0x2c002c00u;
        pin(gu); pin(g0); pin(s16);
        UNROLL for (int i = 0; i < 4; ++i) UNROLL for (int j = 0; j < 4; ++j) { const u32 g = rot(fmaf((float)(16 * j + i), gu, g0)); GR[i][j] = hmul(g, s16); GI[i][j] = quarter(g, s16); }
    }
    pin(GR); pin(GI);
    {
        MagMax m;
        UNROLL for (int s1 = 0; s1 < 8; ++s1) m.add(xv[s1].x, xv[s1].y);
        if (wv == 0) lset(lds, L_SX, ex, lane);
        lset(lds, L_EW + wv, clampe(m.exponent()), lane);
    }
    TRY_STORE_Z(false);
    UNROLL for (int j = 0; j < 4; ++j) FR[j] = rot((float)(n * (4 * q + j)) * (1.0f / 16.0f));
    FI = quarter(FR);
    {
        float hs[4], hu[4];
        UNROLL for (int i = 0; i < 4; ++i) { hs[i] = (float)(16 * n + 4 * wv + i) * (1.0f / 2048.0f); hu[i] = (float)(cls + 32 * q + 8 * i); }
        pin(hs); pin(hu);
        UNROLL for (int i = 0; i < 4; ++i) { UNROLL for (int j = 0; j < 4; ++j) { float a = hs[i] * hu[j]; pin(a); HR[i][j] = rot(a); } HI[i] = quarter(HR[i]); }
        const int d = n + cls - 8 * q;
        UNROLL for (int j = 0; j < 4; ++j) sel1[j] = (d >> 1) == j ? ((d & 1) ? 0xffff0000u : 0xffffu) : 0u;
    }
    pin(FR); pin(FI); pin(HR); pin(HI); pin(sel1);
    TRY_STORE_Z(true);
    STAMP(5);
    VMWAIT(4);
    u4 WA[4], WB[4];
    u32 ew = 0;
    UNROLL for (int i = 0; i < 4; ++i) asm volatile("" : "=v"(WA[i]), "=v"(WB[i]));
#define WIDEN(es) do { \
        u4 wr[4][4]; \
        int wl = L_W / 4 + wv * 1024 + n * 4 + min(q, 2); pin(wl); \
        UNROLL for (int i = 0; i < 4; ++i) UNROLL for (int a = 0; a < 4; ++a) wr[i][a] = ((const u4*)lds)[wl + (i * 4 + a) * 64]; \
        ew = pass ? (vd >> 8) & 0xffu : (es); \
        const u32 sw = ((249u - ew - (packed ? 1u : 0u)) << 7) * 0x10001u;       \
        const u4 sels = sel1 & sw; \
        SB(); \
        f4 wf[4][4]; \
        UNROLL for (int k = 0; k < 10; ++k) { \
            if (k < 8) { wf[k >> 1][2 * (k & 1)] = mfma16b(wr[k >> 1][2 * (k & 1)], sels, f4{}); wf[k >> 1][2 * (k & 1) + 1] = mfma16b(wr[k >> 1][2 * (k & 1) + 1], sels, f4{}); } \
            if (k >= 2) { const int i_ = (k - 2) >> 1, f_ = k & 1; UNROLL for (int r = 0; r < 4; ++r) (f_ ? WB : WA)[i_][r] = pkh(wf[i_][2 * f_][r], wf[i_][2 * f_ + 1][r]); } \
            SB(); } \
        pin(WA); pin(WB); } while (0)
    MEMBAR();
    const u4 pr0 = *(const u4*)&lds[L_PR];
    const bool wearly = rfl(min4u(pr0)) != 0u;
    MEMBAR();
    if (wearly) WIDEN(rfl(max4u(pr0)));
    STAMP(6);
    const u32 vn = valid_bits(epoch, n), vm = valid_bits(epoch, 15 - n);
    u32 x1 = cls > 4 ? vm ^ 0x80000000u : vn, x2 = vm ^ (cls == 4 ? 0x00008000u : cls == 0 ? 0x80000000u : 0u);
    u32 z1[4][4], z2[4][4];
    const int zvm = q * 64 + (63 - tile) * 16384 + (15 - n) * 4;
    const int zv1 = cls <= 4 ? q * 64 + tile * 16384 + n * 4 : zvm;
    int zso = WS_Z + row * 64 * 16384 + wv * 4096;
    asm volatile("" : "+s"(zso));
#define LOAD_Z() do { \
        if (packed) { int a_ = zvm; pin(a_); UNROLL for (int i = 0; i < 4; ++i) UNROLL for (int j = 0; j < 4; ++j) z2[i][j] = __builtin_amdgcn_raw_buffer_load_b32(rws, a_ + (4 * i + j) * 256, zso, SC1); } \
        else { UNROLL for (int i = 0; i < 4; ++i) UNROLL for (int j = 0; j < 4; ++j) z2[i][j] = x2; } \
        int c_ = zv1; pin(c_); UNROLL for (int i = 0; i < 4; ++i) UNROLL for (int j = 0; j < 4; ++j) z1[i][j] = __builtin_amdgcn_raw_buffer_load_b32(rws, c_ + (4 * i + j) * 256, zso, SC1); } while (0)
    pin(x1); pin(x2);
    VMWAIT(0);
    LOAD_Z();
    if (wv == 0) lset(lds, L_ZL, pv, lane);
    SB();
    STAMP(7);
    const int lw = ((wv * 16 + 4 * q) * 16 + n) * 4;
    int lr = (q * 16 + 4 * wv) * 16 + n;
    const u4* l4 = (const u4*)lds;
    pin(lr);
    STAMP(8);
    SB();
    {
        f4 yr[4], yi[4];
        NOUNROLL for (int spin = 0; spin < SPIN_CAP; ++spin) {
            u32 m = 0;
            UNROLL for (int i = 0; i < 4; ++i) {
                u4 d;
                UNROLL for (int j = 0; j < 4; ++j) { z1[i][j] ^= x1; z2[i][j] ^= x2; }
                UNROLL for (int j = 0; j < 4; ++j) d[j] = U(H(z1[i][j]) + H(z2[i][j]));
                UNROLL for (int j = 0; j < 4; ++j) m |= z1[i][j] | z2[i][j];
                yr[i] = mfma16(GR[i], d); yi[i] = mfma16(GI[i], d);
                SB();
            }
            if (__all((m & TM) == 0u)) break;
            MEMBAR();
            LOAD_Z();
        }
        STAMP(9);
        u4 y[4];
        UNROLL for (int i = 0; i < 4; ++i) UNROLL for (int r = 0; r < 4; ++r) y[r][i] = pkh(yr[i][r], yi[i][r]);
        UNROLL for (int r = 0; r < 4; ++r) *(u4*)&lds[L_A2 + lw + r * 64] = y[r];
        lset(lds, L_C3 + wv, pv, lane);
    }
    if (!wearly) { const u32 es = lwait4(lds, L_PR, 1u); WIDEN(es); }
    {
        u4 d[4];
        NOUNROLL for (int spin = 0; spin < LSPIN_CAP; ++spin) {
            MEMBAR();
            const u4 c = *(const u4*)&lds[L_C3];
            MEMBAR();
            UNROLL for (int i = 0; i < 4; ++i) d[i] = l4[L_A2 / 4 + lr + i * 16];
            pin(d[0]);
            if (rfl(min4u(c)) >= pv) break;
        }
        STAMP(10);
        pin(WA); pin(WB);
        f4 xr[4], xi[4];
        u4 xx[4], ya[4], yb[4];
        UNROLL for (int i = 0; i < 4; ++i) {
            xr[i] = mfma16(FR, d[i]); xi[i] = mfma16(FI, d[i]);
            if (i >= 2) { UNROLL for (int r = 0; r < 4; ++r) xx[i - 2][r] = pkh(xr[i - 2][r], xi[i - 2][r]); pin(xx[i - 2]); }
            SB();
        }
        UNROLL for (int r = 0; r < 4; ++r) ya[0][r] = cmul_swap(xx[0][r], WA[0][r]);
        UNROLL for (int i = 2; i < 4; ++i) { UNROLL for (int r = 0; r < 4; ++r) xx[i][r] = pkh(xr[i][r], xi[i][r]); pin(xx[i]); }
        SB();
        f4 p0[4], p1[4], p2[4], p3[4];
        UNROLL for (int i = 0; i < 4; ++i) {
            p0[i] = mfma16(FR, ya[i]); p1[i] = mfma16(FI, ya[i]);
            if (i < 3) { UNROLL for (int r = 0; r < 4; ++r) ya[i + 1][r] = cmul_swap(xx[i + 1][r], WA[i + 1][r]); }
            else { UNROLL for (int r = 0; r < 4; ++r) yb[0][r] = cmul_swap(xx[0][r], WB[0][r]); }
            SB();
        }
        u4 ra[4], rb[4];
        UNROLL for (int i = 0; i < 4; ++i) {
            p2[i] = mfma16(FR, yb[i]); p3[i] = mfma16(FI, yb[i]);
            if (i < 3) { UNROLL for (int r = 0; r < 4; ++r) yb[i + 1][r] = cmul_swap(xx[i + 1][r], WB[i + 1][r]); }
            UNROLL for (int k = 0; k < 4; ++k) ra[i][k] = pkh(p0[k][i], p1[k][i]);
            *(u4*)&lds[L_B + lw + i * 64] = ra[i];
            SB();
        }
        UNROLL for (int i = 0; i < 3; ++i) UNROLL for (int r = 0; r < 4; ++r) rb[r][i] = pkh(p2[i][r], p3[i][r]);
        SB();
        UNROLL for (int r = 0; r < 4; ++r) { rb[r][3] = pkh(p2[3][r], p3[3][r]); *(u4*)&lds[L_B + 4096 + lw + r * 64] = rb[r]; }
        lset(lds, L_C4 + wv, pv, lane);
    }
    STAMP(11);
    {
        u4 da[4], db[4];
        NOUNROLL for (int spin = 0; spin < LSPIN_CAP; ++spin) {
            MEMBAR();
            const u4 c = *(const u4*)&lds[L_C4];
            MEMBAR();
            UNROLL for (int i = 0; i < 4; ++i) { da[i] = l4[L_B / 4 + lr + i * 16]; db[i] = l4[L_B / 4 + 1024 + lr + i * 16]; }
            pin(da[0]);
            if (rfl(min4u(c)) >= pv) break;
        }
        const int vo = 64 * q * 32768 + n * 8, so = WS_V + 4 * wv * 32768 + cls * 4096 + tile * 128;
        f4 p0[4], p1[4], p2[4], p3[4];
        UNROLL for (int i = 0; i < 5; ++i) {
            if (i < 4) { p0[i] = mfma16(HR[i], da[i]); p1[i] = mfma16(HI[i], da[i]); p2[i] = mfma16(HR[i], db[i]); p3[i] = mfma16(HI[i], db[i]); }
            if (i > 0) { UNROLL for (int r = 0; r < 4; ++r)
                __builtin_amdgcn_raw_buffer_store_b64(u2{mark(pkh(p0[i - 1][r], p1[i - 1][r]), vn, tm), mark(pkh(p2[i - 1][r], p3[i - 1][r]), vn, tm)}, rws, vo, so + (16 * r + i - 1) * 32768, SC1); }
            SB();
        }
    }
    if (wv == 0) lset(lds, L_P2, 1u, lane);
    STAMP(12);
    SB();
    {
        pin(xv);
        const f2 g01 = {bflo(gv.x), bfhi(gv.x)}, g23 = {bflo(gv.y), bfhi(gv.y)};
        f4 acc = {};
        UNROLL for (int i = 0; i < 4; ++i) {
            u4 pp;
            UNROLL for (int k = 0; k < 2; ++k) {
                const f2 a = f2{bflo(xv[2 * i + k].x), bfhi(xv[2 * i + k].x)} * g01, c = f2{bflo(xv[2 * i + k].y), bfhi(xv[2 * i + k].y)} * g23;
                pp[2 * k] = pkb(a.x, a.y); pp[2 * k + 1] = pkb(c.x, c.y);
            }
            const u32 s0 = n == 2 * i ? 0x3f803f80u : 0u, s1 = n == 2 * i + 1 ? 0x3f803f80u : 0u;
            acc = mfma16b(pp, u4{s0, s0, s1, s1}, acc);
        }
        if (n < 8) lds[L_GS + n * 16 + (t >> 4)] = __builtin_bit_cast(u32, (acc.x + acc.y) + (acc.z + acc.w));
        lset(lds, L_C5 + wv, pv, lane);
    }
    MR[0] = *(const u4*)&lds[L_M + lane * 4]; MR[1] = *(const u4*)&lds[L_M + 256 + lane * 4];
    UNROLL for (int i = 0; i < 4; ++i) TW[i] = *(const u4*)&lds[L_M + (2 + i) * 256 + lane * 4];
    core_expand(lane, MR, MI);
    pin(MR); pin(MI); pin(TW);
    u4 PM;
    {
        lwait4(lds, L_C5, pv);
        float gg[4];
        UNROLL for (int k = 0; k < 2; ++k) {
            const f4* pp = (const f4*)&lds[L_GS + ((n >> 2) * 2 + k) * 16];
            const f4 ps = (pp[0] + pp[1]) + (pp[2] + pp[3]);
            const float lg = rbf((ps.x + ps.y) + (ps.z + ps.w));
            gg[2 * k] = rbf(1.0f / (1.0f + __expf(-lg)));
            gg[2 * k + 1] = rbf(1.0f - gg[2 * k]);
        }
        UNROLL for (int k = 0; k < 4; ++k) { gg[k] *= 0.03125f; pin(gg[k]); }
        const int r = n & 3, sa = (n >> 2) * 2;
        UNROLL for (int cc = 0; cc < 2; ++cc) {
            const int u1 = 2 * q + cc;
            const float ta = (float)(sa * u1) * 0.125f + ((r & 1) ? 0.25f : (r & 2) ? 0.5f : 0.0f);
            const float tb = (float)((sa + 1) * u1) * 0.125f + ((r & 1) ? ((r & 2) ? 0.0f : 0.5f) : 0.25f);
            const float ca = cosr(ta), sa_ = sinr(ta), cb = cosr(tb), sb_ = sinr(tb);
            float e[4] = {gg[2] * cb, gg[2] * sb_, gg[3] * cb, gg[3] * sb_};
            pin(e);
            e[0] = fmaf(gg[0], ca, e[0]); pin(e[0]); e[1] = fmaf(gg[0], sa_, e[1]); pin(e[1]); e[2] = fmaf(gg[1], ca, e[2]); pin(e[2]); e[3] = fmaf(gg[1], sa_, e[3]);
            PM[2 * cc] = pkh(e[0], e[1]); PM[2 * cc + 1] = pkh(e[2], e[3]);
        }
    }
    int a3d = (q * 8 + wv) * 32 + n, a3m = (q * 8 + 7 - wv) * 32 + 16 - n, a3z = (q * 8 + 7 - wv) * 32 + ((32 - n) & 31), a3r = wv * 256 + lane;
    float osc = __builtin_bit_cast(float, (u32)min(max((int)ex + (int)ew - 130, 1), 254) << 23) * TRUNC_FIX;
    const int ve = 2 * q * 4096 + (128 * wv + n) * 8;
    int vs0 = WS_V + b * 32768, vs1 = vs0 + 4096, vsl = WS_VL + b * 16;
    asm volatile("" : "+s"(vs0), "+s"(vs1), "+s"(vsl));
    int ov = (4 * perm32(lane & 15) + 2 * ((lane >> 4) & 1) + (lane >> 5)) * 16 + (512 * wv + b) * 2048;
    u32 x8 = wv == 3 ? 0u : vn;
    pin(a3d); pin(a3m); pin(a3z); pin(a3r); pin(PM); pin(ov); pin(osc); pin(x8);
    STAMP(13);
    VMWAIT(0);
    {
        f4 z3[9];
        NOUNROLL for (int spin = 0; spin < SPIN_CAP; ++spin) {
            MEMBAR();
            u2 v0[8], v1[8];
            u4 ve8, vo8;
            int a_ = ve; pin(a_);
            if (wv == 3) {
                ve8 = __builtin_amdgcn_raw_buffer_load_b128(rws, q * 8192, vsl, SC1); vo8 = __builtin_amdgcn_raw_buffer_load_b128(rws, q * 8192 + 4096, vsl, SC1);
            } else {
                const u2 e2 = __builtin_amdgcn_raw_buffer_load_b64(rws, a_ + 1024, vs0, SC1), o2 = __builtin_amdgcn_raw_buffer_load_b64(rws, a_ + 1024, vs1, SC1);
                ve8 = u4{e2.x, e2.y, tag0, tag0}; vo8 = u4{o2.x, o2.y, tag0, tag0};
            }
            UNROLL for (int i = 0; i < 8; ++i) {
                v0[i] = __builtin_amdgcn_raw_buffer_load_b64(rws, a_ + 128 * i, vs0, SC1);
                v1[i] = __builtin_amdgcn_raw_buffer_load_b64(rws, a_ + 128 * i, vs1, SC1);
            }
            SB();
            u32 m = (((wv == 3 ? ve8.y : ve8.z) ^ tag0) | (ve8.w ^ tag0) | ((wv == 3 ? vo8.y : vo8.z) ^ tag0) | (vo8.w ^ tag0)) ? TM : 0u;
            UNROLL for (int k = 0; k < 9; ++k) {
                const int i = (k + 8) % 9;
                const u4 d = i == 8 ? u4{ve8.x, wv == 3 ? ve8.z : ve8.y, vo8.x, wv == 3 ? vo8.z : vo8.y} ^ x8 : u4{v0[i & 7].x, v0[i & 7].y, v1[i & 7].x, v1[i & 7].y} ^ vn;
                m |= d.x | d.y; m |= d.z | d.w;
                z3[i] = mfma16(PM, d);
                SB();
            }
            if (__all((m & TM) == 0u)) break;
        }
        STAMP(14);
        if (wv == 0 && n == 0) { z3[0].x = 0.5f * (z3[0].x + z3[0].z); z3[0].y = 0.5f * (z3[0].y + z3[0].w); }
        if (wv == 3) { z3[8].z = 0.5f * (z3[8].x + z3[8].z); z3[8].w = 0.5f * (z3[8].y + z3[8].w); }
        u4* l4w = (u4*)&lds[L_A1];
        UNROLL for (int p = 0; p < 2; ++p)
            l4w[a3d + 16 * p] = u4{pkh(z3[p].x, z3[p + 2].x), pkh(z3[p + 4].x, z3[p + 6].x), pkh(z3[p].y, z3[p + 2].y), pkh(z3[p + 4].y, z3[p + 6].y)};
        l4w[a3m] = u4{pkh(z3[7].z, z3[5].z), pkh(z3[3].z, z3[1].z), pkh(z3[7].w, z3[5].w), pkh(z3[3].w, z3[1].w)};
        f2 m[4];
        UNROLL for (int i = 0; i < 4; ++i) m[i] = n == 0 ? f2{z3[8 - 2 * i].z, z3[8 - 2 * i].w} : f2{z3[6 - 2 * i].z, z3[6 - 2 * i].w};
        l4w[a3z] = u4{pkh(m[0].x, m[1].x), pkh(m[2].x, m[3].x), pkh(m[0].y, m[1].y), pkh(m[2].y, m[3].y)};
        lset(lds, L_C6 + wv, 2u * pv - 1u, lane);
    }
    {
        u4 in[4];
        NOUNROLL for (int spin = 0; spin < LSPIN_CAP; ++spin) {
            MEMBAR();
            const u4 c = *(const u4*)&lds[L_C6];
            MEMBAR();
            UNROLL for (int s = 0; s < 4; ++s) in[s] = ((const u4*)&lds[L_A1])[a3r + s * 64];
            pin(in[0]);
            if (rfl(min4u(c)) >= 2u * pv - 1u) break;
        }
        f16v zr, zi;
        core_fft<false>(in, MR, MI, TW, zr, zi);
        zr *= osc; zi *= osc;
        const rsrc_t ro = mkrs(out, 2048 * 2048);
#define OUT_ROW(z, off) do { u4 lo, hi; \
            UNROLL for (int j = 0; j < 4; ++j) { const auto sw_ = __builtin_amdgcn_permlane16_swap(pkb(z[2 * j], z[2 * j + 1]), pkb(z[8 + 2 * j], z[8 + 2 * j + 1]), false, false); lo[j] = sw_[0]; hi[j] = sw_[1]; } \
            store128<OAUX>(lo, ro, ov + (off)); store128<OAUX>(hi, ro, ov + (off) + 1024); } while (0)
        OUT_ROW(zr, 256 * 2048);
        OUT_ROW(zi, 0);
    }
    STAMP(15);
    vd = lwait(lds, L_VD, 1u);
    if (pass == 1 || !(vd & 0x20000u)) {
        if (b == 0 && t == 0) __builtin_amdgcn_raw_buffer_store_b32(epoch, rws, 0, WS_E, SC1);
        break;
    }
    lset(lds, L_C6 + wv, 2u, lane);
    lwait4(lds, L_C6, 2u);
    VMWAIT(0);
    }
    STAMP_OUT();
}

extern "C" void kernel_launch(void* const* d_in, const int* in_sizes, int n_in,
                              void* d_out, int out_size, void* d_ws, size_t ws_size,
                              hipStream_t stream) {
    gfnet_fused<<<256, 512, 0, stream>>>(d_in[0], d_in[6], d_in[1], d_in[2], d_in[4], d_in[5], (char*)d_ws, d_out);
}
```

```cpp
#define VPOLL 2
#include <hip/hip_runtime.h>
#include <hip/hip_bf16.h>
#include <stdint.h>

typedef _Float16 h2 __attribute__((ext_vector_type(2)));
typedef _Float16 h8 __attribute__((ext_vector_type(8)));
typedef __bf16 b2 __attribute__((ext_vector_type(2)));
typedef __bf16 b8 __attribute__((ext_vector_type(8)));
typedef float f2 __attribute__((ext_vector_type(2)));
typedef float f4 __attribute__((ext_vector_type(4)));
typedef float f16v __attribute__((ext_vector_type(16)));
typedef short s4 __attribute__((ext_vector_type(4)));
typedef unsigned u32;
typedef u32 u2 __attribute__((ext_vector_type(2)));
typedef u32 u4 __attribute__((ext_vector_type(4)));
typedef unsigned short us2 __attribute__((ext_vector_type(2)));
typedef __amdgpu_buffer_rsrc_t rsrc_t;
#define DI __device__ __forceinline__
#define UNROLL _Pragma("unroll")
#define NOUNROLL _Pragma("unroll 1")
#define LAS __attribute__((address_space(3)))
#define SB() __builtin_amdgcn_sched_barrier(0)
#define MEMBAR() asm volatile("" ::: "memory")
#define VMWAIT(n) asm volatile("s_waitcnt vmcnt(%0)" :: "n"(n) : "memory")

#define MARGIN 2
#define FULLLINE 1
#define OAUX 16
#define QUICK 145

constexpr u32 FSTR = 16, HSTR = 32;
constexpr u32 WS_Z = 0;
constexpr u32 WS_V = 4u << 20;
constexpr u32 WS_VL = 12u << 20;
constexpr u32 WS_HB = WS_VL + (32u << 10);
constexpr u32 WS_F = WS_HB + (32u << 10);
constexpr u32 WS_E = WS_F + 2048 * FSTR;
constexpr u32 WS_BYTES = WS_E + 4096;
constexpr int SC1 = 16, NT = 2;
constexpr int SPIN_CAP = 8192, LSPIN_CAP = 1 << 17;
constexpr int OOB = 0x7ffffff0;
constexpr u32 TM = 0x00010001u;
constexpr float TRUNC_FIX = 1.00066f;

template <class T> DI void pin(T& v) { asm volatile("" : "+v"(v)); }
template <class T, int N> DI void pin(T (&v)[N]) { UNROLL for (int i = 0; i < N; ++i) pin(v[i]); }
DI u32 opq(u32 v) { asm("" : "+v"(v)); return v; }
DI u32 pkh(float lo, float hi) { h2 r; r.x = (_Float16)lo; r.y = (_Float16)hi; return __builtin_bit_cast(u32, r); }
DI u32 pkb(float lo, float hi) { b2 r; r.x = (__bf16)lo; r.y = (__bf16)hi; return __builtin_bit_cast(u32, r); }
DI float bflo(u32 v) { return __builtin_bit_cast(float, v << 16); }
DI float bfhi(u32 v) { return __builtin_bit_cast(float, v & 0xffff0000u); }
DI float rbf(float v) { return (float)(__bf16)v; }
DI h2 H(u32 v) { return __builtin_bit_cast(h2, v); }
DI u32 U(h2 v) { return __builtin_bit_cast(u32, v); }
DI f16v mfma32(u4 a, u4 b, f16v c) { return __builtin_amdgcn_mfma_f32_32x32x16_f16(__builtin_bit_cast(h8, a), __builtin_bit_cast(h8, b), c, 0, 0, 0); }
DI f4 mfma16(u4 a, u4 b) { return __builtin_amdgcn_mfma_f32_16x16x32_f16(__builtin_bit_cast(h8, a), __builtin_bit_cast(h8, b), f4{}, 0, 0, 0); }
DI f4 mfma16b(u4 a, u4 b, f4 c) { return __builtin_amdgcn_mfma_f32_16x16x32_bf16(__builtin_bit_cast(b8, a), __builtin_bit_cast(b8, b), c, 0, 0, 0); }
DI u32 hmul(u32 a, u32 b) { return U(H(a) * H(b)); }
DI u32 cmul_swap(u32 x, u32 w) { const h2 a = H(opq(x)), c = H(opq(w)); h2 n = c; n.y = -n.y; return U(__builtin_elementwise_fma(a.yy, n, a.xx * c.yx)); }
DI u32 cmul_conj(u32 x, u32 t) { const h2 a = H(opq(x)), c = H(opq(t)); h2 n = c.yy; n.y = -n.y; return U(__builtin_elementwise_fma(a.yx, n, a * c.xx)); }
DI u32 pkmax(u32 a, u32 b) { return __builtin_bit_cast(u32, __builtin_elementwise_max(__builtin_bit_cast(us2, a), __builtin_bit_cast(us2, b))); }
DI float sinr(float r) { return __builtin_amdgcn_sinf(r); }
DI float cosr(float r) { return __builtin_amdgcn_cosf(r); }
DI u32 rot(float ph) { return pkh(cosr(ph), sinr(ph)); }
DI u32 quarter(u32 r, u32 f) { u32 o; asm("v_pk_mul_f16 %0, %1, %2 op_sel:[1,0] op_sel_hi:[0,1] neg_lo:[1,0]" : "=v"(o) : "v"(r), "v"(f)); return o; }
DI u4 quarter(u4 r) { u32 one = 0x3c003c00u; pin(one); return u4{quarter(r.x, one), quarter(r.y, one), quarter(r.z, one), quarter(r.w, one)}; }
DI int perm32(int n) { return (n & 0x13) | ((n & 4) << 1) | ((n & 8) >> 1); }
DI u32 max4u(u4 v) { return max(max(v.x, v.y), max(v.z, v.w)); }
DI u32 min4u(u4 v) { return min(min(v.x, v.y), min(v.z, v.w)); }
DI u32 mark(u32 v, u32 par, u32 tm) { return (par & tm) | (v & ~tm); }
DI u32 rfl(u32 v) { return __builtin_amdgcn_readfirstlane(v); }
DI rsrc_t mkrs(const void* p, u32 bytes) { return __builtin_amdgcn_make_buffer_rsrc((void*)p, (short)0, (int)bytes, 0x00020000); }
template <int AUX> DI void store128(u4 v, rsrc_t r, int off) { __builtin_amdgcn_raw_buffer_store_b128(v, r, off, 0, AUX); }
template <int CTRL> DI u32 dpp(u32 v) { return __builtin_amdgcn_update_dpp(0u, v, CTRL, 0xf, 0xf, true); }
DI u32 wave_maxu(u32 v) {
    v = max(v, dpp<0xB1>(v)); v = max(v, dpp<0x4E>(v)); v = max(v, dpp<0x141>(v)); v = max(v, dpp<0x140>(v));
    return max(max((u32)__builtin_amdgcn_readlane(v, 0), (u32)__builtin_amdgcn_readlane(v, 16)), max((u32)__builtin_amdgcn_readlane(v, 32), (u32)__builtin_amdgcn_readlane(v, 48)));
}
#define STAMP_DECL
#define STAMP(i)
#define STAMP_OUT()

DI void core_setup(int lane, int s, u4& tw, u2& m) {
    float fc = (float)perm32(lane & 31) * (1.0f / 1024.0f), p0 = (float)(4 * (lane >> 5) + 8 * s) * fc, p1 = (float)(4 * (lane >> 5) + 8 * (s >> 1) + 2 * (s & 1)) * fc * 32.0f;
    pin(fc); pin(p0); pin(p1);
    float tc[4], ts[4];
    UNROLL for (int i = 0; i < 4; ++i) { const float pt = fmaf((float)i, fc, p0); tc[i] = cosr(pt); ts[i] = sinr(pt); }
    tw = u4{pkh(tc[0], tc[1]), pkh(tc[2], tc[3]), pkh(ts[0], ts[1]), pkh(ts[2], ts[3])};
    const float p2 = fmaf(32.0f, fc, p1);
    m = u2{pkh(cosr(p1), cosr(p2)), pkh(sinr(p1), sinr(p2))};
}
DI void core_expand(int lane, u4 (&MR)[4], u4 (&MI)[4]) {
    const u32 sg = (lane & 1) ? 0x80008000u : 0u;
    MR[2] = MR[0] ^ sg; MR[3] = MR[1] ^ sg;
    UNROLL for (int s = 0; s < 4; ++s) MI[s] = u4{MR[s].z ^ 0x80008000u, MR[s].w ^ 0x80008000u, MR[s].x, MR[s].y};
}
template <bool OUT_B> DI void core_fft(const u4 (&in)[4], const u4 (&MR)[4], const u4 (&MI)[4], const u4 (&TW)[4], f16v& outr, f16v& outi) {
    f16v yr = {}, yi = {};
    UNROLL for (int s = 0; s < 4; ++s) yr = mfma32(in[s], MR[s], yr);
    SB();
    yi = mfma32(in[0], MI[0], yi); yi = mfma32(in[1], MI[1], yi);
    SB();
    h2 t0[8], t1[8];
#define TW_RE(p) do { const h2 pr_ = H(pkh(yr[2 * (p)], yr[2 * (p) + 1])); t0[p] = pr_ * H(TW[(p) >> 1][(p) & 1]); t1[p] = pr_ * H(TW[(p) >> 1][2 + ((p) & 1)]); } while (0)
    TW_RE(0); TW_RE(1); SB();
    yi = mfma32(in[2], MI[2], yi);
    TW_RE(2); TW_RE(3); SB();
    yi = mfma32(in[3], MI[3], yi);
    TW_RE(4); TW_RE(5); TW_RE(6); TW_RE(7); SB();
    outr = f16v{}; outi = f16v{};
    u4 d[4];
#define TW_IM(s) do { UNROLL for (int j = 0; j < 2; ++j) { const int p_ = 2 * (s) + j; const h2 pi_ = H(pkh(yi[2 * p_], yi[2 * p_ + 1])); \
        d[s][j] = U(__builtin_elementwise_fma(pi_, H(TW[s][2 + j]), t0[p_])); d[s][2 + j] = U(__builtin_elementwise_fma(pi_, H(TW[s][j]), -t1[p_])); } } while (0)
    TW_IM(0); SB();
    UNROLL for (int s = 0; s < 4; ++s) {
        outr = OUT_B ? mfma32(MR[s], d[s], outr) : mfma32(d[s], MR[s], outr);
        if (s < 3) TW_IM(s + 1);
        SB();
    }
    UNROLL for (int s = 0; s < 4; ++s) outi = OUT_B ? mfma32(MI[s], d[s], outi) : mfma32(d[s], MI[s], outi);
}

DI u32 tag_of(u32 epoch, u32 idx) { u32 h = (epoch ^ (idx * 0x9E3779B1u)) * 0x85EBCA6Bu; return h ^ (h >> 15); }
DI u32 valid_bits(u32 epoch, int slot) { return (((0x6A3Cu >> slot) ^ epoch) & 1u) ? 0x00000001u : 0x00010000u; }
DI int cls_of(int b) { const int row = (b & 7) >> 1; return row == 0 ? 4 * (b >> 7) : ((b & 1) ? 8 - row : row); }
DI int tile_of(int b) { return (b & 6) == 0 ? 16 * (b & 1) + ((b >> 3) & 15) : b >> 3; }

constexpr int L_C1 = 0, L_C3 = 4, L_C4 = 8, L_C5 = 12, L_C6 = 16;
constexpr int L_EW = 20;
constexpr int L_WW = 24;
constexpr int L_PR = 28;
constexpr int L_LD = 32;
constexpr int L_SX = 36;
constexpr int L_ZL = 37;
constexpr int L_VD = 38;
constexpr int L_P2 = 39;
constexpr int L_GS = 64;
constexpr int L_M = 192;
constexpr int L_A1 = L_M + 1536;
constexpr int L_A2 = L_A1 + 4096;
constexpr int L_B = L_A2 + 4096;
constexpr int L_W = L_B + 8192;
constexpr int L_END = L_W + 64 * 256;

DI void lset(u32* lds, int idx, u32 v, int lane) { MEMBAR(); if (lane == 0) lds[idx] = v; MEMBAR(); }
DI u32 lget(u32* lds, int idx) { MEMBAR(); return rfl(lds[idx]); }
DI u32 lwait(u32* lds, int idx, u32 atleast) {
    u32 v = 0;
    NOUNROLL for (int spin = 0; spin < LSPIN_CAP; ++spin) { v = lget(lds, idx); if (v >= atleast) break; __builtin_amdgcn_s_sleep(1); }
    MEMBAR();
    return v;
}
DI u32 lwait4(u32* lds, int idx, u32 atleast) {
    u4 v;
    NOUNROLL for (int spin = 0; spin < LSPIN_CAP; ++spin) { MEMBAR(); v = *(const u4*)&lds[idx]; if (rfl(min4u(v)) >= atleast) break; __builtin_amdgcn_s_sleep(1); }
    MEMBAR();
    return rfl(max4u(v));
}
struct MagMax {
    h2 hi = {}, lo = {};
    DI void add(u32 a, u32 c) {
        hi = __builtin_elementwise_maximum(__builtin_elementwise_maximum(hi, H(a)), H(c));
        lo = __builtin_elementwise_minimum(__builtin_elementwise_minimum(lo, H(a)), H(c));
    }
    DI u32 exponent() const {
        const u32 mx = pkmax(U(hi) & 0x7fff7fffu, U(lo) & 0x7fff7fffu);
        return wave_maxu(max(mx & 0xffffu, mx >> 16)) >> 7;
    }
};
DI u32 clampe(u32 e) { return min(max(e, 8u), 240u); }

__global__ __launch_bounds__(512, 2) void gfnet_fused(const void* x, const void* gw, const void* war, const void* wai, const void* wbr, const void* wbi,
                                                      char* ws, void* out) {
    __shared__ __attribute__((aligned(16))) u32 lds[L_END];
    STAMP_DECL;
    STAMP(0);
    u32 code;
    {
        unsigned long long pc0; int len;
        asm volatile("s_getpc_b64 %0\n.Lgf_begin:\n\ts_mov_b32 %1, .Lgf_end-.Lgf_begin" : "=s"(pc0), "=s"(len));
        code = __builtin_amdgcn_raw_buffer_load_b32(mkrs((const void*)pc0, (u32)max(len, 0)), (threadIdx.x >> 6) == 4 ? ((blockIdx.x >> 5) * 64 + (threadIdx.x & 63)) * 64 : OOB, 0, 0);
        SB();
    }
    asm volatile("" :: "s"(x), "s"(gw), "s"(war), "s"(wai), "s"(wbr), "s"(wbi), "s"(ws), "s"(out));
    const int wv0 = __builtin_amdgcn_readfirstlane(threadIdx.x >> 6);
    const rsrc_t rws = mkrs(ws, WS_BYTES);

    if (wv0 >= 4) {
        const u32 tstart = (u32)__builtin_amdgcn_s_memrealtime();
        const int wv = wv0, b = blockIdx.x, cls = cls_of(b), tile = tile_of(b);
        const int lane = threadIdx.x & 63, q = lane >> 4, n = lane & 15;
        const rsrc_t rw = mkrs(wv == 4 ? war : wv == 5 ? wai : wv == 6 ? wbr : wbi, 2048 * 1026);
        if (wv == 4) { lds[lane] = 0u; asm volatile("s_waitcnt lgkmcnt(0)" ::: "memory"); }
        MEMBAR();
        __builtin_amdgcn_s_barrier();
        MEMBAR();
        const bool helper = wv == 7 && tile == 31;
        u4 GR, GI, FR, FI, HR, HI, T1, T2;
        u32 ev;
        {
            const int wvo = (lane >> 2) * (128 * 1026) + min(lane & 3, 2) * 16, wso = (cls * 513 + 16 * tile - cls) * 2;
#define WLOAD(r) do { UNROLL for (int st = 0; st < 4; ++st) { const int c_ = 4 * st + ((tile + (r)) & 3); \
                __builtin_amdgcn_raw_ptr_buffer_load_lds(rw, (LAS void*)&lds[L_W + (c_ * 4 + wv - 4) * 256], 16, wvo, wso + c_ * 8208, 0, 0); } SB(); } while (0)
            __builtin_amdgcn_s_sleep(2);
            WLOAD(0);
            const bool split = (b & 6) == 0;
            const int er = split ? 16 * (tile & 15) + ((lane >> 1) & 15) : 8 * tile + ((lane >> 1) & 7);
            const int ec = (split ? (tile & 16) * 16 : 0) - cls + ((lane & 1) ? (split ? 262 : 518) : 0);
            u32 edge = __builtin_amdgcn_raw_buffer_load_b32(rw, ((cls + 8 * er) * 513 + ec) * 2, 0, 0);
            ev = __builtin_amdgcn_raw_buffer_load_b32(rws, 0, WS_E, SC1);
            const u4 smp = __builtin_amdgcn_raw_buffer_load_b128(rw, (lane >> 3) * (2 * 131328 + 128) + (lane & 7) * 16, 0, 0);
            MEMBAR();
            if (helper) {
                UNROLL for (int j = 0; j < 4; ++j) {
                    GR[j] = rot((float)((4 * q + j) * (cls + 8 * n)) * (1.0f / 128.0f) + (cls == 4 ? 0.25f : 0.0f));
                    HR[j] = rot((float)(n * (cls + 8 * (4 * q + j))) * (1.0f / 128.0f));
                    FR[j] = rot((float)(n * (4 * q + j)) * (1.0f / 16.0f));
                    T1[j] = rot((float)((4 * q + j) * (cls + 8 * n)) * (1.0f / 2048.0f));
                    T2[j] = rot((float)(n * (cls + 8 * (4 * q + j))) * (1.0f / 2048.0f));
                }
                GI = quarter(GR); HI = quarter(HR); FI = quarter(FR);
                pin(GR); pin(GI); pin(HR); pin(HI); pin(FR); pin(FI); pin(T1); pin(T2);
            }
            VMWAIT(0);
            STAMP(1);
            if ((int)((u32)__builtin_amdgcn_s_memrealtime() - tstart) < QUICK)
                __builtin_amdgcn_raw_buffer_store_b32(0u, rws, lane * 128, WS_V + b * 32768 + (wv - 4) * 8192, SC1);
            MEMBAR();
            WLOAD(1); WLOAD(2); WLOAD(3);
            VMWAIT(12);
            asm volatile("ds_write_b32 %0, %1" :: "v"((u32)(size_t)(LAS u32*)&lds[L_LD + wv - 4]), "v"(1u) : "memory");
            pin(edge); pin(code);
            MagMax ms; ms.add(smp.x, smp.y); ms.add(smp.z, smp.w);
            const u32 es = clampe(ms.exponent() + MARGIN);
            VMWAIT(0);
            lset(lds, L_PR + wv - 4, es, lane);
            STAMP(2);
        }
        const u32 ep0 = rfl(ev) + 1u;
        if (wv == 5 || wv == 6) {
            lwait4(lds, L_PR, 1u);
            MagMax m;
            UNROLL for (int c = 0; c < 32; ++c) { const u4 v = *(const u4*)&lds[L_W + ((wv - 5) * 32 + c) * 256 + lane * 4]; m.add(v.x, v.y); m.add(v.z, v.w); }
            const u32 e = clampe(m.exponent());
            MEMBAR(); if (lane == 0) *(u2*)&lds[L_WW + 2 * (wv - 5)] = u2{e, e}; MEMBAR();
        } else if (wv == 4) {
            const u32 ex = lwait4(lds, L_EW, 1u), ew = lwait4(lds, L_WW, 1u);
            __builtin_amdgcn_raw_buffer_store_b64(u2{ex | (ew << 8), tag_of(ep0, b + 1)}, rws, lane < 8 ? lane * (int)(256 * FSTR) : OOB, WS_F + b * FSTR, SC1);
            if (VPOLL == 0) { lwait(lds, L_P2, 1u); __builtin_amdgcn_s_sleep(24); }
            if (VPOLL == 1) lwait4(lds, L_C3, 1u);
            if (VPOLL == 2) { lwait(lds, L_P2, 1u); __builtin_amdgcn_s_sleep(8); }
            u2 f[4]; u32 tg[4];
            UNROLL for (int j = 0; j < 4; ++j) tg[j] = tag_of(ep0, 4 * lane + j + 1);
            NOUNROLL for (int spin = 0; spin < SPIN_CAP; ++spin) {
                MEMBAR();
                UNROLL for (int j = 0; j < 4; ++j) f[j] = __builtin_amdgcn_raw_buffer_load_b64(rws, lane * (4 * FSTR) + j * FSTR, WS_F + (b & 7) * (256 * FSTR), SC1);
                if (__all(f[0].y == tg[0] && f[1].y == tg[1] && f[2].y == tg[2] && f[3].y == tg[3])) break;
                __builtin_amdgcn_s_sleep(8);
            }
            const u4 e4 = {f[0].x, f[1].x, f[2].x, f[3].x};
            const u32 gx = wave_maxu(max4u(e4 & 0xffu)), gwm = wave_maxu(max4u((e4 >> 8) & 0xffu));
            const bool redo = gx > lget(lds, L_SX) || gwm > lwait4(lds, L_PR, 1u);
            lset(lds, L_VD, gx | (gwm << 8) | (redo ? 0x20000u : 0x10000u), lane);
            if (b == 0 && lane == 0) __builtin_amdgcn_raw_buffer_store_b32(ep0 + (redo ? 1u : 0u), rws, 0, WS_E, SC1);
            STAMP(5);
        } else if (helper) {
            __builtin_amdgcn_s_setprio(2);
            u32 ew = lwait4(lds, L_PR, 1u);
            NOUNROLL for (u32 pass = 0; pass < 2; ++pass) {
                u4 WH[2];
                const float sw = __builtin_bit_cast(float, (249u - ew) << 23);
                UNROLL for (int r = 0; r < 4; ++r) UNROLL for (int f = 0; f < 2; ++f) {
                    const unsigned short* wp = (const unsigned short*)&lds[L_W + (n * 4 + 2 * f) * 256 + (4 * q + r) * 16] + 16 + cls;
                    float re = bflo(wp[0]) * sw, im = bflo(wp[512]) * sw;
                    pin(re);
                    WH[f][r] = pkh(re, im);
                }
                pin(WH);
                STAMP(3);
                lwait(lds, L_ZL, pass + 1u);
                const u32 tg = tag_of(ep0 + pass, 0u);
                u4 hz;
                NOUNROLL for (int spin = 0; spin < SPIN_CAP; ++spin) {
                    MEMBAR();
                    u32 bad = 0;
                    UNROLL for (int j = 0; j < 4; ++j) { const u2 g = __builtin_amdgcn_raw_buffer_load_b64(rws, (64 * q + n + 16 * j) * HSTR, WS_HB + ((b & 7) >> 1) * (256 * HSTR), SC1); hz[j] = g.x & ~TM; bad |= g.y ^ tg; }
                    if (__all(bad == 0u)) break;
                    __builtin_amdgcn_s_sleep(4);
                }
                STAMP(4);
                const u32 f16 = cls == 0 ? 0x00002c00u : cls == 4 ? 0x2c000000u : cls > 4 ? 0xac002c00u : 0x2c002c00u;
                u4 d, o[2];
                UNROLL for (int j = 0; j < 4; ++j) d[j] = hmul(hz[j], f16);
                f4 yr = mfma16(d, GR), yi = mfma16(d, GI);
                UNROLL for (int r = 0; r < 4; ++r) d[r] = cmul_conj(pkh(yr[r], yi[r]), T1[r]);
                const f4 xr = mfma16(FR, d), xi = mfma16(FI, d);
                UNROLL for (int f = 0; f < 2; ++f) {
                    UNROLL for (int r = 0; r < 4; ++r) d[r] = cmul_swap(pkh(xr[r], xi[r]), WH[f][r]);
                    yr = mfma16(d, FR); yi = mfma16(d, FI);
                    UNROLL for (int r = 0; r < 4; ++r) o[f][r] = cmul_conj(pkh(yr[r], yi[r]), T2[r]);
                }
                const f4 p0 = mfma16(HR, o[0]), p1 = mfma16(HI, o[0]), p2 = mfma16(HR, o[1]), p3 = mfma16(HI, o[1]);
                UNROLL for (int r = 0; r < 4; ++r)
                    store128<SC1>(u4{pkh(p0[r], p1[r]) & ~TM, tg, pkh(p2[r], p3[r]) & ~TM, tg}, rws, (64 * q + n) * 16 + WS_VL + r * 256 + cls * 4096);
                STAMP(5);
                const u32 vd = lwait(lds, L_VD, 1u);
                if (!(vd & 0x20000u)) break;
                ew = (vd >> 8) & 0xffu;
            }
        }
        asm volatile(".Lgf_end:");
        STAMP_OUT();
        return;
    }

    u32 vd = 0, epoch = 0;
    NOUNROLL for (u32 pass = 0; pass < 2; ++pass) {
    int t = threadIdx.x, wv = wv0, b = blockIdx.x;
    pin(t);
    asm volatile("" : "+s"(wv));
    asm volatile("" : "+s"(b));
    const int lane = t & 63, q = lane >> 4, n = lane & 15;
    const u32 pv = pass + 1u;
    u2 xv[8];
    const rsrc_t rx = mkrs(x, 2048 * 2048);
    const u4 smp = __builtin_amdgcn_raw_buffer_load_b128(rx, (lane >> 3) * (524288 + 2048 + 256) + (lane & 7) * 16, 0, 0);
    UNROLL for (int s1 = 0; s1 < 8; ++s1) xv[s1] = __builtin_amdgcn_raw_buffer_load_b64(rx, t * 8, (256 * s1 + b) * 2048, NT);
    const u32 ev = __builtin_amdgcn_raw_buffer_load_b32(rws, 0, WS_E, SC1);
    const u2 gv = __builtin_amdgcn_raw_buffer_load_b64(mkrs(gw, 2048), t * 8, 0, 0);
    MEMBAR();
    SB();
    if (pass == 0) { __builtin_amdgcn_s_barrier(); MEMBAR(); __builtin_amdgcn_s_setprio(1); }
    u4 MR[4], MI[4], TW[4];
    {
        u4 tw; u2 m;
        core_setup(lane, wv, tw, m);
        *(u4*)&lds[L_M + (2 + wv) * 256 + lane * 4] = tw;
        lds[L_M + (wv >> 1) * 256 + lane * 4 + (wv & 1)] = m.x; lds[L_M + (wv >> 1) * 256 + lane * 4 + 2 + (wv & 1)] = m.y;
    }
    const int zc = perm32(lane & 31) + (lane >> 5) * 256, zcu = zc + ((lane & 31) == 0 ? 543 : 511);
    int zl = (zc >> 4) * 16384 + (zc & 15) * 4, zu = (zcu >> 4) * 16384 + (zcu & 15) * 4;
    int zh = lane == 0 ? 0 : OOB;
    int tro = L_A1 + wv * 1024 + (4 * (lane >> 5) + ((lane & 15) >> 2)) * 16 + ((lane >> 4) & 1) * 8 + (lane & 3) * 2;
    u32 tm = TM, vl0 = valid_bits(0u, zc & 15), vu0 = valid_bits(0u, zcu & 15);
    pin(zl); pin(zu); pin(zh); pin(tro); pin(tm); pin(vl0); pin(vu0);
    STAMP(1);
    SB();
    pin(code);
    u32 ex;
    {
        MagMax ms; ms.add(smp.x, smp.y); ms.add(smp.z, smp.w);
        ex = pass ? (vd & 0xffu) : clampe(ms.exponent() + MARGIN);
    }
    STAMP(2);
    u32 tag0, flip;
    u32 z[16], z512;
    {
        const float sc = __builtin_bit_cast(float, (250u - ex) << 23);
        const float rs = sc * 0.70710678118654752f;
        u32 o[8][2];
        UNROLL for (int j = 0; j < 2; ++j) {
            f2 v[8];
            UNROLL for (int s1 = 0; s1 < 8; ++s1) v[s1] = f2{bflo(xv[s1][j]), bfhi(xv[s1][j])};
            const f2 a0 = v[0] + v[4], a1 = v[1] + v[5], a2 = v[2] + v[6], a3 = v[3] + v[7];
            const f2 b0 = (v[0] - v[4]) * sc, b1 = v[1] - v[5], b2_ = (v[2] - v[6]) * sc, b3 = v[3] - v[7];
            const f2 e0 = a0 + a2, e1 = (a1 + a3) * sc, dm = b1 - b3, dp = b1 + b3;
            f2 r[8];
            r[0] = e0 * sc + e1; r[1] = e0 * sc - e1;
            r[2] = dm * rs + b0; r[3] = -(dp * rs) - b2_;
            r[4] = (a0 - a2) * sc; r[5] = (a3 - a1) * sc;
            r[6] = b0 - dm * rs; r[7] = b2_ - dp * rs;
            UNROLL for (int p = 0; p < 8; ++p) o[p][j] = pkh(r[p].x, r[p].y);
        }
        UNROLL for (int p = 0; p < 8; ++p) *(u2*)&lds[L_A1 + p * 512 + 2 * t] = u2{o[p][0], o[p][1]};
        lset(lds, L_C1 + wv, pv, lane);
        epoch = (pass ? epoch : rfl(ev)) + 1u; tag0 = tag_of(epoch, 0u); flip = (epoch & 1u) ? TM : 0u;
        const u32 vl = vl0 ^ flip, vu = vu0 ^ flip;
        u4 in[4];
        NOUNROLL for (int spin = 0; spin < LSPIN_CAP; ++spin) {
            MEMBAR();
            const u4 c = *(const u4*)&lds[L_C1];
            MEMBAR();
            MR[0] = *(const u4*)&lds[L_M + lane * 4]; MR[1] = *(const u4*)&lds[L_M + 256 + lane * 4];
            UNROLL for (int s = 0; s < 4; ++s) {
                const u2 re = __builtin_bit_cast(u2, __builtin_amdgcn_ds_read_tr16_b64_v4i16((LAS s4*)&lds[tro + s * 128]));
                const u2 im = __builtin_bit_cast(u2, __builtin_amdgcn_ds_read_tr16_b64_v4i16((LAS s4*)&lds[tro + s * 128 + 512]));
                in[s] = u4{re.x, re.y, im.x, im.y};
            }
            pin(MR[0]); pin(MR[1]);
            if (rfl(min4u(c)) >= pv) break;
        }
        UNROLL for (int i = 0; i < 4; ++i) TW[i] = *(const u4*)&lds[L_M + (2 + i) * 256 + lane * 4];
        STAMP(3);
        core_expand(lane, MR, MI);
        f16v zr, zi;
        core_fft<true>(in, MR, MI, TW, zr, zi);
        UNROLL for (int r = 0; r < 16; ++r) z[r] = pkh(zr[r], zi[r]);
        z512 = z[8];
        const u32 za = __builtin_amdgcn_readlane(z[8], 32), zb = __builtin_amdgcn_readlane(z[0], 0);
        MEMBAR();
        const u32 zt = lane < 32 ? za : zb;
        UNROLL for (int r = 0; r < 8; ++r) z[r] = mark(z[r], vl, tm);
        UNROLL for (int r = 8; r < 16; ++r) z[r] = mark((lane & 31) == 0 ? (r < 15 ? z[r + 1] : zt) : z[r], vu, tm);
    }
    pin(z);
    STAMP(4);
    const int zs = WS_Z + wv * 64 * 16384 + (((b >> 2) & 3) * 64 + (b & 3) * 16 + ((b >> 4) & 3) * 4 + (b >> 6)) * 64;
    bool zsent = false;
#define TRY_STORE_Z(force) do { if (!zsent) { \
        bool go = true; \
        if (force) lwait4(lds, L_LD, 1u); else { MEMBAR(); go = rfl(min4u(*(const u4*)&lds[L_LD])) != 0u; MEMBAR(); } \
        if (go) { \
            UNROLL for (int r = 0; r < 16; ++r) __builtin_amdgcn_raw_buffer_store_b32(z[r], rws, r < 8 ? zl : zu, zs + (r & 7) * 32768, SC1); \
            __builtin_amdgcn_raw_buffer_store_b64(u2{z512, tag0}, rws, zh, WS_HB + (wv * 256 + b) * HSTR, SC1); \
            zsent = true; } } SB(); } while (0)
    TRY_STORE_Z(false);
    const int row = (b & 7) >> 1, cls = cls_of(b), tile = tile_of(b);
    const bool packed = row == 0;
    u4 FR, FI, GR[4], GI[4], HR[4], HI[4], sel1;
    {
        const float ofs = cls == 4 ? 0.25f : 0.0f;
        float gu = (float)(cls + 8 * n) * (1.0f / 2048.0f), g0 = (float)(64 * q + 4 * wv) * gu + ofs;
        u32 s16 = 0x2c002c00u;
        pin(gu); pin(g0); pin(s16);
        UNROLL for (int i = 0; i < 4; ++i) UNROLL for (int j = 0; j < 4; ++j) { const u32 g = rot(fmaf((float)(16 * j + i), gu, g0)); GR[i][j] = hmul(g, s16); GI[i][j] = quarter(g, s16); }
    }
    pin(GR); pin(GI);
    {
        MagMax m;
        UNROLL for (int s1 = 0; s1 < 8; ++s1) m.add(xv[s1].x, xv[s1].y);
        if (wv == 0) lset(lds, L_SX, ex, lane);
        lset(lds, L_EW + wv, clampe(m.exponent()), lane);
    }
    TRY_STORE_Z(false);
    UNROLL for (int j = 0; j < 4; ++j) FR[j] = rot((float)(n * (4 * q + j)) * (1.0f / 16.0f));
    FI = quarter(FR);
    {
        float hs[4], hu[4];
        UNROLL for (int i = 0; i < 4; ++i) { hs[i] = (float)(16 * n + 4 * wv + i) * (1.0f / 2048.0f); hu[i] = (float)(cls + 32 * q + 8 * i); }
        pin(hs); pin(hu);
        UNROLL for (int i = 0; i < 4; ++i) { UNROLL for (int j = 0; j < 4; ++j) { float a = hs[i] * hu[j]; pin(a); HR[i][j] = rot(a); } HI[i] = quarter(HR[i]); }
        const int d = n + cls - 8 * q;
        UNROLL for (int j = 0; j < 4; ++j) sel1[j] = (d >> 1) == j ? ((d & 1) ? 0xffff0000u : 0xffffu) : 0u;
    }
    pin(FR); pin(FI); pin(HR); pin(HI); pin(sel1);
    TRY_STORE_Z(true);
    STAMP(5);
    VMWAIT(4);
    u4 WA[4], WB[4];
    u32 ew = 0;
    UNROLL for (int i = 0; i < 4; ++i) asm volatile("" : "=v"(WA[i]), "=v"(WB[i]));
#define WIDEN(es) do { \
        u4 wr[4][4]; \
        int wl = L_W / 4 + wv * 1024 + n * 4 + min(q, 2); pin(wl); \
        UNROLL for (int i = 0; i < 4; ++i) UNROLL for (int a = 0; a < 4; ++a) wr[i][a] = ((const u4*)lds)[wl + (i * 4 + a) * 64]; \
        ew = pass ? (vd >> 8) & 0xffu : (es); \
        const u32 sw = ((249u - ew - (packed ? 1u : 0u)) << 7) * 0x10001u;       \
        const u4 sels = sel1 & sw; \
        SB(); \
        f4 wf[4][4]; \
        UNROLL for (int k = 0; k < 10; ++k) { \
            if (k < 8) { wf[k >> 1][2 * (k & 1)] = mfma16b(wr[k >> 1][2 * (k & 1)], sels, f4{}); wf[k >> 1][2 * (k & 1) + 1] = mfma16b(wr[k >> 1][2 * (k & 1) + 1], sels, f4{}); } \
            if (k >= 2) { const int i_ = (k - 2) >> 1, f_ = k & 1; UNROLL for (int r = 0; r < 4; ++r) (f_ ? WB : WA)[i_][r] = pkh(wf[i_][2 * f_][r], wf[i_][2 * f_ + 1][r]); } \
            SB(); } \
        pin(WA); pin(WB); } while (0)
    MEMBAR();
    const u4 pr0 = *(const u4*)&lds[L_PR];
    const bool wearly = rfl(min4u(pr0)) != 0u;
    MEMBAR();
    if (wearly) WIDEN(rfl(max4u(pr0)));
    STAMP(6);
    const u32 vn = valid_bits(epoch, n), vm = valid_bits(epoch, 15 - n);
    u32 x1 = cls > 4 ? vm ^ 0x80000000u : vn, x2 = vm ^ (cls == 4 ? 0x00008000u : cls == 0 ? 0x80000000u : 0u);
    u32 z1[4][4], z2[4][4];
    const int zvm = q * 64 + (63 - tile) * 16384 + (15 - n) * 4;
    const int zv1 = cls <= 4 ? q * 64 + tile * 16384 + n * 4 : zvm;
    int zso = WS_Z + row * 64 * 16384 + wv * 4096;
    asm volatile("" : "+s"(zso));
#define LOAD_Z() do { \
        if (packed) { int a_ = zvm; pin(a_); UNROLL for (int i = 0; i < 4; ++i) UNROLL for (int j = 0; j < 4; ++j) z2[i][j] = __builtin_amdgcn_raw_buffer_load_b32(rws, a_ + (4 * i + j) * 256, zso, SC1); } \
        else { UNROLL for (int i = 0; i < 4; ++i) UNROLL for (int j = 0; j < 4; ++j) z2[i][j] = x2; } \
        int c_ = zv1; pin(c_); UNROLL for (int i = 0; i < 4; ++i) UNROLL for (int j = 0; j < 4; ++j) z1[i][j] = __builtin_amdgcn_raw_buffer_load_b32(rws, c_ + (4 * i + j) * 256, zso, SC1); } while (0)
    pin(x1); pin(x2);
    VMWAIT(0);
    LOAD_Z();
    if (wv == 0) lset(lds, L_ZL, pv, lane);
    SB();
    STAMP(7);
    const int lw = ((wv * 16 + 4 * q) * 16 + n) * 4;
    int lr = (q * 16 + 4 * wv) * 16 + n;
    const u4* l4 = (const u4*)lds;
    pin(lr);
    STAMP(8);
    SB();
    {
        f4 yr[4], yi[4];
        NOUNROLL for (int spin = 0; spin < SPIN_CAP; ++spin) {
            u32 m = 0;
            UNROLL for (int i = 0; i < 4; ++i) {
                u4 d;
                UNROLL for (int j = 0; j < 4; ++j) { z1[i][j] ^= x1; z2[i][j] ^= x2; }
                UNROLL for (int j = 0; j < 4; ++j) d[j] = U(H(z1[i][j]) + H(z2[i][j]));
                UNROLL for (int j = 0; j < 4; ++j) m |= z1[i][j] | z2[i][j];
                yr[i] = mfma16(GR[i], d); yi[i] = mfma16(GI[i], d);
                SB();
            }
            if (__all((m & TM) == 0u)) break;
            MEMBAR();
            LOAD_Z();
        }
        STAMP(9);
        u4 y[4];
        UNROLL for (int i = 0; i < 4; ++i) UNROLL for (int r = 0; r < 4; ++r) y[r][i] = pkh(yr[i][r], yi[i][r]);
        UNROLL for (int r = 0; r < 4; ++r) *(u4*)&lds[L_A2 + lw + r * 64] = y[r];
        lset(lds, L_C3 + wv, pv, lane);
    }
    if (!wearly) { const u32 es = lwait4(lds, L_PR, 1u); WIDEN(es); }
    {
        u4 d[4];
        NOUNROLL for (int spin = 0; spin < LSPIN_CAP; ++spin) {
            MEMBAR();
            const u4 c = *(const u4*)&lds[L_C3];
            MEMBAR();
            UNROLL for (int i = 0; i < 4; ++i) d[i] = l4[L_A2 / 4 + lr + i * 16];
            pin(d[0]);
            if (rfl(min4u(c)) >= pv) break;
        }
        STAMP(10);
        pin(WA); pin(WB);
        f4 xr[4], xi[4];
        u4 xx[4], ya[4], yb[4];
        UNROLL for (int i = 0; i < 4; ++i) {
            xr[i] = mfma16(FR, d[i]); xi[i] = mfma16(FI, d[i]);
            if (i >= 2) { UNROLL for (int r = 0; r < 4; ++r) xx[i - 2][r] = pkh(xr[i - 2][r], xi[i - 2][r]); pin(xx[i - 2]); }
            SB();
        }
        UNROLL for (int r = 0; r < 4; ++r) ya[0][r] = cmul_swap(xx[0][r], WA[0][r]);
        UNROLL for (int i = 2; i < 4; ++i) { UNROLL for (int r = 0; r < 4; ++r) xx[i][r] = pkh(xr[i][r], xi[i][r]); pin(xx[i]); }
        SB();
        f4 p0[4], p1[4], p2[4], p3[4];
        UNROLL for (int i = 0; i < 4; ++i) {
            p0[i] = mfma16(FR, ya[i]); p1[i] = mfma16(FI, ya[i]);
            if (i < 3) { UNROLL for (int r = 0; r < 4; ++r) ya[i + 1][r] = cmul_swap(xx[i + 1][r], WA[i + 1][r]); }
            else { UNROLL for (int r = 0; r < 4; ++r) yb[0][r] = cmul_swap(xx[0][r], WB[0][r]); }
            SB();
        }
        u4 ra[4], rb[4];
        UNROLL for (int i = 0; i < 4; ++i) {
            p2[i] = mfma16(FR, yb[i]); p3[i] = mfma16(FI, yb[i]);
            if (i < 3) { UNROLL for (int r = 0; r < 4; ++r) yb[i + 1][r] = cmul_swap(xx[i + 1][r], WB[i + 1][r]); }
            UNROLL for (int k = 0; k < 4; ++k) ra[i][k] = pkh(p0[k][i], p1[k][i]);
            *(u4*)&lds[L_B + lw + i * 64] = ra[i];
            SB();
        }
        UNROLL for (int i = 0; i < 3; ++i) UNROLL for (int r = 0; r < 4; ++r) rb[r][i] = pkh(p2[i][r], p3[i][r]);
        SB();
        UNROLL for (int r = 0; r < 4; ++r) { rb[r][3] = pkh(p2[3][r], p3[3][r]); *(u4*)&lds[L_B + 4096 + lw + r * 64] = rb[r]; }
        lset(lds, L_C4 + wv, pv, lane);
    }
    STAMP(11);
    {
        u4 da[4], db[4];
        NOUNROLL for (int spin = 0; spin < LSPIN_CAP; ++spin) {
            MEMBAR();
            const u4 c = *(const u4*)&lds[L_C4];
            MEMBAR();
            UNROLL for (int i = 0; i < 4; ++i) { da[i] = l4[L_B / 4 + lr + i * 16]; db[i] = l4[L_B / 4 + 1024 + lr + i * 16]; }
            pin(da[0]);
            if (rfl(min4u(c)) >= pv) break;
        }
        const int vo = 64 * q * 32768 + n * 8, so = WS_V + 4 * wv * 32768 + cls * 4096 + tile * 128;
        f4 p0[4], p1[4], p2[4], p3[4];
        UNROLL for (int i = 0; i < 5; ++i) {
            if (i < 4) { p0[i] = mfma16(HR[i], da[i]); p1[i] = mfma16(HI[i], da[i]); p2[i] = mfma16(HR[i], db[i]); p3[i] = mfma16(HI[i], db[i]); }
            if (i > 0) { UNROLL for (int r = 0; r < 4; ++r)
                __builtin_amdgcn_raw_buffer_store_b64(u2{mark(pkh(p0[i - 1][r], p1[i - 1][r]), vn, tm), mark(pkh(p2[i - 1][r], p3[i - 1][r]), vn, tm)}, rws, vo, so + (16 * r + i - 1) * 32768, SC1); }
            SB();
        }
    }
    if (wv == 0) lset(lds, L_P2, 1u, lane);
    STAMP(12);
    SB();
    {
        pin(xv);
        const f2 g01 = {bflo(gv.x), bfhi(gv.x)}, g23 = {bflo(gv.y), bfhi(gv.y)};
        f4 acc = {};
        UNROLL for (int i = 0; i < 4; ++i) {
            u4 pp;
            UNROLL for (int k = 0; k < 2; ++k) {
                const f2 a = f2{bflo(xv[2 * i + k].x), bfhi(xv[2 * i + k].x)} * g01, c = f2{bflo(xv[2 * i + k].y), bfhi(xv[2 * i + k].y)} * g23;
                pp[2 * k] = pkb(a.x, a.y); pp[2 * k + 1] = pkb(c.x, c.y);
            }
            const u32 s0 = n == 2 * i ? 0x3f803f80u : 0u, s1 = n == 2 * i + 1 ? 0x3f803f80u : 0u;
            acc = mfma16b(pp, u4{s0, s0, s1, s1}, acc);
        }
        if (n < 8) lds[L_GS + n * 16 + (t >> 4)] = __builtin_bit_cast(u32, (acc.x + acc.y) + (acc.z + acc.w));
        lset(lds, L_C5 + wv, pv, lane);
    }
    MR[0] = *(const u4*)&lds[L_M + lane * 4]; MR[1] = *(const u4*)&lds[L_M + 256 + lane * 4];
    UNROLL for (int i = 0; i < 4; ++i) TW[i] = *(const u4*)&lds[L_M + (2 + i) * 256 + lane * 4];
    core_expand(lane, MR, MI);
    pin(MR); pin(MI); pin(TW);
    u4 PM;
    {
        lwait4(lds, L_C5, pv);
        float gg[4];
        UNROLL for (int k = 0; k < 2; ++k) {
            const f4* pp = (const f4*)&lds[L_GS + ((n >> 2) * 2 + k) * 16];
            const f4 ps = (pp[0] + pp[1]) + (pp[2] + pp[3]);
            const float lg = rbf((ps.x + ps.y) + (ps.z + ps.w));
            gg[2 * k] = rbf(1.0f / (1.0f + __expf(-lg)));
            gg[2 * k + 1] = rbf(1.0f - gg[2 * k]);
        }
        UNROLL for (int k = 0; k < 4; ++k) { gg[k] *= 0.03125f; pin(gg[k]); }
        const int r = n & 3, sa = (n >> 2) * 2;
        UNROLL for (int cc = 0; cc < 2; ++cc) {
            const int u1 = 2 * q + cc;
            const float ta = (float)(sa * u1) * 0.125f + ((r & 1) ? 0.25f : (r & 2) ? 0.5f : 0.0f);
            const float tb = (float)((sa + 1) * u1) * 0.125f + ((r & 1) ? ((r & 2) ? 0.0f : 0.5f) : 0.25f);
            const float ca = cosr(ta), sa_ = sinr(ta), cb = cosr(tb), sb_ = sinr(tb);
            float e[4] = {gg[2] * cb, gg[2] * sb_, gg[3] * cb, gg[3] * sb_};
            pin(e);
            e[0] = fmaf(gg[0], ca, e[0]); pin(e[0]); e[1] = fmaf(gg[0], sa_, e[1]); pin(e[1]); e[2] = fmaf(gg[1], ca, e[2]); pin(e[2]); e[3] = fmaf(gg[1], sa_, e[3]);
            PM[2 * cc] = pkh(e[0], e[1]); PM[2 * cc + 1] = pkh(e[2], e[3]);
        }
    }
    int a3d = (q * 8 + wv) * 32 + n, a3m = (q * 8 + 7 - wv) * 32 + 16 - n, a3z = (q * 8 + 7 - wv) * 32 + ((32 - n) & 31), a3r = wv * 256 + lane;
    float osc = __builtin_bit_cast(float, (u32)min(max((int)ex + (int)ew - 130, 1), 254) << 23) * TRUNC_FIX;
    const int ve = 2 * q * 4096 + (128 * wv + n) * 8;
    int vs0 = WS_V + b * 32768, vs1 = vs0 + 4096, vsl = WS_VL + b * 16;
    asm volatile("" : "+s"(vs0), "+s"(vs1), "+s"(vsl));
    int ov = (4 * perm32(lane & 15) + 2 * ((lane >> 4) & 1) + (lane >> 5)) * 16 + (512 * wv + b) * 2048;
    u32 x8 = wv == 3 ? 0u : vn;
    pin(a3d); pin(a3m); pin(a3z); pin(a3r); pin(PM); pin(ov); pin(osc); pin(x8);
    STAMP(13);
    VMWAIT(0);
    {
        f4 z3[9];
        NOUNROLL for (int spin = 0; spin < SPIN_CAP; ++spin) {
            MEMBAR();
            u2 v0[8], v1[8];
            u4 ve8, vo8;
            int a_ = ve; pin(a_);
            if (wv == 3) {
                ve8 = __builtin_amdgcn_raw_buffer_load_b128(rws, q * 8192, vsl, SC1); vo8 = __builtin_amdgcn_raw_buffer_load_b128(rws, q * 8192 + 4096, vsl, SC1);
            } else {
                const u2 e2 = __builtin_amdgcn_raw_buffer_load_b64(rws, a_ + 1024, vs0, SC1), o2 = __builtin_amdgcn_raw_buffer_load_b64(rws, a_ + 1024, vs1, SC1);
                ve8 = u4{e2.x, e2.y, tag0, tag0}; vo8 = u4{o2.x, o2.y, tag0, tag0};
            }
            UNROLL for (int i = 0; i < 8; ++i) {
                v0[i] = __builtin_amdgcn_raw_buffer_load_b64(rws, a_ + 128 * i, vs0, SC1);
                v1[i] = __builtin_amdgcn_raw_buffer_load_b64(rws, a_ + 128 * i, vs1, SC1);
            }
            SB();
            u32 m = (((wv == 3 ? ve8.y : ve8.z) ^ tag0) | (ve8.w ^ tag0) | ((wv == 3 ? vo8.y : vo8.z) ^ tag0) | (vo8.w ^ tag0)) ? TM : 0u;
            UNROLL for (int k = 0; k < 9; ++k) {
                const int i = (k + 8) % 9;
                const u4 d = i == 8 ? u4{ve8.x, wv == 3 ? ve8.z : ve8.y, vo8.x, wv == 3 ? vo8.z : vo8.y} ^ x8 : u4{v0[i & 7].x, v0[i & 7].y, v1[i & 7].x, v1[i & 7].y} ^ vn;
                m |= d.x | d.y; m |= d.z | d.w;
                z3[i] = mfma16(PM, d);
                SB();
            }
            if (__all((m & TM) == 0u)) break;
        }
        STAMP(14);
        if (wv == 0 && n == 0) { z3[0].x = 0.5f * (z3[0].x + z3[0].z); z3[0].y = 0.5f * (z3[0].y + z3[0].w); }
        if (wv == 3) { z3[8].z = 0.5f * (z3[8].x + z3[8].z); z3[8].w = 0.5f * (z3[8].y + z3[8].w); }
        u4* l4w = (u4*)&lds[L_A1];
        UNROLL for (int p = 0; p < 2; ++p)
            l4w[a3d + 16 * p] = u4{pkh(z3[p].x, z3[p + 2].x), pkh(z3[p + 4].x, z3[p + 6].x), pkh(z3[p].y, z3[p + 2].y), pkh(z3[p + 4].y, z3[p + 6].y)};
        l4w[a3m] = u4{pkh(z3[7].z, z3[5].z), pkh(z3[3].z, z3[1].z), pkh(z3[7].w, z3[5].w), pkh(z3[3].w, z3[1].w)};
        f2 m[4];
        UNROLL for (int i = 0; i < 4; ++i) m[i] = n == 0 ? f2{z3[8 - 2 * i].z, z3[8 - 2 * i].w} : f2{z3[6 - 2 * i].z, z3[6 - 2 * i].w};
        l4w[a3z] = u4{pkh(m[0].x, m[1].x), pkh(m[2].x, m[3].x), pkh(m[0].y, m[1].y), pkh(m[2].y, m[3].y)};
        lset(lds, L_C6 + wv, 2u * pv - 1u, lane);
    }
    {
        u4 in[4];
        NOUNROLL for (int spin = 0; spin < LSPIN_CAP; ++spin) {
            MEMBAR();
            const u4 c = *(const u4*)&lds[L_C6];
            MEMBAR();
            UNROLL for (int s = 0; s < 4; ++s) in[s] = ((const u4*)&lds[L_A1])[a3r + s * 64];
            pin(in[0]);
            if (rfl(min4u(c)) >= 2u * pv - 1u) break;
        }
        f16v zr, zi;
        core_fft<false>(in, MR, MI, TW, zr, zi);
        zr *= osc; zi *= osc;
        const rsrc_t ro = mkrs(out, 2048 * 2048);
#define OUT_ROW(z, off) do { u4 lo, hi; \
            UNROLL for (int j = 0; j < 4; ++j) { const auto sw_ = __builtin_amdgcn_permlane16_swap(pkb(z[2 * j], z[2 * j + 1]), pkb(z[8 + 2 * j], z[8 + 2 * j + 1]), false, false); lo[j] = sw_[0]; hi[j] = sw_[1]; } \
            store128<OAUX>(lo, ro, ov + (off)); store128<OAUX>(hi, ro, ov + (off) + 1024); } while (0)
        OUT_ROW(zr, 256 * 2048);
        OUT_ROW(zi, 0);
    }
    STAMP(15);
    vd = lwait(lds, L_VD, 1u);
    if (pass == 1 || !(vd & 0x20000u)) break;
    lset(lds, L_C6 + wv, 2u, lane);
    lwait4(lds, L_C6, 2u);
    VMWAIT(0);
    }
    STAMP_OUT();
}

extern "C" void kernel_launch(void* const* d_in, const int* in_sizes, int n_in,
                              void* d_out, int out_size, void* d_ws, size_t ws_size,
                              hipStream_t stream) {
    gfnet_fused<<<256, 512, 0, stream>>>(d_in[0], d_in[6], d_in[1], d_in[2], d_in[4], d_in[5], (char*)d_ws, d_out);
}
```

```cpp
#include <hip/hip_runtime.h>
#include <hip/hip_bf16.h>
#include <stdint.h>

typedef _Float16 h2 __attribute__((ext_vector_type(2)));
typedef _Float16 h8 __attribute__((ext_vector_type(8)));
typedef __bf16 b2 __attribute__((ext_vector_type(2)));
typedef __bf16 b8 __attribute__((ext_vector_type(8)));
typedef float f2 __attribute__((ext_vector_type(2)));
typedef float f4 __attribute__((ext_vector_type(4)));
typedef float f16v __attribute__((ext_vector_type(16)));
typedef short s4 __attribute__((ext_vector_type(4)));
typedef unsigned u32;
typedef u32 u2 __attribute__((ext_vector_type(2)));
typedef u32 u4 __attribute__((ext_vector_type(4)));
typedef unsigned short us2 __attribute__((ext_vector_type(2)));
typedef __amdgpu_buffer_rsrc_t rsrc_t;
#define DI __device__ __forceinline__
#define UNROLL _Pragma("unroll")
#define NOUNROLL _Pragma("unroll 1")
#define LAS __attribute__((address_space(3)))
#define SB() __builtin_amdgcn_sched_barrier(0)
#define MEMBAR() asm volatile("" ::: "memory")
#define VMWAIT(n) asm volatile("s_waitcnt vmcnt(%0)" :: "n"(n) : "memory")

#define MARGIN 2
#define QUICK 145

constexpr u32 FSTR = 16, HSTR = 32;
constexpr u32 WS_Z = 0;
constexpr u32 WS_V = 4u << 20;
constexpr u32 WS_VL = 12u << 20;
constexpr u32 WS_HB = WS_VL + (32u << 10);
constexpr u32 WS_F = WS_HB + (32u << 10);
constexpr u32 WS_E = WS_F + 2048 * FSTR;
constexpr u32 WS_BYTES = WS_E + 4096;
constexpr int SC1 = 16, NT = 2;
constexpr int SPIN_CAP = 8192, LSPIN_CAP = 1 << 17;
constexpr int OOB = 0x7ffffff0;
constexpr u32 TM = 0x00010001u;
constexpr float TRUNC_FIX = 1.00066f;

template <class T> DI void pin(T& v) { asm volatile("" : "+v"(v)); }
template <class T, int N> DI void pin(T (&v)[N]) { UNROLL for (int i = 0; i < N; ++i) pin(v[i]); }
DI u32 opq(u32 v) { asm("" : "+v"(v)); return v; }
DI u32 pkh(float lo, float hi) { h2 r; r.x = (_Float16)lo; r.y = (_Float16)hi; return __builtin_bit_cast(u32, r); }
DI u32 pkb(float lo, float hi) { b2 r; r.x = (__bf16)lo; r.y = (__bf16)hi; return __builtin_bit_cast(u32, r); }
DI float bflo(u32 v) { return __builtin_bit_cast(float, v << 16); }
DI float bfhi(u32 v) { return __builtin_bit_cast(float, v & 0xffff0000u); }
DI float rbf(float v) { return (float)(__bf16)v; }
DI h2 H(u32 v) { return __builtin_bit_cast(h2, v); }
DI u32 U(h2 v) { return __builtin_bit_cast(u32, v); }
DI f16v mfma32(u4 a, u4 b, f16v c) { return __builtin_amdgcn_mfma_f32_32x32x16_f16(__builtin_bit_cast(h8, a), __builtin_bit_cast(h8, b), c, 0, 0, 0); }
DI f4 mfma16(u4 a, u4 b) { return __builtin_amdgcn_mfma_f32_16x16x32_f16(__builtin_bit_cast(h8, a), __builtin_bit_cast(h8, b), f4{}, 0, 0, 0); }
DI f4 mfma16b(u4 a, u4 b, f4 c) { return __builtin_amdgcn_mfma_f32_16x16x32_bf16(__builtin_bit_cast(b8, a), __builtin_bit_cast(b8, b), c, 0, 0, 0); }
DI u32 hmul(u32 a, u32 b) { return U(H(a) * H(b)); }
DI u32 cmul_swap(u32 x, u32 w) { const h2 a = H(opq(x)), c = H(opq(w)); h2 n = c; n.y = -n.y; return U(__builtin_elementwise_fma(a.yy, n, a.xx * c.yx)); }
DI u32 cmul_conj(u32 x, u32 t) { const h2 a = H(opq(x)), c = H(opq(t)); h2 n = c.yy; n.y = -n.y; return U(__builtin_elementwise_fma(a.yx, n, a * c.xx)); }
DI u32 pkmax(u32 a, u32 b) { return __builtin_bit_cast(u32, __builtin_elementwise_max(__builtin_bit_cast(us2, a), __builtin_bit_cast(us2, b))); }
DI float sinr(float r) { return __builtin_amdgcn_sinf(r); }
DI float cosr(float r) { return __builtin_amdgcn_cosf(r); }
DI u32 rot(float ph) { return pkh(cosr(ph), sinr(ph)); }
DI u32 quarter(u32 r, u32 f) { u32 o; asm("v_pk_mul_f16 %0, %1, %2 op_sel:[1,0] op_sel_hi:[0,1] neg_lo:[1,0]" : "=v"(o) : "v"(r), "v"(f)); return o; }
DI u4 quarter(u4 r) { u32 one = 0x3c003c00u; pin(one); return u4{quarter(r.x, one), quarter(r.y, one), quarter(r.z, one), quarter(r.w, one)}; }
DI int perm32(int n) { return (n & 0x13) | ((n & 4) << 1) | ((n & 8) >> 1); }
DI u32 max4u(u4 v) { return max(max(v.x, v.y), max(v.z, v.w)); }
DI u32 min4u(u4 v) { return min(min(v.x, v.y), min(v.z, v.w)); }
DI u32 mark(u32 v, u32 par, u32 tm) { return (par & tm) | (v & ~tm); }
DI u32 rfl(u32 v) { return __builtin_amdgcn_readfirstlane(v); }
DI rsrc_t mkrs(const void* p, u32 bytes) { return __builtin_amdgcn_make_buffer_rsrc((void*)p, (short)0, (int)bytes, 0x00020000); }
template <int AUX> DI void store128(u4 v, rsrc_t r, int off) { __builtin_amdgcn_raw_buffer_store_b128(v, r, off, 0, AUX); }
template <int CTRL> DI u32 dpp(u32 v) { return __builtin_amdgcn_update_dpp(0u, v, CTRL, 0xf, 0xf, true); }
DI u32 wave_maxu(u32 v) {
    v = max(v, dpp<0xB1>(v)); v = max(v, dpp<0x4E>(v)); v = max(v, dpp<0x141>(v)); v = max(v, dpp<0x140>(v));
    return max(max((u32)__builtin_amdgcn_readlane(v, 0), (u32)__builtin_amdgcn_readlane(v, 16)), max((u32)__builtin_amdgcn_readlane(v, 32), (u32)__builtin_amdgcn_readlane(v, 48)));
}

DI void core_setup(int lane, int s, u4& tw, u2& m) {
    float fc = (float)perm32(lane & 31) * (1.0f / 1024.0f), p0 = (float)(4 * (lane >> 5) + 8 * s) * fc, p1 = (float)(4 * (lane >> 5) + 8 * (s >> 1) + 2 * (s & 1)) * fc * 32.0f;
    pin(fc); pin(p0); pin(p1);
    float tc[4], ts[4];
    UNROLL for (int i = 0; i < 4; ++i) { const float pt = fmaf((float)i, fc, p0); tc[i] = cosr(pt); ts[i] = sinr(pt); }
    tw = u4{pkh(tc[0], tc[1]), pkh(tc[2], tc[3]), pkh(ts[0], ts[1]), pkh(ts[2], ts[3])};
    const float p2 = fmaf(32.0f, fc, p1);
    m = u2{pkh(cosr(p1), cosr(p2)), pkh(sinr(p1), sinr(p2))};
}
DI void core_expand(int lane, u4 (&MR)[4], u4 (&MI)[4]) {
    const u32 sg = (lane & 1) ? 0x80008000u : 0u;
    MR[2] = MR[0] ^ sg; MR[3] = MR[1] ^ sg;
    UNROLL for (int s = 0; s < 4; ++s) MI[s] = u4{MR[s].z ^ 0x80008000u, MR[s].w ^ 0x80008000u, MR[s].x, MR[s].y};
}
template <bool OUT_B> DI void core_fft(const u4 (&in)[4], const u4 (&MR)[4], const u4 (&MI)[4], const u4 (&TW)[4], f16v& outr, f16v& outi) {
    f16v yr = {}, yi = {};
    UNROLL for (int s = 0; s < 4; ++s) yr = mfma32(in[s], MR[s], yr);
    SB();
    yi = mfma32(in[0], MI[0], yi); yi = mfma32(in[1], MI[1], yi);
    SB();
    h2 t0[8], t1[8];
#define TW_RE(p) do { const h2 pr_ = H(pkh(yr[2 * (p)], yr[2 * (p) + 1])); t0[p] = pr_ * H(TW[(p) >> 1][(p) & 1]); t1[p] = pr_ * H(TW[(p) >> 1][2 + ((p) & 1)]); } while (0)
    TW_RE(0); TW_RE(1); SB();
    yi = mfma32(in[2], MI[2], yi);
    TW_RE(2); TW_RE(3); SB();
    yi = mfma32(in[3], MI[3], yi);
    TW_RE(4); TW_RE(5); TW_RE(6); TW_RE(7); SB();
    outr = f16v{}; outi = f16v{};
    u4 d[4];
#define TW_IM(s) do { UNROLL for (int j = 0; j < 2; ++j) { const int p_ = 2 * (s) + j; const h2 pi_ = H(pkh(yi[2 * p_], yi[2 * p_ + 1])); \
        d[s][j] = U(__builtin_elementwise_fma(pi_, H(TW[s][2 + j]), t0[p_])); d[s][2 + j] = U(__builtin_elementwise_fma(pi_, H(TW[s][j]), -t1[p_])); } } while (0)
    TW_IM(0); SB();
    UNROLL for (int s = 0; s < 4; ++s) {
        outr = OUT_B ? mfma32(MR[s], d[s], outr) : mfma32(d[s], MR[s], outr);
        if (s < 3) TW_IM(s + 1);
        SB();
    }
    UNROLL for (int s = 0; s < 4; ++s) outi = OUT_B ? mfma32(MI[s], d[s], outi) : mfma32(d[s], MI[s], outi);
}

DI u32 tag_of(u32 epoch, u32 idx) { u32 h = (epoch ^ (idx * 0x9E3779B1u)) * 0x85EBCA6Bu; return h ^ (h >> 15); }
DI u32 valid_bits(u32 epoch, int slot) { return (((0x6A3Cu >> slot) ^ epoch) & 1u) ? 0x00000001u : 0x00010000u; }
DI int cls_of(int b) { const int row = (b & 7) >> 1; return row == 0 ? 4 * (b >> 7) : ((b & 1) ? 8 - row : row); }
DI int tile_of(int b) { return (b & 6) == 0 ? 16 * (b & 1) + ((b >> 3) & 15) : b >> 3; }

constexpr int L_C1 = 0, L_C3 = 4, L_C4 = 8, L_C5 = 12, L_C6 = 16;
constexpr int L_EW = 20;
constexpr int L_WW = 24;
constexpr int L_PR = 28;
constexpr int L_LD = 32;
constexpr int L_SX = 36;
constexpr int L_ZL = 37;
constexpr int L_VD = 38;
constexpr int L_P2 = 39;
constexpr int L_GS = 64;
constexpr int L_M = 192;
constexpr int L_A1 = L_M + 1536;
constexpr int L_A2 = L_A1 + 4096;
constexpr int L_B = L_A2 + 4096;
constexpr int L_W = L_B + 8192;
constexpr int L_END = L_W + 64 * 256;

DI void lset(u32* lds, int idx, u32 v, int lane) { MEMBAR(); if (lane == 0) lds[idx] = v; MEMBAR(); }
DI u32 lget(u32* lds, int idx) { MEMBAR(); return rfl(lds[idx]); }
DI u32 lwait(u32* lds, int idx, u32 atleast) {
    u32 v = 0;
    NOUNROLL for (int spin = 0; spin < LSPIN_CAP; ++spin) { v = lget(lds, idx); if (v >= atleast) break; __builtin_amdgcn_s_sleep(1); }
    MEMBAR();
    return v;
}
DI u32 lwait4(u32* lds, int idx, u32 atleast) {
    u4 v;
    NOUNROLL for (int spin = 0; spin < LSPIN_CAP; ++spin) { MEMBAR(); v = *(const u4*)&lds[idx]; if (rfl(min4u(v)) >= atleast) break; __builtin_amdgcn_s_sleep(1); }
    MEMBAR();
    return rfl(max4u(v));
}
struct MagMax {
    h2 hi = {}, lo = {};
    DI void add(u32 a, u32 c) {
        hi = __builtin_elementwise_maximum(__builtin_elementwise_maximum(hi, H(a)), H(c));
        lo = __builtin_elementwise_minimum(__builtin_elementwise_minimum(lo, H(a)), H(c));
    }
    DI u32 exponent() const {
        const u32 mx = pkmax(U(hi) & 0x7fff7fffu, U(lo) & 0x7fff7fffu);
        return wave_maxu(max(mx & 0xffffu, mx >> 16)) >> 7;
    }
};
DI u32 clampe(u32 e) { return min(max(e, 8u), 240u); }

__global__ __launch_bounds__(512, 2) void gfnet_fused(const void* x, const void* gw, const void* war, const void* wai, const void* wbr, const void* wbi,
                                                      char* ws, void* out) {
    __shared__ __attribute__((aligned(16))) u32 lds[L_END];
    u32 code;
    {
        unsigned long long pc0; int len;
        asm volatile("s_getpc_b64 %0\n.Lgf_begin:\n\ts_mov_b32 %1, .Lgf_end-.Lgf_begin" : "=s"(pc0), "=s"(len));
        code = __builtin_amdgcn_raw_buffer_load_b32(mkrs((const void*)pc0, (u32)max(len, 0)), (threadIdx.x >> 6) == 4 ? ((blockIdx.x >> 5) * 64 + (threadIdx.x & 63)) * 64 : OOB, 0, 0);
        SB();
    }
    asm volatile("" :: "s"(x), "s"(gw), "s"(war), "s"(wai), "s"(wbr), "s"(wbi), "s"(ws), "s"(out));
    const int wv0 = __builtin_amdgcn_readfirstlane(threadIdx.x >> 6);
    const rsrc_t rws = mkrs(ws, WS_BYTES);

    if (wv0 >= 4) {
        const u32 tstart = (u32)__builtin_amdgcn_s_memrealtime();
        const int wv = wv0, b = blockIdx.x, cls = cls_of(b), tile = tile_of(b);
        const int lane = threadIdx.x & 63, q = lane >> 4, n = lane & 15;
        const rsrc_t rw = mkrs(wv == 4 ? war : wv == 5 ? wai : wv == 6 ? wbr : wbi, 2048 * 1026);
        if (wv == 4) { lds[lane] = 0u; asm volatile("s_waitcnt lgkmcnt(0)" ::: "memory"); }
        MEMBAR();
        __builtin_amdgcn_s_barrier();
        MEMBAR();
        const bool helper = wv == 7 && tile == 31;
        u4 GR, GI, FR, FI, HR, HI, T1, T2;
        u32 ev;
        {
            const int wvo = (lane >> 2) * (128 * 1026) + min(lane & 3, 2) * 16, wso = (cls * 513 + 16 * tile - cls) * 2;
#define WLOAD(r) do { UNROLL for (int st = 0; st < 4; ++st) { const int c_ = 4 * st + ((tile + (r)) & 3); \
                __builtin_amdgcn_raw_ptr_buffer_load_lds(rw, (LAS void*)&lds[L_W + (c_ * 4 + wv - 4) * 256], 16, wvo, wso + c_ * 8208, 0, 0); } SB(); } while (0)
            __builtin_amdgcn_s_sleep(2);
            WLOAD(0);
            const bool split = (b & 6) == 0;
            const int er = split ? 16 * (tile & 15) + ((lane >> 1) & 15) : 8 * tile + ((lane >> 1) & 7);
            const int ec = (split ? (tile & 16) * 16 : 0) - cls + ((lane & 1) ? (split ? 262 : 518) : 0);
            u32 edge = __builtin_amdgcn_raw_buffer_load_b32(rw, ((cls + 8 * er) * 513 + ec) * 2, 0, 0);
            ev = __builtin_amdgcn_raw_buffer_load_b32(rws, 0, WS_E, SC1);
            const u4 smp = __builtin_amdgcn_raw_buffer_load_b128(rw, (lane >> 3) * (2 * 131328 + 128) + (lane & 7) * 16, 0, 0);
            MEMBAR();
            if (helper) {
                UNROLL for (int j = 0; j < 4; ++j) {
                    GR[j] = rot((float)((4 * q + j) * (cls + 8 * n)) * (1.0f / 128.0f) + (cls == 4 ? 0.25f : 0.0f));
                    HR[j] = rot((float)(n * (cls + 8 * (4 * q + j))) * (1.0f / 128.0f));
                    FR[j] = rot((float)(n * (4 * q + j)) * (1.0f / 16.0f));
                    T1[j] = rot((float)((4 * q + j) * (cls + 8 * n)) * (1.0f / 2048.0f));
                    T2[j] = rot((float)(n * (cls + 8 * (4 * q + j))) * (1.0f / 2048.0f));
                }
                GI = quarter(GR); HI = quarter(HR); FI = quarter(FR);
                pin(GR); pin(GI); pin(HR); pin(HI); pin(FR); pin(FI); pin(T1); pin(T2);
            }
            VMWAIT(0);
            if ((int)((u32)__builtin_amdgcn_s_memrealtime() - tstart) < QUICK)
                __builtin_amdgcn_raw_buffer_store_b32(0u, rws, lane * 128, WS_V + b * 32768 + (wv - 4) * 8192, SC1);
            MEMBAR();
            WLOAD(1); WLOAD(2); WLOAD(3);
            VMWAIT(12);
            asm volatile("ds_write_b32 %0, %1" :: "v"((u32)(size_t)(LAS u32*)&lds[L_LD + wv - 4]), "v"(1u) : "memory");
            pin(edge); pin(code);
            MagMax ms; ms.add(smp.x, smp.y); ms.add(smp.z, smp.w);
            const u32 es = clampe(ms.exponent() + MARGIN);
            VMWAIT(0);
            lset(lds, L_PR + wv - 4, es, lane);
        }
        const u32 ep0 = rfl(ev) + 1u;
        if (wv == 5 || wv == 6) {
            lwait4(lds, L_PR, 1u);
            MagMax m;
            UNROLL for (int c = 0; c < 32; ++c) { const u4 v = *(const u4*)&lds[L_W + ((wv - 5) * 32 + c) * 256 + lane * 4]; m.add(v.x, v.y); m.add(v.z, v.w); }
            const u32 e = clampe(m.exponent());
            MEMBAR(); if (lane == 0) *(u2*)&lds[L_WW + 2 * (wv - 5)] = u2{e, e}; MEMBAR();
        } else if (wv == 4) {
            const u32 ex = lwait4(lds, L_EW, 1u), ew = lwait4(lds, L_WW, 1u);
            __builtin_amdgcn_raw_buffer_store_b64(u2{ex | (ew << 8), tag_of(ep0, b + 1)}, rws, lane < 8 ? lane * (int)(256 * FSTR) : OOB, WS_F + b * FSTR, SC1);
            lwait(lds, L_P2, 1u);
            __builtin_amdgcn_s_sleep(8);
            u2 f[4]; u32 tg[4];
            UNROLL for (int j = 0; j < 4; ++j) tg[j] = tag_of(ep0, 4 * lane + j + 1);
            NOUNROLL for (int spin = 0; spin < SPIN_CAP; ++spin) {
                MEMBAR();
                UNROLL for (int j = 0; j < 4; ++j) f[j] = __builtin_amdgcn_raw_buffer_load_b64(rws, lane * (4 * FSTR) + j * FSTR, WS_F + (b & 7) * (256 * FSTR), SC1);
                if (__all(f[0].y == tg[0] && f[1].y == tg[1] && f[2].y == tg[2] && f[3].y == tg[3])) break;
                __builtin_amdgcn_s_sleep(8);
            }
            const u4 e4 = {f[0].x, f[1].x, f[2].x, f[3].x};
            const u32 gx = wave_maxu(max4u(e4 & 0xffu)), gwm = wave_maxu(max4u((e4 >> 8) & 0xffu));
            const bool redo = gx > lget(lds, L_SX) || gwm > lwait4(lds, L_PR, 1u);
            lset(lds, L_VD, gx | (gwm << 8) | (redo ? 0x20000u : 0x10000u), lane);
            if (b == 0 && lane == 0) __builtin_amdgcn_raw_buffer_store_b32(ep0 + (redo ? 1u : 0u), rws, 0, WS_E, SC1);
        } else if (helper) {
            __builtin_amdgcn_s_setprio(2);
            u32 ew = lwait4(lds, L_PR, 1u);
            NOUNROLL for (u32 pass = 0; pass < 2; ++pass) {
                u4 WH[2];
                const float sw = __builtin_bit_cast(float, (249u - ew) << 23);
                UNROLL for (int r = 0; r < 4; ++r) UNROLL for (int f = 0; f < 2; ++f) {
                    const unsigned short* wp = (const unsigned short*)&lds[L_W + (n * 4 + 2 * f) * 256 + (4 * q + r) * 16] + 16 + cls;
                    float re = bflo(wp[0]) * sw, im = bflo(wp[512]) * sw;
                    pin(re);
                    WH[f][r] = pkh(re, im);
                }
                pin(WH);
                lwait(lds, L_ZL, pass + 1u);
                const u32 tg = tag_of(ep0 + pass, 0u);
                u4 hz;
                NOUNROLL for (int spin = 0; spin < SPIN_CAP; ++spin) {
                    MEMBAR();
                    u32 bad = 0;
                    UNROLL for (int j = 0; j < 4; ++j) { const u2 g = __builtin_amdgcn_raw_buffer_load_b64(rws, (64 * q + n + 16 * j) * HSTR, WS_HB + ((b & 7) >> 1) * (256 * HSTR), SC1); hz[j] = g.x & ~TM; bad |= g.y ^ tg; }
                    if (__all(bad == 0u)) break;
                    __builtin_amdgcn_s_sleep(4);
                }
                const u32 f16 = cls == 0 ? 0x00002c00u : cls == 4 ? 0x2c000000u : cls > 4 ? 0xac002c00u : 0x2c002c00u;
                u4 d, o[2];
                UNROLL for (int j = 0; j < 4; ++j) d[j] = hmul(hz[j], f16);
                f4 yr = mfma16(d, GR), yi = mfma16(d, GI);
                UNROLL for (int r = 0; r < 4; ++r) d[r] = cmul_conj(pkh(yr[r], yi[r]), T1[r]);
                const f4 xr = mfma16(FR, d), xi = mfma16(FI, d);
                UNROLL for (int f = 0; f < 2; ++f) {
                    UNROLL for (int r = 0; r < 4; ++r) d[r] = cmul_swap(pkh(xr[r], xi[r]), WH[f][r]);
                    yr = mfma16(d, FR); yi = mfma16(d, FI);
                    UNROLL for (int r = 0; r < 4; ++r) o[f][r] = cmul_conj(pkh(yr[r], yi[r]), T2[r]);
                }
                const f4 p0 = mfma16(HR, o[0]), p1 = mfma16(HI, o[0]), p2 = mfma16(HR, o[1]), p3 = mfma16(HI, o[1]);
                UNROLL for (int r = 0; r < 4; ++r)
                    store128<SC1>(u4{pkh(p0[r], p1[r]) & ~TM, tg, pkh(p2[r], p3[r]) & ~TM, tg}, rws, (64 * q + n) * 16 + WS_VL + r * 256 + cls * 4096);
                const u32 vd = lwait(lds, L_VD, 1u);
                if (!(vd & 0x20000u)) break;
                ew = (vd >> 8) & 0xffu;
            }
        }
        asm volatile(".Lgf_end:");
        return;
    }

    u32 vd = 0, epoch = 0;
    NOUNROLL for (u32 pass = 0; pass < 2; ++pass) {
    int t = threadIdx.x, wv = wv0, b = blockIdx.x;
    pin(t);
    asm volatile("" : "+s"(wv));
    asm volatile("" : "+s"(b));
    const int lane = t & 63, q = lane >> 4, n = lane & 15;
    const u32 pv = pass + 1u;
    u2 xv[8];
    const rsrc_t rx = mkrs(x, 2048 * 2048);
    const u4 smp = __builtin_amdgcn_raw_buffer_load_b128(rx, (lane >> 3) * (524288 + 2048 + 256) + (lane & 7) * 16, 0, 0);
    UNROLL for (int s1 = 0; s1 < 8; ++s1) xv[s1] = __builtin_amdgcn_raw_buffer_load_b64(rx, t * 8, (256 * s1 + b) * 2048, NT);
    const u32 ev = __builtin_amdgcn_raw_buffer_load_b32(rws, 0, WS_E, SC1);
    const u2 gv = __builtin_amdgcn_raw_buffer_load_b64(mkrs(gw, 2048), t * 8, 0, 0);
    MEMBAR();
    SB();
    if (pass == 0) { __builtin_amdgcn_s_barrier(); MEMBAR(); __builtin_amdgcn_s_setprio(1); }
    u4 MR[4], MI[4], TW[4];
    {
        u4 tw; u2 m;
        core_setup(lane, wv, tw, m);
        *(u4*)&lds[L_M + (2 + wv) * 256 + lane * 4] = tw;
        lds[L_M + (wv >> 1) * 256 + lane * 4 + (wv & 1)] = m.x; lds[L_M + (wv >> 1) * 256 + lane * 4 + 2 + (wv & 1)] = m.y;
    }
    const int zc = perm32(lane & 31) + (lane >> 5) * 256, zcu = zc + ((lane & 31) == 0 ? 543 : 511);
    int zl = (zc >> 4) * 16384 + (zc & 15) * 4, zu = (zcu >> 4) * 16384 + (zcu & 15) * 4;
    int zh = lane == 0 ? 0 : OOB;
    int tro = L_A1 + wv * 1024 + (4 * (lane >> 5) + ((lane & 15) >> 2)) * 16 + ((lane >> 4) & 1) * 8 + (lane & 3) * 2;
    u32 tm = TM, vl0 = valid_bits(0u, zc & 15), vu0 = valid_bits(0u, zcu & 15);
    pin(zl); pin(zu); pin(zh); pin(tro); pin(tm); pin(vl0); pin(vu0);
    SB();
    pin(code);
    u32 ex;
    {
        MagMax ms; ms.add(smp.x, smp.y); ms.add(smp.z, smp.w);
        ex = pass ? (vd & 0xffu) : clampe(ms.exponent() + MARGIN);
    }
    u32 tag0, flip;
    u32 z[16], z512;
    {
        const float sc = __builtin_bit_cast(float, (250u - ex) << 23);
        const float rs = sc * 0.70710678118654752f;
        u32 o[8][2];
        UNROLL for (int j = 0; j < 2; ++j) {
            f2 v[8];
            UNROLL for (int s1 = 0; s1 < 8; ++s1) v[s1] = f2{bflo(xv[s1][j]), bfhi(xv[s1][j])};
            const f2 a0 = v[0] + v[4], a1 = v[1] + v[5], a2 = v[2] + v[6], a3 = v[3] + v[7];
            const f2 b0 = (v[0] - v[4]) * sc, b1 = v[1] - v[5], b2_ = (v[2] - v[6]) * sc, b3 = v[3] - v[7];
            const f2 e0 = a0 + a2, e1 = (a1 + a3) * sc, dm = b1 - b3, dp = b1 + b3;
            f2 r[8];
            r[0] = e0 * sc + e1; r[1] = e0 * sc - e1;
            r[2] = dm * rs + b0; r[3] = -(dp * rs) - b2_;
            r[4] = (a0 - a2) * sc; r[5] = (a3 - a1) * sc;
            r[6] = b0 - dm * rs; r[7] = b2_ - dp * rs;
            UNROLL for (int p = 0; p < 8; ++p) o[p][j] = pkh(r[p].x, r[p].y);
        }
        UNROLL for (int p = 0; p < 8; ++p) *(u2*)&lds[L_A1 + p * 512 + 2 * t] = u2{o[p][0], o[p][1]};
        lset(lds, L_C1 + wv, pv, lane);
        epoch = (pass ? epoch : rfl(ev)) + 1u; tag0 = tag_of(epoch, 0u); flip = (epoch & 1u) ? TM : 0u;
        const u32 vl = vl0 ^ flip, vu = vu0 ^ flip;
        u4 in[4];
        NOUNROLL for (int spin = 0; spin < LSPIN_CAP; ++spin) {
            MEMBAR();
            const u4 c = *(const u4*)&lds[L_C1];
            MEMBAR();
            MR[0] = *(const u4*)&lds[L_M + lane * 4]; MR[1] = *(const u4*)&lds[L_M + 256 + lane * 4];
            UNROLL for (int s = 0; s < 4; ++s) {
                const u2 re = __builtin_bit_cast(u2, __builtin_amdgcn_ds_read_tr16_b64_v4i16((LAS s4*)&lds[tro + s * 128]));
                const u2 im = __builtin_bit_cast(u2, __builtin_amdgcn_ds_read_tr16_b64_v4i16((LAS s4*)&lds[tro + s * 128 + 512]));
                in[s] = u4{re.x, re.y, im.x, im.y};
            }
            pin(MR[0]); pin(MR[1]);
            if (rfl(min4u(c)) >= pv) break;
        }
        UNROLL for (int i = 0; i < 4; ++i) TW[i] = *(const u4*)&lds[L_M + (2 + i) * 256 + lane * 4];
        core_expand(lane, MR, MI);
        f16v zr, zi;
        core_fft<true>(in, MR, MI, TW, zr, zi);
        UNROLL for (int r = 0; r < 16; ++r) z[r] = pkh(zr[r], zi[r]);
        z512 = z[8];
        const u32 za = __builtin_amdgcn_readlane(z[8], 32), zb = __builtin_amdgcn_readlane(z[0], 0);
        MEMBAR();
        const u32 zt = lane < 32 ? za : zb;
        UNROLL for (int r = 0; r < 8; ++r) z[r] = mark(z[r], vl, tm);
        UNROLL for (int r = 8; r < 16; ++r) z[r] = mark((lane & 31) == 0 ? (r < 15 ? z[r + 1] : zt) : z[r], vu, tm);
    }
    pin(z);
    const int zs = WS_Z + wv * 64 * 16384 + (((b >> 2) & 3) * 64 + (b & 3) * 16 + ((b >> 4) & 3) * 4 + (b >> 6)) * 64;
    bool zsent = false;
#define TRY_STORE_Z(force) do { if (!zsent) { \
        bool go = true; \
        if (force) lwait4(lds, L_LD, 1u); else { MEMBAR(); go = rfl(min4u(*(const u4*)&lds[L_LD])) != 0u; MEMBAR(); } \
        if (go) { \
            UNROLL for (int r = 0; r < 16; ++r) __builtin_amdgcn_raw_buffer_store_b32(z[r], rws, r < 8 ? zl : zu, zs + (r & 7) * 32768, SC1); \
            __builtin_amdgcn_raw_buffer_store_b64(u2{z512, tag0}, rws, zh, WS_HB + (wv * 256 + b) * HSTR, SC1); \
            zsent = true; } } SB(); } while (0)
    TRY_STORE_Z(false);
    const int row = (b & 7) >> 1, cls = cls_of(b), tile = tile_of(b);
    const bool packed = row == 0;
    u4 FR, FI, GR[4], GI[4], HR[4], HI[4], sel1;
    {
        const float ofs = cls == 4 ? 0.25f : 0.0f;
        float gu = (float)(cls + 8 * n) * (1.0f / 2048.0f), g0 = (float)(64 * q + 4 * wv) * gu + ofs;
        u32 s16 = 0x2c002c00u;
        pin(gu); pin(g0); pin(s16);
        UNROLL for (int i = 0; i < 4; ++i) UNROLL for (int j = 0; j < 4; ++j) { const u32 g = rot(fmaf((float)(16 * j + i), gu, g0)); GR[i][j] = hmul(g, s16); GI[i][j] = quarter(g, s16); }
    }
    pin(GR); pin(GI);
    {
        MagMax m;
        UNROLL for (int s1 = 0; s1 < 8; ++s1) m.add(xv[s1].x, xv[s1].y);
        if (wv == 0) lset(lds, L_SX, ex, lane);
        lset(lds, L_EW + wv, clampe(m.exponent()), lane);
    }
    TRY_STORE_Z(false);
    UNROLL for (int j = 0; j < 4; ++j) FR[j] = rot((float)(n * (4 * q + j)) * (1.0f / 16.0f));
    FI = quarter(FR);
    {
        float hs[4], hu[4];
        UNROLL for (int i = 0; i < 4; ++i) { hs[i] = (float)(16 * n + 4 * wv + i) * (1.0f / 2048.0f); hu[i] = (float)(cls + 32 * q + 8 * i); }
        pin(hs); pin(hu);
        UNROLL for (int i = 0; i < 4; ++i) { UNROLL for (int j = 0; j < 4; ++j) { float a = hs[i] * hu[j]; pin(a); HR[i][j] = rot(a); } HI[i] = quarter(HR[i]); }
        const int d = n + cls - 8 * q;
        UNROLL for (int j = 0; j < 4; ++j) sel1[j] = (d >> 1) == j ? ((d & 1) ? 0xffff0000u : 0xffffu) : 0u;
    }
    pin(FR); pin(FI); pin(HR); pin(HI); pin(sel1);
    TRY_STORE_Z(true);
    VMWAIT(4);
    u4 WA[4], WB[4];
    u32 ew = 0;
    UNROLL for (int i = 0; i < 4; ++i) asm volatile("" : "=v"(WA[i]), "=v"(WB[i]));
#define WIDEN(es) do { \
        u4 wr[4][4]; \
        int wl = L_W / 4 + wv * 1024 + n * 4 + min(q, 2); pin(wl); \
        UNROLL for (int i = 0; i < 4; ++i) UNROLL for (int a = 0; a < 4; ++a) wr[i][a] = ((const u4*)lds)[wl + (i * 4 + a) * 64]; \
        ew = pass ? (vd >> 8) & 0xffu : (es); \
        const u32 sw = ((249u - ew - (packed ? 1u : 0u)) << 7) * 0x10001u;       \
        const u4 sels = sel1 & sw; \
        SB(); \
        f4 wf[4][4]; \
        UNROLL for (int k = 0; k < 10; ++k) { \
            if (k < 8) { wf[k >> 1][2 * (k & 1)] = mfma16b(wr[k >> 1][2 * (k & 1)], sels, f4{}); wf[k >> 1][2 * (k & 1) + 1] = mfma16b(wr[k >> 1][2 * (k & 1) + 1], sels, f4{}); } \
            if (k >= 2) { const int i_ = (k - 2) >> 1, f_ = k & 1; UNROLL for (int r = 0; r < 4; ++r) (f_ ? WB : WA)[i_][r] = pkh(wf[i_][2 * f_][r], wf[i_][2 * f_ + 1][r]); } \
            SB(); } \
        pin(WA); pin(WB); } while (0)
    MEMBAR();
    const u4 pr0 = *(const u4*)&lds[L_PR];
    const bool wearly = rfl(min4u(pr0)) != 0u;
    MEMBAR();
    if (wearly) WIDEN(rfl(max4u(pr0)));
    const u32 vn = valid_bits(epoch, n), vm = valid_bits(epoch, 15 - n);
    u32 x1 = cls > 4 ? vm ^ 0x80000000u : vn, x2 = vm ^ (cls == 4 ? 0x00008000u : cls == 0 ? 0x80000000u : 0u);
    u32 z1[4][4], z2[4][4];
    const int zvm = q * 64 + (63 - tile) * 16384 + (15 - n) * 4;
    const int zv1 = cls <= 4 ? q * 64 + tile * 16384 + n * 4 : zvm;
    int zso = WS_Z + row * 64 * 16384 + wv * 4096;
    asm volatile("" : "+s"(zso));
#define LOAD_Z() do { \
        if (packed) { int a_ = zvm; pin(a_); UNROLL for (int i = 0; i < 4; ++i) UNROLL for (int j = 0; j < 4; ++j) z2[i][j] = __builtin_amdgcn_raw_buffer_load_b32(rws, a_ + (4 * i + j) * 256, zso, SC1); } \
        else { UNROLL for (int i = 0; i < 4; ++i) UNROLL for (int j = 0; j < 4; ++j) z2[i][j] = x2; } \
        int c_ = zv1; pin(c_); UNROLL for (int i = 0; i < 4; ++i) UNROLL for (int j = 0; j < 4; ++j) z1[i][j] = __builtin_amdgcn_raw_buffer_load_b32(rws, c_ + (4 * i + j) * 256, zso, SC1); } while (0)
    pin(x1); pin(x2);
    VMWAIT(0);
    LOAD_Z();
    if (wv == 0) lset(lds, L_ZL, pv, lane);
    SB();
    const int lw = ((wv * 16 + 4 * q) * 16 + n) * 4;
    int lr = (q * 16 + 4 * wv) * 16 + n;
    const u4* l4 = (const u4*)lds;
    pin(lr);
    SB();
    {
        f4 yr[4], yi[4];
        NOUNROLL for (int spin = 0; spin < SPIN_CAP; ++spin) {
            u32 m = 0;
            UNROLL for (int i = 0; i < 4; ++i) {
                u4 d;
                UNROLL for (int j = 0; j < 4; ++j) { z1[i][j] ^= x1; z2[i][j] ^= x2; }
                UNROLL for (int j = 0; j < 4; ++j) d[j] = U(H(z1[i][j]) + H(z2[i][j]));
                UNROLL for (int j = 0; j < 4; ++j) m |= z1[i][j] | z2[i][j];
                yr[i] = mfma16(GR[i], d); yi[i] = mfma16(GI[i], d);
                SB();
            }
            if (__all((m & TM) == 0u)) break;
            MEMBAR();
            LOAD_Z();
        }
        u4 y[4];
        UNROLL for (int i = 0; i < 4; ++i) UNROLL for (int r = 0; r < 4; ++r) y[r][i] = pkh(yr[i][r], yi[i][r]);
        UNROLL for (int r = 0; r < 4; ++r) *(u4*)&lds[L_A2 + lw + r * 64] = y[r];
        lset(lds, L_C3 + wv, pv, lane);
    }
    if (!wearly) { const u32 es = lwait4(lds, L_PR, 1u); WIDEN(es); }
    {
        u4 d[4];
        NOUNROLL for (int spin = 0; spin < LSPIN_CAP; ++spin) {
            MEMBAR();
            const u4 c = *(const u4*)&lds[L_C3];
            MEMBAR();
            UNROLL for (int i = 0; i < 4; ++i) d[i] = l4[L_A2 / 4 + lr + i * 16];
            pin(d[0]);
            if (rfl(min4u(c)) >= pv) break;
        }
        pin(WA); pin(WB);
        f4 xr[4], xi[4];
        u4 xx[4], ya[4], yb[4];
        UNROLL for (int i = 0; i < 4; ++i) {
            xr[i] = mfma16(FR, d[i]); xi[i] = mfma16(FI, d[i]);
            if (i >= 2) { UNROLL for (int r = 0; r < 4; ++r) xx[i - 2][r] = pkh(xr[i - 2][r], xi[i - 2][r]); pin(xx[i - 2]); }
            SB();
        }
        UNROLL for (int r = 0; r < 4; ++r) ya[0][r] = cmul_swap(xx[0][r], WA[0][r]);
        UNROLL for (int i = 2; i < 4; ++i) { UNROLL for (int r = 0; r < 4; ++r) xx[i][r] = pkh(xr[i][r], xi[i][r]); pin(xx[i]); }
        SB();
        f4 p0[4], p1[4], p2[4], p3[4];
        UNROLL for (int i = 0; i < 4; ++i) {
            p0[i] = mfma16(FR, ya[i]); p1[i] = mfma16(FI, ya[i]);
            if (i < 3) { UNROLL for (int r = 0; r < 4; ++r) ya[i + 1][r] = cmul_swap(xx[i + 1][r], WA[i + 1][r]); }
            else { UNROLL for (int r = 0; r < 4; ++r) yb[0][r] = cmul_swap(xx[0][r], WB[0][r]); }
            SB();
        }
        u4 ra[4], rb[4];
        UNROLL for (int i = 0; i < 4; ++i) {
            p2[i] = mfma16(FR, yb[i]); p3[i] = mfma16(FI, yb[i]);
            if (i < 3) { UNROLL for (int r = 0; r < 4; ++r) yb[i + 1][r] = cmul_swap(xx[i + 1][r], WB[i + 1][r]); }
            UNROLL for (int k = 0; k < 4; ++k) ra[i][k] = pkh(p0[k][i], p1[k][i]);
            *(u4*)&lds[L_B + lw + i * 64] = ra[i];
            SB();
        }
        UNROLL for (int i = 0; i < 3; ++i) UNROLL for (int r = 0; r < 4; ++r) rb[r][i] = pkh(p2[i][r], p3[i][r]);
        SB();
        UNROLL for (int r = 0; r < 4; ++r) { rb[r][3] = pkh(p2[3][r], p3[3][r]); *(u4*)&lds[L_B + 4096 + lw + r * 64] = rb[r]; }
        lset(lds, L_C4 + wv, pv, lane);
    }
    {
        u4 da[4], db[4];
        NOUNROLL for (int spin = 0; spin < LSPIN_CAP; ++spin) {
            MEMBAR();
            const u4 c = *(const u4*)&lds[L_C4];
            MEMBAR();
            UNROLL for (int i = 0; i < 4; ++i) { da[i] = l4[L_B / 4 + lr + i * 16]; db[i] = l4[L_B / 4 + 1024 + lr + i * 16]; }
            pin(da[0]);
            if (rfl(min4u(c)) >= pv) break;
        }
        const int vo = 64 * q * 32768 + n * 8, so = WS_V + 4 * wv * 32768 + cls * 4096 + tile * 128;
        f4 p0[4], p1[4], p2[4], p3[4];
        UNROLL for (int i = 0; i < 5; ++i) {
            if (i < 4) { p0[i] = mfma16(HR[i], da[i]); p1[i] = mfma16(HI[i], da[i]); p2[i] = mfma16(HR[i], db[i]); p3[i] = mfma16(HI[i], db[i]); }
            if (i > 0) { UNROLL for (int r = 0; r < 4; ++r)
                __builtin_amdgcn_raw_buffer_store_b64(u2{mark(pkh(p0[i - 1][r], p1[i - 1][r]), vn, tm), mark(pkh(p2[i - 1][r], p3[i - 1][r]), vn, tm)}, rws, vo, so + (16 * r + i - 1) * 32768, SC1); }
            SB();
        }
    }
    if (wv == 0) lset(lds, L_P2, 1u, lane);
    SB();
    {
        pin(xv);
        const f2 g01 = {bflo(gv.x), bfhi(gv.x)}, g23 = {bflo(gv.y), bfhi(gv.y)};
        f4 acc = {};
        UNROLL for (int i = 0; i < 4; ++i) {
            u4 pp;
            UNROLL for (int k = 0; k < 2; ++k) {
                const f2 a = f2{bflo(xv[2 * i + k].x), bfhi(xv[2 * i + k].x)} * g01, c = f2{bflo(xv[2 * i + k].y), bfhi(xv[2 * i + k].y)} * g23;
                pp[2 * k] = pkb(a.x, a.y); pp[2 * k + 1] = pkb(c.x, c.y);
            }
            const u32 s0 = n == 2 * i ? 0x3f803f80u : 0u, s1 = n == 2 * i + 1 ? 0x3f803f80u : 0u;
            acc = mfma16b(pp, u4{s0, s0, s1, s1}, acc);
        }
        if (n < 8) lds[L_GS + n * 16 + (t >> 4)] = __builtin_bit_cast(u32, (acc.x + acc.y) + (acc.z + acc.w));
        lset(lds, L_C5 + wv, pv, lane);
    }
    MR[0] = *(const u4*)&lds[L_M + lane * 4]; MR[1] = *(const u4*)&lds[L_M + 256 + lane * 4];
    UNROLL for (int i = 0; i < 4; ++i) TW[i] = *(const u4*)&lds[L_M + (2 + i) * 256 + lane * 4];
    core_expand(lane, MR, MI);
    pin(MR); pin(MI); pin(TW);
    u4 PM;
    {
        lwait4(lds, L_C5, pv);
        float gg[4];
        UNROLL for (int k = 0; k < 2; ++k) {
            const f4* pp = (const f4*)&lds[L_GS + ((n >> 2) * 2 + k) * 16];
            const f4 ps = (pp[0] + pp[1]) + (pp[2] + pp[3]);
            const float lg = rbf((ps.x + ps.y) + (ps.z + ps.w));
            gg[2 * k] = rbf(1.0f / (1.0f + __expf(-lg)));
            gg[2 * k + 1] = rbf(1.0f - gg[2 * k]);
        }
        UNROLL for (int k = 0; k < 4; ++k) { gg[k] *= 0.03125f; pin(gg[k]); }
        const int r = n & 3, sa = (n >> 2) * 2;
        UNROLL for (int cc = 0; cc < 2; ++cc) {
            const int u1 = 2 * q + cc;
            const float ta = (float)(sa * u1) * 0.125f + ((r & 1) ? 0.25f : (r & 2) ? 0.5f : 0.0f);
            const float tb = (float)((sa + 1) * u1) * 0.125f + ((r & 1) ? ((r & 2) ? 0.0f : 0.5f) : 0.25f);
            const float ca = cosr(ta), sa_ = sinr(ta), cb = cosr(tb), sb_ = sinr(tb);
            float e[4] = {gg[2] * cb, gg[2] * sb_, gg[3] * cb, gg[3] * sb_};
            pin(e);
            e[0] = fmaf(gg[0], ca, e[0]); pin(e[0]); e[1] = fmaf(gg[0], sa_, e[1]); pin(e[1]); e[2] = fmaf(gg[1], ca, e[2]); pin(e[2]); e[3] = fmaf(gg[1], sa_, e[3]);
            PM[2 * cc] = pkh(e[0], e[1]); PM[2 * cc + 1] = pkh(e[2], e[3]);
        }
    }
    int a3d = (q * 8 + wv) * 32 + n, a3m = (q * 8 + 7 - wv) * 32 + 16 - n, a3z = (q * 8 + 7 - wv) * 32 + ((32 - n) & 31), a3r = wv * 256 + lane;
    float osc = __builtin_bit_cast(float, (u32)min(max((int)ex + (int)ew - 130, 1), 254) << 23) * TRUNC_FIX;
    const int ve = 2 * q * 4096 + (128 * wv + n) * 8;
    int vs0 = WS_V + b * 32768, vs1 = vs0 + 4096, vsl = WS_VL + b * 16;
    asm volatile("" : "+s"(vs0), "+s"(vs1), "+s"(vsl));
    int ov = (4 * perm32(lane & 15) + 2 * ((lane >> 4) & 1) + (lane >> 5)) * 16 + (512 * wv + b) * 2048;
    u32 x8 = wv == 3 ? 0u : vn;
    pin(a3d); pin(a3m); pin(a3z); pin(a3r); pin(PM); pin(ov); pin(osc); pin(x8);
    VMWAIT(0);
    {
        f4 z3[9];
        NOUNROLL for (int spin = 0; spin < SPIN_CAP; ++spin) {
            MEMBAR();
            u2 v0[8], v1[8];
            u4 ve8, vo8;
            int a_ = ve; pin(a_);
            if (wv == 3) {
                ve8 = __builtin_amdgcn_raw_buffer_load_b128(rws, q * 8192, vsl, SC1); vo8 = __builtin_amdgcn_raw_buffer_load_b128(rws, q * 8192 + 4096, vsl, SC1);
            } else {
                const u2 e2 = __builtin_amdgcn_raw_buffer_load_b64(rws, a_ + 1024, vs0, SC1), o2 = __builtin_amdgcn_raw_buffer_load_b64(rws, a_ + 1024, vs1, SC1);
                ve8 = u4{e2.x, e2.y, tag0, tag0}; vo8 = u4{o2.x, o2.y, tag0, tag0};
            }
            UNROLL for (int i = 0; i < 8; ++i) {
                v0[i] = __builtin_amdgcn_raw_buffer_load_b64(rws, a_ + 128 * i, vs0, SC1);
                v1[i] = __builtin_amdgcn_raw_buffer_load_b64(rws, a_ + 128 * i, vs1, SC1);
            }
            SB();
            u32 m = (((wv == 3 ? ve8.y : ve8.z) ^ tag0) | (ve8.w ^ tag0) | ((wv == 3 ? vo8.y : vo8.z) ^ tag0) | (vo8.w ^ tag0)) ? TM : 0u;
            UNROLL for (int k = 0; k < 9; ++k) {
                const int i = (k + 8) % 9;
                const u4 d = i == 8 ? u4{ve8.x, wv == 3 ? ve8.z : ve8.y, vo8.x, wv == 3 ? vo8.z : vo8.y} ^ x8 : u4{v0[i & 7].x, v0[i & 7].y, v1[i & 7].x, v1[i & 7].y} ^ vn;
                m |= d.x | d.y; m |= d.z | d.w;
                z3[i] = mfma16(PM, d);
                SB();
            }
            if (__all((m & TM) == 0u)) break;
        }
        if (wv == 0 && n == 0) { z3[0].x = 0.5f * (z3[0].x + z3[0].z); z3[0].y = 0.5f * (z3[0].y + z3[0].w); }
        if (wv == 3) { z3[8].z = 0.5f * (z3[8].x + z3[8].z); z3[8].w = 0.5f * (z3[8].y + z3[8].w); }
        u4* l4w = (u4*)&lds[L_A1];
        UNROLL for (int p = 0; p < 2; ++p)
            l4w[a3d + 16 * p] = u4{pkh(z3[p].x, z3[p + 2].x), pkh(z3[p + 4].x, z3[p + 6].x), pkh(z3[p].y, z3[p + 2].y), pkh(z3[p + 4].y, z3[p + 6].y)};
        l4w[a3m] = u4{pkh(z3[7].z, z3[5].z), pkh(z3[3].z, z3[1].z), pkh(z3[7].w, z3[5].w), pkh(z3[3].w, z3[1].w)};
        f2 m[4];
        UNROLL for (int i = 0; i < 4; ++i) m[i] = n == 0 ? f2{z3[8 - 2 * i].z, z3[8 - 2 * i].w} : f2{z3[6 - 2 * i].z, z3[6 - 2 * i].w};
        l4w[a3z] = u4{pkh(m[0].x, m[1].x), pkh(m[2].x, m[3].x), pkh(m[0].y, m[1].y), pkh(m[2].y, m[3].y)};
        lset(lds, L_C6 + wv, 2u * pv - 1u, lane);
    }
    {
        u4 in[4];
        NOUNROLL for (int spin = 0; spin < LSPIN_CAP; ++spin) {
            MEMBAR();
            const u4 c = *(const u4*)&lds[L_C6];
            MEMBAR();
            UNROLL for (int s = 0; s < 4; ++s) in[s] = ((const u4*)&lds[L_A1])[a3r + s * 64];
            pin(in[0]);
            if (rfl(min4u(c)) >= 2u * pv - 1u) break;
        }
        f16v zr, zi;
        core_fft<false>(in, MR, MI, TW, zr, zi);
        zr *= osc; zi *= osc;
        const rsrc_t ro = mkrs(out, 2048 * 2048);
#define OUT_ROW(z, off) do { u4 lo, hi; \
            UNROLL for (int j = 0; j < 4; ++j) { const auto sw_ = __builtin_amdgcn_permlane16_swap(pkb(z[2 * j], z[2 * j + 1]), pkb(z[8 + 2 * j], z[8 + 2 * j + 1]), false, false); lo[j] = sw_[0]; hi[j] = sw_[1]; } \
            store128<SC1>(lo, ro, ov + (off)); store128<SC1>(hi, ro, ov + (off) + 1024); } while (0)
        OUT_ROW(zr, 256 * 2048);
        OUT_ROW(zi, 0);
    }
    vd = lwait(lds, L_VD, 1u);
    if (pass == 1 || !(vd & 0x20000u)) break;
    lset(lds, L_C6 + wv, 2u, lane);
    lwait4(lds, L_C6, 2u);
    VMWAIT(0);
    }
}

extern "C" void kernel_launch(void* const* d_in, const int* in_sizes, int n_in,
                              void* d_out, int out_size, void* d_ws, size_t ws_size,
                              hipStream_t stream) {
    gfnet_fused<<<256, 512, 0, stream>>>(d_in[0], d_in[6], d_in[1], d_in[2], d_in[4], d_in[5], (char*)d_ws, d_out);
}
```

```cpp
#include <hip/hip_runtime.h>
#include <hip/hip_bf16.h>
#include <stdint.h>

typedef _Float16 h2 __attribute__((ext_vector_type(2)));
typedef _Float16 h8 __attribute__((ext_vector_type(8)));
typedef __bf16 b2 __attribute__((ext_vector_type(2)));
typedef __bf16 b8 __attribute__((ext_vector_type(8)));
typedef float f2 __attribute__((ext_vector_type(2)));
typedef float f4 __attribute__((ext_vector_type(4)));
typedef float f16v __attribute__((ext_vector_type(16)));
typedef short s4 __attribute__((ext_vector_type(4)));
typedef unsigned u32;
typedef u32 u2 __attribute__((ext_vector_type(2)));
typedef u32 u4 __attribute__((ext_vector_type(4)));
typedef unsigned short us2 __attribute__((ext_vector_type(2)));
typedef __amdgpu_buffer_rsrc_t rsrc_t;
#define DI __device__ __forceinline__
#define UNROLL _Pragma("unroll")
#define NOUNROLL _Pragma("unroll 1")
#define LAS __attribute__((address_space(3)))
#define SB() __builtin_amdgcn_sched_barrier(0)
#define MEMBAR() asm volatile("" ::: "memory")
#define VMWAIT(n) asm volatile("s_waitcnt vmcnt(%0)" :: "n"(n) : "memory")

#define MARGIN 2
#define QUICK 145

constexpr u32 FSTR = 16, HSTR = 32;
constexpr u32 WS_Z = 0;
constexpr u32 WS_V = 4u << 20;
constexpr u32 WS_VL = 12u << 20;
constexpr u32 WS_HB = WS_VL + (32u << 10);
constexpr u32 WS_F = WS_HB + (32u << 10);
constexpr u32 WS_E = WS_F + 2048 * FSTR;
constexpr u32 WS_BYTES = WS_E + 4096;
constexpr int SC1 = 16, NT = 2;
constexpr int SPIN_CAP = 8192, LSPIN_CAP = 1 << 17;
constexpr int OOB = 0x7ffffff0;
constexpr u32 TM = 0x00010001u;
constexpr float TRUNC_FIX = 1.00066f;

template <class T> DI void pin(T& v) { asm volatile("" : "+v"(v)); }
template <class T, int N> DI void pin(T (&v)[N]) { UNROLL for (int i = 0; i < N; ++i) pin(v[i]); }
DI u32 opq(u32 v) { asm("" : "+v"(v)); return v; }
DI u32 pkh(float lo, float hi) { h2 r; r.x = (_Float16)lo; r.y = (_Float16)hi; return __builtin_bit_cast(u32, r); }
DI u32 pkb(float lo, float hi) { b2 r; r.x = (__bf16)lo; r.y = (__bf16)hi; return __builtin_bit_cast(u32, r); }
DI float bflo(u32 v) { return __builtin_bit_cast(float, v << 16); }
DI float bfhi(u32 v) { return __builtin_bit_cast(float, v & 0xffff0000u); }
DI float rbf(float v) { return (float)(__bf16)v; }
DI h2 H(u32 v) { return __builtin_bit_cast(h2, v); }
DI u32 U(h2 v) { return __builtin_bit_cast(u32, v); }
DI f16v mfma32(u4 a, u4 b, f16v c) { return __builtin_amdgcn_mfma_f32_32x32x16_f16(__builtin_bit_cast(h8, a), __builtin_bit_cast(h8, b), c, 0, 0, 0); }
DI f4 mfma16(u4 a, u4 b) { return __builtin_amdgcn_mfma_f32_16x16x32_f16(__builtin_bit_cast(h8, a), __builtin_bit_cast(h8, b), f4{}, 0, 0, 0); }
DI f4 mfma16b(u4 a, u4 b, f4 c) { return __builtin_amdgcn_mfma_f32_16x16x32_bf16(__builtin_bit_cast(b8, a), __builtin_bit_cast(b8, b), c, 0, 0, 0); }
DI u32 hmul(u32 a, u32 b) { return U(H(a) * H(b)); }
DI u32 cmul_swap(u32 x, u32 w) { const h2 a = H(opq(x)), c = H(opq(w)); h2 n = c; n.y = -n.y; return U(__builtin_elementwise_fma(a.yy, n, a.xx * c.yx)); }
DI u32 cmul_conj(u32 x, u32 t) { const h2 a = H(opq(x)), c = H(opq(t)); h2 n = c.yy; n.y = -n.y; return U(__builtin_elementwise_fma(a.yx, n, a * c.xx)); }
DI u32 pkmax(u32 a, u32 b) { return __builtin_bit_cast(u32, __builtin_elementwise_max(__builtin_bit_cast(us2, a), __builtin_bit_cast(us2, b))); }
DI float sinr(float r) { return __builtin_amdgcn_sinf(r); }
DI float cosr(float r) { return __builtin_amdgcn_cosf(r); }
DI u32 rot(float ph) { return pkh(cosr(ph), sinr(ph)); }
DI u32 quarter(u32 r, u32 f) { u32 o; asm("v_pk_mul_f16 %0, %1, %2 op_sel:[1,0] op_sel_hi:[0,1] neg_lo:[1,0]" : "=v"(o) : "v"(r), "v"(f)); return o; }
DI u4 quarter(u4 r) { u32 one = 0x3c003c00u; pin(one); return u4{quarter(r.x, one), quarter(r.y, one), quarter(r.z, one), quarter(r.w, one)}; }
DI int perm32(int n) { return (n & 0x13) | ((n & 4) << 1) | ((n & 8) >> 1); }
DI u32 max4u(u4 v) { return max(max(v.x, v.y), max(v.z, v.w)); }
DI u32 min4u(u4 v) { return min(min(v.x, v.y), min(v.z, v.w)); }
DI u32 mark(u32 v, u32 par, u32 tm) { return (par & tm) | (v & ~tm); }
DI u32 rfl(u32 v) { return __builtin_amdgcn_readfirstlane(v); }
DI rsrc_t mkrs(const void* p, u32 bytes) { return __builtin_amdgcn_make_buffer_rsrc((void*)p, (short)0, (int)bytes, 0x00020000); }
template <int AUX> DI void store128(u4 v, rsrc_t r, int off) { __builtin_amdgcn_raw_buffer_store_b128(v, r, off, 0, AUX); }
template <int CTRL> DI u32 dpp(u32 v) { return __builtin_amdgcn_update_dpp(0u, v, CTRL, 0xf, 0xf, true); }
DI u32 wave_maxu(u32 v) {
    v = max(v, dpp<0xB1>(v)); v = max(v, dpp<0x4E>(v)); v = max(v, dpp<0x141>(v)); v = max(v, dpp<0x140>(v));
    return max(max((u32)__builtin_amdgcn_readlane(v, 0), (u32)__builtin_amdgcn_readlane(v, 16)), max((u32)__builtin_amdgcn_readlane(v, 32), (u32)__builtin_amdgcn_readlane(v, 48)));
}
#define STAMP_DECL
#define STAMP(i)
#define STAMP_OUT()

DI void core_setup(int lane, int s, u4& tw, u2& m) {
    float fc = (float)perm32(lane & 31) * (1.0f / 1024.0f), p0 = (float)(4 * (lane >> 5) + 8 * s) * fc, p1 = (float)(4 * (lane >> 5) + 8 * (s >> 1) + 2 * (s & 1)) * fc * 32.0f;
    pin(fc); pin(p0); pin(p1);
    float tc[4], ts[4];
    UNROLL for (int i = 0; i < 4; ++i) { const float pt = fmaf((float)i, fc, p0); tc[i] = cosr(pt); ts[i] = sinr(pt); }
    tw = u4{pkh(tc[0], tc[1]), pkh(tc[2], tc[3]), pkh(ts[0], ts[1]), pkh(ts[2], ts[3])};
    const float p2 = fmaf(32.0f, fc, p1);
    m = u2{pkh(cosr(p1), cosr(p2)), pkh(sinr(p1), sinr(p2))};
}
DI void core_expand(int lane, u4 (&MR)[4], u4 (&MI)[4]) {
    const u32 sg = (lane & 1) ? 0x80008000u : 0u;
    MR[2] = MR[0] ^ sg; MR[3] = MR[1] ^ sg;
    UNROLL for (int s = 0; s < 4; ++s) MI[s] = u4{MR[s].z ^ 0x80008000u, MR[s].w ^ 0x80008000u, MR[s].x, MR[s].y};
}
template <bool OUT_B> DI void core_fft(const u4 (&in)[4], const u4 (&MR)[4], const u4 (&MI)[4], const u4 (&TW)[4], f16v& outr, f16v& outi) {
    f16v yr = {}, yi = {};
    UNROLL for (int s = 0; s < 4; ++s) yr = mfma32(in[s], MR[s], yr);
    SB();
    yi = mfma32(in[0], MI[0], yi); yi = mfma32(in[1], MI[1], yi);
    SB();
    h2 t0[8], t1[8];
#define TW_RE(p) do { const h2 pr_ = H(pkh(yr[2 * (p)], yr[2 * (p) + 1])); t0[p] = pr_ * H(TW[(p) >> 1][(p) & 1]); t1[p] = pr_ * H(TW[(p) >> 1][2 + ((p) & 1)]); } while (0)
    TW_RE(0); TW_RE(1); SB();
    yi = mfma32(in[2], MI[2], yi);
    TW_RE(2); TW_RE(3); SB();
    yi = mfma32(in[3], MI[3], yi);
    TW_RE(4); TW_RE(5); TW_RE(6); TW_RE(7); SB();
    outr = f16v{}; outi = f16v{};
    u4 d[4];
#define TW_IM(s) do { UNROLL for (int j = 0; j < 2; ++j) { const int p_ = 2 * (s) + j; const h2 pi_ = H(pkh(yi[2 * p_], yi[2 * p_ + 1])); \
        d[s][j] = U(__builtin_elementwise_fma(pi_, H(TW[s][2 + j]), t0[p_])); d[s][2 + j] = U(__builtin_elementwise_fma(pi_, H(TW[s][j]), -t1[p_])); } } while (0)
    TW_IM(0); SB();
    UNROLL for (int s = 0; s < 4; ++s) {
        outr = OUT_B ? mfma32(MR[s], d[s], outr) : mfma32(d[s], MR[s], outr);
        if (s < 3) TW_IM(s + 1);
        SB();
    }
    UNROLL for (int s = 0; s < 4; ++s) outi = OUT_B ? mfma32(MI[s], d[s], outi) : mfma32(d[s], MI[s], outi);
}

DI u32 tag_of(u32 epoch, u32 idx) { u32 h = (epoch ^ (idx * 0x9E3779B1u)) * 0x85EBCA6Bu; return h ^ (h >> 15); }
DI u32 valid_bits(u32 epoch, int slot) { return (((0x6A3Cu >> slot) ^ epoch) & 1u) ? 0x00000001u : 0x00010000u; }
DI int cls_of(int b) { const int row = (b & 7) >> 1; return row == 0 ? 4 * (b >> 7) : ((b & 1) ? 8 - row : row); }
DI int tile_of(int b) { return (b & 6) == 0 ? 16 * (b & 1) + ((b >> 3) & 15) : b >> 3; }

constexpr int L_C1 = 0, L_C3 = 4, L_C4 = 8, L_C5 = 12, L_C6 = 16;
constexpr int L_EW = 20;
constexpr int L_WW = 24;
constexpr int L_PR = 28;
constexpr int L_LD = 32;
constexpr int L_SX = 36;
constexpr int L_ZL = 37;
constexpr int L_VD = 38;
constexpr int L_P2 = 39;
constexpr int L_GS = 64;
constexpr int L_M = 192;
constexpr int L_A1 = L_M + 1536;
constexpr int L_A2 = L_A1 + 4096;
constexpr int L_B = L_A2 + 4096;
constexpr int L_W = L_B + 8192;
constexpr int L_END = L_W + 64 * 256;

DI void lset(u32* lds, int idx, u32 v, int lane) { MEMBAR(); if (lane == 0) lds[idx] = v; MEMBAR(); }
DI u32 lget(u32* lds, int idx) { MEMBAR(); return rfl(lds[idx]); }
DI u32 lwait(u32* lds, int idx, u32 atleast) {
    u32 v = 0;
    NOUNROLL for (int spin = 0; spin < LSPIN_CAP; ++spin) { v = lget(lds, idx); if (v >= atleast) break; __builtin_amdgcn_s_sleep(1); }
    MEMBAR();
    return v;
}
DI u32 lwait4(u32* lds, int idx, u32 atleast) {
    u4 v;
    NOUNROLL for (int spin = 0; spin < LSPIN_CAP; ++spin) { MEMBAR(); v = *(const u4*)&lds[idx]; if (rfl(min4u(v)) >= atleast) break; __builtin_amdgcn_s_sleep(1); }
    MEMBAR();
    return rfl(max4u(v));
}
struct MagMax {
    h2 hi = {}, lo = {};
    DI void add(u32 a, u32 c) {
        hi = __builtin_elementwise_maximum(__builtin_elementwise_maximum(hi, H(a)), H(c));
        lo = __builtin_elementwise_minimum(__builtin_elementwise_minimum(lo, H(a)), H(c));
    }
    DI u32 exponent() const {
        const u32 mx = pkmax(U(hi) & 0x7fff7fffu, U(lo) & 0x7fff7fffu);
        return wave_maxu(max(mx & 0xffffu, mx >> 16)) >> 7;
    }
};
DI u32 clampe(u32 e) { return min(max(e, 8u), 240u); }

__global__ __launch_bounds__(384, 2) void gfnet_fused(const void* x, const void* gw, const void* war, const void* wai, const void* wbr, const void* wbi,
                                                      char* ws, void* out) {
    __shared__ __attribute__((aligned(16))) u32 lds[L_END];
    STAMP_DECL;
    STAMP(0);
    u32 code;
    {
        unsigned long long pc0; int len;
        asm volatile("s_getpc_b64 %0\n.Lgf_begin:\n\ts_mov_b32 %1, .Lgf_end-.Lgf_begin" : "=s"(pc0), "=s"(len));
        code = __builtin_amdgcn_raw_buffer_load_b32(mkrs((const void*)pc0, (u32)max(len, 0)), (threadIdx.x >> 6) == 4 ? ((blockIdx.x >> 5) * 64 + (threadIdx.x & 63)) * 64 : OOB, 0, 0);
        SB();
    }
    asm volatile("" :: "s"(x), "s"(gw), "s"(war), "s"(wai), "s"(wbr), "s"(wbi), "s"(ws), "s"(out));
    const int wv0 = __builtin_amdgcn_readfirstlane(threadIdx.x >> 6);
    const rsrc_t rws = mkrs(ws, WS_BYTES);

    if (wv0 >= 4) {
        const u32 tstart = (u32)__builtin_amdgcn_s_memrealtime();
        const int wv = wv0, b = blockIdx.x, cls = cls_of(b), tile = tile_of(b);
        const int lane = threadIdx.x & 63, q = lane >> 4, n = lane & 15;
        const rsrc_t rw[2] = {mkrs(wv == 4 ? war : wbr, 2048 * 1026), mkrs(wv == 4 ? wai : wbi, 2048 * 1026)};
        if (wv == 4) { lds[lane] = 0u; asm volatile("s_waitcnt lgkmcnt(0)" ::: "memory"); }
        MEMBAR();
        __builtin_amdgcn_s_barrier();
        MEMBAR();
        const bool helper = wv == 5 && tile == 31;
        u4 GR, GI, FR, FI, HR, HI, T1, T2;
        u32 ev;
        {
            const int wvo = (lane >> 2) * (128 * 1026) + min(lane & 3, 2) * 16, wso = (cls * 513 + 16 * tile - cls) * 2;
#define WLOAD(r) do { UNROLL for (int st = 0; st < 4; ++st) { const int c_ = 4 * st + ((tile + (r)) & 3); \
                UNROLL for (int a = 0; a < 2; ++a) __builtin_amdgcn_raw_ptr_buffer_load_lds(rw[a], (LAS void*)&lds[L_W + (c_ * 4 + 2 * (wv - 4) + a) * 256], 16, wvo, wso + c_ * 8208, 0, 0); } SB(); } while (0)
            __builtin_amdgcn_s_sleep(2);
            WLOAD(0);
            const bool split = (b & 6) == 0;
            const int er = split ? 16 * (tile & 15) + ((lane >> 1) & 15) : 8 * tile + ((lane >> 1) & 7);
            const int ec = (split ? (tile & 16) * 16 : 0) - cls + ((lane & 1) ? (split ? 262 : 518) : 0);
            u32 edge[2];
            UNROLL for (int a = 0; a < 2; ++a) edge[a] = __builtin_amdgcn_raw_buffer_load_b32(rw[a], ((cls + 8 * er) * 513 + ec) * 2, 0, 0);
            ev = __builtin_amdgcn_raw_buffer_load_b32(rws, 0, WS_E, SC1);
            const u4 smp = __builtin_amdgcn_raw_buffer_load_b128(rw[0], (lane >> 3) * (2 * 131328 + 128) + (lane & 7) * 16, 0, 0);
            const u4 smp1 = __builtin_amdgcn_raw_buffer_load_b128(rw[1], (lane >> 3) * (2 * 131328 + 128) + (lane & 7) * 16, 0, 0);
            MEMBAR();
            if (helper) {
                UNROLL for (int j = 0; j < 4; ++j) {
                    GR[j] = rot((float)((4 * q + j) * (cls + 8 * n)) * (1.0f / 128.0f) + (cls == 4 ? 0.25f : 0.0f));
                    HR[j] = rot((float)(n * (cls + 8 * (4 * q + j))) * (1.0f / 128.0f));
                    FR[j] = rot((float)(n * (4 * q + j)) * (1.0f / 16.0f));
                    T1[j] = rot((float)((4 * q + j) * (cls + 8 * n)) * (1.0f / 2048.0f));
                    T2[j] = rot((float)(n * (cls + 8 * (4 * q + j))) * (1.0f / 2048.0f));
                }
                GI = quarter(GR); HI = quarter(HR); FI = quarter(FR);
                pin(GR); pin(GI); pin(HR); pin(HI); pin(FR); pin(FI); pin(T1); pin(T2);
            }
            VMWAIT(0);
            STAMP(1);
            if ((int)((u32)__builtin_amdgcn_s_memrealtime() - tstart) < QUICK)
                UNROLL for (int a = 0; a < 2; ++a) __builtin_amdgcn_raw_buffer_store_b32(0u, rws, lane * 128, WS_V + b * 32768 + (2 * (wv - 4) + a) * 8192, SC1);
            MEMBAR();
            WLOAD(1); WLOAD(2); WLOAD(3);
            VMWAIT(24);
            asm volatile("ds_write_b64 %0, %1" :: "v"((u32)(size_t)(LAS u32*)&lds[L_LD + 2 * (wv - 4)]), "v"(u2{1u, 1u}) : "memory");
            pin(edge); pin(code);
            MagMax ms; ms.add(smp.x, smp.y); ms.add(smp.z, smp.w); ms.add(smp1.x, smp1.y); ms.add(smp1.z, smp1.w);
            const u32 es = clampe(ms.exponent() + MARGIN);
            VMWAIT(0);
            MEMBAR(); if (lane == 0) *(u2*)&lds[L_PR + 2 * (wv - 4)] = u2{es, es}; MEMBAR();
            STAMP(2);
        }
        const u32 ep0 = rfl(ev) + 1u;
        if (wv == 4) {
            lwait4(lds, L_PR, 1u);
            MagMax m;
            NOUNROLL for (int c0 = 0; c0 < 64; c0 += 16) { UNROLL for (int c = 0; c < 16; ++c) { const u4 v = *(const u4*)&lds[L_W + (c0 + c) * 256 + lane * 4]; m.add(v.x, v.y); m.add(v.z, v.w); } }
            const u32 ew = clampe(m.exponent()), ex = lwait4(lds, L_EW, 1u);
            __builtin_amdgcn_raw_buffer_store_b64(u2{ex | (ew << 8), tag_of(ep0, b + 1)}, rws, lane < 8 ? lane * (int)(256 * FSTR) : OOB, WS_F + b * FSTR, SC1);
            lwait(lds, L_P2, 1u);
            __builtin_amdgcn_s_sleep(8);
            u2 f[4]; u32 tg[4];
            UNROLL for (int j = 0; j < 4; ++j) tg[j] = tag_of(ep0, 4 * lane + j + 1);
            NOUNROLL for (int spin = 0; spin < SPIN_CAP; ++spin) {
                MEMBAR();
                UNROLL for (int j = 0; j < 4; ++j) f[j] = __builtin_amdgcn_raw_buffer_load_b64(rws, lane * (4 * FSTR) + j * FSTR, WS_F + (b & 7) * (256 * FSTR), SC1);
                if (__all(f[0].y == tg[0] && f[1].y == tg[1] && f[2].y == tg[2] && f[3].y == tg[3])) break;
                __builtin_amdgcn_s_sleep(8);
            }
            const u4 e4 = {f[0].x, f[1].x, f[2].x, f[3].x};
            const u32 gx = wave_maxu(max4u(e4 & 0xffu)), gwm = wave_maxu(max4u((e4 >> 8) & 0xffu));
            const bool redo = gx > lget(lds, L_SX) || gwm > lwait4(lds, L_PR, 1u);
            lset(lds, L_VD, gx | (gwm << 8) | (redo ? 0x20000u : 0x10000u), lane);
            if (b == 0 && lane == 0) __builtin_amdgcn_raw_buffer_store_b32(ep0 + (redo ? 1u : 0u), rws, 0, WS_E, SC1);
            STAMP(5);
        } else if (helper) {
            __builtin_amdgcn_s_setprio(2);
            u32 ew = lwait4(lds, L_PR, 1u);
            NOUNROLL for (u32 pass = 0; pass < 2; ++pass) {
                u4 WH[2];
                const float sw = __builtin_bit_cast(float, (249u - ew) << 23);
                UNROLL for (int r = 0; r < 4; ++r) UNROLL for (int f = 0; f < 2; ++f) {
                    const unsigned short* wp = (const unsigned short*)&lds[L_W + (n * 4 + 2 * f) * 256 + (4 * q + r) * 16] + 16 + cls;
                    float re = bflo(wp[0]) * sw, im = bflo(wp[512]) * sw;
                    pin(re);
                    WH[f][r] = pkh(re, im);
                }
                pin(WH);
                STAMP(3);
                lwait(lds, L_ZL, pass + 1u);
                const u32 tg = tag_of(ep0 + pass, 0u);
                u4 hz;
                NOUNROLL for (int spin = 0; spin < SPIN_CAP; ++spin) {
                    MEMBAR();
                    u32 bad = 0;
                    UNROLL for (int j = 0; j < 4; ++j) { const u2 g = __builtin_amdgcn_raw_buffer_load_b64(rws, (64 * q + n + 16 * j) * HSTR, WS_HB + ((b & 7) >> 1) * (256 * HSTR), SC1); hz[j] = g.x & ~TM; bad |= g.y ^ tg; }
                    if (__all(bad == 0u)) break;
                    __builtin_amdgcn_s_sleep(4);
                }
                STAMP(4);
                const u32 f16 = cls == 0 ? 0x00002c00u : cls == 4 ? 0x2c000000u : cls > 4 ? 0xac002c00u : 0x2c002c00u;
                u4 d, o[2];
                UNROLL for (int j = 0; j < 4; ++j) d[j] = hmul(hz[j], f16);
                f4 yr = mfma16(d, GR), yi = mfma16(d, GI);
                UNROLL for (int r = 0; r < 4; ++r) d[r] = cmul_conj(pkh(yr[r], yi[r]), T1[r]);
                const f4 xr = mfma16(FR, d), xi = mfma16(FI, d);
                UNROLL for (int f = 0; f < 2; ++f) {
                    UNROLL for (int r = 0; r < 4; ++r) d[r] = cmul_swap(pkh(xr[r], xi[r]), WH[f][r]);
                    yr = mfma16(d, FR); yi = mfma16(d, FI);
                    UNROLL for (int r = 0; r < 4; ++r) o[f][r] = cmul_conj(pkh(yr[r], yi[r]), T2[r]);
                }
                const f4 p0 = mfma16(HR, o[0]), p1 = mfma16(HI, o[0]), p2 = mfma16(HR, o[1]), p3 = mfma16(HI, o[1]);
                UNROLL for (int r = 0; r < 4; ++r)
                    store128<SC1>(u4{pkh(p0[r], p1[r]) & ~TM, tg, pkh(p2[r], p3[r]) & ~TM, tg}, rws, (64 * q + n) * 16 + WS_VL + r * 256 + cls * 4096);
                STAMP(5);
                const u32 vd = lwait(lds, L_VD, 1u);
                if (!(vd & 0x20000u)) break;
                ew = (vd >> 8) & 0xffu;
            }
        }
        asm volatile(".Lgf_end:");
        STAMP_OUT();
        return;
    }

    u32 vd = 0, epoch = 0;
    NOUNROLL for (u32 pass = 0; pass < 2; ++pass) {
    int t = threadIdx.x, wv = wv0, b = blockIdx.x;
    pin(t);
    asm volatile("" : "+s"(wv));
    asm volatile("" : "+s"(b));
    const int lane = t & 63, q = lane >> 4, n = lane & 15;
    const u32 pv = pass + 1u;
    u2 xv[8];
    const rsrc_t rx = mkrs(x, 2048 * 2048);
    const u4 smp = __builtin_amdgcn_raw_buffer_load_b128(rx, (lane >> 3) * (524288 + 2048 + 256) + (lane & 7) * 16, 0, 0);
    UNROLL for (int s1 = 0; s1 < 8; ++s1) xv[s1] = __builtin_amdgcn_raw_buffer_load_b64(rx, t * 8, (256 * s1 + b) * 2048, NT);
    const u32 ev = __builtin_amdgcn_raw_buffer_load_b32(rws, 0, WS_E, SC1);
    const u2 gv = __builtin_amdgcn_raw_buffer_load_b64(mkrs(gw, 2048), t * 8, 0, 0);
    MEMBAR();
    SB();
    if (pass == 0) { __builtin_amdgcn_s_barrier(); MEMBAR(); __builtin_amdgcn_s_setprio(1); }
    u4 MR[4], MI[4], TW[4];
    {
        u4 tw; u2 m;
        core_setup(lane, wv, tw, m);
        *(u4*)&lds[L_M + (2 + wv) * 256 + lane * 4] = tw;
        lds[L_M + (wv >> 1) * 256 + lane * 4 + (wv & 1)] = m.x; lds[L_M + (wv >> 1) * 256 + lane * 4 + 2 + (wv & 1)] = m.y;
    }
    const int zc = perm32(lane & 31) + (lane >> 5) * 256, zcu = zc + ((lane & 31) == 0 ? 543 : 511);
    int zl = (zc >> 4) * 16384 + (zc & 15) * 4, zu = (zcu >> 4) * 16384 + (zcu & 15) * 4;
    int zh = lane == 0 ? 0 : OOB;
    int tro = L_A1 + wv * 1024 + (4 * (lane >> 5) + ((lane & 15) >> 2)) * 16 + ((lane >> 4) & 1) * 8 + (lane & 3) * 2;
    u32 tm = TM, vl0 = valid_bits(0u, zc & 15), vu0 = valid_bits(0u, zcu & 15);
    pin(zl); pin(zu); pin(zh); pin(tro); pin(tm); pin(vl0); pin(vu0);
    STAMP(1);
    SB();
    pin(code);
    u32 ex;
    {
        MagMax ms; ms.add(smp.x, smp.y); ms.add(smp.z, smp.w);
        ex = pass ? (vd & 0xffu) : clampe(ms.exponent() + MARGIN);
    }
    STAMP(2);
    u32 tag0, flip;
    u32 z[16], z512;
    {
        const float sc = __builtin_bit_cast(float, (250u - ex) << 23);
        const float rs = sc * 0.70710678118654752f;
        u32 o[8][2];
        UNROLL for (int j = 0; j < 2; ++j) {
            f2 v[8];
            UNROLL for (int s1 = 0; s1 < 8; ++s1) v[s1] = f2{bflo(xv[s1][j]), bfhi(xv[s1][j])};
            const f2 a0 = v[0] + v[4], a1 = v[1] + v[5], a2 = v[2] + v[6], a3 = v[3] + v[7];
            const f2 b0 = (v[0] - v[4]) * sc, b1 = v[1] - v[5], b2_ = (v[2] - v[6]) * sc, b3 = v[3] - v[7];
            const f2 e0 = a0 + a2, e1 = (a1 + a3) * sc, dm = b1 - b3, dp = b1 + b3;
            f2 r[8];
            r[0] = e0 * sc + e1; r[1] = e0 * sc - e1;
            r[2] = dm * rs + b0; r[3] = -(dp * rs) - b2_;
            r[4] = (a0 - a2) * sc; r[5] = (a3 - a1) * sc;
            r[6] = b0 - dm * rs; r[7] = b2_ - dp * rs;
            UNROLL for (int p = 0; p < 8; ++p) o[p][j] = pkh(r[p].x, r[p].y);
        }
        UNROLL for (int p = 0; p < 8; ++p) *(u2*)&lds[L_A1 + p * 512 + 2 * t] = u2{o[p][0], o[p][1]};
        lset(lds, L_C1 + wv, pv, lane);
        epoch = (pass ? epoch : rfl(ev)) + 1u; tag0 = tag_of(epoch, 0u); flip = (epoch & 1u) ? TM : 0u;
        const u32 vl = vl0 ^ flip, vu = vu0 ^ flip;
        u4 in[4];
        NOUNROLL for (int spin = 0; spin < LSPIN_CAP; ++spin) {
            MEMBAR();
            const u4 c = *(const u4*)&lds[L_C1];
            MEMBAR();
            MR[0] = *(const u4*)&lds[L_M + lane * 4]; MR[1] = *(const u4*)&lds[L_M + 256 + lane * 4];
            UNROLL for (int s = 0; s < 4; ++s) {
                const u2 re = __builtin_bit_cast(u2, __builtin_amdgcn_ds_read_tr16_b64_v4i16((LAS s4*)&lds[tro + s * 128]));
                const u2 im = __builtin_bit_cast(u2, __builtin_amdgcn_ds_read_tr16_b64_v4i16((LAS s4*)&lds[tro + s * 128 + 512]));
                in[s] = u4{re.x, re.y, im.x, im.y};
            }
            pin(MR[0]); pin(MR[1]);
            if (rfl(min4u(c)) >= pv) break;
        }
        UNROLL for (int i = 0; i < 4; ++i) TW[i] = *(const u4*)&lds[L_M + (2 + i) * 256 + lane * 4];
        STAMP(3);
        core_expand(lane, MR, MI);
        f16v zr, zi;
        core_fft<true>(in, MR, MI, TW, zr, zi);
        UNROLL for (int r = 0; r < 16; ++r) z[r] = pkh(zr[r], zi[r]);
        z512 = z[8];
        const u32 za = __builtin_amdgcn_readlane(z[8], 32), zb = __builtin_amdgcn_readlane(z[0], 0);
        MEMBAR();
        const u32 zt = lane < 32 ? za : zb;
        UNROLL for (int r = 0; r < 8; ++r) z[r] = mark(z[r], vl, tm);
        UNROLL for (int r = 8; r < 16; ++r) z[r] = mark((lane & 31) == 0 ? (r < 15 ? z[r + 1] : zt) : z[r], vu, tm);
    }
    pin(z);
    STAMP(4);
    const int zs = WS_Z + wv * 64 * 16384 + (((b >> 2) & 3) * 64 + (b & 3) * 16 + ((b >> 4) & 3) * 4 + (b >> 6)) * 64;
    bool zsent = false;
#define TRY_STORE_Z(force) do { if (!zsent) { \
        bool go = true; \
        if (force) lwait4(lds, L_LD, 1u); else { MEMBAR(); go = rfl(min4u(*(const u4*)&lds[L_LD])) != 0u; MEMBAR(); } \
        if (go) { \
            UNROLL for (int r = 0; r < 16; ++r) __builtin_amdgcn_raw_buffer_store_b32(z[r], rws, r < 8 ? zl : zu, zs + (r & 7) * 32768, SC1); \
            __builtin_amdgcn_raw_buffer_store_b64(u2{z512, tag0}, rws, zh, WS_HB + (wv * 256 + b) * HSTR, SC1); \
            zsent = true; } } SB(); } while (0)
    TRY_STORE_Z(false);
    const int row = (b & 7) >> 1, cls = cls_of(b), tile = tile_of(b);
    const bool packed = row == 0;
    u4 FR, FI, GR[4], GI[4], HR[4], HI[4], sel1;
    {
        const float ofs = cls == 4 ? 0.25f : 0.0f;
        float gu = (float)(cls + 8 * n) * (1.0f / 2048.0f), g0 = (float)(64 * q + 4 * wv) * gu + ofs;
        u32 s16 = 0x2c002c00u;
        pin(gu); pin(g0); pin(s16);
        UNROLL for (int i = 0; i < 4; ++i) UNROLL for (int j = 0; j < 4; ++j) { const u32 g = rot(fmaf((float)(16 * j + i), gu, g0)); GR[i][j] = hmul(g, s16); GI[i][j] = quarter(g, s16); }
    }
    pin(GR); pin(GI);
    {
        MagMax m;
        UNROLL for (int s1 = 0; s1 < 8; ++s1) m.add(xv[s1].x, xv[s1].y);
        if (wv == 0) lset(lds, L_SX, ex, lane);
        lset(lds, L_EW + wv, clampe(m.exponent()), lane);
    }
    TRY_STORE_Z(false);
    UNROLL for (int j = 0; j < 4; ++j) FR[j] = rot((float)(n * (4 * q + j)) * (1.0f / 16.0f));
    FI = quarter(FR);
    {
        float hs[4], hu[4];
        UNROLL for (int i = 0; i < 4; ++i) { hs[i] = (float)(16 * n + 4 * wv + i) * (1.0f / 2048.0f); hu[i] = (float)(cls + 32 * q + 8 * i); }
        pin(hs); pin(hu);
        UNROLL for (int i = 0; i < 4; ++i) { UNROLL for (int j = 0; j < 4; ++j) { float a = hs[i] * hu[j]; pin(a); HR[i][j] = rot(a); } HI[i] = quarter(HR[i]); }
        const int d = n + cls - 8 * q;
        UNROLL for (int j = 0; j < 4; ++j) sel1[j] = (d >> 1) == j ? ((d & 1) ? 0xffff0000u : 0xffffu) : 0u;
    }
    pin(FR); pin(FI); pin(HR); pin(HI); pin(sel1);
    TRY_STORE_Z(true);
    STAMP(5);
    VMWAIT(4);
    u4 WA[4], WB[4];
    u32 ew = 0;
    UNROLL for (int i = 0; i < 4; ++i) asm volatile("" : "=v"(WA[i]), "=v"(WB[i]));
#define WIDEN(es) do { \
        u4 wr[4][4]; \
        int wl = L_W / 4 + wv * 1024 + n * 4 + min(q, 2); pin(wl); \
        UNROLL for (int i = 0; i < 4; ++i) UNROLL for (int a = 0; a < 4; ++a) wr[i][a] = ((const u4*)lds)[wl + (i * 4 + a) * 64]; \
        ew = pass ? (vd >> 8) & 0xffu : (es); \
        const u32 sw = ((249u - ew - (packed ? 1u : 0u)) << 7) * 0x10001u;       \
        const u4 sels = sel1 & sw; \
        SB(); \
        f4 wf[4][4]; \
        UNROLL for (int k = 0; k < 10; ++k) { \
            if (k < 8) { wf[k >> 1][2 * (k & 1)] = mfma16b(wr[k >> 1][2 * (k & 1)], sels, f4{}); wf[k >> 1][2 * (k & 1) + 1] = mfma16b(wr[k >> 1][2 * (k & 1) + 1], sels, f4{}); } \
            if (k >= 2) { const int i_ = (k - 2) >> 1, f_ = k & 1; UNROLL for (int r = 0; r < 4; ++r) (f_ ? WB : WA)[i_][r] = pkh(wf[i_][2 * f_][r], wf[i_][2 * f_ + 1][r]); } \
            SB(); } \
        pin(WA); pin(WB); } while (0)
    MEMBAR();
    const u4 pr0 = *(const u4*)&lds[L_PR];
    const bool wearly = rfl(min4u(pr0)) != 0u;
    MEMBAR();
    if (wearly) WIDEN(rfl(max4u(pr0)));
    STAMP(6);
    const u32 vn = valid_bits(epoch, n), vm = valid_bits(epoch, 15 - n);
    u32 x1 = cls > 4 ? vm ^ 0x80000000u : vn, x2 = vm ^ (cls == 4 ? 0x00008000u : cls == 0 ? 0x80000000u : 0u);
    u32 z1[4][4], z2[4][4];
    const int zvm = q * 64 + (63 - tile) * 16384 + (15 - n) * 4;
    const int zv1 = cls <= 4 ? q * 64 + tile * 16384 + n * 4 : zvm;
    int zso = WS_Z + row * 64 * 16384 + wv * 4096;
    asm volatile("" : "+s"(zso));
#define LOAD_Z() do { \
        if (packed) { int a_ = zvm; pin(a_); UNROLL for (int i = 0; i < 4; ++i) UNROLL for (int j = 0; j < 4; ++j) z2[i][j] = __builtin_amdgcn_raw_buffer_load_b32(rws, a_ + (4 * i + j) * 256, zso, SC1); } \
        else { UNROLL for (int i = 0; i < 4; ++i) UNROLL for (int j = 0; j < 4; ++j) z2[i][j] = x2; } \
        int c_ = zv1; pin(c_); UNROLL for (int i = 0; i < 4; ++i) UNROLL for (int j = 0; j < 4; ++j) z1[i][j] = __builtin_amdgcn_raw_buffer_load_b32(rws, c_ + (4 * i + j) * 256, zso, SC1); } while (0)
    pin(x1); pin(x2);
    VMWAIT(0);
    LOAD_Z();
    if (wv == 0) lset(lds, L_ZL, pv, lane);
    SB();
    STAMP(7);
    const int lw = ((wv * 16 + 4 * q) * 16 + n) * 4;
    int lr = (q * 16 + 4 * wv) * 16 + n;
    const u4* l4 = (const u4*)lds;
    pin(lr);
    STAMP(8);
    SB();
    {
        f4 yr[4], yi[4];
        NOUNROLL for (int spin = 0; spin < SPIN_CAP; ++spin) {
            u32 m = 0;
            UNROLL for (int i = 0; i < 4; ++i) {
                u4 d;
                UNROLL for (int j = 0; j < 4; ++j) { z1[i][j] ^= x1; z2[i][j] ^= x2; }
                UNROLL for (int j = 0; j < 4; ++j) d[j] = U(H(z1[i][j]) + H(z2[i][j]));
                UNROLL for (int j = 0; j < 4; ++j) m |= z1[i][j] | z2[i][j];
                yr[i] = mfma16(GR[i], d); yi[i] = mfma16(GI[i], d);
                SB();
            }
            if (__all((m & TM) == 0u)) break;
            MEMBAR();
            LOAD_Z();
        }
        STAMP(9);
        u4 y[4];
        UNROLL for (int i = 0; i < 4; ++i) UNROLL for (int r = 0; r < 4; ++r) y[r][i] = pkh(yr[i][r], yi[i][r]);
        UNROLL for (int r = 0; r < 4; ++r) *(u4*)&lds[L_A2 + lw + r * 64] = y[r];
        lset(lds, L_C3 + wv, pv, lane);
    }
    if (!wearly) { const u32 es = lwait4(lds, L_PR, 1u); WIDEN(es); }
    {
        u4 d[4];
        NOUNROLL for (int spin = 0; spin < LSPIN_CAP; ++spin) {
            MEMBAR();
            const u4 c = *(const u4*)&lds[L_C3];
            MEMBAR();
            UNROLL for (int i = 0; i < 4; ++i) d[i] = l4[L_A2 / 4 + lr + i * 16];
            pin(d[0]);
            if (rfl(min4u(c)) >= pv) break;
        }
        STAMP(10);
        pin(WA); pin(WB);
        f4 xr[4], xi[4];
        u4 xx[4], ya[4], yb[4];
        UNROLL for (int i = 0; i < 4; ++i) {
            xr[i] = mfma16(FR, d[i]); xi[i] = mfma16(FI, d[i]);
            if (i >= 2) { UNROLL for (int r = 0; r < 4; ++r) xx[i - 2][r] = pkh(xr[i - 2][r], xi[i - 2][r]); pin(xx[i - 2]); }
            SB();
        }
        UNROLL for (int r = 0; r < 4; ++r) ya[0][r] = cmul_swap(xx[0][r], WA[0][r]);
        UNROLL for (int i = 2; i < 4; ++i) { UNROLL for (int r = 0; r < 4; ++r) xx[i][r] = pkh(xr[i][r], xi[i][r]); pin(xx[i]); }
        SB();
        f4 p0[4], p1[4], p2[4], p3[4];
        UNROLL for (int i = 0; i < 4; ++i) {
            p0[i] = mfma16(FR, ya[i]); p1[i] = mfma16(FI, ya[i]);
            if (i < 3) { UNROLL for (int r = 0; r < 4; ++r) ya[i + 1][r] = cmul_swap(xx[i + 1][r], WA[i + 1][r]); }
            else { UNROLL for (int r = 0; r < 4; ++r) yb[0][r] = cmul_swap(xx[0][r], WB[0][r]); }
            SB();
        }
        u4 ra[4], rb[4];
        UNROLL for (int i = 0; i < 4; ++i) {
            p2[i] = mfma16(FR, yb[i]); p3[i] = mfma16(FI, yb[i]);
            if (i < 3) { UNROLL for (int r = 0; r < 4; ++r) yb[i + 1][r] = cmul_swap(xx[i + 1][r], WB[i + 1][r]); }
            UNROLL for (int k = 0; k < 4; ++k) ra[i][k] = pkh(p0[k][i], p1[k][i]);
            *(u4*)&lds[L_B + lw + i * 64] = ra[i];
            SB();
        }
        UNROLL for (int i = 0; i < 3; ++i) UNROLL for (int r = 0; r < 4; ++r) rb[r][i] = pkh(p2[i][r], p3[i][r]);
        SB();
        UNROLL for (int r = 0; r < 4; ++r) { rb[r][3] = pkh(p2[3][r], p3[3][r]); *(u4*)&lds[L_B + 4096 + lw + r * 64] = rb[r]; }
        lset(lds, L_C4 + wv, pv, lane);
    }
    STAMP(11);
    {
        u4 da[4], db[4];
        NOUNROLL for (int spin = 0; spin < LSPIN_CAP; ++spin) {
            MEMBAR();
            const u4 c = *(const u4*)&lds[L_C4];
            MEMBAR();
            UNROLL for (int i = 0; i < 4; ++i) { da[i] = l4[L_B / 4 + lr + i * 16]; db[i] = l4[L_B / 4 + 1024 + lr + i * 16]; }
            pin(da[0]);
            if (rfl(min4u(c)) >= pv) break;
        }
        const int vo = 64 * q * 32768 + n * 8, so = WS_V + 4 * wv * 32768 + cls * 4096 + tile * 128;
        f4 p0[4], p1[4], p2[4], p3[4];
        UNROLL for (int i = 0; i < 5; ++i) {
            if (i < 4) { p0[i] = mfma16(HR[i], da[i]); p1[i] = mfma16(HI[i], da[i]); p2[i] = mfma16(HR[i], db[i]); p3[i] = mfma16(HI[i], db[i]); }
            if (i > 0) { UNROLL for (int r = 0; r < 4; ++r)
                __builtin_amdgcn_raw_buffer_store_b64(u2{mark(pkh(p0[i - 1][r], p1[i - 1][r]), vn, tm), mark(pkh(p2[i - 1][r], p3[i - 1][r]), vn, tm)}, rws, vo, so + (16 * r + i - 1) * 32768, SC1); }
            SB();
        }
    }
    if (wv == 0) lset(lds, L_P2, 1u, lane);
    STAMP(12);
    SB();
    {
        pin(xv);
        const f2 g01 = {bflo(gv.x), bfhi(gv.x)}, g23 = {bflo(gv.y), bfhi(gv.y)};
        f4 acc = {};
        UNROLL for (int i = 0; i < 4; ++i) {
            u4 pp;
            UNROLL for (int k = 0; k < 2; ++k) {
                const f2 a = f2{bflo(xv[2 * i + k].x), bfhi(xv[2 * i + k].x)} * g01, c = f2{bflo(xv[2 * i + k].y), bfhi(xv[2 * i + k].y)} * g23;
                pp[2 * k] = pkb(a.x, a.y); pp[2 * k + 1] = pkb(c.x, c.y);
            }
            const u32 s0 = n == 2 * i ? 0x3f803f80u : 0u, s1 = n == 2 * i + 1 ? 0x3f803f80u : 0u;
            acc = mfma16b(pp, u4{s0, s0, s1, s1}, acc);
        }
        if (n < 8) lds[L_GS + n * 16 + (t >> 4)] = __builtin_bit_cast(u32, (acc.x + acc.y) + (acc.z + acc.w));
        lset(lds, L_C5 + wv, pv, lane);
    }
    MR[0] = *(const u4*)&lds[L_M + lane * 4]; MR[1] = *(const u4*)&lds[L_M + 256 + lane * 4];
    UNROLL for (int i = 0; i < 4; ++i) TW[i] = *(const u4*)&lds[L_M + (2 + i) * 256 + lane * 4];
    core_expand(lane, MR, MI);
    pin(MR); pin(MI); pin(TW);
    u4 PM;
    {
        lwait4(lds, L_C5, pv);
        float gg[4];
        UNROLL for (int k = 0; k < 2; ++k) {
            const f4* pp = (const f4*)&lds[L_GS + ((n >> 2) * 2 + k) * 16];
            const f4 ps = (pp[0] + pp[1]) + (pp[2] + pp[3]);
            const float lg = rbf((ps.x + ps.y) + (ps.z + ps.w));
            gg[2 * k] = rbf(1.0f / (1.0f + __expf(-lg)));
            gg[2 * k + 1] = rbf(1.0f - gg[2 * k]);
        }
        UNROLL for (int k = 0; k < 4; ++k) { gg[k] *= 0.03125f; pin(gg[k]); }
        const int r = n & 3, sa = (n >> 2) * 2;
        UNROLL for (int cc = 0; cc < 2; ++cc) {
            const int u1 = 2 * q + cc;
            const float ta = (float)(sa * u1) * 0.125f + ((r & 1) ? 0.25f : (r & 2) ? 0.5f : 0.0f);
            const float tb = (float)((sa + 1) * u1) * 0.125f + ((r & 1) ? ((r & 2) ? 0.0f : 0.5f) : 0.25f);
            const float ca = cosr(ta), sa_ = sinr(ta), cb = cosr(tb), sb_ = sinr(tb);
            float e[4] = {gg[2] * cb, gg[2] * sb_, gg[3] * cb, gg[3] * sb_};
            pin(e);
            e[0] = fmaf(gg[0], ca, e[0]); pin(e[0]); e[1] = fmaf(gg[0], sa_, e[1]); pin(e[1]); e[2] = fmaf(gg[1], ca, e[2]); pin(e[2]); e[3] = fmaf(gg[1], sa_, e[3]);
            PM[2 * cc] = pkh(e[0], e[1]); PM[2 * cc + 1] = pkh(e[2], e[3]);
        }
    }
    int a3d = (q * 8 + wv) * 32 + n, a3m = (q * 8 + 7 - wv) * 32 + 16 - n, a3z = (q * 8 + 7 - wv) * 32 + ((32 - n) & 31), a3r = wv * 256 + lane;
    float osc = __builtin_bit_cast(float, (u32)min(max((int)ex + (int)ew - 130, 1), 254) << 23) * TRUNC_FIX;
    const int ve = 2 * q * 4096 + (128 * wv + n) * 8;
    int vs0 = WS_V + b * 32768, vs1 = vs0 + 4096, vsl = WS_VL + b * 16;
    asm volatile("" : "+s"(vs0), "+s"(vs1), "+s"(vsl));
    int ov = (4 * perm32(lane & 15) + 2 * ((lane >> 4) & 1) + (lane >> 5)) * 16 + (512 * wv + b) * 2048;
    u32 x8 = wv == 3 ? 0u : vn;
    pin(a3d); pin(a3m); pin(a3z); pin(a3r); pin(PM); pin(ov); pin(osc); pin(x8);
    STAMP(13);
    VMWAIT(0);
    {
        f4 z3[9];
        NOUNROLL for (int spin = 0; spin < SPIN_CAP; ++spin) {
            MEMBAR();
            u2 v0[8], v1[8];
            u4 ve8, vo8;
            int a_ = ve; pin(a_);
            if (wv == 3) {
                ve8 = __builtin_amdgcn_raw_buffer_load_b128(rws, q * 8192, vsl, SC1); vo8 = __builtin_amdgcn_raw_buffer_load_b128(rws, q * 8192 + 4096, vsl, SC1);
            } else {
                const u2 e2 = __builtin_amdgcn_raw_buffer_load_b64(rws, a_ + 1024, vs0, SC1), o2 = __builtin_amdgcn_raw_buffer_load_b64(rws, a_ + 1024, vs1, SC1);
                ve8 = u4{e2.x, e2.y, tag0, tag0}; vo8 = u4{o2.x, o2.y, tag0, tag0};
            }
            UNROLL for (int i = 0; i < 8; ++i) {
                v0[i] = __builtin_amdgcn_raw_buffer_load_b64(rws, a_ + 128 * i, vs0, SC1);
                v1[i] = __builtin_amdgcn_raw_buffer_load_b64(rws, a_ + 128 * i, vs1, SC1);
            }
            SB();
            u32 m = (((wv == 3 ? ve8.y : ve8.z) ^ tag0) | (ve8.w ^ tag0) | ((wv == 3 ? vo8.y : vo8.z) ^ tag0) | (vo8.w ^ tag0)) ? TM : 0u;
            UNROLL for (int k = 0; k < 9; ++k) {
                const int i = (k + 8) % 9;
                const u4 d = i == 8 ? u4{ve8.x, wv == 3 ? ve8.z : ve8.y, vo8.x, wv == 3 ? vo8.z : vo8.y} ^ x8 : u4{v0[i & 7].x, v0[i & 7].y, v1[i & 7].x, v1[i & 7].y} ^ vn;
                m |= d.x | d.y; m |= d.z | d.w;
                z3[i] = mfma16(PM, d);
                SB();
            }
            if (__all((m & TM) == 0u)) break;
        }
        STAMP(14);
        if (wv == 0 && n == 0) { z3[0].x = 0.5f * (z3[0].x + z3[0].z); z3[0].y = 0.5f * (z3[0].y + z3[0].w); }
        if (wv == 3) { z3[8].z = 0.5f * (z3[8].x + z3[8].z); z3[8].w = 0.5f * (z3[8].y + z3[8].w); }
        u4* l4w = (u4*)&lds[L_A1];
        UNROLL for (int p = 0; p < 2; ++p)
            l4w[a3d + 16 * p] = u4{pkh(z3[p].x, z3[p + 2].x), pkh(z3[p + 4].x, z3[p + 6].x), pkh(z3[p].y, z3[p + 2].y), pkh(z3[p + 4].y, z3[p + 6].y)};
        l4w[a3m] = u4{pkh(z3[7].z, z3[5].z), pkh(z3[3].z, z3[1].z), pkh(z3[7].w, z3[5].w), pkh(z3[3].w, z3[1].w)};
        f2 m[4];
        UNROLL for (int i = 0; i < 4; ++i) m[i] = n == 0 ? f2{z3[8 - 2 * i].z, z3[8 - 2 * i].w} : f2{z3[6 - 2 * i].z, z3[6 - 2 * i].w};
        l4w[a3z] = u4{pkh(m[0].x, m[1].x), pkh(m[2].x, m[3].x), pkh(m[0].y, m[1].y), pkh(m[2].y, m[3].y)};
        lset(lds, L_C6 + wv, 2u * pv - 1u, lane);
    }
    {
        u4 in[4];
        NOUNROLL for (int spin = 0; spin < LSPIN_CAP; ++spin) {
            MEMBAR();
            const u4 c = *(const u4*)&lds[L_C6];
            MEMBAR();
            UNROLL for (int s = 0; s < 4; ++s) in[s] = ((const u4*)&lds[L_A1])[a3r + s * 64];
            pin(in[0]);
            if (rfl(min4u(c)) >= 2u * pv - 1u) break;
        }
        f16v zr, zi;
        core_fft<false>(in, MR, MI, TW, zr, zi);
        zr *= osc; zi *= osc;
        const rsrc_t ro = mkrs(out, 2048 * 2048);
#define OUT_ROW(z, off) do { u4 lo, hi; \
            UNROLL for (int j = 0; j < 4; ++j) { const auto sw_ = __builtin_amdgcn_permlane16_swap(pkb(z[2 * j], z[2 * j + 1]), pkb(z[8 + 2 * j], z[8 + 2 * j + 1]), false, false); lo[j] = sw_[0]; hi[j] = sw_[1]; } \
            store128<SC1>(lo, ro, ov + (off)); store128<SC1>(hi, ro, ov + (off) + 1024); } while (0)
        OUT_ROW(zr, 256 * 2048);
        OUT_ROW(zi, 0);
    }
    STAMP(15);
    vd = lwait(lds, L_VD, 1u);
    if (pass == 1 || !(vd & 0x20000u)) break;
    lset(lds, L_C6 + wv, 2u, lane);
    lwait4(lds, L_C6, 2u);
    VMWAIT(0);
    }
    STAMP_OUT();
}

extern "C" void kernel_launch(void* const* d_in, const int* in_sizes, int n_in,
                              void* d_out, int out_size, void* d_ws, size_t ws_size,
                              hipStream_t stream) {
    gfnet_fused<<<256, 384, 0, stream>>>(d_in[0], d_in[6], d_in[1], d_in[2], d_in[4], d_in[5], (char*)d_ws, d_out);
}
```

```cpp
#include <hip/hip_runtime.h>
#include <hip/hip_bf16.h>
#include <stdint.h>

typedef _Float16 h2 __attribute__((ext_vector_type(2)));
typedef _Float16 h8 __attribute__((ext_vector_type(8)));
typedef __bf16 b2 __attribute__((ext_vector_type(2)));
typedef __bf16 b8 __attribute__((ext_vector_type(8)));
typedef float f2 __attribute__((ext_vector_type(2)));
typedef float f4 __attribute__((ext_vector_type(4)));
typedef float f16v __attribute__((ext_vector_type(16)));
typedef short s4 __attribute__((ext_vector_type(4)));
typedef unsigned u32;
typedef u32 u2 __attribute__((ext_vector_type(2)));
typedef u32 u4 __attribute__((ext_vector_type(4)));
typedef unsigned short us2 __attribute__((ext_vector_type(2)));
typedef __amdgpu_buffer_rsrc_t rsrc_t;
#define DI __device__ __forceinline__
#define UNROLL _Pragma("unroll")
#define NOUNROLL _Pragma("unroll 1")
#define LAS __attribute__((address_space(3)))
#define SB() __builtin_amdgcn_sched_barrier(0)
#define MEMBAR() asm volatile("" ::: "memory")
#define VMWAIT(n) asm volatile("s_waitcnt vmcnt(%0)" :: "n"(n) : "memory")

#define MARGIN 2

constexpr u32 FSTR = 16, HSTR = 32;
constexpr u32 WS_Z = 0;
constexpr u32 WS_V = 4u << 20;
constexpr u32 WS_VL = 12u << 20;
constexpr u32 WS_HB = WS_VL + (32u << 10);
constexpr u32 WS_F = WS_HB + (32u << 10);
constexpr u32 WS_E = WS_F + 2048 * FSTR;
constexpr u32 WS_BYTES = WS_E + 4096;
constexpr int SC1 = 16, NT = 2;
constexpr int SPIN_CAP = 8192, LSPIN_CAP = 1 << 17;
constexpr int OOB = 0x7ffffff0;
constexpr u32 TM = 0x00010001u;
constexpr float TRUNC_FIX = 1.00066f;

template <class T> DI void pin(T& v) { asm volatile("" : "+v"(v)); }
template <class T, int N> DI void pin(T (&v)[N]) { UNROLL for (int i = 0; i < N; ++i) pin(v[i]); }
DI u32 opq(u32 v) { asm("" : "+v"(v)); return v; }
DI u32 pkh(float lo, float hi) { h2 r; r.x = (_Float16)lo; r.y = (_Float16)hi; return __builtin_bit_cast(u32, r); }
DI u32 pkb(float lo, float hi) { b2 r; r.x = (__bf16)lo; r.y = (__bf16)hi; return __builtin_bit_cast(u32, r); }
DI float bflo(u32 v) { return __builtin_bit_cast(float, v << 16); }
DI float bfhi(u32 v) { return __builtin_bit_cast(float, v & 0xffff0000u); }
DI float rbf(float v) { return (float)(__bf16)v; }
DI h2 H(u32 v) { return __builtin_bit_cast(h2, v); }
DI u32 U(h2 v) { return __builtin_bit_cast(u32, v); }
DI f16v mfma32(u4 a, u4 b, f16v c) { return __builtin_amdgcn_mfma_f32_32x32x16_f16(__builtin_bit_cast(h8, a), __builtin_bit_cast(h8, b), c, 0, 0, 0); }
DI f4 mfma16(u4 a, u4 b) { return __builtin_amdgcn_mfma_f32_16x16x32_f16(__builtin_bit_cast(h8, a), __builtin_bit_cast(h8, b), f4{}, 0, 0, 0); }
DI f4 mfma16b(u4 a, u4 b, f4 c) { return __builtin_amdgcn_mfma_f32_16x16x32_bf16(__builtin_bit_cast(b8, a), __builtin_bit_cast(b8, b), c, 0, 0, 0); }
DI u32 hmul(u32 a, u32 b) { return U(H(a) * H(b)); }
DI u32 cmul_swap(u32 x, u32 w) { const h2 a = H(opq(x)), c = H(opq(w)); h2 n = c; n.y = -n.y; return U(__builtin_elementwise_fma(a.yy, n, a.xx * c.yx)); }
DI u32 cmul_conj(u32 x, u32 t) { const h2 a = H(opq(x)), c = H(opq(t)); h2 n = c.yy; n.y = -n.y; return U(__builtin_elementwise_fma(a.yx, n, a * c.xx)); }
DI u32 pkmax(u32 a, u32 b) { return __builtin_bit_cast(u32, __builtin_elementwise_max(__builtin_bit_cast(us2, a), __builtin_bit_cast(us2, b))); }
DI float sinr(float r) { return __builtin_amdgcn_sinf(r); }
DI float cosr(float r) { return __builtin_amdgcn_cosf(r); }
DI u32 rot(float ph) { return pkh(cosr(ph), sinr(ph)); }
DI u32 quarter(u32 r, u32 f) { u32 o; asm("v_pk_mul_f16 %0, %1, %2 op_sel:[1,0] op_sel_hi:[0,1] neg_lo:[1,0]" : "=v"(o) : "v"(r), "v"(f)); return o; }
DI u4 quarter(u4 r) { u32 one = 0x3c003c00u; pin(one); return u4{quarter(r.x, one), quarter(r.y, one), quarter(r.z, one), quarter(r.w, one)}; }
DI int perm32(int n) { return (n & 0x13) | ((n & 4) << 1) | ((n & 8) >> 1); }
DI u32 max4u(u4 v) { return max(max(v.x, v.y), max(v.z, v.w)); }
DI u32 min4u(u4 v) { return min(min(v.x, v.y), min(v.z, v.w)); }
DI u32 mark(u32 v, u32 par, u32 tm) { return (par & tm) | (v & ~tm); }
DI u32 rfl(u32 v) { return __builtin_amdgcn_readfirstlane(v); }
DI rsrc_t mkrs(const void* p, u32 bytes) { return __builtin_amdgcn_make_buffer_rsrc((void*)p, (short)0, (int)bytes, 0x00020000); }
template <int AUX> DI void store128(u4 v, rsrc_t r, int off) { __builtin_amdgcn_raw_buffer_store_b128(v, r, off, 0, AUX); }
template <int CTRL> DI u32 dpp(u32 v) { return __builtin_amdgcn_update_dpp(0u, v, CTRL, 0xf, 0xf, true); }
DI u32 wave_maxu(u32 v) {
    v = max(v, dpp<0xB1>(v)); v = max(v, dpp<0x4E>(v)); v = max(v, dpp<0x141>(v)); v = max(v, dpp<0x140>(v));
    return max(max((u32)__builtin_amdgcn_readlane(v, 0), (u32)__builtin_amdgcn_readlane(v, 16)), max((u32)__builtin_amdgcn_readlane(v, 32), (u32)__builtin_amdgcn_readlane(v, 48)));
}

DI void core_setup(int lane, int s, u4& tw, u2& m) {
    float fc = (float)perm32(lane & 31) * (1.0f / 1024.0f), p0 = (float)(4 * (lane >> 5) + 8 * s) * fc, p1 = (float)(4 * (lane >> 5) + 8 * (s >> 1) + 2 * (s & 1)) * fc * 32.0f;
    pin(fc); pin(p0); pin(p1);
    float tc[4], ts[4];
    UNROLL for (int i = 0; i < 4; ++i) { const float pt = fmaf((float)i, fc, p0); tc[i] = cosr(pt); ts[i] = sinr(pt); }
    tw = u4{pkh(tc[0], tc[1]), pkh(tc[2], tc[3]), pkh(ts[0], ts[1]), pkh(ts[2], ts[3])};
    const float p2 = fmaf(32.0f, fc, p1);
    m = u2{pkh(cosr(p1), cosr(p2)), pkh(sinr(p1), sinr(p2))};
}
DI void core_expand(int lane, u4 (&MR)[4], u4 (&MI)[4]) {
    const u32 sg = (lane & 1) ? 0x80008000u : 0u;
    MR[2] = MR[0] ^ sg; MR[3] = MR[1] ^ sg;
    UNROLL for (int s = 0; s < 4; ++s) MI[s] = u4{MR[s].z ^ 0x80008000u, MR[s].w ^ 0x80008000u, MR[s].x, MR[s].y};
}
template <bool OUT_B> DI void core_fft(const u4 (&in)[4], const u4 (&MR)[4], const u4 (&MI)[4], const u4 (&TW)[4], f16v& outr, f16v& outi) {
    f16v yr = {}, yi = {};
    UNROLL for (int s = 0; s < 4; ++s) yr = mfma32(in[s], MR[s], yr);
    SB();
    yi = mfma32(in[0], MI[0], yi); yi = mfma32(in[1], MI[1], yi);
    SB();
    h2 t0[8], t1[8];
#define TW_RE(p) do { const h2 pr_ = H(pkh(yr[2 * (p)], yr[2 * (p) + 1])); t0[p] = pr_ * H(TW[(p) >> 1][(p) & 1]); t1[p] = pr_ * H(TW[(p) >> 1][2 + ((p) & 1)]); } while (0)
    TW_RE(0); TW_RE(1); SB();
    yi = mfma32(in[2], MI[2], yi);
    TW_RE(2); TW_RE(3); SB();
    yi = mfma32(in[3], MI[3], yi);
    TW_RE(4); TW_RE(5); TW_RE(6); TW_RE(7); SB();
    outr = f16v{}; outi = f16v{};
    u4 d[4];
#define TW_IM(s) do { UNROLL for (int j = 0; j < 2; ++j) { const int p_ = 2 * (s) + j; const h2 pi_ = H(pkh(yi[2 * p_], yi[2 * p_ + 1])); \
        d[s][j] = U(__builtin_elementwise_fma(pi_, H(TW[s][2 + j]), t0[p_])); d[s][2 + j] = U(__builtin_elementwise_fma(pi_, H(TW[s][j]), -t1[p_])); } } while (0)
    TW_IM(0); SB();
    UNROLL for (int s = 0; s < 4; ++s) {
        outr = OUT_B ? mfma32(MR[s], d[s], outr) : mfma32(d[s], MR[s], outr);
        if (s < 3) TW_IM(s + 1);
        SB();
    }
    UNROLL for (int s = 0; s < 4; ++s) outi = OUT_B ? mfma32(MI[s], d[s], outi) : mfma32(d[s], MI[s], outi);
}

DI u32 tag_of(u32 epoch, u32 idx) { u32 h = (epoch ^ (idx * 0x9E3779B1u)) * 0x85EBCA6Bu; return h ^ (h >> 15); }
DI u32 valid_bits(u32 epoch, int slot) { return (((0x6A3Cu >> slot) ^ epoch) & 1u) ? 0x00000001u : 0x00010000u; }
DI int cls_of(int b) { const int row = (b & 7) >> 1; return row == 0 ? 4 * (b >> 7) : ((b & 1) ? 8 - row : row); }
DI int tile_of(int b) { return (b & 6) == 0 ? 16 * (b & 1) + ((b >> 3) & 15) : b >> 3; }

constexpr int L_C1 = 0, L_C3 = 4, L_C4 = 8, L_C5 = 12, L_C6 = 16;
constexpr int L_EW = 20;
constexpr int L_PR = 28;
constexpr int L_LD = 32;
constexpr int L_SX = 36;
constexpr int L_ZL = 37;
constexpr int L_VD = 38;
constexpr int L_P2 = 39;
constexpr int L_GS = 64;
constexpr int L_M = 192;
constexpr int L_A1 = L_M + 1536;
constexpr int L_A2 = L_A1 + 4096;
constexpr int L_B = L_A2 + 4096;
constexpr int L_W = L_B + 8192;
constexpr int L_END = L_W + 64 * 256;

DI void lset(u32* lds, int idx, u32 v, int lane) { MEMBAR(); if (lane == 0) lds[idx] = v; MEMBAR(); }
DI u32 lget(u32* lds, int idx) { MEMBAR(); return rfl(lds[idx]); }
DI u32 lwait(u32* lds, int idx, u32 atleast) {
    u32 v = 0;
    NOUNROLL for (int spin = 0; spin < LSPIN_CAP; ++spin) { v = lget(lds, idx); if (v >= atleast) break; __builtin_amdgcn_s_sleep(1); }
    MEMBAR();
    return v;
}
DI u32 lwait4(u32* lds, int idx, u32 atleast) {
    u4 v;
    NOUNROLL for (int spin = 0; spin < LSPIN_CAP; ++spin) { MEMBAR(); v = *(const u4*)&lds[idx]; if (rfl(min4u(v)) >= atleast) break; __builtin_amdgcn_s_sleep(1); }
    MEMBAR();
    return rfl(max4u(v));
}
struct MagMax {
    h2 hi = {}, lo = {};
    DI void add(u32 a, u32 c) {
        hi = __builtin_elementwise_maximum(__builtin_elementwise_maximum(hi, H(a)), H(c));
        lo = __builtin_elementwise_minimum(__builtin_elementwise_minimum(lo, H(a)), H(c));
    }
    DI u32 exponent() const {
        const u32 mx = pkmax(U(hi) & 0x7fff7fffu, U(lo) & 0x7fff7fffu);
        return wave_maxu(max(mx & 0xffffu, mx >> 16)) >> 7;
    }
};
DI u32 clampe(u32 e) { return min(max(e, 8u), 240u); }

__global__ __launch_bounds__(384, 2) void gfnet_fused(const void* x, const void* gw, const void* war, const void* wai, const void* wbr, const void* wbi,
                                                      char* ws, void* out) {
    __shared__ __attribute__((aligned(16))) u32 lds[L_END];
    u32 code;
    {
        unsigned long long pc0; int len;
        asm volatile("s_getpc_b64 %0\n.Lgf_begin:\n\ts_mov_b32 %1, .Lgf_end-.Lgf_begin" : "=s"(pc0), "=s"(len));
        code = __builtin_amdgcn_raw_buffer_load_b32(mkrs((const void*)pc0, (u32)max(len, 0)), (threadIdx.x >> 6) == 4 ? ((blockIdx.x >> 5) * 64 + (threadIdx.x & 63)) * 64 : OOB, 0, 0);
        SB();
    }
    asm volatile("" :: "s"(x), "s"(gw), "s"(war), "s"(wai), "s"(wbr), "s"(wbi), "s"(ws), "s"(out));
    const int wv0 = __builtin_amdgcn_readfirstlane(threadIdx.x >> 6);
    const rsrc_t rws = mkrs(ws, WS_BYTES);

    if (wv0 >= 4) {
        const int wv = wv0, b = blockIdx.x, cls = cls_of(b), tile = tile_of(b);
        const int lane = threadIdx.x & 63, q = lane >> 4, n = lane & 15;
        const rsrc_t rw[2] = {mkrs(wv == 4 ? war : wbr, 2048 * 1026), mkrs(wv == 4 ? wai : wbi, 2048 * 1026)};
        if (wv == 4) { lds[lane] = 0u; asm volatile("s_waitcnt lgkmcnt(0)" ::: "memory"); }
        MEMBAR();
        __builtin_amdgcn_s_barrier();
        MEMBAR();
        const bool helper = wv == 5 && tile == 31;
        u4 GR, GI, FR, FI, HR, HI, T1, T2;
        u32 ev;
        {
            const int wvo = (lane >> 2) * (128 * 1026) + min(lane & 3, 2) * 16, wso = (cls * 513 + 16 * tile - cls) * 2;
#define WLOAD(r) do { UNROLL for (int st = 0; st < 4; ++st) { const int c_ = 4 * st + ((tile + (r)) & 3); \
                UNROLL for (int a = 0; a < 2; ++a) __builtin_amdgcn_raw_ptr_buffer_load_lds(rw[a], (LAS void*)&lds[L_W + (c_ * 4 + 2 * (wv - 4) + a) * 256], 16, wvo, wso + c_ * 8208, 0, 0); } SB(); } while (0)
            __builtin_amdgcn_s_sleep(2);
            WLOAD(0);
            const bool split = (b & 6) == 0;
            const int er = split ? 16 * (tile & 15) + ((lane >> 1) & 15) : 8 * tile + ((lane >> 1) & 7);
            const int ec = (split ? (tile & 16) * 16 : 0) - cls + ((lane & 1) ? (split ? 262 : 518) : 0);
            u32 edge[2];
            UNROLL for (int a = 0; a < 2; ++a) edge[a] = __builtin_amdgcn_raw_buffer_load_b32(rw[a], ((cls + 8 * er) * 513 + ec) * 2, 0, 0);
            ev = __builtin_amdgcn_raw_buffer_load_b32(rws, 0, WS_E, SC1);
            const u4 smp = __builtin_amdgcn_raw_buffer_load_b128(rw[0], (lane >> 3) * (2 * 131328 + 128) + (lane & 7) * 16, 0, 0);
            const u4 smp1 = __builtin_amdgcn_raw_buffer_load_b128(rw[1], (lane >> 3) * (2 * 131328 + 128) + (lane & 7) * 16, 0, 0);
            MEMBAR();
            if (helper) {
                UNROLL for (int j = 0; j < 4; ++j) {
                    GR[j] = rot((float)((4 * q + j) * (cls + 8 * n)) * (1.0f / 128.0f) + (cls == 4 ? 0.25f : 0.0f));
                    HR[j] = rot((float)(n * (cls + 8 * (4 * q + j))) * (1.0f / 128.0f));
                    FR[j] = rot((float)(n * (4 * q + j)) * (1.0f / 16.0f));
                    T1[j] = rot((float)((4 * q + j) * (cls + 8 * n)) * (1.0f / 2048.0f));
                    T2[j] = rot((float)(n * (cls + 8 * (4 * q + j))) * (1.0f / 2048.0f));
                }
                GI = quarter(GR); HI = quarter(HR); FI = quarter(FR);
                pin(GR); pin(GI); pin(HR); pin(HI); pin(FR); pin(FI); pin(T1); pin(T2);
            }
            VMWAIT(0);
            WLOAD(1); WLOAD(2); WLOAD(3);
            asm volatile("ds_write_b64 %0, %1" :: "v"((u32)(size_t)(LAS u32*)&lds[L_LD + 2 * (wv - 4)]), "v"(u2{1u, 1u}) : "memory");
            pin(edge); pin(code);
            MagMax ms; ms.add(smp.x, smp.y); ms.add(smp.z, smp.w); ms.add(smp1.x, smp1.y); ms.add(smp1.z, smp1.w);
            const u32 es = clampe(ms.exponent() + MARGIN);
            VMWAIT(0);
            MEMBAR(); if (lane == 0) *(u2*)&lds[L_PR + 2 * (wv - 4)] = u2{es, es}; MEMBAR();
        }
        const u32 ep0 = rfl(ev) + 1u;
        if (wv == 4) {
            lwait4(lds, L_PR, 1u);
            MagMax m;
            NOUNROLL for (int c0 = 0; c0 < 64; c0 += 16) { UNROLL for (int c = 0; c < 16; ++c) { const u4 v = *(const u4*)&lds[L_W + (c0 + c) * 256 + lane * 4]; m.add(v.x, v.y); m.add(v.z, v.w); } }
            const u32 ew = clampe(m.exponent()), ex = lwait4(lds, L_EW, 1u);
            __builtin_amdgcn_raw_buffer_store_b64(u2{ex | (ew << 8), tag_of(ep0, b + 1)}, rws, lane < 8 ? lane * (int)(256 * FSTR) : OOB, WS_F + b * FSTR, SC1);
            lwait(lds, L_P2, 1u);
            __builtin_amdgcn_s_sleep(8);
            u2 f[4]; u32 tg[4];
            UNROLL for (int j = 0; j < 4; ++j) tg[j] = tag_of(ep0, 4 * lane + j + 1);
            NOUNROLL for (int spin = 0; spin < SPIN_CAP; ++spin) {
                MEMBAR();
                UNROLL for (int j = 0; j < 4; ++j) f[j] = __builtin_amdgcn_raw_buffer_load_b64(rws, lane * (4 * FSTR) + j * FSTR, WS_F + (b & 7) * (256 * FSTR), SC1);
                if (__all(f[0].y == tg[0] && f[1].y == tg[1] && f[2].y == tg[2] && f[3].y == tg[3])) break;
                __builtin_amdgcn_s_sleep(8);
            }
            const u4 e4 = {f[0].x, f[1].x, f[2].x, f[3].x};
            const u32 gx = wave_maxu(max4u(e4 & 0xffu)), gwm = wave_maxu(max4u((e4 >> 8) & 0xffu));
            const bool redo = gx > lget(lds, L_SX) || gwm > lwait4(lds, L_PR, 1u);
            lset(lds, L_VD, gx | (gwm << 8) | (redo ? 0x20000u : 0x10000u), lane);
            if (b == 0 && lane == 0) __builtin_amdgcn_raw_buffer_store_b32(ep0 + (redo ? 1u : 0u), rws, 0, WS_E, SC1);
        } else if (helper) {
            __builtin_amdgcn_s_setprio(2);
            u32 ew = lwait4(lds, L_PR, 1u);
            NOUNROLL for (u32 pass = 0; pass < 2; ++pass) {
                u4 WH[2];
                const float sw = __builtin_bit_cast(float, (249u - ew) << 23);
                UNROLL for (int r = 0; r < 4; ++r) UNROLL for (int f = 0; f < 2; ++f) {
                    const unsigned short* wp = (const unsigned short*)&lds[L_W + (n * 4 + 2 * f) * 256 + (4 * q + r) * 16] + 16 + cls;
                    float re = bflo(wp[0]) * sw, im = bflo(wp[512]) * sw;
                    pin(re);
                    WH[f][r] = pkh(re, im);
                }
                pin(WH);
                lwait(lds, L_ZL, pass + 1u);
                const u32 tg = tag_of(ep0 + pass, 0u);
                u4 hz;
                NOUNROLL for (int spin = 0; spin < SPIN_CAP; ++spin) {
                    MEMBAR();
                    u32 bad = 0;
                    UNROLL for (int j = 0; j < 4; ++j) { const u2 g = __builtin_amdgcn_raw_buffer_load_b64(rws, (64 * q + n + 16 * j) * HSTR, WS_HB + ((b & 7) >> 1) * (256 * HSTR), SC1); hz[j] = g.x & ~TM; bad |= g.y ^ tg; }
                    if (__all(bad == 0u)) break;
                    __builtin_amdgcn_s_sleep(4);
                }
                const u32 f16 = cls == 0 ? 0x00002c00u : cls == 4 ? 0x2c000000u : cls > 4 ? 0xac002c00u : 0x2c002c00u;
                u4 d, o[2];
                UNROLL for (int j = 0; j < 4; ++j) d[j] = hmul(hz[j], f16);
                f4 yr = mfma16(d, GR), yi = mfma16(d, GI);
                UNROLL for (int r = 0; r < 4; ++r) d[r] = cmul_conj(pkh(yr[r], yi[r]), T1[r]);
                const f4 xr = mfma16(FR, d), xi = mfma16(FI, d);
                UNROLL for (int f = 0; f < 2; ++f) {
                    UNROLL for (int r = 0; r < 4; ++r) d[r] = cmul_swap(pkh(xr[r], xi[r]), WH[f][r]);
                    yr = mfma16(d, FR); yi = mfma16(d, FI);
                    UNROLL for (int r = 0; r < 4; ++r) o[f][r] = cmul_conj(pkh(yr[r], yi[r]), T2[r]);
                }
                const f4 p0 = mfma16(HR, o[0]), p1 = mfma16(HI, o[0]), p2 = mfma16(HR, o[1]), p3 = mfma16(HI, o[1]);
                UNROLL for (int r = 0; r < 4; ++r)
                    store128<SC1>(u4{pkh(p0[r], p1[r]) & ~TM, tg, pkh(p2[r], p3[r]) & ~TM, tg}, rws, (64 * q + n) * 16 + WS_VL + r * 256 + cls * 4096);
                const u32 vd = lwait(lds, L_VD, 1u);
                if (!(vd & 0x20000u)) break;
                ew = (vd >> 8) & 0xffu;
            }
        }
        asm volatile(".Lgf_end:");
        return;
    }

    u32 vd = 0, epoch = 0;
    NOUNROLL for (u32 pass = 0; pass < 2; ++pass) {
    int t = threadIdx.x, wv = wv0, b = blockIdx.x;
    pin(t);
    asm volatile("" : "+s"(wv));
    asm volatile("" : "+s"(b));
    const int lane = t & 63, q = lane >> 4, n = lane & 15;
    const u32 pv = pass + 1u;
    u2 xv[8];
    const rsrc_t rx = mkrs(x, 2048 * 2048);
    const u4 smp = __builtin_amdgcn_raw_buffer_load_b128(rx, (lane >> 3) * (524288 + 2048 + 256) + (lane & 7) * 16, 0, 0);
    UNROLL for (int s1 = 0; s1 < 8; ++s1) xv[s1] = __builtin_amdgcn_raw_buffer_load_b64(rx, t * 8, (256 * s1 + b) * 2048, NT);
    const u32 ev = __builtin_amdgcn_raw_buffer_load_b32(rws, 0, WS_E, SC1);
    const u2 gv = __builtin_amdgcn_raw_buffer_load_b64(mkrs(gw, 2048), t * 8, 0, 0);
    MEMBAR();
    SB();
    if (pass == 0) { __builtin_amdgcn_s_barrier(); MEMBAR(); __builtin_amdgcn_s_setprio(1); }
    u4 MR[4], MI[4], TW[4];
    {
        u4 tw; u2 m;
        core_setup(lane, wv, tw, m);
        *(u4*)&lds[L_M + (2 + wv) * 256 + lane * 4] = tw;
        lds[L_M + (wv >> 1) * 256 + lane * 4 + (wv & 1)] = m.x; lds[L_M + (wv >> 1) * 256 + lane * 4 + 2 + (wv & 1)] = m.y;
    }
    const int zc = perm32(lane & 31) + (lane >> 5) * 256, zcu = zc + ((lane & 31) == 0 ? 543 : 511);
    int zl = (zc >> 4) * 16384 + (zc & 15) * 4, zu = (zcu >> 4) * 16384 + (zcu & 15) * 4;
    int zh = lane == 0 ? 0 : OOB;
    int tro = L_A1 + wv * 1024 + (4 * (lane >> 5) + ((lane & 15) >> 2)) * 16 + ((lane >> 4) & 1) * 8 + (lane & 3) * 2;
    u32 tm = TM, vl0 = valid_bits(0u, zc & 15), vu0 = valid_bits(0u, zcu & 15);
    pin(zl); pin(zu); pin(zh); pin(tro); pin(tm); pin(vl0); pin(vu0);
    SB();
    pin(code);
    u32 ex;
    {
        MagMax ms; ms.add(smp.x, smp.y); ms.add(smp.z, smp.w);
        ex = pass ? (vd & 0xffu) : clampe(ms.exponent() + MARGIN);
    }
    u32 tag0, flip;
    u32 z[16], z512;
    {
        const float sc = __builtin_bit_cast(float, (250u - ex) << 23);
        const float rs = sc * 0.70710678118654752f;
        u32 o[8][2];
        UNROLL for (int j = 0; j < 2; ++j) {
            f2 v[8];
            UNROLL for (int s1 = 0; s1 < 8; ++s1) v[s1] = f2{bflo(xv[s1][j]), bfhi(xv[s1][j])};
            const f2 a0 = v[0] + v[4], a1 = v[1] + v[5], a2 = v[2] + v[6], a3 = v[3] + v[7];
            const f2 b0 = (v[0] - v[4]) * sc, b1 = v[1] - v[5], b2_ = (v[2] - v[6]) * sc, b3 = v[3] - v[7];
            const f2 e0 = a0 + a2, e1 = (a1 + a3) * sc, dm = b1 - b3, dp = b1 + b3;
            f2 r[8];
            r[0] = e0 * sc + e1; r[1] = e0 * sc - e1;
            r[2] = dm * rs + b0; r[3] = -(dp * rs) - b2_;
            r[4] = (a0 - a2) * sc; r[5] = (a3 - a1) * sc;
            r[6] = b0 - dm * rs; r[7] = b2_ - dp * rs;
            UNROLL for (int p = 0; p < 8; ++p) o[p][j] = pkh(r[p].x, r[p].y);
        }
        UNROLL for (int p = 0; p < 8; ++p) *(u2*)&lds[L_A1 + p * 512 + 2 * t] = u2{o[p][0], o[p][1]};
        lset(lds, L_C1 + wv, pv, lane);
        epoch = (pass ? epoch : rfl(ev)) + 1u; tag0 = tag_of(epoch, 0u); flip = (epoch & 1u) ? TM : 0u;
        const u32 vl = vl0 ^ flip, vu = vu0 ^ flip;
        u4 in[4];
        NOUNROLL for (int spin = 0; spin < LSPIN_CAP; ++spin) {
            MEMBAR();
            const u4 c = *(const u4*)&lds[L_C1];
            MEMBAR();
            MR[0] = *(const u4*)&lds[L_M + lane * 4]; MR[1] = *(const u4*)&lds[L_M + 256 + lane * 4];
            UNROLL for (int s = 0; s < 4; ++s) {
                const u2 re = __builtin_bit_cast(u2, __builtin_amdgcn_ds_read_tr16_b64_v4i16((LAS s4*)&lds[tro + s * 128]));
                const u2 im = __builtin_bit_cast(u2, __builtin_amdgcn_ds_read_tr16_b64_v4i16((LAS s4*)&lds[tro + s * 128 + 512]));
                in[s] = u4{re.x, re.y, im.x, im.y};
            }
            pin(MR[0]); pin(MR[1]);
            if (rfl(min4u(c)) >= pv) break;
        }
        UNROLL for (int i = 0; i < 4; ++i) TW[i] = *(const u4*)&lds[L_M + (2 + i) * 256 + lane * 4];
        core_expand(lane, MR, MI);
        f16v zr, zi;
        core_fft<true>(in, MR, MI, TW, zr, zi);
        UNROLL for (int r = 0; r < 16; ++r) z[r] = pkh(zr[r], zi[r]);
        z512 = z[8];
        const u32 za = __builtin_amdgcn_readlane(z[8], 32), zb = __builtin_amdgcn_readlane(z[0], 0);
        MEMBAR();
        const u32 zt = lane < 32 ? za : zb;
        UNROLL for (int r = 0; r < 8; ++r) z[r] = mark(z[r], vl, tm);
        UNROLL for (int r = 8; r < 16; ++r) z[r] = mark((lane & 31) == 0 ? (r < 15 ? z[r + 1] : zt) : z[r], vu, tm);
    }
    pin(z);
    const int zs = WS_Z + wv * 64 * 16384 + (((b >> 2) & 3) * 64 + (b & 3) * 16 + ((b >> 4) & 3) * 4 + (b >> 6)) * 64;
    bool zsent = false;
#define TRY_STORE_Z(force) do { if (!zsent) { \
        bool go = true; \
        if (force) lwait4(lds, L_LD, 1u); else { MEMBAR(); go = rfl(min4u(*(const u4*)&lds[L_LD])) != 0u; MEMBAR(); } \
        if (go) { \
            UNROLL for (int r = 0; r < 16; ++r) __builtin_amdgcn_raw_buffer_store_b32(z[r], rws, r < 8 ? zl : zu, zs + (r & 7) * 32768, SC1); \
            __builtin_amdgcn_raw_buffer_store_b64(u2{z512, tag0}, rws, zh, WS_HB + (wv * 256 + b) * HSTR, SC1); \
            zsent = true; } } SB(); } while (0)
    TRY_STORE_Z(false);
    const int row = (b & 7) >> 1, cls = cls_of(b), tile = tile_of(b);
    const bool packed = row == 0;
    u4 FR, FI, GR[4], GI[4], HR[4], HI[4], sel1;
    {
        const float ofs = cls == 4 ? 0.25f : 0.0f;
        float gu = (float)(cls + 8 * n) * (1.0f / 2048.0f), g0 = (float)(64 * q + 4 * wv) * gu + ofs;
        u32 s16 = 0x2c002c00u;
        pin(gu); pin(g0); pin(s16);
        UNROLL for (int i = 0; i < 4; ++i) UNROLL for (int j = 0; j < 4; ++j) { const u32 g = rot(fmaf((float)(16 * j + i), gu, g0)); GR[i][j] = hmul(g, s16); GI[i][j] = quarter(g, s16); }
    }
    pin(GR); pin(GI);
    {
        MagMax m;
        UNROLL for (int s1 = 0; s1 < 8; ++s1) m.add(xv[s1].x, xv[s1].y);
        if (wv == 0) lset(lds, L_SX, ex, lane);
        lset(lds, L_EW + wv, clampe(m.exponent()), lane);
    }
    TRY_STORE_Z(false);
    UNROLL for (int j = 0; j < 4; ++j) FR[j] = rot((float)(n * (4 * q + j)) * (1.0f / 16.0f));
    FI = quarter(FR);
    {
        float hs[4], hu[4];
        UNROLL for (int i = 0; i < 4; ++i) { hs[i] = (float)(16 * n + 4 * wv + i) * (1.0f / 2048.0f); hu[i] = (float)(cls + 32 * q + 8 * i); }
        pin(hs); pin(hu);
        UNROLL for (int i = 0; i < 4; ++i) { UNROLL for (int j = 0; j < 4; ++j) { float a = hs[i] * hu[j]; pin(a); HR[i][j] = rot(a); } HI[i] = quarter(HR[i]); }
        const int d = n + cls - 8 * q;
        UNROLL for (int j = 0; j < 4; ++j) sel1[j] = (d >> 1) == j ? ((d & 1) ? 0xffff0000u : 0xffffu) : 0u;
    }
    pin(FR); pin(FI); pin(HR); pin(HI); pin(sel1);
    TRY_STORE_Z(true);
    VMWAIT(4);
    u4 WA[4], WB[4];
    u32 ew = 0;
    UNROLL for (int i = 0; i < 4; ++i) asm volatile("" : "=v"(WA[i]), "=v"(WB[i]));
#define WIDEN(es) do { \
        u4 wr[4][4]; \
        int wl = L_W / 4 + wv * 1024 + n * 4 + min(q, 2); pin(wl); \
        UNROLL for (int i = 0; i < 4; ++i) UNROLL for (int a = 0; a < 4; ++a) wr[i][a] = ((const u4*)lds)[wl + (i * 4 + a) * 64]; \
        ew = pass ? (vd >> 8) & 0xffu : (es); \
        const u32 sw = ((249u - ew - (packed ? 1u : 0u)) << 7) * 0x10001u;       \
        const u4 sels = sel1 & sw; \
        SB(); \
        f4 wf[4][4]; \
        UNROLL for (int k = 0; k < 10; ++k) { \
            if (k < 8) { wf[k >> 1][2 * (k & 1)] = mfma16b(wr[k >> 1][2 * (k & 1)], sels, f4{}); wf[k >> 1][2 * (k & 1) + 1] = mfma16b(wr[k >> 1][2 * (k & 1) + 1], sels, f4{}); } \
            if (k >= 2) { const int i_ = (k - 2) >> 1, f_ = k & 1; UNROLL for (int r = 0; r < 4; ++r) (f_ ? WB : WA)[i_][r] = pkh(wf[i_][2 * f_][r], wf[i_][2 * f_ + 1][r]); } \
            SB(); } \
        pin(WA); pin(WB); } while (0)
    MEMBAR();
    const u4 pr0 = *(const u4*)&lds[L_PR];
    const bool wearly = rfl(min4u(pr0)) != 0u;
    MEMBAR();
    if (wearly) WIDEN(rfl(max4u(pr0)));
    const u32 vn = valid_bits(epoch, n), vm = valid_bits(epoch, 15 - n);
    u32 x1 = cls > 4 ? vm ^ 0x80000000u : vn, x2 = vm ^ (cls == 4 ? 0x00008000u : cls == 0 ? 0x80000000u : 0u);
    u32 z1[4][4], z2[4][4];
    const int zvm = q * 64 + (63 - tile) * 16384 + (15 - n) * 4;
    const int zv1 = cls <= 4 ? q * 64 + tile * 16384 + n * 4 : zvm;
    int zso = WS_Z + row * 64 * 16384 + wv * 4096;
    asm volatile("" : "+s"(zso));
#define LOAD_Z() do { \
        if (packed) { int a_ = zvm; pin(a_); UNROLL for (int i = 0; i < 4; ++i) UNROLL for (int j = 0; j < 4; ++j) z2[i][j] = __builtin_amdgcn_raw_buffer_load_b32(rws, a_ + (4 * i + j) * 256, zso, SC1); } \
        else { UNROLL for (int i = 0; i < 4; ++i) UNROLL for (int j = 0; j < 4; ++j) z2[i][j] = x2; } \
        int c_ = zv1; pin(c_); UNROLL for (int i = 0; i < 4; ++i) UNROLL for (int j = 0; j < 4; ++j) z1[i][j] = __builtin_amdgcn_raw_buffer_load_b32(rws, c_ + (4 * i + j) * 256, zso, SC1); } while (0)
    pin(x1); pin(x2);
    VMWAIT(0);
    LOAD_Z();
    if (wv == 0) lset(lds, L_ZL, pv, lane);
    SB();
    const int lw = ((wv * 16 + 4 * q) * 16 + n) * 4;
    int lr = (q * 16 + 4 * wv) * 16 + n;
    const u4* l4 = (const u4*)lds;
    pin(lr);
    SB();
    {
        f4 yr[4], yi[4];
        NOUNROLL for (int spin = 0; spin < SPIN_CAP; ++spin) {
            u32 m = 0;
            UNROLL for (int i = 0; i < 4; ++i) {
                u4 d;
                UNROLL for (int j = 0; j < 4; ++j) { z1[i][j] ^= x1; z2[i][j] ^= x2; }
                UNROLL for (int j = 0; j < 4; ++j) d[j] = U(H(z1[i][j]) + H(z2[i][j]));
                UNROLL for (int j = 0; j < 4; ++j) m |= z1[i][j] | z2[i][j];
                yr[i] = mfma16(GR[i], d); yi[i] = mfma16(GI[i], d);
                SB();
            }
            if (__all((m & TM) == 0u)) break;
            MEMBAR();
            LOAD_Z();
        }
        u4 y[4];
        UNROLL for (int i = 0; i < 4; ++i) UNROLL for (int r = 0; r < 4; ++r) y[r][i] = pkh(yr[i][r], yi[i][r]);
        UNROLL for (int r = 0; r < 4; ++r) *(u4*)&lds[L_A2 + lw + r * 64] = y[r];
        lset(lds, L_C3 + wv, pv, lane);
    }
    if (!wearly) { const u32 es = lwait4(lds, L_PR, 1u); WIDEN(es); }
    {
        u4 d[4];
        NOUNROLL for (int spin = 0; spin < LSPIN_CAP; ++spin) {
            MEMBAR();
            const u4 c = *(const u4*)&lds[L_C3];
            MEMBAR();
            UNROLL for (int i = 0; i < 4; ++i) d[i] = l4[L_A2 / 4 + lr + i * 16];
            pin(d[0]);
            if (rfl(min4u(c)) >= pv) break;
        }
        pin(WA); pin(WB);
        f4 xr[4], xi[4];
        u4 xx[4], ya[4], yb[4];
        UNROLL for (int i = 0; i < 4; ++i) {
            xr[i] = mfma16(FR, d[i]); xi[i] = mfma16(FI, d[i]);
            if (i >= 2) { UNROLL for (int r = 0; r < 4; ++r) xx[i - 2][r] = pkh(xr[i - 2][r], xi[i - 2][r]); pin(xx[i - 2]); }
            SB();
        }
        UNROLL for (int r = 0; r < 4; ++r) ya[0][r] = cmul_swap(xx[0][r], WA[0][r]);
        UNROLL for (int i = 2; i < 4; ++i) { UNROLL for (int r = 0; r < 4; ++r) xx[i][r] = pkh(xr[i][r], xi[i][r]); pin(xx[i]); }
        SB();
        f4 p0[4], p1[4], p2[4], p3[4];
        UNROLL for (int i = 0; i < 4; ++i) {
            p0[i] = mfma16(FR, ya[i]); p1[i] = mfma16(FI, ya[i]);
            if (i < 3) { UNROLL for (int r = 0; r < 4; ++r) ya[i + 1][r] = cmul_swap(xx[i + 1][r], WA[i + 1][r]); }
            else { UNROLL for (int r = 0; r < 4; ++r) yb[0][r] = cmul_swap(xx[0][r], WB[0][r]); }
            SB();
        }
        u4 ra[4], rb[4];
        UNROLL for (int i = 0; i < 4; ++i) {
            p2[i] = mfma16(FR, yb[i]); p3[i] = mfma16(FI, yb[i]);
            if (i < 3) { UNROLL for (int r = 0; r < 4; ++r) yb[i + 1][r] = cmul_swap(xx[i + 1][r], WB[i + 1][r]); }
            UNROLL for (int k = 0; k < 4; ++k) ra[i][k] = pkh(p0[k][i], p1[k][i]);
            *(u4*)&lds[L_B + lw + i * 64] = ra[i];
            SB();
        }
        UNROLL for (int i = 0; i < 3; ++i) UNROLL for (int r = 0; r < 4; ++r) rb[r][i] = pkh(p2[i][r], p3[i][r]);
        SB();
        UNROLL for (int r = 0; r < 4; ++r) { rb[r][3] = pkh(p2[3][r], p3[3][r]); *(u4*)&lds[L_B + 4096 + lw + r * 64] = rb[r]; }
        lset(lds, L_C4 + wv, pv, lane);
    }
    {
        u4 da[4], db[4];
        NOUNROLL for (int spin = 0; spin < LSPIN_CAP; ++spin) {
            MEMBAR();
            const u4 c = *(const u4*)&lds[L_C4];
            MEMBAR();
            UNROLL for (int i = 0; i < 4; ++i) { da[i] = l4[L_B / 4 + lr + i * 16]; db[i] = l4[L_B / 4 + 1024 + lr + i * 16]; }
            pin(da[0]);
            if (rfl(min4u(c)) >= pv) break;
        }
        const int vo = 64 * q * 32768 + n * 8, so = WS_V + 4 * wv * 32768 + cls * 4096 + tile * 128;
        f4 p0[4], p1[4], p2[4], p3[4];
        UNROLL for (int i = 0; i < 5; ++i) {
            if (i < 4) { p0[i] = mfma16(HR[i], da[i]); p1[i] = mfma16(HI[i], da[i]); p2[i] = mfma16(HR[i], db[i]); p3[i] = mfma16(HI[i], db[i]); }
            if (i > 0) { UNROLL for (int r = 0; r < 4; ++r)
                __builtin_amdgcn_raw_buffer_store_b64(u2{mark(pkh(p0[i - 1][r], p1[i - 1][r]), vn, tm), mark(pkh(p2[i - 1][r], p3[i - 1][r]), vn, tm)}, rws, vo, so + (16 * r + i - 1) * 32768, SC1); }
            SB();
        }
    }
    if (wv == 0) lset(lds, L_P2, 1u, lane);
    SB();
    {
        pin(xv);
        const f2 g01 = {bflo(gv.x), bfhi(gv.x)}, g23 = {bflo(gv.y), bfhi(gv.y)};
        f4 acc = {};
        UNROLL for (int i = 0; i < 4; ++i) {
            u4 pp;
            UNROLL for (int k = 0; k < 2; ++k) {
                const f2 a = f2{bflo(xv[2 * i + k].x), bfhi(xv[2 * i + k].x)} * g01, c = f2{bflo(xv[2 * i + k].y), bfhi(xv[2 * i + k].y)} * g23;
                pp[2 * k] = pkb(a.x, a.y); pp[2 * k + 1] = pkb(c.x, c.y);
            }
            const u32 s0 = n == 2 * i ? 0x3f803f80u : 0u, s1 = n == 2 * i + 1 ? 0x3f803f80u : 0u;
            acc = mfma16b(pp, u4{s0, s0, s1, s1}, acc);
        }
        if (n < 8) lds[L_GS + n * 16 + (t >> 4)] = __builtin_bit_cast(u32, (acc.x + acc.y) + (acc.z + acc.w));
        lset(lds, L_C5 + wv, pv, lane);
    }
    MR[0] = *(const u4*)&lds[L_M + lane * 4]; MR[1] = *(const u4*)&lds[L_M + 256 + lane * 4];
    UNROLL for (int i = 0; i < 4; ++i) TW[i] = *(const u4*)&lds[L_M + (2 + i) * 256 + lane * 4];
    core_expand(lane, MR, MI);
    pin(MR); pin(MI); pin(TW);
    u4 PM;
    {
        lwait4(lds, L_C5, pv);
        float gg[4];
        UNROLL for (int k = 0; k < 2; ++k) {
            const f4* pp = (const f4*)&lds[L_GS + ((n >> 2) * 2 + k) * 16];
            const f4 ps = (pp[0] + pp[1]) + (pp[2] + pp[3]);
            const float lg = rbf((ps.x + ps.y) + (ps.z + ps.w));
            gg[2 * k] = rbf(1.0f / (1.0f + __expf(-lg)));
            gg[2 * k + 1] = rbf(1.0f - gg[2 * k]);
        }
        UNROLL for (int k = 0; k < 4; ++k) { gg[k] *= 0.03125f; pin(gg[k]); }
        const int r = n & 3, sa = (n >> 2) * 2;
        UNROLL for (int cc = 0; cc < 2; ++cc) {
            const int u1 = 2 * q + cc;
            const float ta = (float)(sa * u1) * 0.125f + ((r & 1) ? 0.25f : (r & 2) ? 0.5f : 0.0f);
            const float tb = (float)((sa + 1) * u1) * 0.125f + ((r & 1) ? ((r & 2) ? 0.0f : 0.5f) : 0.25f);
            const float ca = cosr(ta), sa_ = sinr(ta), cb = cosr(tb), sb_ = sinr(tb);
            float e[4] = {gg[2] * cb, gg[2] * sb_, gg[3] * cb, gg[3] * sb_};
            pin(e);
            e[0] = fmaf(gg[0], ca, e[0]); pin(e[0]); e[1] = fmaf(gg[0], sa_, e[1]); pin(e[1]); e[2] = fmaf(gg[1], ca, e[2]); pin(e[2]); e[3] = fmaf(gg[1], sa_, e[3]);
            PM[2 * cc] = pkh(e[0], e[1]); PM[2 * cc + 1] = pkh(e[2], e[3]);
        }
    }
    int a3d = (q * 8 + wv) * 32 + n, a3m = (q * 8 + 7 - wv) * 32 + 16 - n, a3z = (q * 8 + 7 - wv) * 32 + ((32 - n) & 31), a3r = wv * 256 + lane;
    float osc = __builtin_bit_cast(float, (u32)min(max((int)ex + (int)ew - 130, 1), 254) << 23) * TRUNC_FIX;
    const int ve = 2 * q * 4096 + (128 * wv + n) * 8;
    int vs0 = WS_V + b * 32768, vs1 = vs0 + 4096, vsl = WS_VL + b * 16;
    asm volatile("" : "+s"(vs0), "+s"(vs1), "+s"(vsl));
    int ov = (4 * perm32(lane & 15) + 2 * ((lane >> 4) & 1) + (lane >> 5)) * 16 + (512 * wv + b) * 2048;
    u32 x8 = wv == 3 ? 0u : vn;
    pin(a3d); pin(a3m); pin(a3z); pin(a3r); pin(PM); pin(ov); pin(osc); pin(x8);
    VMWAIT(0);
    {
        f4 z3[9];
        NOUNROLL for (int spin = 0; spin < SPIN_CAP; ++spin) {
            MEMBAR();
            u2 v0[8], v1[8];
            u4 ve8, vo8;
            int a_ = ve; pin(a_);
            if (wv == 3) {
                ve8 = __builtin_amdgcn_raw_buffer_load_b128(rws, q * 8192, vsl, SC1); vo8 = __builtin_amdgcn_raw_buffer_load_b128(rws, q * 8192 + 4096, vsl, SC1);
            } else {
                const u2 e2 = __builtin_amdgcn_raw_buffer_load_b64(rws, a_ + 1024, vs0, SC1), o2 = __builtin_amdgcn_raw_buffer_load_b64(rws, a_ + 1024, vs1, SC1);
                ve8 = u4{e2.x, e2.y, tag0, tag0}; vo8 = u4{o2.x, o2.y, tag0, tag0};
            }
            UNROLL for (int i = 0; i < 8; ++i) {
                v0[i] = __builtin_amdgcn_raw_buffer_load_b64(rws, a_ + 128 * i, vs0, SC1);
                v1[i] = __builtin_amdgcn_raw_buffer_load_b64(rws, a_ + 128 * i, vs1, SC1);
            }
            SB();
            u32 m = (((wv == 3 ? ve8.y : ve8.z) ^ tag0) | (ve8.w ^ tag0) | ((wv == 3 ? vo8.y : vo8.z) ^ tag0) | (vo8.w ^ tag0)) ? TM : 0u;
            UNROLL for (int k = 0; k < 9; ++k) {
                const int i = (k + 8) % 9;
                const u4 d = i == 8 ? u4{ve8.x, wv == 3 ? ve8.z : ve8.y, vo8.x, wv == 3 ? vo8.z : vo8.y} ^ x8 : u4{v0[i & 7].x, v0[i & 7].y, v1[i & 7].x, v1[i & 7].y} ^ vn;
                m |= d.x | d.y; m |= d.z | d.w;
                z3[i] = mfma16(PM, d);
                SB();
            }
            if (__all((m & TM) == 0u)) break;
        }
        if (wv == 0 && n == 0) { z3[0].x = 0.5f * (z3[0].x + z3[0].z); z3[0].y = 0.5f * (z3[0].y + z3[0].w); }
        if (wv == 3) { z3[8].z = 0.5f * (z3[8].x + z3[8].z); z3[8].w = 0.5f * (z3[8].y + z3[8].w); }
        u4* l4w = (u4*)&lds[L_A1];
        UNROLL for (int p = 0; p < 2; ++p)
            l4w[a3d + 16 * p] = u4{pkh(z3[p].x, z3[p + 2].x), pkh(z3[p + 4].x, z3[p + 6].x), pkh(z3[p].y, z3[p + 2].y), pkh(z3[p + 4].y, z3[p + 6].y)};
        l4w[a3m] = u4{pkh(z3[7].z, z3[5].z), pkh(z3[3].z, z3[1].z), pkh(z3[7].w, z3[5].w), pkh(z3[3].w, z3[1].w)};
        f2 m[4];
        UNROLL for (int i = 0; i < 4; ++i) m[i] = n == 0 ? f2{z3[8 - 2 * i].z, z3[8 - 2 * i].w} : f2{z3[6 - 2 * i].z, z3[6 - 2 * i].w};
        l4w[a3z] = u4{pkh(m[0].x, m[1].x), pkh(m[2].x, m[3].x), pkh(m[0].y, m[1].y), pkh(m[2].y, m[3].y)};
        lset(lds, L_C6 + wv, 2u * pv - 1u, lane);
    }
    {
        u4 in[4];
        NOUNROLL for (int spin = 0; spin < LSPIN_CAP; ++spin) {
            MEMBAR();
            const u4 c = *(const u4*)&lds[L_C6];
            MEMBAR();
            UNROLL for (int s = 0; s < 4; ++s) in[s] = ((const u4*)&lds[L_A1])[a3r + s * 64];
            pin(in[0]);
            if (rfl(min4u(c)) >= 2u * pv - 1u) break;
        }
        f16v zr, zi;
        core_fft<false>(in, MR, MI, TW, zr, zi);
        zr *= osc; zi *= osc;
        const rsrc_t ro = mkrs(out, 2048 * 2048);
#define OUT_ROW(z, off) do { u4 lo, hi; \
            UNROLL for (int j = 0; j < 4; ++j) { const auto sw_ = __builtin_amdgcn_permlane16_swap(pkb(z[2 * j], z[2 * j + 1]), pkb(z[8 + 2 * j], z[8 + 2 * j + 1]), false, false); lo[j] = sw_[0]; hi[j] = sw_[1]; } \
            store128<SC1>(lo, ro, ov + (off)); store128<SC1>(hi, ro, ov + (off) + 1024); } while (0)
        OUT_ROW(zr, 256 * 2048);
        OUT_ROW(zi, 0);
    }
    vd = lwait(lds, L_VD, 1u);
    if (pass == 1 || !(vd & 0x20000u)) break;
    lset(lds, L_C6 + wv, 2u, lane);
    lwait4(lds, L_C6, 2u);
    VMWAIT(0);
    }
}

extern "C" void kernel_launch(void* const* d_in, const int* in_sizes, int n_in,
                              void* d_out, int out_size, void* d_ws, size_t ws_size,
                              hipStream_t stream) {
    gfnet_fused<<<256, 384, 0, stream>>>(d_in[0], d_in[6], d_in[1], d_in[2], d_in[4], d_in[5], (char*)d_ws, d_out);
}
```

```cpp
#include <hip/hip_runtime.h>
#include <hip/hip_bf16.h>
#include <stdint.h>

typedef _Float16 h2 __attribute__((ext_vector_type(2)));
typedef _Float16 h8 __attribute__((ext_vector_type(8)));
typedef __bf16 b2 __attribute__((ext_vector_type(2)));
typedef __bf16 b8 __attribute__((ext_vector_type(8)));
typedef float f2 __attribute__((ext_vector_type(2)));
typedef float f4 __attribute__((ext_vector_type(4)));
typedef float f16v __attribute__((ext_vector_type(16)));
typedef short s4 __attribute__((ext_vector_type(4)));
typedef unsigned u32;
typedef u32 u2 __attribute__((ext_vector_type(2)));
typedef u32 u4 __attribute__((ext_vector_type(4)));
typedef unsigned short us2 __attribute__((ext_vector_type(2)));
typedef __amdgpu_buffer_rsrc_t rsrc_t;
#define DI __device__ __forceinline__
#define UNROLL _Pragma("unroll")
#define NOUNROLL _Pragma("unroll 1")
#define LAS __attribute__((address_space(3)))
#define SB() __builtin_amdgcn_sched_barrier(0)
#define MEMBAR() asm volatile("" ::: "memory")
#define VMWAIT(n) asm volatile("s_waitcnt vmcnt(%0)" :: "n"(n) : "memory")

#define MARGIN 2

constexpr u32 FSTR = 16, HSTR = 32;
constexpr u32 WS_Z = 0;
constexpr u32 WS_V = 4u << 20;
constexpr u32 WS_VL = 12u << 20;
constexpr u32 WS_HB = WS_VL + (32u << 10);
constexpr u32 WS_F = WS_HB + (32u << 10);
constexpr u32 WS_E = WS_F + 2048 * FSTR;
constexpr u32 WS_BYTES = WS_E + 4096;
constexpr int SC1 = 16, NT = 2;
constexpr int SPIN_CAP = 8192, LSPIN_CAP = 1 << 17;
constexpr int OOB = 0x7ffffff0;
constexpr u32 TM = 0x00010001u;
constexpr float TRUNC_FIX = 1.00066f;

template <class T> DI void pin(T& v) { asm volatile("" : "+v"(v)); }
template <class T, int N> DI void pin(T (&v)[N]) { UNROLL for (int i = 0; i < N; ++i) pin(v[i]); }
DI u32 opq(u32 v) { asm("" : "+v"(v)); return v; }
DI u32 pkh(float lo, float hi) { h2 r; r.x = (_Float16)lo; r.y = (_Float16)hi; return __builtin_bit_cast(u32, r); }
DI u32 pkb(float lo, float hi) { b2 r; r.x = (__bf16)lo; r.y = (__bf16)hi; return __builtin_bit_cast(u32, r); }
DI float bflo(u32 v) { return __builtin_bit_cast(float, v << 16); }
DI float bfhi(u32 v) { return __builtin_bit_cast(float, v & 0xffff0000u); }
DI float rbf(float v) { return (float)(__bf16)v; }
DI h2 H(u32 v) { return __builtin_bit_cast(h2, v); }
DI u32 U(h2 v) { return __builtin_bit_cast(u32, v); }
DI f16v mfma32(u4 a, u4 b, f16v c) { return __builtin_amdgcn_mfma_f32_32x32x16_f16(__builtin_bit_cast(h8, a), __builtin_bit_cast(h8, b), c, 0, 0, 0); }
DI f4 mfma16(u4 a, u4 b) { return __builtin_amdgcn_mfma_f32_16x16x32_f16(__builtin_bit_cast(h8, a), __builtin_bit_cast(h8, b), f4{}, 0, 0, 0); }
DI f4 mfma16b(u4 a, u4 b, f4 c) { return __builtin_amdgcn_mfma_f32_16x16x32_bf16(__builtin_bit_cast(b8, a), __builtin_bit_cast(b8, b), c, 0, 0, 0); }
DI u32 hmul(u32 a, u32 b) { return U(H(a) * H(b)); }
DI u32 cmul_swap(u32 x, u32 w) { const h2 a = H(opq(x)), c = H(opq(w)); h2 n = c; n.y = -n.y; return U(__builtin_elementwise_fma(a.yy, n, a.xx * c.yx)); }
DI u32 cmul_conj(u32 x, u32 t) { const h2 a = H(opq(x)), c = H(opq(t)); h2 n = c.yy; n.y = -n.y; return U(__builtin_elementwise_fma(a.yx, n, a * c.xx)); }
DI u32 pkmax(u32 a, u32 b) { return __builtin_bit_cast(u32, __builtin_elementwise_max(__builtin_bit_cast(us2, a), __builtin_bit_cast(us2, b))); }
DI float sinr(float r) { return __builtin_amdgcn_sinf(r); }
DI float cosr(float r) { return __builtin_amdgcn_cosf(r); }
DI u32 rot(float ph) { return pkh(cosr(ph), sinr(ph)); }
DI u32 quarter(u32 r, u32 f) { u32 o; asm("v_pk_mul_f16 %0, %1, %2 op_sel:[1,0] op_sel_hi:[0,1] neg_lo:[1,0]" : "=v"(o) : "v"(r), "v"(f)); return o; }
DI u4 quarter(u4 r) { u32 one = 0x3c003c00u; pin(one); return u4{quarter(r.x, one), quarter(r.y, one), quarter(r.z, one), quarter(r.w, one)}; }
DI int perm32(int n) { return (n & 0x13) | ((n & 4) << 1) | ((n & 8) >> 1); }
DI u32 max4u(u4 v) { return max(max(v.x, v.y), max(v.z, v.w)); }
DI u32 min4u(u4 v) { return min(min(v.x, v.y), min(v.z, v.w)); }
DI u32 mark(u32 v, u32 par, u32 tm) { return (par & tm) | (v & ~tm); }
DI u32 rfl(u32 v) { return __builtin_amdgcn_readfirstlane(v); }
DI rsrc_t mkrs(const void* p, u32 bytes) { return __builtin_amdgcn_make_buffer_rsrc((void*)p, (short)0, (int)bytes, 0x00020000); }
template <int AUX> DI void store128(u4 v, rsrc_t r, int off) { __builtin_amdgcn_raw_buffer_store_b128(v, r, off, 0, AUX); }
template <int CTRL> DI u32 dpp(u32 v) { return __builtin_amdgcn_update_dpp(0u, v, CTRL, 0xf, 0xf, true); }
DI u32 wave_maxu(u32 v) {
    v = max(v, dpp<0xB1>(v)); v = max(v, dpp<0x4E>(v)); v = max(v, dpp<0x141>(v)); v = max(v, dpp<0x140>(v));
    return max(max((u32)__builtin_amdgcn_readlane(v, 0), (u32)__builtin_amdgcn_readlane(v, 16)), max((u32)__builtin_amdgcn_readlane(v, 32), (u32)__builtin_amdgcn_readlane(v, 48)));
}

DI void core_setup(int lane, int s, u4& tw, u2& m) {
    float fc = (float)perm32(lane & 31) * (1.0f / 1024.0f), p0 = (float)(4 * (lane >> 5) + 8 * s) * fc, p1 = (float)(4 * (lane >> 5) + 8 * (s >> 1) + 2 * (s & 1)) * fc * 32.0f;
    pin(fc); pin(p0); pin(p1);
    float tc[4], ts[4];
    UNROLL for (int i = 0; i < 4; ++i) { const float pt = fmaf((float)i, fc, p0); tc[i] = cosr(pt); ts[i] = sinr(pt); }
    tw = u4{pkh(tc[0], tc[1]), pkh(tc[2], tc[3]), pkh(ts[0], ts[1]), pkh(ts[2], ts[3])};
    const float p2 = fmaf(32.0f, fc, p1);
    m = u2{pkh(cosr(p1), cosr(p2)), pkh(sinr(p1), sinr(p2))};
}
DI void core_expand(int lane, u4 (&MR)[4], u4 (&MI)[4]) {
    const u32 sg = (lane & 1) ? 0x80008000u : 0u;
    MR[2] = MR[0] ^ sg; MR[3] = MR[1] ^ sg;
    UNROLL for (int s = 0; s < 4; ++s) MI[s] = u4{MR[s].z ^ 0x80008000u, MR[s].w ^ 0x80008000u, MR[s].x, MR[s].y};
}
template <bool OUT_B> DI void core_fft(const u4 (&in)[4], const u4 (&MR)[4], const u4 (&MI)[4], const u4 (&TW)[4], f16v& outr, f16v& outi) {
    f16v yr = {}, yi = {};
    UNROLL for (int s = 0; s < 4; ++s) yr = mfma32(in[s], MR[s], yr);
    SB();
    yi = mfma32(in[0], MI[0], yi); yi = mfma32(in[1], MI[1], yi);
    SB();
    h2 t0[8], t1[8];
#define TW_RE(p) do { const h2 pr_ = H(pkh(yr[2 * (p)], yr[2 * (p) + 1])); t0[p] = pr_ * H(TW[(p) >> 1][(p) & 1]); t1[p] = pr_ * H(TW[(p) >> 1][2 + ((p) & 1)]); } while (0)
    TW_RE(0); TW_RE(1); SB();
    yi = mfma32(in[2], MI[2], yi);
    TW_RE(2); TW_RE(3); SB();
    yi = mfma32(in[3], MI[3], yi);
    TW_RE(4); TW_RE(5); TW_RE(6); TW_RE(7); SB();
    outr = f16v{}; outi = f16v{};
    u4 d[4];
#define TW_IM(s) do { UNROLL for (int j = 0; j < 2; ++j) { const int p_ = 2 * (s) + j; const h2 pi_ = H(pkh(yi[2 * p_], yi[2 * p_ + 1])); \
        d[s][j] = U(__builtin_elementwise_fma(pi_, H(TW[s][2 + j]), t0[p_])); d[s][2 + j] = U(__builtin_elementwise_fma(pi_, H(TW[s][j]), -t1[p_])); } } while (0)
    TW_IM(0); SB();
    UNROLL for (int s = 0; s < 4; ++s) {
        outr = OUT_B ? mfma32(MR[s], d[s], outr) : mfma32(d[s], MR[s], outr);
        if (s < 3) TW_IM(s + 1);
        SB();
    }
    UNROLL for (int s = 0; s < 4; ++s) outi = OUT_B ? mfma32(MI[s], d[s], outi) : mfma32(d[s], MI[s], outi);
}

DI u32 tag_of(u32 epoch, u32 idx) { u32 h = (epoch ^ (idx * 0x9E3779B1u)) * 0x85EBCA6Bu; return h ^ (h >> 15); }
DI u32 valid_bits(u32 epoch, int slot) { return (((0x6A3Cu >> slot) ^ epoch) & 1u) ? 0x00000001u : 0x00010000u; }
DI int cls_of(int b) { const int row = (b & 7) >> 1; return row == 0 ? 4 * (b >> 7) : ((b & 1) ? 8 - row : row); }
DI int tile_of(int b) { return (b & 6) == 0 ? 16 * (b & 1) + ((b >> 3) & 15) : b >> 3; }

constexpr int L_C1 = 0, L_C3 = 4, L_C4 = 8, L_C5 = 12, L_C6 = 16;
constexpr int L_EW = 20;
constexpr int L_PR = 28;
constexpr int L_LD = 32;
constexpr int L_SX = 36;
constexpr int L_ZL = 37;
constexpr int L_VD = 38;
constexpr int L_P2 = 39;
constexpr int L_GS = 64;
constexpr int L_M = 192;
constexpr int L_A1 = L_M + 1536;
constexpr int L_A2 = L_A1 + 4096;
constexpr int L_B = L_A2 + 4096;
constexpr int L_W = L_B + 8192;
constexpr int L_END = L_W + 64 * 256;

DI void lset(u32* lds, int idx, u32 v, int lane) { MEMBAR(); if (lane == 0) lds[idx] = v; MEMBAR(); }
DI u32 lget(u32* lds, int idx) { MEMBAR(); return rfl(lds[idx]); }
DI u32 lwait(u32* lds, int idx, u32 atleast) {
    u32 v = 0;
    NOUNROLL for (int spin = 0; spin < LSPIN_CAP; ++spin) { v = lget(lds, idx); if (v >= atleast) break; __builtin_amdgcn_s_sleep(1); }
    MEMBAR();
    return v;
}
DI u32 lwait4(u32* lds, int idx, u32 atleast) {
    u4 v;
    NOUNROLL for (int spin = 0; spin < LSPIN_CAP; ++spin) { MEMBAR(); v = *(const u4*)&lds[idx]; if (rfl(min4u(v)) >= atleast) break; __builtin_amdgcn_s_sleep(1); }
    MEMBAR();
    return rfl(max4u(v));
}
struct MagMax {
    h2 hi = {}, lo = {};
    DI void add(u32 a, u32 c) {
        hi = __builtin_elementwise_maximum(__builtin_elementwise_maximum(hi, H(a)), H(c));
        lo = __builtin_elementwise_minimum(__builtin_elementwise_minimum(lo, H(a)), H(c));
    }
    DI u32 exponent() const {
        const u32 mx = pkmax(U(hi) & 0x7fff7fffu, U(lo) & 0x7fff7fffu);
        return wave_maxu(max(mx & 0xffffu, mx >> 16)) >> 7;
    }
};
DI u32 clampe(u32 e) { return min(max(e, 8u), 240u); }

__global__ __launch_bounds__(384, 2) void gfnet_fused(const void* x, const void* gw, const void* war, const void* wai, const void* wbr, const void* wbi,
                                                      char* ws, void* out) {
    __shared__ __attribute__((aligned(16))) u32 lds[L_END];
    u32 code;
    {
        unsigned long long pc0; int len, w3;
        asm volatile("s_getpc_b64 %0\n.Lgf_begin:\n\ts_mov_b32 %1, .Lgf_end-.Lgf_begin\n\ts_mov_b32 %2, 0x20000" : "=s"(pc0), "=s"(len), "=s"(w3));
        code = __builtin_amdgcn_raw_buffer_load_b32(__builtin_amdgcn_make_buffer_rsrc((void*)pc0, (short)0, max(len, 0), w3), (threadIdx.x >> 6) == 4 ? ((blockIdx.x >> 5) * 64 + (threadIdx.x & 63)) * 64 : OOB, 0, 0);
        SB();
    }
    asm volatile("" :: "s"(x), "s"(gw), "s"(war), "s"(wai), "s"(wbr), "s"(wbi), "s"(ws), "s"(out));
    int wv0 = __builtin_amdgcn_readfirstlane(threadIdx.x >> 6);
    const rsrc_t rws = mkrs(ws, WS_BYTES);
    u2 xv[8], gv; u4 smp, smp0, smp1; u32 ev, eva, edge[2];
#define LOAD_X(t, b) do { const rsrc_t rx = mkrs(x, 2048 * 2048); \
        smp = __builtin_amdgcn_raw_buffer_load_b128(rx, (((t) & 63) >> 3) * (524288 + 2048 + 256) + ((t) & 7) * 16, 0, 0); \
        UNROLL for (int s1 = 0; s1 < 8; ++s1) xv[s1] = __builtin_amdgcn_raw_buffer_load_b64(rx, (t) * 8, (256 * s1 + (b)) * 2048, NT); \
        ev = __builtin_amdgcn_raw_buffer_load_b32(rws, 0, WS_E, SC1); \
        gv = __builtin_amdgcn_raw_buffer_load_b64(mkrs(gw, 2048), (t) * 8, 0, 0); MEMBAR(); } while (0)
#define W_ROLE() const int wv = wv0, b = blockIdx.x, cls = cls_of(b), tile = tile_of(b), lane = threadIdx.x & 63; \
        const rsrc_t rw[2] = {mkrs(wv == 4 ? war : wbr, 2048 * 1026), mkrs(wv == 4 ? wai : wbi, 2048 * 1026)}; \
        const int wvo = (lane >> 2) * (128 * 1026) + min(lane & 3, 2) * 16, wso = (cls * 513 + 16 * tile - cls) * 2
#define WLOAD(r) do { UNROLL for (int st = 0; st < 4; ++st) { const int c_ = 4 * st + ((tile + (r)) & 3); \
        UNROLL for (int a = 0; a < 2; ++a) __builtin_amdgcn_raw_ptr_buffer_load_lds(rw[a], (LAS void*)&lds[L_W + (c_ * 4 + 2 * (wv - 4) + a) * 256], 16, wvo, wso + c_ * 8208, 0, 0); } SB(); } while (0)
    if (wv0 < 4) {
        LOAD_X((int)threadIdx.x, (int)blockIdx.x);
        __builtin_amdgcn_s_barrier();
        MEMBAR();
        __builtin_amdgcn_s_setprio(1);
    } else {
        W_ROLE();
        if (wv == 4) { lds[lane] = 0u; asm volatile("s_waitcnt lgkmcnt(0)" ::: "memory"); }
        const bool split = (b & 6) == 0;
        const int er = split ? 16 * (tile & 15) + ((lane >> 1) & 15) : 8 * tile + ((lane >> 1) & 7);
        int eo = ((cls + 8 * er) * 513 + (split ? (tile & 16) * 16 : 0) - cls + ((lane & 1) ? (split ? 262 : 518) : 0)) * 2;
        pin(eo);
        MEMBAR();
        __builtin_amdgcn_s_barrier();
        MEMBAR();
        WLOAD(0);
        UNROLL for (int a = 0; a < 2; ++a) edge[a] = __builtin_amdgcn_raw_buffer_load_b32(rw[a], eo, 0, 0);
        eva = __builtin_amdgcn_raw_buffer_load_b32(rws, 0, WS_E, SC1);
        smp0 = __builtin_amdgcn_raw_buffer_load_b128(rw[0], (lane >> 3) * (2 * 131328 + 128) + (lane & 7) * 16, 0, 0);
        smp1 = __builtin_amdgcn_raw_buffer_load_b128(rw[1], (lane >> 3) * (2 * 131328 + 128) + (lane & 7) * 16, 0, 0);
        MEMBAR();
    }
    asm volatile("" : "+s"(wv0));

    if (wv0 >= 4) {
        W_ROLE();
        const int q = lane >> 4, n = lane & 15;
        const bool helper = wv == 5 && tile == 31;
        u4 GR, GI, FR, FI, HR, HI, T1, T2;
        {
            if (helper) {
                UNROLL for (int j = 0; j < 4; ++j) {
                    GR[j] = rot((float)((4 * q + j) * (cls + 8 * n)) * (1.0f / 128.0f) + (cls == 4 ? 0.25f : 0.0f));
                    HR[j] = rot((float)(n * (cls + 8 * (4 * q + j))) * (1.0f / 128.0f));
                    FR[j] = rot((float)(n * (4 * q + j)) * (1.0f / 16.0f));
                    T1[j] = rot((float)((4 * q + j) * (cls + 8 * n)) * (1.0f / 2048.0f));
                    T2[j] = rot((float)(n * (cls + 8 * (4 * q + j))) * (1.0f / 2048.0f));
                }
                GI = quarter(GR); HI = quarter(HR); FI = quarter(FR);
                pin(GR); pin(GI); pin(HR); pin(HI); pin(FR); pin(FI); pin(T1); pin(T2);
            }
            VMWAIT(0);
            WLOAD(1); WLOAD(2); WLOAD(3);
            asm volatile("ds_write_b64 %0, %1" :: "v"((u32)(size_t)(LAS u32*)&lds[L_LD + 2 * (wv - 4)]), "v"(u2{1u, 1u}) : "memory");
            pin(edge); pin(code);
            MagMax ms; ms.add(smp0.x, smp0.y); ms.add(smp0.z, smp0.w); ms.add(smp1.x, smp1.y); ms.add(smp1.z, smp1.w);
            const u32 es = clampe(ms.exponent() + MARGIN);
            VMWAIT(0);
            MEMBAR(); if (lane == 0) *(u2*)&lds[L_PR + 2 * (wv - 4)] = u2{es, es}; MEMBAR();
        }
        const u32 ep0 = rfl(eva) + 1u;
        if (wv == 4) {
            lwait4(lds, L_PR, 1u);
            MagMax m;
            NOUNROLL for (int c0 = 0; c0 < 64; c0 += 16) { UNROLL for (int c = 0; c < 16; ++c) { const u4 v = *(const u4*)&lds[L_W + (c0 + c) * 256 + lane * 4]; m.add(v.x, v.y); m.add(v.z, v.w); } }
            const u32 ew = clampe(m.exponent()), ex = lwait4(lds, L_EW, 1u);
            __builtin_amdgcn_raw_buffer_store_b64(u2{ex | (ew << 8), tag_of(ep0, b + 1)}, rws, lane < 8 ? lane * (int)(256 * FSTR) : OOB, WS_F + b * FSTR, SC1);
            lwait(lds, L_P2, 1u);
            __builtin_amdgcn_s_sleep(8);
            u2 f[4]; u32 tg[4];
            UNROLL for (int j = 0; j < 4; ++j) tg[j] = tag_of(ep0, 4 * lane + j + 1);
            NOUNROLL for (int spin = 0; spin < SPIN_CAP; ++spin) {
                MEMBAR();
                UNROLL for (int j = 0; j < 4; ++j) f[j] = __builtin_amdgcn_raw_buffer_load_b64(rws, lane * (4 * FSTR) + j * FSTR, WS_F + (b & 7) * (256 * FSTR), SC1);
                if (__all(f[0].y == tg[0] && f[1].y == tg[1] && f[2].y == tg[2] && f[3].y == tg[3])) break;
                __builtin_amdgcn_s_sleep(8);
            }
            const u4 e4 = {f[0].x, f[1].x, f[2].x, f[3].x};
            const u32 gx = wave_maxu(max4u(e4 & 0xffu)), gwm = wave_maxu(max4u((e4 >> 8) & 0xffu));
            const bool redo = gx > lget(lds, L_SX) || gwm > lwait4(lds, L_PR, 1u);
            lset(lds, L_VD, gx | (gwm << 8) | (redo ? 0x20000u : 0x10000u), lane);
            if (b == 0 && lane == 0) __builtin_amdgcn_raw_buffer_store_b32(ep0 + (redo ? 1u : 0u), rws, 0, WS_E, SC1);
        } else if (helper) {
            __builtin_amdgcn_s_setprio(2);
            u32 ew = lwait4(lds, L_PR, 1u);
            NOUNROLL for (u32 pass = 0; pass < 2; ++pass) {
                u4 WH[2];
                const float sw = __builtin_bit_cast(float, (249u - ew) << 23);
                UNROLL for (int r = 0; r < 4; ++r) UNROLL for (int f = 0; f < 2; ++f) {
                    const unsigned short* wp = (const unsigned short*)&lds[L_W + (n * 4 + 2 * f) * 256 + (4 * q + r) * 16] + 16 + cls;
                    float re = bflo(wp[0]) * sw, im = bflo(wp[512]) * sw;
                    pin(re);
                    WH[f][r] = pkh(re, im);
                }
                pin(WH);
                lwait(lds, L_ZL, pass + 1u);
                const u32 tg = tag_of(ep0 + pass, 0u);
                u4 hz;
                NOUNROLL for (int spin = 0; spin < SPIN_CAP; ++spin) {
                    MEMBAR();
                    u32 bad = 0;
                    UNROLL for (int j = 0; j < 4; ++j) { const u2 g = __builtin_amdgcn_raw_buffer_load_b64(rws, (64 * q + n + 16 * j) * HSTR, WS_HB + ((b & 7) >> 1) * (256 * HSTR), SC1); hz[j] = g.x & ~TM; bad |= g.y ^ tg; }
                    if (__all(bad == 0u)) break;
                    __builtin_amdgcn_s_sleep(4);
                }
                const u32 f16 = cls == 0 ? 0x00002c00u : cls == 4 ? 0x2c000000u : cls > 4 ? 0xac002c00u : 0x2c002c00u;
                u4 d, o[2];
                UNROLL for (int j = 0; j < 4; ++j) d[j] = hmul(hz[j], f16);
                f4 yr = mfma16(d, GR), yi = mfma16(d, GI);
                UNROLL for (int r = 0; r < 4; ++r) d[r] = cmul_conj(pkh(yr[r], yi[r]), T1[r]);
                const f4 xr = mfma16(FR, d), xi = mfma16(FI, d);
                UNROLL for (int f = 0; f < 2; ++f) {
                    UNROLL for (int r = 0; r < 4; ++r) d[r] = cmul_swap(pkh(xr[r], xi[r]), WH[f][r]);
                    yr = mfma16(d, FR); yi = mfma16(d, FI);
                    UNROLL for (int r = 0; r < 4; ++r) o[f][r] = cmul_conj(pkh(yr[r], yi[r]), T2[r]);
                }
                const f4 p0 = mfma16(HR, o[0]), p1 = mfma16(HI, o[0]), p2 = mfma16(HR, o[1]), p3 = mfma16(HI, o[1]);
                UNROLL for (int r = 0; r < 4; ++r)
                    store128<SC1>(u4{pkh(p0[r], p1[r]) & ~TM, tg, pkh(p2[r], p3[r]) & ~TM, tg}, rws, (64 * q + n) * 16 + WS_VL + r * 256 + cls * 4096);
                const u32 vd = lwait(lds, L_VD, 1u);
                if (!(vd & 0x20000u)) break;
                ew = (vd >> 8) & 0xffu;
            }
        }
        asm volatile(".Lgf_end:");
        return;
    }

    {
        const int lane = threadIdx.x & 63, wv = wv0;
        u4 tw; u2 m;
        core_setup(lane, wv, tw, m);
        *(u4*)&lds[L_M + (2 + wv) * 256 + lane * 4] = tw;
        lds[L_M + (wv >> 1) * 256 + lane * 4 + (wv & 1)] = m.x; lds[L_M + (wv >> 1) * 256 + lane * 4 + 2 + (wv & 1)] = m.y;
    }
    SB();
    u32 vd = 0;
    NOUNROLL for (u32 pass = 0; pass < 2; ++pass) {
    int t = threadIdx.x, wv = wv0, b = blockIdx.x;
    pin(t);
    asm volatile("" : "+s"(wv));
    asm volatile("" : "+s"(b));
    const int lane = t & 63, q = lane >> 4, n = lane & 15;
    const u32 pv = pass + 1u;
    u4 MR[4], MI[4], TW[4];
    const int zc = perm32(lane & 31) + (lane >> 5) * 256, zcu = zc + ((lane & 31) == 0 ? 543 : 511);
    int zl = (zc >> 4) * 16384 + (zc & 15) * 4, zu = (zcu >> 4) * 16384 + (zcu & 15) * 4;
    int zh = lane == 0 ? 0 : OOB;
    int tro = L_A1 + wv * 1024 + (4 * (lane >> 5) + ((lane & 15) >> 2)) * 16 + ((lane >> 4) & 1) * 8 + (lane & 3) * 2;
    u32 tm = TM, vl0 = valid_bits(0u, zc & 15), vu0 = valid_bits(0u, zcu & 15);
    pin(zl); pin(zu); pin(zh); pin(tro); pin(tm); pin(vl0); pin(vu0);
    SB();
    pin(smp);
    u32 ex;
    {
        MagMax ms; ms.add(smp.x, smp.y); ms.add(smp.z, smp.w);
        ex = pass ? (vd & 0xffu) : clampe(ms.exponent() + MARGIN);
    }
    u32 epoch, tag0, flip;
    u32 z[16], z512;
    {
        const float sc = __builtin_bit_cast(float, (250u - ex) << 23);
        const float rs = sc * 0.70710678118654752f;
        u32 o[8][2];
        pin(xv);
        UNROLL for (int j = 0; j < 2; ++j) {
            f2 v[8];
            UNROLL for (int s1 = 0; s1 < 8; ++s1) v[s1] = f2{bflo(xv[s1][j]), bfhi(xv[s1][j])};
            const f2 a0 = v[0] + v[4], a1 = v[1] + v[5], a2 = v[2] + v[6], a3 = v[3] + v[7];
            const f2 b0 = (v[0] - v[4]) * sc, b1 = v[1] - v[5], b2_ = (v[2] - v[6]) * sc, b3 = v[3] - v[7];
            const f2 e0 = a0 + a2, e1 = (a1 + a3) * sc, dm = b1 - b3, dp = b1 + b3;
            f2 r[8];
            r[0] = e0 * sc + e1; r[1] = e0 * sc - e1;
            r[2] = dm * rs + b0; r[3] = -(dp * rs) - b2_;
            r[4] = (a0 - a2) * sc; r[5] = (a3 - a1) * sc;
            r[6] = b0 - dm * rs; r[7] = b2_ - dp * rs;
            UNROLL for (int p = 0; p < 8; ++p) o[p][j] = pkh(r[p].x, r[p].y);
        }
        UNROLL for (int p = 0; p < 8; ++p) *(u2*)&lds[L_A1 + p * 512 + 2 * t] = u2{o[p][0], o[p][1]};
        lset(lds, L_C1 + wv, pv, lane);
        pin(ev);
        epoch = rfl(ev) + pv; tag0 = tag_of(epoch, 0u); flip = (epoch & 1u) ? TM : 0u;
        const u32 vl = vl0 ^ flip, vu = vu0 ^ flip;
        u4 in[4];
        NOUNROLL for (int spin = 0; spin < LSPIN_CAP; ++spin) {
            MEMBAR();
            const u4 c = *(const u4*)&lds[L_C1];
            MEMBAR();
            MR[0] = *(const u4*)&lds[L_M + lane * 4]; MR[1] = *(const u4*)&lds[L_M + 256 + lane * 4];
            UNROLL for (int s = 0; s < 4; ++s) {
                const u2 re = __builtin_bit_cast(u2, __builtin_amdgcn_ds_read_tr16_b64_v4i16((LAS s4*)&lds[tro + s * 128]));
                const u2 im = __builtin_bit_cast(u2, __builtin_amdgcn_ds_read_tr16_b64_v4i16((LAS s4*)&lds[tro + s * 128 + 512]));
                in[s] = u4{re.x, re.y, im.x, im.y};
            }
            pin(MR[0]); pin(MR[1]);
            if (rfl(min4u(c)) >= pv) break;
        }
        UNROLL for (int i = 0; i < 4; ++i) TW[i] = *(const u4*)&lds[L_M + (2 + i) * 256 + lane * 4];
        core_expand(lane, MR, MI);
        f16v zr, zi;
        core_fft<true>(in, MR, MI, TW, zr, zi);
        UNROLL for (int r = 0; r < 16; ++r) z[r] = pkh(zr[r], zi[r]);
        z512 = z[8];
        const u32 za = __builtin_amdgcn_readlane(z[8], 32), zb = __builtin_amdgcn_readlane(z[0], 0);
        MEMBAR();
        const u32 zt = lane < 32 ? za : zb;
        UNROLL for (int r = 0; r < 8; ++r) z[r] = mark(z[r], vl, tm);
        UNROLL for (int r = 8; r < 16; ++r) z[r] = mark((lane & 31) == 0 ? (r < 15 ? z[r + 1] : zt) : z[r], vu, tm);
    }
    pin(z);
    const int zs = WS_Z + wv * 64 * 16384 + (((b >> 2) & 3) * 64 + (b & 3) * 16 + ((b >> 4) & 3) * 4 + (b >> 6)) * 64;
    bool zsent = false;
#define TRY_STORE_Z(force) do { if (!zsent) { \
        bool go = true; \
        if (force) lwait4(lds, L_LD, 1u); else { MEMBAR(); go = rfl(min4u(*(const u4*)&lds[L_LD])) != 0u; MEMBAR(); } \
        if (go) { \
            UNROLL for (int r = 0; r < 16; ++r) __builtin_amdgcn_raw_buffer_store_b32(z[r], rws, r < 8 ? zl : zu, zs + (r & 7) * 32768, SC1); \
            __builtin_amdgcn_raw_buffer_store_b64(u2{z512, tag0}, rws, zh, WS_HB + (wv * 256 + b) * HSTR, SC1); \
            zsent = true; } } SB(); } while (0)
    TRY_STORE_Z(false);
    const int row = (b & 7) >> 1, cls = cls_of(b), tile = tile_of(b);
    const bool packed = row == 0;
    u4 FR, FI, GR[4], GI[4], HR[4], HI[4], sel1;
    {
        const float ofs = cls == 4 ? 0.25f : 0.0f;
        float gu = (float)(cls + 8 * n) * (1.0f / 2048.0f), g0 = (float)(64 * q + 4 * wv) * gu + ofs;
        u32 s16 = 0x2c002c00u;
        pin(gu); pin(g0); pin(s16);
        UNROLL for (int i = 0; i < 4; ++i) UNROLL for (int j = 0; j < 4; ++j) { const u32 g = rot(fmaf((float)(16 * j + i), gu, g0)); GR[i][j] = hmul(g, s16); GI[i][j] = quarter(g, s16); }
    }
    pin(GR); pin(GI);
    {
        MagMax m;
        UNROLL for (int s1 = 0; s1 < 8; ++s1) m.add(xv[s1].x, xv[s1].y);
        if (wv == 0) lset(lds, L_SX, ex, lane);
        lset(lds, L_EW + wv, clampe(m.exponent()), lane);
    }
    TRY_STORE_Z(false);
    UNROLL for (int j = 0; j < 4; ++j) FR[j] = rot((float)(n * (4 * q + j)) * (1.0f / 16.0f));
    FI = quarter(FR);
    {
        float hs[4], hu[4];
        UNROLL for (int i = 0; i < 4; ++i) { hs[i] = (float)(16 * n + 4 * wv + i) * (1.0f / 2048.0f); hu[i] = (float)(cls + 32 * q + 8 * i); }
        pin(hs); pin(hu);
        UNROLL for (int i = 0; i < 4; ++i) { UNROLL for (int j = 0; j < 4; ++j) { float a = hs[i] * hu[j]; pin(a); HR[i][j] = rot(a); } HI[i] = quarter(HR[i]); }
        const int d = n + cls - 8 * q;
        UNROLL for (int j = 0; j < 4; ++j) sel1[j] = (d >> 1) == j ? ((d & 1) ? 0xffff0000u : 0xffffu) : 0u;
    }
    pin(FR); pin(FI); pin(HR); pin(HI); pin(sel1);
    TRY_STORE_Z(true);
    VMWAIT(4);
    u4 WA[4], WB[4];
    u32 ew = 0;
    UNROLL for (int i = 0; i < 4; ++i) asm volatile("" : "=v"(WA[i]), "=v"(WB[i]));
#define WIDEN(es) do { \
        u4 wr[4][4]; \
        int wl = L_W / 4 + wv * 1024 + n * 4 + min(q, 2); pin(wl); \
        UNROLL for (int i = 0; i < 4; ++i) UNROLL for (int a = 0; a < 4; ++a) wr[i][a] = ((const u4*)lds)[wl + (i * 4 + a) * 64]; \
        ew = pass ? (vd >> 8) & 0xffu : (es); \
        const u32 sw = ((249u - ew - (packed ? 1u : 0u)) << 7) * 0x10001u;       \
        const u4 sels = sel1 & sw; \
        SB(); \
        f4 wf[4][4]; \
        UNROLL for (int k = 0; k < 10; ++k) { \
            if (k < 8) { wf[k >> 1][2 * (k & 1)] = mfma16b(wr[k >> 1][2 * (k & 1)], sels, f4{}); wf[k >> 1][2 * (k & 1) + 1] = mfma16b(wr[k >> 1][2 * (k & 1) + 1], sels, f4{}); } \
            if (k >= 2) { const int i_ = (k - 2) >> 1, f_ = k & 1; UNROLL for (int r = 0; r < 4; ++r) (f_ ? WB : WA)[i_][r] = pkh(wf[i_][2 * f_][r], wf[i_][2 * f_ + 1][r]); } \
            SB(); } \
        pin(WA); pin(WB); } while (0)
    MEMBAR();
    const u4 pr0 = *(const u4*)&lds[L_PR];
    const bool wearly = rfl(min4u(pr0)) != 0u;
    MEMBAR();
    if (wearly) WIDEN(rfl(max4u(pr0)));
    const u32 vn = valid_bits(epoch, n), vm = valid_bits(epoch, 15 - n);
    u32 x1 = cls > 4 ? vm ^ 0x80000000u : vn, x2 = vm ^ (cls == 4 ? 0x00008000u : cls == 0 ? 0x80000000u : 0u);
    u32 z1[4][4], z2[4][4];
    const int zvm = q * 64 + (63 - tile) * 16384 + (15 - n) * 4;
    const int zv1 = cls <= 4 ? q * 64 + tile * 16384 + n * 4 : zvm;
    int zso = WS_Z + row * 64 * 16384 + wv * 4096;
    asm volatile("" : "+s"(zso));
#define LOAD_Z() do { \
        if (packed) { int a_ = zvm; pin(a_); UNROLL for (int i = 0; i < 4; ++i) UNROLL for (int j = 0; j < 4; ++j) z2[i][j] = __builtin_amdgcn_raw_buffer_load_b32(rws, a_ + (4 * i + j) * 256, zso, SC1); } \
        else { UNROLL for (int i = 0; i < 4; ++i) UNROLL for (int j = 0; j < 4; ++j) z2[i][j] = x2; } \
        int c_ = zv1; pin(c_); UNROLL for (int i = 0; i < 4; ++i) UNROLL for (int j = 0; j < 4; ++j) z1[i][j] = __builtin_amdgcn_raw_buffer_load_b32(rws, c_ + (4 * i + j) * 256, zso, SC1); } while (0)
    pin(x1); pin(x2);
    VMWAIT(0);
    LOAD_Z();
    if (wv == 0) lset(lds, L_ZL, pv, lane);
    SB();
    const int lw = ((wv * 16 + 4 * q) * 16 + n) * 4;
    int lr = (q * 16 + 4 * wv) * 16 + n;
    const u4* l4 = (const u4*)lds;
    pin(lr);
    SB();
    {
        f4 yr[4], yi[4];
        NOUNROLL for (int spin = 0; spin < SPIN_CAP; ++spin) {
            u32 m = 0;
            UNROLL for (int i = 0; i < 4; ++i) {
                u4 d;
                UNROLL for (int j = 0; j < 4; ++j) { z1[i][j] ^= x1; z2[i][j] ^= x2; }
                UNROLL for (int j = 0; j < 4; ++j) d[j] = U(H(z1[i][j]) + H(z2[i][j]));
                UNROLL for (int j = 0; j < 4; ++j) m |= z1[i][j] | z2[i][j];
                yr[i] = mfma16(GR[i], d); yi[i] = mfma16(GI[i], d);
                SB();
            }
            if (__all((m & TM) == 0u)) break;
            MEMBAR();
            LOAD_Z();
        }
        u4 y[4];
        UNROLL for (int i = 0; i < 4; ++i) UNROLL for (int r = 0; r < 4; ++r) y[r][i] = pkh(yr[i][r], yi[i][r]);
        UNROLL for (int r = 0; r < 4; ++r) *(u4*)&lds[L_A2 + lw + r * 64] = y[r];
        lset(lds, L_C3 + wv, pv, lane);
    }
    if (!wearly) { const u32 es = lwait4(lds, L_PR, 1u); WIDEN(es); }
    {
        u4 d[4];
        NOUNROLL for (int spin = 0; spin < LSPIN_CAP; ++spin) {
            MEMBAR();
            const u4 c = *(const u4*)&lds[L_C3];
            MEMBAR();
            UNROLL for (int i = 0; i < 4; ++i) d[i] = l4[L_A2 / 4 + lr + i * 16];
            pin(d[0]);
            if (rfl(min4u(c)) >= pv) break;
        }
        pin(WA); pin(WB);
        f4 xr[4], xi[4];
        u4 xx[4], ya[4], yb[4];
        UNROLL for (int i = 0; i < 4; ++i) {
            xr[i] = mfma16(FR, d[i]); xi[i] = mfma16(FI, d[i]);
            if (i >= 2) { UNROLL for (int r = 0; r < 4; ++r) xx[i - 2][r] = pkh(xr[i - 2][r], xi[i - 2][r]); pin(xx[i - 2]); }
            SB();
        }
        UNROLL for (int r = 0; r < 4; ++r) ya[0][r] = cmul_swap(xx[0][r], WA[0][r]);
        UNROLL for (int i = 2; i < 4; ++i) { UNROLL for (int r = 0; r < 4; ++r) xx[i][r] = pkh(xr[i][r], xi[i][r]); pin(xx[i]); }
        SB();
        f4 p0[4], p1[4], p2[4], p3[4];
        UNROLL for (int i = 0; i < 4; ++i) {
            p0[i] = mfma16(FR, ya[i]); p1[i] = mfma16(FI, ya[i]);
            if (i < 3) { UNROLL for (int r = 0; r < 4; ++r) ya[i + 1][r] = cmul_swap(xx[i + 1][r], WA[i + 1][r]); }
            else { UNROLL for (int r = 0; r < 4; ++r) yb[0][r] = cmul_swap(xx[0][r], WB[0][r]); }
            SB();
        }
        u4 ra[4], rb[4];
        UNROLL for (int i = 0; i < 4; ++i) {
            p2[i] = mfma16(FR, yb[i]); p3[i] = mfma16(FI, yb[i]);
            if (i < 3) { UNROLL for (int r = 0; r < 4; ++r) yb[i + 1][r] = cmul_swap(xx[i + 1][r], WB[i + 1][r]); }
            UNROLL for (int k = 0; k < 4; ++k) ra[i][k] = pkh(p0[k][i], p1[k][i]);
            *(u4*)&lds[L_B + lw + i * 64] = ra[i];
            SB();
        }
        UNROLL for (int i = 0; i < 3; ++i) UNROLL for (int r = 0; r < 4; ++r) rb[r][i] = pkh(p2[i][r], p3[i][r]);
        SB();
        UNROLL for (int r = 0; r < 4; ++r) { rb[r][3] = pkh(p2[3][r], p3[3][r]); *(u4*)&lds[L_B + 4096 + lw + r * 64] = rb[r]; }
        lset(lds, L_C4 + wv, pv, lane);
    }
    {
        u4 da[4], db[4];
        NOUNROLL for (int spin = 0; spin < LSPIN_CAP; ++spin) {
            MEMBAR();
            const u4 c = *(const u4*)&lds[L_C4];
            MEMBAR();
            UNROLL for (int i = 0; i < 4; ++i) { da[i] = l4[L_B / 4 + lr + i * 16]; db[i] = l4[L_B / 4 + 1024 + lr + i * 16]; }
            pin(da[0]);
            if (rfl(min4u(c)) >= pv) break;
        }
        const int vo = 64 * q * 32768 + n * 8, so = WS_V + 4 * wv * 32768 + cls * 4096 + tile * 128;
        f4 p0[4], p1[4], p2[4], p3[4];
        UNROLL for (int i = 0; i < 5; ++i) {
            if (i < 4) { p0[i] = mfma16(HR[i], da[i]); p1[i] = mfma16(HI[i], da[i]); p2[i] = mfma16(HR[i], db[i]); p3[i] = mfma16(HI[i], db[i]); }
            if (i > 0) { UNROLL for (int r = 0; r < 4; ++r)
                __builtin_amdgcn_raw_buffer_store_b64(u2{mark(pkh(p0[i - 1][r], p1[i - 1][r]), vn, tm), mark(pkh(p2[i - 1][r], p3[i - 1][r]), vn, tm)}, rws, vo, so + (16 * r + i - 1) * 32768, SC1); }
            SB();
        }
    }
    if (wv == 0) lset(lds, L_P2, 1u, lane);
    SB();
    {
        pin(xv); pin(gv);
        const f2 g01 = {bflo(gv.x), bfhi(gv.x)}, g23 = {bflo(gv.y), bfhi(gv.y)};
        f4 acc = {};
        UNROLL for (int i = 0; i < 4; ++i) {
            u4 pp;
            UNROLL for (int k = 0; k < 2; ++k) {
                const f2 a = f2{bflo(xv[2 * i + k].x), bfhi(xv[2 * i + k].x)} * g01, c = f2{bflo(xv[2 * i + k].y), bfhi(xv[2 * i + k].y)} * g23;
                pp[2 * k] = pkb(a.x, a.y); pp[2 * k + 1] = pkb(c.x, c.y);
            }
            const u32 s0 = n == 2 * i ? 0x3f803f80u : 0u, s1 = n == 2 * i + 1 ? 0x3f803f80u : 0u;
            acc = mfma16b(pp, u4{s0, s0, s1, s1}, acc);
        }
        if (n < 8) lds[L_GS + n * 16 + (t >> 4)] = __builtin_bit_cast(u32, (acc.x + acc.y) + (acc.z + acc.w));
        lset(lds, L_C5 + wv, pv, lane);
    }
    MR[0] = *(const u4*)&lds[L_M + lane * 4]; MR[1] = *(const u4*)&lds[L_M + 256 + lane * 4];
    UNROLL for (int i = 0; i < 4; ++i) TW[i] = *(const u4*)&lds[L_M + (2 + i) * 256 + lane * 4];
    core_expand(lane, MR, MI);
    pin(MR); pin(MI); pin(TW);
    u4 PM;
    {
        lwait4(lds, L_C5, pv);
        float gg[4];
        UNROLL for (int k = 0; k < 2; ++k) {
            const f4* pp = (const f4*)&lds[L_GS + ((n >> 2) * 2 + k) * 16];
            const f4 ps = (pp[0] + pp[1]) + (pp[2] + pp[3]);
            const float lg = rbf((ps.x + ps.y) + (ps.z + ps.w));
            gg[2 * k] = rbf(1.0f / (1.0f + __expf(-lg)));
            gg[2 * k + 1] = rbf(1.0f - gg[2 * k]);
        }
        UNROLL for (int k = 0; k < 4; ++k) { gg[k] *= 0.03125f; pin(gg[k]); }
        const int r = n & 3, sa = (n >> 2) * 2;
        UNROLL for (int cc = 0; cc < 2; ++cc) {
            const int u1 = 2 * q + cc;
            const float ta = (float)(sa * u1) * 0.125f + ((r & 1) ? 0.25f : (r & 2) ? 0.5f : 0.0f);
            const float tb = (float)((sa + 1) * u1) * 0.125f + ((r & 1) ? ((r & 2) ? 0.0f : 0.5f) : 0.25f);
            const float ca = cosr(ta), sa_ = sinr(ta), cb = cosr(tb), sb_ = sinr(tb);
            float e[4] = {gg[2] * cb, gg[2] * sb_, gg[3] * cb, gg[3] * sb_};
            pin(e);
            e[0] = fmaf(gg[0], ca, e[0]); pin(e[0]); e[1] = fmaf(gg[0], sa_, e[1]); pin(e[1]); e[2] = fmaf(gg[1], ca, e[2]); pin(e[2]); e[3] = fmaf(gg[1], sa_, e[3]);
            PM[2 * cc] = pkh(e[0], e[1]); PM[2 * cc + 1] = pkh(e[2], e[3]);
        }
    }
    int a3d = (q * 8 + wv) * 32 + n, a3m = (q * 8 + 7 - wv) * 32 + 16 - n, a3z = (q * 8 + 7 - wv) * 32 + ((32 - n) & 31), a3r = wv * 256 + lane;
    float osc = __builtin_bit_cast(float, (u32)min(max((int)ex + (int)ew - 130, 1), 254) << 23) * TRUNC_FIX;
    const int ve = 2 * q * 4096 + (128 * wv + n) * 8;
    int vs0 = WS_V + b * 32768, vs1 = vs0 + 4096, vsl = WS_VL + b * 16;
    asm volatile("" : "+s"(vs0), "+s"(vs1), "+s"(vsl));
    int ov = (4 * perm32(lane & 15) + 2 * ((lane >> 4) & 1) + (lane >> 5)) * 16 + (512 * wv + b) * 2048;
    u32 x8 = wv == 3 ? 0u : vn;
    pin(a3d); pin(a3m); pin(a3z); pin(a3r); pin(PM); pin(ov); pin(osc); pin(x8);
    VMWAIT(0);
    {
        f4 z3[9];
        NOUNROLL for (int spin = 0; spin < SPIN_CAP; ++spin) {
            MEMBAR();
            u2 v0[8], v1[8];
            u4 ve8, vo8;
            int a_ = ve; pin(a_);
            if (wv == 3) {
                ve8 = __builtin_amdgcn_raw_buffer_load_b128(rws, q * 8192, vsl, SC1); vo8 = __builtin_amdgcn_raw_buffer_load_b128(rws, q * 8192 + 4096, vsl, SC1);
            } else {
                const u2 e2 = __builtin_amdgcn_raw_buffer_load_b64(rws, a_ + 1024, vs0, SC1), o2 = __builtin_amdgcn_raw_buffer_load_b64(rws, a_ + 1024, vs1, SC1);
                ve8 = u4{e2.x, e2.y, tag0, tag0}; vo8 = u4{o2.x, o2.y, tag0, tag0};
            }
            UNROLL for (int i = 0; i < 8; ++i) {
                v0[i] = __builtin_amdgcn_raw_buffer_load_b64(rws, a_ + 128 * i, vs0, SC1);
                v1[i] = __builtin_amdgcn_raw_buffer_load_b64(rws, a_ + 128 * i, vs1, SC1);
            }
            SB();
            u32 m = (((wv == 3 ? ve8.y : ve8.z) ^ tag0) | (ve8.w ^ tag0) | ((wv == 3 ? vo8.y : vo8.z) ^ tag0) | (vo8.w ^ tag0)) ? TM : 0u;
            UNROLL for (int k = 0; k < 9; ++k) {
                const int i = (k + 8) % 9;
                const u4 d = i == 8 ? u4{ve8.x, wv == 3 ? ve8.z : ve8.y, vo8.x, wv == 3 ? vo8.z : vo8.y} ^ x8 : u4{v0[i & 7].x, v0[i & 7].y, v1[i & 7].x, v1[i & 7].y} ^ vn;
                m |= d.x | d.y; m |= d.z | d.w;
                z3[i] = mfma16(PM, d);
                SB();
            }
            if (__all((m & TM) == 0u)) break;
        }
        if (wv == 0 && n == 0) { z3[0].x = 0.5f * (z3[0].x + z3[0].z); z3[0].y = 0.5f * (z3[0].y + z3[0].w); }
        if (wv == 3) { z3[8].z = 0.5f * (z3[8].x + z3[8].z); z3[8].w = 0.5f * (z3[8].y + z3[8].w); }
        u4* l4w = (u4*)&lds[L_A1];
        UNROLL for (int p = 0; p < 2; ++p)
            l4w[a3d + 16 * p] = u4{pkh(z3[p].x, z3[p + 2].x), pkh(z3[p + 4].x, z3[p + 6].x), pkh(z3[p].y, z3[p + 2].y), pkh(z3[p + 4].y, z3[p + 6].y)};
        l4w[a3m] = u4{pkh(z3[7].z, z3[5].z), pkh(z3[3].z, z3[1].z), pkh(z3[7].w, z3[5].w), pkh(z3[3].w, z3[1].w)};
        f2 m[4];
        UNROLL for (int i = 0; i < 4; ++i) m[i] = n == 0 ? f2{z3[8 - 2 * i].z, z3[8 - 2 * i].w} : f2{z3[6 - 2 * i].z, z3[6 - 2 * i].w};
        l4w[a3z] = u4{pkh(m[0].x, m[1].x), pkh(m[2].x, m[3].x), pkh(m[0].y, m[1].y), pkh(m[2].y, m[3].y)};
        lset(lds, L_C6 + wv, 2u * pv - 1u, lane);
    }
    {
        u4 in[4];
        NOUNROLL for (int spin = 0; spin < LSPIN_CAP; ++spin) {
            MEMBAR();
            const u4 c = *(const u4*)&lds[L_C6];
            MEMBAR();
            UNROLL for (int s = 0; s < 4; ++s) in[s] = ((const u4*)&lds[L_A1])[a3r + s * 64];
            pin(in[0]);
            if (rfl(min4u(c)) >= 2u * pv - 1u) break;
        }
        f16v zr, zi;
        core_fft<false>(in, MR, MI, TW, zr, zi);
        zr *= osc; zi *= osc;
        const rsrc_t ro = mkrs(out, 2048 * 2048);
#define OUT_ROW(z, off) do { u4 lo, hi; \
            UNROLL for (int j = 0; j < 4; ++j) { const auto sw_ = __builtin_amdgcn_permlane16_swap(pkb(z[2 * j], z[2 * j + 1]), pkb(z[8 + 2 * j], z[8 + 2 * j + 1]), false, false); lo[j] = sw_[0]; hi[j] = sw_[1]; } \
            store128<SC1>(lo, ro, ov + (off)); store128<SC1>(hi, ro, ov + (off) + 1024); } while (0)
        OUT_ROW(zr, 256 * 2048);
        OUT_ROW(zi, 0);
    }
    vd = lwait(lds, L_VD, 1u);
    if (pass == 1 || !(vd & 0x20000u)) break;
    lset(lds, L_C6 + wv, 2u, lane);
    lwait4(lds, L_C6, 2u);
    VMWAIT(0);
    }
}

extern "C" void kernel_launch(void* const* d_in, const int* in_sizes, int n_in,
                              void* d_out, int out_size, void* d_ws, size_t ws_size,
                              hipStream_t stream) {
    gfnet_fused<<<256, 384, 0, stream>>>(d_in[0], d_in[6], d_in[1], d_in[2], d_in[4], d_in[5], (char*)d_ws, d_out);
}
```

```cpp
#include <hip/hip_runtime.h>
#include <hip/hip_bf16.h>
#include <stdint.h>

typedef _Float16 h2 __attribute__((ext_vector_type(2)));
typedef _Float16 h8 __attribute__((ext_vector_type(8)));
typedef __bf16 b2 __attribute__((ext_vector_type(2)));
typedef __bf16 b8 __attribute__((ext_vector_type(8)));
typedef float f2 __attribute__((ext_vector_type(2)));
typedef float f4 __attribute__((ext_vector_type(4)));
typedef float f16v __attribute__((ext_vector_type(16)));
typedef short s4 __attribute__((ext_vector_type(4)));
typedef unsigned u32;
typedef u32 u2 __attribute__((ext_vector_type(2)));
typedef u32 u4 __attribute__((ext_vector_type(4)));
typedef unsigned short us2 __attribute__((ext_vector_type(2)));
typedef __amdgpu_buffer_rsrc_t rsrc_t;
#define DI __device__ __forceinline__
#define UNROLL _Pragma("unroll")
#define NOUNROLL _Pragma("unroll 1")
#define LAS __attribute__((address_space(3)))
#define SB() __builtin_amdgcn_sched_barrier(0)
#define MEMBAR() asm volatile("" ::: "memory")
#define VMWAIT(n) asm volatile("s_waitcnt vmcnt(%0)" :: "n"(n) : "memory")

#define MARGIN 2

constexpr u32 FSTR = 16, HSTR = 32;
constexpr u32 WS_Z = 0;
constexpr u32 WS_V = 4u << 20;
constexpr u32 WS_VL = 12u << 20;
constexpr u32 WS_HB = WS_VL + (32u << 10);
constexpr u32 WS_F = WS_HB + (32u << 10);
constexpr u32 WS_E = WS_F + 2048 * FSTR;
constexpr u32 WS_BYTES = WS_E + 4096;
constexpr int SC1 = 16, NT = 2;
constexpr int SPIN_CAP = 8192, LSPIN_CAP = 1 << 17;
constexpr int OOB = 0x7ffffff0;
constexpr u32 TM = 0x00010001u;
constexpr float TRUNC_FIX = 1.00066f;

template <class T> DI void pin(T& v) { asm volatile("" : "+v"(v)); }
template <class T, int N> DI void pin(T (&v)[N]) { UNROLL for (int i = 0; i < N; ++i) pin(v[i]); }
DI u32 opq(u32 v) { asm("" : "+v"(v)); return v; }
DI u32 pkh(float lo, float hi) { h2 r; r.x = (_Float16)lo; r.y = (_Float16)hi; return __builtin_bit_cast(u32, r); }
DI u32 pkb(float lo, float hi) { b2 r; r.x = (__bf16)lo; r.y = (__bf16)hi; return __builtin_bit_cast(u32, r); }
DI float bflo(u32 v) { return __builtin_bit_cast(float, v << 16); }
DI float bfhi(u32 v) { return __builtin_bit_cast(float, v & 0xffff0000u); }
DI float rbf(float v) { return (float)(__bf16)v; }
DI h2 H(u32 v) { return __builtin_bit_cast(h2, v); }
DI u32 U(h2 v) { return __builtin_bit_cast(u32, v); }
DI f16v mfma32(u4 a, u4 b, f16v c) { return __builtin_amdgcn_mfma_f32_32x32x16_f16(__builtin_bit_cast(h8, a), __builtin_bit_cast(h8, b), c, 0, 0, 0); }
DI f4 mfma16(u4 a, u4 b) { return __builtin_amdgcn_mfma_f32_16x16x32_f16(__builtin_bit_cast(h8, a), __builtin_bit_cast(h8, b), f4{}, 0, 0, 0); }
DI f4 mfma16b(u4 a, u4 b, f4 c) { return __builtin_amdgcn_mfma_f32_16x16x32_bf16(__builtin_bit_cast(b8, a), __builtin_bit_cast(b8, b), c, 0, 0, 0); }
DI u32 hmul(u32 a, u32 b) { return U(H(a) * H(b)); }
DI u32 cmul_swap(u32 x, u32 w) { const h2 a = H(opq(x)), c = H(opq(w)); h2 n = c; n.y = -n.y; return U(__builtin_elementwise_fma(a.yy, n, a.xx * c.yx)); }
DI u32 cmul_conj(u32 x, u32 t) { const h2 a = H(opq(x)), c = H(opq(t)); h2 n = c.yy; n.y = -n.y; return U(__builtin_elementwise_fma(a.yx, n, a * c.xx)); }
DI u32 pkmax(u32 a, u32 b) { return __builtin_bit_cast(u32, __builtin_elementwise_max(__builtin_bit_cast(us2, a), __builtin_bit_cast(us2, b))); }
DI float sinr(float r) { return __builtin_amdgcn_sinf(r); }
DI float cosr(float r) { return __builtin_amdgcn_cosf(r); }
DI u32 rot(float ph) { return pkh(cosr(ph), sinr(ph)); }
DI u32 quarter(u32 r, u32 f) { u32 o; asm("v_pk_mul_f16 %0, %1, %2 op_sel:[1,0] op_sel_hi:[0,1] neg_lo:[1,0]" : "=v"(o) : "v"(r), "v"(f)); return o; }
DI u4 quarter(u4 r) { u32 one = 0x3c003c00u; pin(one); return u4{quarter(r.x, one), quarter(r.y, one), quarter(r.z, one), quarter(r.w, one)}; }
DI int perm32(int n) { return (n & 0x13) | ((n & 4) << 1) | ((n & 8) >> 1); }
DI u32 max4u(u4 v) { return max(max(v.x, v.y), max(v.z, v.w)); }
DI u32 min4u(u4 v) { return min(min(v.x, v.y), min(v.z, v.w)); }
DI u32 mark(u32 v, u32 par, u32 tm) { return (par & tm) | (v & ~tm); }
DI u32 rfl(u32 v) { return __builtin_amdgcn_readfirstlane(v); }
DI rsrc_t mkrs(const void* p, u32 bytes) { return __builtin_amdgcn_make_buffer_rsrc((void*)p, (short)0, (int)bytes, 0x00020000); }
template <int AUX> DI void store128(u4 v, rsrc_t r, int off) { __builtin_amdgcn_raw_buffer_store_b128(v, r, off, 0, AUX); }
template <int CTRL> DI u32 dpp(u32 v) { return __builtin_amdgcn_update_dpp(0u, v, CTRL, 0xf, 0xf, true); }
DI u32 wave_maxu(u32 v) {
    v = max(v, dpp<0xB1>(v)); v = max(v, dpp<0x4E>(v)); v = max(v, dpp<0x141>(v)); v = max(v, dpp<0x140>(v));
    return max(max((u32)__builtin_amdgcn_readlane(v, 0), (u32)__builtin_amdgcn_readlane(v, 16)), max((u32)__builtin_amdgcn_readlane(v, 32), (u32)__builtin_amdgcn_readlane(v, 48)));
}

DI void core_setup(int lane, int s, u4& tw, u2& m) {
    float fc = (float)perm32(lane & 31) * (1.0f / 1024.0f), p0 = (float)(4 * (lane >> 5) + 8 * s) * fc, p1 = (float)(4 * (lane >> 5) + 8 * (s >> 1) + 2 * (s & 1)) * fc * 32.0f;
    pin(fc); pin(p0); pin(p1);
    float tc[4], ts[4];
    UNROLL for (int i = 0; i < 4; ++i) { const float pt = fmaf((float)i, fc, p0); tc[i] = cosr(pt); ts[i] = sinr(pt); }
    tw = u4{pkh(tc[0], tc[1]), pkh(tc[2], tc[3]), pkh(ts[0], ts[1]), pkh(ts[2], ts[3])};
    const float p2 = fmaf(32.0f, fc, p1);
    m = u2{pkh(cosr(p1), cosr(p2)), pkh(sinr(p1), sinr(p2))};
}
DI void core_expand(int lane, u4 (&MR)[4], u4 (&MI)[4]) {
    const u32 sg = (lane & 1) ? 0x80008000u : 0u;
    MR[2] = MR[0] ^ sg; MR[3] = MR[1] ^ sg;
    UNROLL for (int s = 0; s < 4; ++s) MI[s] = u4{MR[s].z ^ 0x80008000u, MR[s].w ^ 0x80008000u, MR[s].x, MR[s].y};
}
template <bool OUT_B> DI void core_fft(const u4 (&in)[4], const u4 (&MR)[4], const u4 (&MI)[4], const u4 (&TW)[4], f16v& outr, f16v& outi) {
    f16v yr = {}, yi = {};
    UNROLL for (int s = 0; s < 4; ++s) yr = mfma32(in[s], MR[s], yr);
    SB();
    yi = mfma32(in[0], MI[0], yi); yi = mfma32(in[1], MI[1], yi);
    SB();
    h2 t0[8], t1[8];
#define TW_RE(p) do { const h2 pr_ = H(pkh(yr[2 * (p)], yr[2 * (p) + 1])); t0[p] = pr_ * H(TW[(p) >> 1][(p) & 1]); t1[p] = pr_ * H(TW[(p) >> 1][2 + ((p) & 1)]); } while (0)
    TW_RE(0); TW_RE(1); SB();
    yi = mfma32(in[2], MI[2], yi);
    TW_RE(2); TW_RE(3); SB();
    yi = mfma32(in[3], MI[3], yi);
    TW_RE(4); TW_RE(5); TW_RE(6); TW_RE(7); SB();
    outr = f16v{}; outi = f16v{};
    u4 d[4];
#define TW_IM(s) do { UNROLL for (int j = 0; j < 2; ++j) { const int p_ = 2 * (s) + j; const h2 pi_ = H(pkh(yi[2 * p_], yi[2 * p_ + 1])); \
        d[s][j] = U(__builtin_elementwise_fma(pi_, H(TW[s][2 + j]), t0[p_])); d[s][2 + j] = U(__builtin_elementwise_fma(pi_, H(TW[s][j]), -t1[p_])); } } while (0)
    TW_IM(0); SB();
    UNROLL for (int s = 0; s < 4; ++s) {
        outr = OUT_B ? mfma32(MR[s], d[s], outr) : mfma32(d[s], MR[s], outr);
        if (s < 3) TW_IM(s + 1);
        SB();
    }
    UNROLL for (int s = 0; s < 4; ++s) outi = OUT_B ? mfma32(MI[s], d[s], outi) : mfma32(d[s], MI[s], outi);
}

DI u32 tag_of(u32 epoch, u32 idx) { u32 h = (epoch ^ (idx * 0x9E3779B1u)) * 0x85EBCA6Bu; return h ^ (h >> 15); }
DI u32 valid_bits(u32 epoch, int slot) { return (((0x6A3Cu >> slot) ^ epoch) & 1u) ? 0x00000001u : 0x00010000u; }
DI int split_mask(int b) { return (((b & 6) - 1) >> 31); }
DI int cls_of(int b) { return (int)((0x53627100u >> (4 * (b & 7))) & 7u) | (((b >> 7) << 2) & split_mask(b)); }
DI int tile_of(int b) { return ((b >> 3) & 15) | ((((b & split_mask(b)) | ((b >> 7) & ~split_mask(b))) & 1) << 4); }

constexpr int L_C1 = 0, L_C3 = 4, L_C4 = 8, L_C5 = 12, L_C6 = 16;
constexpr int L_EW = 20;
constexpr int L_PR = 28;
constexpr int L_LD = 32;
constexpr int L_SX = 36;
constexpr int L_ZL = 37;
constexpr int L_VD = 38;
constexpr int L_P2 = 39;
constexpr int L_GS = 64;
constexpr int L_M = 192;
constexpr int L_A1 = L_M + 1536;
constexpr int L_A2 = L_A1 + 4096;
constexpr int L_B = L_A2 + 4096;
constexpr int L_W = L_B + 8192;
constexpr int L_END = L_W + 64 * 256;

DI void lset(u32* lds, int idx, u32 v, int lane) { MEMBAR(); if (lane == 0) lds[idx] = v; MEMBAR(); }
DI u32 lget(u32* lds, int idx) { MEMBAR(); return rfl(lds[idx]); }
DI u32 lwait(u32* lds, int idx, u32 atleast) {
    u32 v = 0;
    NOUNROLL for (int spin = 0; spin < LSPIN_CAP; ++spin) { v = lget(lds, idx); if (v >= atleast) break; __builtin_amdgcn_s_sleep(1); }
    MEMBAR();
    return v;
}
DI u32 lwait4(u32* lds, int idx, u32 atleast) {
    u4 v;
    NOUNROLL for (int spin = 0; spin < LSPIN_CAP; ++spin) { MEMBAR(); v = *(const u4*)&lds[idx]; if (rfl(min4u(v)) >= atleast) break; __builtin_amdgcn_s_sleep(1); }
    MEMBAR();
    return rfl(max4u(v));
}
struct MagMax {
    h2 hi = {}, lo = {};
    DI void add(u32 a, u32 c) {
        hi = __builtin_elementwise_maximum(__builtin_elementwise_maximum(hi, H(a)), H(c));
        lo = __builtin_elementwise_minimum(__builtin_elementwise_minimum(lo, H(a)), H(c));
    }
    DI u32 exponent() const {
        const u32 mx = pkmax(U(hi) & 0x7fff7fffu, U(lo) & 0x7fff7fffu);
        return wave_maxu(max(mx & 0xffffu, mx >> 16)) >> 7;
    }
};
DI u32 clampe(u32 e) { return min(max(e, 8u), 240u); }

__global__ __launch_bounds__(384, 2) void gfnet_fused(const void* x, const void* gw, const void* war, const void* wai, const void* wbr, const void* wbi,
                                                      char* ws, void* out) {
    __shared__ __attribute__((aligned(16))) u32 lds[L_END];
    u32 code;
    {
        unsigned long long pc0; int len;
        asm volatile("s_getpc_b64 %0\n.Lgf_begin:\n\ts_mov_b32 %1, .Lgf_end-.Lgf_begin" : "=s"(pc0), "=s"(len));
        code = __builtin_amdgcn_raw_buffer_load_b32(mkrs((const void*)pc0, (u32)max(len, 0)), (threadIdx.x >> 6) == 4 ? ((blockIdx.x >> 5) * 64 + (threadIdx.x & 63)) * 64 : OOB, 0, 0);
        SB();
    }
    asm volatile("" :: "s"(x), "s"(gw), "s"(war), "s"(wai), "s"(wbr), "s"(wbi), "s"(ws), "s"(out));
    int wv0 = __builtin_amdgcn_readfirstlane(threadIdx.x >> 6);
    const rsrc_t rws = mkrs(ws, WS_BYTES);
    u2 xv[8], gv; u4 smp, smp0, smp1; u32 ev, eva, edge[2];
#define LOAD_X(t, b) do { const rsrc_t rx = mkrs(x, 2048 * 2048); \
        smp = __builtin_amdgcn_raw_buffer_load_b128(rx, (((t) & 63) >> 3) * (524288 + 2048 + 256) + ((t) & 7) * 16, 0, 0); \
        UNROLL for (int s1 = 0; s1 < 8; ++s1) xv[s1] = __builtin_amdgcn_raw_buffer_load_b64(rx, (t) * 8, (256 * s1 + (b)) * 2048, NT); \
        ev = __builtin_amdgcn_raw_buffer_load_b32(rws, 0, WS_E, SC1); \
        gv = __builtin_amdgcn_raw_buffer_load_b64(mkrs(gw, 2048), (t) * 8, 0, 0); MEMBAR(); } while (0)
#define W_ROLE() const int wv = wv0, b = blockIdx.x, cls = cls_of(b), tile = tile_of(b), lane = threadIdx.x & 63; \
        const rsrc_t rw[2] = {mkrs(wv == 4 ? war : wbr, 2048 * 1026), mkrs(wv == 4 ? wai : wbi, 2048 * 1026)}; \
        const int wvo = (lane >> 2) * (128 * 1026) + min(lane & 3, 2) * 16, wso = (cls * 513 + 16 * tile - cls) * 2
#define WLOAD(r) do { UNROLL for (int st = 0; st < 4; ++st) { const int c_ = 4 * st + ((tile + (r)) & 3); \
        UNROLL for (int a = 0; a < 2; ++a) __builtin_amdgcn_raw_ptr_buffer_load_lds(rw[a], (LAS void*)&lds[L_W + (c_ * 4 + 2 * (wv - 4) + a) * 256], 16, wvo, wso + c_ * 8208, 0, 0); } SB(); } while (0)
    if (wv0 < 4) {
        LOAD_X((int)threadIdx.x, (int)blockIdx.x);
        __builtin_amdgcn_s_barrier();
        MEMBAR();
        __builtin_amdgcn_s_setprio(1);
    } else {
        W_ROLE();
        if (wv == 4) { lds[lane] = 0u; asm volatile("s_waitcnt lgkmcnt(0)" ::: "memory"); }
        const int sm = split_mask(b);
        const int er = ((16 * (tile & 15)) & sm) + ((8 * tile) & ~sm) + ((lane >> 1) & (7 | (8 & sm)));
        int eo = ((cls + 8 * er) * 513 + (((tile & 16) * 16) & sm) - cls + (lane & 1) * (518 - (256 & sm))) * 2;
        pin(eo);
        MEMBAR();
        __builtin_amdgcn_s_barrier();
        MEMBAR();
        WLOAD(0);
        UNROLL for (int a = 0; a < 2; ++a) edge[a] = __builtin_amdgcn_raw_buffer_load_b32(rw[a], eo, 0, 0);
        eva = __builtin_amdgcn_raw_buffer_load_b32(rws, 0, WS_E, SC1);
        smp0 = __builtin_amdgcn_raw_buffer_load_b128(rw[0], (lane >> 3) * (2 * 131328 + 128) + (lane & 7) * 16, 0, 0);
        smp1 = __builtin_amdgcn_raw_buffer_load_b128(rw[1], (lane >> 3) * (2 * 131328 + 128) + (lane & 7) * 16, 0, 0);
        MEMBAR();
    }
    asm volatile("" : "+s"(wv0));

    if (wv0 >= 4) {
        W_ROLE();
        const int q = lane >> 4, n = lane & 15;
        const bool helper = wv == 5 && tile == 31;
        u4 GR, GI, FR, FI, HR, HI, T1, T2;
        {
            if (helper) {
                UNROLL for (int j = 0; j < 4; ++j) {
                    GR[j] = rot((float)((4 * q + j) * (cls + 8 * n)) * (1.0f / 128.0f) + (cls == 4 ? 0.25f : 0.0f));
                    HR[j] = rot((float)(n * (cls + 8 * (4 * q + j))) * (1.0f / 128.0f));
                    FR[j] = rot((float)(n * (4 * q + j)) * (1.0f / 16.0f));
                    T1[j] = rot((float)((4 * q + j) * (cls + 8 * n)) * (1.0f / 2048.0f));
                    T2[j] = rot((float)(n * (cls + 8 * (4 * q + j))) * (1.0f / 2048.0f));
                }
                GI = quarter(GR); HI = quarter(HR); FI = quarter(FR);
                pin(GR); pin(GI); pin(HR); pin(HI); pin(FR); pin(FI); pin(T1); pin(T2);
            }
            VMWAIT(0);
            WLOAD(1); WLOAD(2); WLOAD(3);
            asm volatile("ds_write_b64 %0, %1" :: "v"((u32)(size_t)(LAS u32*)&lds[L_LD + 2 * (wv - 4)]), "v"(u2{1u, 1u}) : "memory");
            pin(edge); pin(code);
            MagMax ms; ms.add(smp0.x, smp0.y); ms.add(smp0.z, smp0.w); ms.add(smp1.x, smp1.y); ms.add(smp1.z, smp1.w);
            const u32 es = clampe(ms.exponent() + MARGIN);
            VMWAIT(0);
            MEMBAR(); if (lane == 0) *(u2*)&lds[L_PR + 2 * (wv - 4)] = u2{es, es}; MEMBAR();
        }
        const u32 ep0 = rfl(eva) + 1u;
        if (wv == 4) {
            lwait4(lds, L_PR, 1u);
            MagMax m;
            NOUNROLL for (int c0 = 0; c0 < 64; c0 += 16) { UNROLL for (int c = 0; c < 16; ++c) { const u4 v = *(const u4*)&lds[L_W + (c0 + c) * 256 + lane * 4]; m.add(v.x, v.y); m.add(v.z, v.w); } }
            const u32 ew = clampe(m.exponent()), ex = lwait4(lds, L_EW, 1u);
            __builtin_amdgcn_raw_buffer_store_b64(u2{ex | (ew << 8), tag_of(ep0, b + 1)}, rws, lane < 8 ? lane * (int)(256 * FSTR) : OOB, WS_F + b * FSTR, SC1);
            lwait(lds, L_P2, 1u);
            __builtin_amdgcn_s_sleep(8);
            u2 f[4]; u32 tg[4];
            UNROLL for (int j = 0; j < 4; ++j) tg[j] = tag_of(ep0, 4 * lane + j + 1);
            NOUNROLL for (int spin = 0; spin < SPIN_CAP; ++spin) {
                MEMBAR();
                UNROLL for (int j = 0; j < 4; ++j) f[j] = __builtin_amdgcn_raw_buffer_load_b64(rws, lane * (4 * FSTR) + j * FSTR, WS_F + (b & 7) * (256 * FSTR), SC1);
                if (__all(f[0].y == tg[0] && f[1].y == tg[1] && f[2].y == tg[2] && f[3].y == tg[3])) break;
                __builtin_amdgcn_s_sleep(8);
            }
            const u4 e4 = {f[0].x, f[1].x, f[2].x, f[3].x};
            const u32 gx = wave_maxu(max4u(e4 & 0xffu)), gwm = wave_maxu(max4u((e4 >> 8) & 0xffu));
            const bool redo = gx > lget(lds, L_SX) || gwm > lwait4(lds, L_PR, 1u);
            lset(lds, L_VD, gx | (gwm << 8) | (redo ? 0x20000u : 0x10000u), lane);
            if (b == 0 && lane == 0) __builtin_amdgcn_raw_buffer_store_b32(ep0 + (redo ? 1u : 0u), rws, 0, WS_E, SC1);
        } else if (helper) {
            __builtin_amdgcn_s_setprio(2);
            u32 ew = lwait4(lds, L_PR, 1u);
            NOUNROLL for (u32 pass = 0; pass < 2; ++pass) {
                u4 WH[2];
                const float sw = __builtin_bit_cast(float, (249u - ew) << 23);
                UNROLL for (int r = 0; r < 4; ++r) UNROLL for (int f = 0; f < 2; ++f) {
                    const unsigned short* wp = (const unsigned short*)&lds[L_W + (n * 4 + 2 * f) * 256 + (4 * q + r) * 16] + 16 + cls;
                    float re = bflo(wp[0]) * sw, im = bflo(wp[512]) * sw;
                    pin(re);
                    WH[f][r] = pkh(re, im);
                }
                pin(WH);
                lwait(lds, L_ZL, pass + 1u);
                const u32 tg = tag_of(ep0 + pass, 0u);
                u4 hz;
                NOUNROLL for (int spin = 0; spin < SPIN_CAP; ++spin) {
                    MEMBAR();
                    u32 bad = 0;
                    UNROLL for (int j = 0; j < 4; ++j) { const u2 g = __builtin_amdgcn_raw_buffer_load_b64(rws, (64 * q + n + 16 * j) * HSTR, WS_HB + ((b & 7) >> 1) * (256 * HSTR), SC1); hz[j] = g.x & ~TM; bad |= g.y ^ tg; }
                    if (__all(bad == 0u)) break;
                    __builtin_amdgcn_s_sleep(4);
                }
                const u32 f16 = cls == 0 ? 0x00002c00u : cls == 4 ? 0x2c000000u : cls > 4 ? 0xac002c00u : 0x2c002c00u;
                u4 d, o[2];
                UNROLL for (int j = 0; j < 4; ++j) d[j] = hmul(hz[j], f16);
                f4 yr = mfma16(d, GR), yi = mfma16(d, GI);
                UNROLL for (int r = 0; r < 4; ++r) d[r] = cmul_conj(pkh(yr[r], yi[r]), T1[r]);
                const f4 xr = mfma16(FR, d), xi = mfma16(FI, d);
                UNROLL for (int f = 0; f < 2; ++f) {
                    UNROLL for (int r = 0; r < 4; ++r) d[r] = cmul_swap(pkh(xr[r], xi[r]), WH[f][r]);
                    yr = mfma16(d, FR); yi = mfma16(d, FI);
                    UNROLL for (int r = 0; r < 4; ++r) o[f][r] = cmul_conj(pkh(yr[r], yi[r]), T2[r]);
                }
                const f4 p0 = mfma16(HR, o[0]), p1 = mfma16(HI, o[0]), p2 = mfma16(HR, o[1]), p3 = mfma16(HI, o[1]);
                UNROLL for (int r = 0; r < 4; ++r)
                    store128<SC1>(u4{pkh(p0[r], p1[r]) & ~TM, tg, pkh(p2[r], p3[r]) & ~TM, tg}, rws, (64 * q + n) * 16 + WS_VL + r * 256 + cls * 4096);
                const u32 vd = lwait(lds, L_VD, 1u);
                if (!(vd & 0x20000u)) break;
                ew = (vd >> 8) & 0xffu;
            }
        }
        asm volatile(".Lgf_end:");
        return;
    }

    {
        const int lane = threadIdx.x & 63, wv = wv0;
        u4 tw; u2 m;
        core_setup(lane, wv, tw, m);
        *(u4*)&lds[L_M + (2 + wv) * 256 + lane * 4] = tw;
        lds[L_M + (wv >> 1) * 256 + lane * 4 + (wv & 1)] = m.x; lds[L_M + (wv >> 1) * 256 + lane * 4 + 2 + (wv & 1)] = m.y;
    }
    SB();
    u32 vd = 0;
    NOUNROLL for (u32 pass = 0; pass < 2; ++pass) {
    int t = threadIdx.x, wv = wv0, b = blockIdx.x;
    pin(t);
    asm volatile("" : "+s"(wv));
    asm volatile("" : "+s"(b));
    const int lane = t & 63, q = lane >> 4, n = lane & 15;
    const u32 pv = pass + 1u;
    u4 MR[4], MI[4], TW[4];
    const int zc = perm32(lane & 31) + (lane >> 5) * 256, zcu = zc + ((lane & 31) == 0 ? 543 : 511);
    int zl = (zc >> 4) * 16384 + (zc & 15) * 4, zu = (zcu >> 4) * 16384 + (zcu & 15) * 4;
    int zh = lane == 0 ? 0 : OOB;
    int tro = L_A1 + wv * 1024 + (4 * (lane >> 5) + ((lane & 15) >> 2)) * 16 + ((lane >> 4) & 1) * 8 + (lane & 3) * 2;
    u32 tm = TM, vl0 = valid_bits(0u, zc & 15), vu0 = valid_bits(0u, zcu & 15);
    pin(zl); pin(zu); pin(zh); pin(tro); pin(tm); pin(vl0); pin(vu0);
    SB();
    pin(smp);
    u32 ex;
    {
        MagMax ms; ms.add(smp.x, smp.y); ms.add(smp.z, smp.w);
        ex = pass ? (vd & 0xffu) : clampe(ms.exponent() + MARGIN);
    }
    u32 epoch, tag0, flip;
    u32 z[16], z512;
    {
        const float sc = __builtin_bit_cast(float, (250u - ex) << 23);
        const float rs = sc * 0.70710678118654752f;
        u32 o[8][2];
        pin(xv);
        UNROLL for (int j = 0; j < 2; ++j) {
            f2 v[8];
            UNROLL for (int s1 = 0; s1 < 8; ++s1) v[s1] = f2{bflo(xv[s1][j]), bfhi(xv[s1][j])};
            const f2 a0 = v[0] + v[4], a1 = v[1] + v[5], a2 = v[2] + v[6], a3 = v[3] + v[7];
            const f2 b0 = (v[0] - v[4]) * sc, b1 = v[1] - v[5], b2_ = (v[2] - v[6]) * sc, b3 = v[3] - v[7];
            const f2 e0 = a0 + a2, e1 = (a1 + a3) * sc, dm = b1 - b3, dp = b1 + b3;
            f2 r[8];
            r[0] = e0 * sc + e1; r[1] = e0 * sc - e1;
            r[2] = dm * rs + b0; r[3] = -(dp * rs) - b2_;
            r[4] = (a0 - a2) * sc; r[5] = (a3 - a1) * sc;
            r[6] = b0 - dm * rs; r[7] = b2_ - dp * rs;
            UNROLL for (int p = 0; p < 8; ++p) o[p][j] = pkh(r[p].x, r[p].y);
        }
        UNROLL for (int p = 0; p < 8; ++p) *(u2*)&lds[L_A1 + p * 512 + 2 * t] = u2{o[p][0], o[p][1]};
        lset(lds, L_C1 + wv, pv, lane);
        pin(ev);
        epoch = rfl(ev) + pv; tag0 = tag_of(epoch, 0u); flip = (epoch & 1u) ? TM : 0u;
        const u32 vl = vl0 ^ flip, vu = vu0 ^ flip;
        u4 in[4];
        NOUNROLL for (int spin = 0; spin < LSPIN_CAP; ++spin) {
            MEMBAR();
            const u4 c = *(const u4*)&lds[L_C1];
            MEMBAR();
            MR[0] = *(const u4*)&lds[L_M + lane * 4]; MR[1] = *(const u4*)&lds[L_M + 256 + lane * 4];
            UNROLL for (int s = 0; s < 4; ++s) {
                const u2 re = __builtin_bit_cast(u2, __builtin_amdgcn_ds_read_tr16_b64_v4i16((LAS s4*)&lds[tro + s * 128]));
                const u2 im = __builtin_bit_cast(u2, __builtin_amdgcn_ds_read_tr16_b64_v4i16((LAS s4*)&lds[tro + s * 128 + 512]));
                in[s] = u4{re.x, re.y, im.x, im.y};
            }
            pin(MR[0]); pin(MR[1]);
            if (rfl(min4u(c)) >= pv) break;
        }
        UNROLL for (int i = 0; i < 4; ++i) TW[i] = *(const u4*)&lds[L_M + (2 + i) * 256 + lane * 4];
        core_expand(lane, MR, MI);
        f16v zr, zi;
        core_fft<true>(in, MR, MI, TW, zr, zi);
        UNROLL for (int r = 0; r < 16; ++r) z[r] = pkh(zr[r], zi[r]);
        z512 = z[8];
        const u32 za = __builtin_amdgcn_readlane(z[8], 32), zb = __builtin_amdgcn_readlane(z[0], 0);
        MEMBAR();
        const u32 zt = lane < 32 ? za : zb;
        UNROLL for (int r = 0; r < 8; ++r) z[r] = mark(z[r], vl, tm);
        UNROLL for (int r = 8; r < 16; ++r) z[r] = mark((lane & 31) == 0 ? (r < 15 ? z[r + 1] : zt) : z[r], vu, tm);
    }
    pin(z);
    const int zs = WS_Z + wv * 64 * 16384 + (((b >> 2) & 3) * 64 + (b & 3) * 16 + ((b >> 4) & 3) * 4 + (b >> 6)) * 64;
    bool zsent = false;
#define TRY_STORE_Z(force) do { if (!zsent) { \
        bool go = true; \
        if (force) lwait4(lds, L_LD, 1u); else { MEMBAR(); go = rfl(min4u(*(const u4*)&lds[L_LD])) != 0u; MEMBAR(); } \
        if (go) { \
            UNROLL for (int r = 0; r < 16; ++r) __builtin_amdgcn_raw_buffer_store_b32(z[r], rws, r < 8 ? zl : zu, zs + (r & 7) * 32768, SC1); \
            __builtin_amdgcn_raw_buffer_store_b64(u2{z512, tag0}, rws, zh, WS_HB + (wv * 256 + b) * HSTR, SC1); \
            zsent = true; } } SB(); } while (0)
    TRY_STORE_Z(false);
    const int row = (b & 7) >> 1, cls = cls_of(b), tile = tile_of(b);
    const bool packed = row == 0;
    u4 FR, FI, GR[4], GI[4], HR[4], HI[4], sel1;
    {
        const float ofs = cls == 4 ? 0.25f : 0.0f;
        float gu = (float)(cls + 8 * n) * (1.0f / 2048.0f), g0 = (float)(64 * q + 4 * wv) * gu + ofs;
        u32 s16 = 0x2c002c00u;
        pin(gu); pin(g0); pin(s16);
        UNROLL for (int i = 0; i < 4; ++i) UNROLL for (int j = 0; j < 4; ++j) { const u32 g = rot(fmaf((float)(16 * j + i), gu, g0)); GR[i][j] = hmul(g, s16); GI[i][j] = quarter(g, s16); }
    }
    pin(GR); pin(GI);
    {
        MagMax m;
        UNROLL for (int s1 = 0; s1 < 8; ++s1) m.add(xv[s1].x, xv[s1].y);
        if (wv == 0) lset(lds, L_SX, ex, lane);
        lset(lds, L_EW + wv, clampe(m.exponent()), lane);
    }
    TRY_STORE_Z(false);
    UNROLL for (int j = 0; j < 4; ++j) FR[j] = rot((float)(n * (4 * q + j)) * (1.0f / 16.0f));
    FI = quarter(FR);
    {
        float hs[4], hu[4];
        UNROLL for (int i = 0; i < 4; ++i) { hs[i] = (float)(16 * n + 4 * wv + i) * (1.0f / 2048.0f); hu[i] = (float)(cls + 32 * q + 8 * i); }
        pin(hs); pin(hu);
        UNROLL for (int i = 0; i < 4; ++i) { UNROLL for (int j = 0; j < 4; ++j) { float a = hs[i] * hu[j]; pin(a); HR[i][j] = rot(a); } HI[i] = quarter(HR[i]); }
        const int d = n + cls - 8 * q;
        UNROLL for (int j = 0; j < 4; ++j) sel1[j] = (d >> 1) == j ? ((d & 1) ? 0xffff0000u : 0xffffu) : 0u;
    }
    pin(FR); pin(FI); pin(HR); pin(HI); pin(sel1);
    TRY_STORE_Z(true);
    VMWAIT(4);
    u4 WA[4], WB[4];
    u32 ew = 0;
    UNROLL for (int i = 0; i < 4; ++i) asm volatile("" : "=v"(WA[i]), "=v"(WB[i]));
#define WIDEN(es) do { \
        u4 wr[4][4]; \
        int wl = L_W / 4 + wv * 1024 + n * 4 + min(q, 2); pin(wl); \
        UNROLL for (int i = 0; i < 4; ++i) UNROLL for (int a = 0; a < 4; ++a) wr[i][a] = ((const u4*)lds)[wl + (i * 4 + a) * 64]; \
        ew = pass ? (vd >> 8) & 0xffu : (es); \
        const u32 sw = ((249u - ew - (packed ? 1u : 0u)) << 7) * 0x10001u;       \
        const u4 sels = sel1 & sw; \
        SB(); \
        f4 wf[4][4]; \
        UNROLL for (int k = 0; k < 10; ++k) { \
            if (k < 8) { wf[k >> 1][2 * (k & 1)] = mfma16b(wr[k >> 1][2 * (k & 1)], sels, f4{}); wf[k >> 1][2 * (k & 1) + 1] = mfma16b(wr[k >> 1][2 * (k & 1) + 1], sels, f4{}); } \
            if (k >= 2) { const int i_ = (k - 2) >> 1, f_ = k & 1; UNROLL for (int r = 0; r < 4; ++r) (f_ ? WB : WA)[i_][r] = pkh(wf[i_][2 * f_][r], wf[i_][2 * f_ + 1][r]); } \
            SB(); } \
        pin(WA); pin(WB); } while (0)
    MEMBAR();
    const u4 pr0 = *(const u4*)&lds[L_PR];
    const bool wearly = rfl(min4u(pr0)) != 0u;
    MEMBAR();
    if (wearly) WIDEN(rfl(max4u(pr0)));
    const u32 vn = valid_bits(epoch, n), vm = valid_bits(epoch, 15 - n);
    u32 x1 = cls > 4 ? vm ^ 0x80000000u : vn, x2 = vm ^ (cls == 4 ? 0x00008000u : cls == 0 ? 0x80000000u : 0u);
    u32 z1[4][4], z2[4][4];
    const int zvm = q * 64 + (63 - tile) * 16384 + (15 - n) * 4;
    const int zv1 = cls <= 4 ? q * 64 + tile * 16384 + n * 4 : zvm;
    int zso = WS_Z + row * 64 * 16384 + wv * 4096;
    asm volatile("" : "+s"(zso));
#define LOAD_Z() do { \
        if (packed) { int a_ = zvm; pin(a_); UNROLL for (int i = 0; i < 4; ++i) UNROLL for (int j = 0; j < 4; ++j) z2[i][j] = __builtin_amdgcn_raw_buffer_load_b32(rws, a_ + (4 * i + j) * 256, zso, SC1); } \
        else { UNROLL for (int i = 0; i < 4; ++i) UNROLL for (int j = 0; j < 4; ++j) z2[i][j] = x2; } \
        int c_ = zv1; pin(c_); UNROLL for (int i = 0; i < 4; ++i) UNROLL for (int j = 0; j < 4; ++j) z1[i][j] = __builtin_amdgcn_raw_buffer_load_b32(rws, c_ + (4 * i + j) * 256, zso, SC1); } while (0)
    pin(x1); pin(x2);
    VMWAIT(0);
    LOAD_Z();
    if (wv == 0) lset(lds, L_ZL, pv, lane);
    SB();
    const int lw = ((wv * 16 + 4 * q) * 16 + n) * 4;
    int lr = (q * 16 + 4 * wv) * 16 + n;
    const u4* l4 = (const u4*)lds;
    pin(lr);
    SB();
    {
        f4 yr[4], yi[4];
        NOUNROLL for (int spin = 0; spin < SPIN_CAP; ++spin) {
            u32 m = 0;
            UNROLL for (int i = 0; i < 4; ++i) {
                u4 d;
                UNROLL for (int j = 0; j < 4; ++j) { z1[i][j] ^= x1; z2[i][j] ^= x2; }
                UNROLL for (int j = 0; j < 4; ++j) d[j] = U(H(z1[i][j]) + H(z2[i][j]));
                UNROLL for (int j = 0; j < 4; ++j) m |= z1[i][j] | z2[i][j];
                yr[i] = mfma16(GR[i], d); yi[i] = mfma16(GI[i], d);
                SB();
            }
            if (__all((m & TM) == 0u)) break;
            MEMBAR();
            LOAD_Z();
        }
        u4 y[4];
        UNROLL for (int i = 0; i < 4; ++i) UNROLL for (int r = 0; r < 4; ++r) y[r][i] = pkh(yr[i][r], yi[i][r]);
        UNROLL for (int r = 0; r < 4; ++r) *(u4*)&lds[L_A2 + lw + r * 64] = y[r];
        lset(lds, L_C3 + wv, pv, lane);
    }
    if (!wearly) { const u32 es = lwait4(lds, L_PR, 1u); WIDEN(es); }
    {
        u4 d[4];
        NOUNROLL for (int spin = 0; spin < LSPIN_CAP; ++spin) {
            MEMBAR();
            const u4 c = *(const u4*)&lds[L_C3];
            MEMBAR();
            UNROLL for (int i = 0; i < 4; ++i) d[i] = l4[L_A2 / 4 + lr + i * 16];
            pin(d[0]);
            if (rfl(min4u(c)) >= pv) break;
        }
        pin(WA); pin(WB);
        f4 xr[4], xi[4];
        u4 xx[4], ya[4], yb[4];
        UNROLL for (int i = 0; i < 4; ++i) {
            xr[i] = mfma16(FR, d[i]); xi[i] = mfma16(FI, d[i]);
            if (i >= 2) { UNROLL for (int r = 0; r < 4; ++r) xx[i - 2][r] = pkh(xr[i - 2][r], xi[i - 2][r]); pin(xx[i - 2]); }
            SB();
        }
        UNROLL for (int r = 0; r < 4; ++r) ya[0][r] = cmul_swap(xx[0][r], WA[0][r]);
        UNROLL for (int i = 2; i < 4; ++i) { UNROLL for (int r = 0; r < 4; ++r) xx[i][r] = pkh(xr[i][r], xi[i][r]); pin(xx[i]); }
        SB();
        f4 p0[4], p1[4], p2[4], p3[4];
        UNROLL for (int i = 0; i < 4; ++i) {
            p0[i] = mfma16(FR, ya[i]); p1[i] = mfma16(FI, ya[i]);
            if (i < 3) { UNROLL for (int r = 0; r < 4; ++r) ya[i + 1][r] = cmul_swap(xx[i + 1][r], WA[i + 1][r]); }
            else { UNROLL for (int r = 0; r < 4; ++r) yb[0][r] = cmul_swap(xx[0][r], WB[0][r]); }
            SB();
        }
        u4 ra[4], rb[4];
        UNROLL for (int i = 0; i < 4; ++i) {
            p2[i] = mfma16(FR, yb[i]); p3[i] = mfma16(FI, yb[i]);
            if (i < 3) { UNROLL for (int r = 0; r < 4; ++r) yb[i + 1][r] = cmul_swap(xx[i + 1][r], WB[i + 1][r]); }
            UNROLL for (int k = 0; k < 4; ++k) ra[i][k] = pkh(p0[k][i], p1[k][i]);
            *(u4*)&lds[L_B + lw + i * 64] = ra[i];
            SB();
        }
        UNROLL for (int i = 0; i < 3; ++i) UNROLL for (int r = 0; r < 4; ++r) rb[r][i] = pkh(p2[i][r], p3[i][r]);
        SB();
        UNROLL for (int r = 0; r < 4; ++r) { rb[r][3] = pkh(p2[3][r], p3[3][r]); *(u4*)&lds[L_B + 4096 + lw + r * 64] = rb[r]; }
        lset(lds, L_C4 + wv, pv, lane);
    }
    {
        u4 da[4], db[4];
        NOUNROLL for (int spin = 0; spin < LSPIN_CAP; ++spin) {
            MEMBAR();
            const u4 c = *(const u4*)&lds[L_C4];
            MEMBAR();
            UNROLL for (int i = 0; i < 4; ++i) { da[i] = l4[L_B / 4 + lr + i * 16]; db[i] = l4[L_B / 4 + 1024 + lr + i * 16]; }
            pin(da[0]);
            if (rfl(min4u(c)) >= pv) break;
        }
        const int vo = 64 * q * 32768 + n * 8, so = WS_V + 4 * wv * 32768 + cls * 4096 + tile * 128;
        f4 p0[4], p1[4], p2[4], p3[4];
        UNROLL for (int i = 0; i < 5; ++i) {
            if (i < 4) { p0[i] = mfma16(HR[i], da[i]); p1[i] = mfma16(HI[i], da[i]); p2[i] = mfma16(HR[i], db[i]); p3[i] = mfma16(HI[i], db[i]); }
            if (i > 0) { UNROLL for (int r = 0; r < 4; ++r)
                __builtin_amdgcn_raw_buffer_store_b64(u2{mark(pkh(p0[i - 1][r], p1[i - 1][r]), vn, tm), mark(pkh(p2[i - 1][r], p3[i - 1][r]), vn, tm)}, rws, vo, so + (16 * r + i - 1) * 32768, SC1); }
            SB();
        }
    }
    if (wv == 0) lset(lds, L_P2, 1u, lane);
    SB();
    {
        pin(xv); pin(gv);
        const f2 g01 = {bflo(gv.x), bfhi(gv.x)}, g23 = {bflo(gv.y), bfhi(gv.y)};
        f4 acc = {};
        UNROLL for (int i = 0; i < 4; ++i) {
            u4 pp;
            UNROLL for (int k = 0; k < 2; ++k) {
                const f2 a = f2{bflo(xv[2 * i + k].x), bfhi(xv[2 * i + k].x)} * g01, c = f2{bflo(xv[2 * i + k].y), bfhi(xv[2 * i + k].y)} * g23;
                pp[2 * k] = pkb(a.x, a.y); pp[2 * k + 1] = pkb(c.x, c.y);
            }
            const u32 s0 = n == 2 * i ? 0x3f803f80u : 0u, s1 = n == 2 * i + 1 ? 0x3f803f80u : 0u;
            acc = mfma16b(pp, u4{s0, s0, s1, s1}, acc);
        }
        if (n < 8) lds[L_GS + n * 16 + (t >> 4)] = __builtin_bit_cast(u32, (acc.x + acc.y) + (acc.z + acc.w));
        lset(lds, L_C5 + wv, pv, lane);
    }
    MR[0] = *(const u4*)&lds[L_M + lane * 4]; MR[1] = *(const u4*)&lds[L_M + 256 + lane * 4];
    UNROLL for (int i = 0; i < 4; ++i) TW[i] = *(const u4*)&lds[L_M + (2 + i) * 256 + lane * 4];
    core_expand(lane, MR, MI);
    pin(MR); pin(MI); pin(TW);
    u4 PM;
    {
        lwait4(lds, L_C5, pv);
        float gg[4];
        UNROLL for (int k = 0; k < 2; ++k) {
            const f4* pp = (const f4*)&lds[L_GS + ((n >> 2) * 2 + k) * 16];
            const f4 ps = (pp[0] + pp[1]) + (pp[2] + pp[3]);
            const float lg = rbf((ps.x + ps.y) + (ps.z + ps.w));
            gg[2 * k] = rbf(1.0f / (1.0f + __expf(-lg)));
            gg[2 * k + 1] = rbf(1.0f - gg[2 * k]);
        }
        UNROLL for (int k = 0; k < 4; ++k) { gg[k] *= 0.03125f; pin(gg[k]); }
        const int r = n & 3, sa = (n >> 2) * 2;
        UNROLL for (int cc = 0; cc < 2; ++cc) {
            const int u1 = 2 * q + cc;
            const float ta = (float)(sa * u1) * 0.125f + ((r & 1) ? 0.25f : (r & 2) ? 0.5f : 0.0f);
            const float tb = (float)((sa + 1) * u1) * 0.125f + ((r & 1) ? ((r & 2) ? 0.0f : 0.5f) : 0.25f);
            const float ca = cosr(ta), sa_ = sinr(ta), cb = cosr(tb), sb_ = sinr(tb);
            float e[4] = {gg[2] * cb, gg[2] * sb_, gg[3] * cb, gg[3] * sb_};
            pin(e);
            e[0] = fmaf(gg[0], ca, e[0]); pin(e[0]); e[1] = fmaf(gg[0], sa_, e[1]); pin(e[1]); e[2] = fmaf(gg[1], ca, e[2]); pin(e[2]); e[3] = fmaf(gg[1], sa_, e[3]);
            PM[2 * cc] = pkh(e[0], e[1]); PM[2 * cc + 1] = pkh(e[2], e[3]);
        }
    }
    int a3d = (q * 8 + wv) * 32 + n, a3m = (q * 8 + 7 - wv) * 32 + 16 - n, a3z = (q * 8 + 7 - wv) * 32 + ((32 - n) & 31), a3r = wv * 256 + lane;
    float osc = __builtin_bit_cast(float, (u32)min(max((int)ex + (int)ew - 130, 1), 254) << 23) * TRUNC_FIX;
    const int ve = 2 * q * 4096 + (128 * wv + n) * 8;
    int vs0 = WS_V + b * 32768, vs1 = vs0 + 4096, vsl = WS_VL + b * 16;
    asm volatile("" : "+s"(vs0), "+s"(vs1), "+s"(vsl));
    int ov = (4 * perm32(lane & 15) + 2 * ((lane >> 4) & 1) + (lane >> 5)) * 16 + (512 * wv + b) * 2048;
    u32 x8 = wv == 3 ? 0u : vn;
    pin(a3d); pin(a3m); pin(a3z); pin(a3r); pin(PM); pin(ov); pin(osc); pin(x8);
    VMWAIT(0);
    {
        f4 z3[9];
        NOUNROLL for (int spin = 0; spin < SPIN_CAP; ++spin) {
            MEMBAR();
            u2 v0[8], v1[8];
            u4 ve8, vo8;
            int a_ = ve; pin(a_);
            if (wv == 3) {
                ve8 = __builtin_amdgcn_raw_buffer_load_b128(rws, q * 8192, vsl, SC1); vo8 = __builtin_amdgcn_raw_buffer_load_b128(rws, q * 8192 + 4096, vsl, SC1);
            } else {
                const u2 e2 = __builtin_amdgcn_raw_buffer_load_b64(rws, a_ + 1024, vs0, SC1), o2 = __builtin_amdgcn_raw_buffer_load_b64(rws, a_ + 1024, vs1, SC1);
                ve8 = u4{e2.x, e2.y, tag0, tag0}; vo8 = u4{o2.x, o2.y, tag0, tag0};
            }
            UNROLL for (int i = 0; i < 8; ++i) {
                v0[i] = __builtin_amdgcn_raw_buffer_load_b64(rws, a_ + 128 * i, vs0, SC1);
                v1[i] = __builtin_amdgcn_raw_buffer_load_b64(rws, a_ + 128 * i, vs1, SC1);
            }
            SB();
            u32 m = (((wv == 3 ? ve8.y : ve8.z) ^ tag0) | (ve8.w ^ tag0) | ((wv == 3 ? vo8.y : vo8.z) ^ tag0) | (vo8.w ^ tag0)) ? TM : 0u;
            UNROLL for (int k = 0; k < 9; ++k) {
                const int i = (k + 8) % 9;
                const u4 d = i == 8 ? u4{ve8.x, wv == 3 ? ve8.z : ve8.y, vo8.x, wv == 3 ? vo8.z : vo8.y} ^ x8 : u4{v0[i & 7].x, v0[i & 7].y, v1[i & 7].x, v1[i & 7].y} ^ vn;
                m |= d.x | d.y; m |= d.z | d.w;
                z3[i] = mfma16(PM, d);
                SB();
            }
            if (__all((m & TM) == 0u)) break;
        }
        if (wv == 0 && n == 0) { z3[0].x = 0.5f * (z3[0].x + z3[0].z); z3[0].y = 0.5f * (z3[0].y + z3[0].w); }
        if (wv == 3) { z3[8].z = 0.5f * (z3[8].x + z3[8].z); z3[8].w = 0.5f * (z3[8].y + z3[8].w); }
        u4* l4w = (u4*)&lds[L_A1];
        UNROLL for (int p = 0; p < 2; ++p)
            l4w[a3d + 16 * p] = u4{pkh(z3[p].x, z3[p + 2].x), pkh(z3[p + 4].x, z3[p + 6].x), pkh(z3[p].y, z3[p + 2].y), pkh(z3[p + 4].y, z3[p + 6].y)};
        l4w[a3m] = u4{pkh(z3[7].z, z3[5].z), pkh(z3[3].z, z3[1].z), pkh(z3[7].w, z3[5].w), pkh(z3[3].w, z3[1].w)};
        f2 m[4];
        UNROLL for (int i = 0; i < 4; ++i) m[i] = n == 0 ? f2{z3[8 - 2 * i].z, z3[8 - 2 * i].w} : f2{z3[6 - 2 * i].z, z3[6 - 2 * i].w};
        l4w[a3z] = u4{pkh(m[0].x, m[1].x), pkh(m[2].x, m[3].x), pkh(m[0].y, m[1].y), pkh(m[2].y, m[3].y)};
        lset(lds, L_C6 + wv, 2u * pv - 1u, lane);
    }
    {
        u4 in[4];
        NOUNROLL for (int spin = 0; spin < LSPIN_CAP; ++spin) {
            MEMBAR();
            const u4 c = *(const u4*)&lds[L_C6];
            MEMBAR();
            UNROLL for (int s = 0; s < 4; ++s) in[s] = ((const u4*)&lds[L_A1])[a3r + s * 64];
            pin(in[0]);
            if (rfl(min4u(c)) >= 2u * pv - 1u) break;
        }
        f16v zr, zi;
        core_fft<false>(in, MR, MI, TW, zr, zi);
        zr *= osc; zi *= osc;
        const rsrc_t ro = mkrs(out, 2048 * 2048);
#define OUT_ROW(z, off) do { u4 lo, hi; \
            UNROLL for (int j = 0; j < 4; ++j) { const auto sw_ = __builtin_amdgcn_permlane16_swap(pkb(z[2 * j], z[2 * j + 1]), pkb(z[8 + 2 * j], z[8 + 2 * j + 1]), false, false); lo[j] = sw_[0]; hi[j] = sw_[1]; } \
            store128<SC1>(lo, ro, ov + (off)); store128<SC1>(hi, ro, ov + (off) + 1024); } while (0)
        OUT_ROW(zr, 256 * 2048);
        OUT_ROW(zi, 0);
    }
    vd = lwait(lds, L_VD, 1u);
    if (pass == 1 || !(vd & 0x20000u)) break;
    lset(lds, L_C6 + wv, 2u, lane);
    lwait4(lds, L_C6, 2u);
    VMWAIT(0);
    }
}

extern "C" void kernel_launch(void* const* d_in, const int* in_sizes, int n_in,
                              void* d_out, int out_size, void* d_ws, size_t ws_size,
                              hipStream_t stream) {
    gfnet_fused<<<256, 384, 0, stream>>>(d_in[0], d_in[6], d_in[1], d_in[2], d_in[4], d_in[5], (char*)d_ws, d_out);
}
```
